```python
import torch

def _softmax(x, axis=-1):
    return torch.softmax(x, dim=axis)

def _gelu(x):
    return torch.nn.functional.gelu(x, approximate='tanh')

def _silu(x):
    return torch.nn.functional.silu(x)

def _sigmoid(x):
    return torch.sigmoid(x)

def _layernorm(x, axis=-1, eps=1e-05):
    m = x.mean(dim=axis, keepdim=True)
    v = x.var(dim=axis, keepdim=True, unbiased=False)
    return (x - m) / torch.sqrt(v + eps)

def _scatter_add(tgt, idx, src, axis=0):
    return tgt.clone().index_add_(axis, idx, src)

def _segment_sum(src, seg, n):
    out = torch.zeros((n,) + src.shape[1:], dtype=src.dtype)
    return out.index_add_(0, seg, src)

def _pad(x, pw):
    flat = tuple((v for pair in reversed(pw) for v in pair))
    return torch.nn.functional.pad(x, flat)
kernel_name = 'adaLN_cond__ssm_conv_s4__dense_activation_silu__prompt'

def setup_inputs():

    _w = lambda a: (lambda t: t.to(torch.bfloat16) if t.is_floating_point() else t)(torch.as_tensor(a))

    def _A_setup():
        B, L, D, N = (1, 2048, 512, 128)
        A_re = -(lambda _x: torch.exp(torch.as_tensor(_x)))((lambda *s: torch.randn(*s, dtype=torch.bfloat16) * 3.2)(D, N))
        A_im = torch.pi * (lambda *s: torch.randn(*s, dtype=torch.bfloat16) * 0.05)(D, N)
        B_re = (lambda *s: torch.randn(*s, dtype=torch.bfloat16) * 0.1)(D, N)
        B_im = (lambda *s: torch.randn(*s, dtype=torch.bfloat16) * 0.1)(D, N)
        C_re = (lambda *s: torch.randn(*s, dtype=torch.bfloat16) * 0.1)(D, N)
        C_im = (lambda *s: torch.randn(*s, dtype=torch.bfloat16) * 0.1)(D, N)
        log_dt = (lambda *s: torch.randn(*s, dtype=torch.bfloat16) * 0.05)(D) * 0.5 - 4.0
        Dskip = (lambda *s: torch.randn(*s, dtype=torch.bfloat16) * 0.05)(D)
        return {'u': _w((lambda *s: torch.randn(*s, dtype=torch.bfloat16) * 0.05)(B, L, D)), 'A_re': _w(A_re), 'A_im': _w(A_im), 'B_re': _w(B_re), 'B_im': _w(B_im), 'C_re': _w(C_re), 'C_im': _w(C_im), 'log_dt': _w(log_dt), 'Dskip': _w(Dskip)}
    _A_in = _A_setup()

    def _B_setup():
        B, S, D, F = (1, 2048, 512, 1792)
        return {'x': _w((lambda *s: torch.randn(*s, dtype=torch.bfloat16) * 0.05)(B, S, D)), 'w1': _w((lambda *s: torch.randn(*s, dtype=torch.bfloat16) * 3.2)(D, F) * D ** (-0.5)), 'w2': _w((lambda *s: torch.randn(*s, dtype=torch.bfloat16) * 0.4)(F, D) * F ** (-0.5))}
    _B_in = _B_setup()
    return {'A_u': _A_in['u'], 'A_A_re': _A_in['A_re'], 'A_A_im': _A_in['A_im'], 'A_B_re': _A_in['B_re'], 'A_B_im': _A_in['B_im'], 'A_C_re': _A_in['C_re'], 'A_C_im': _A_in['C_im'], 'A_log_dt': _A_in['log_dt'], 'A_Dskip': _A_in['Dskip'], 'B_x': _B_in['x'], 'B_w1': _B_in['w1'], 'B_w2': _B_in['w2'], 'c': _w((lambda *s: torch.randn(*s, dtype=torch.bfloat16) * 0.2)(1, 512) * 8.0), 'w_mod': _w((lambda *s: torch.randn(*s, dtype=torch.bfloat16) * 0.2)(512, 6 * 512) * 512 ** (-0.5) * 8.0)}

def reference(A_u, A_A_re, A_A_im, A_B_re, A_B_im, A_C_re, A_C_im, A_log_dt, A_Dskip, B_x, B_w1, B_w2, c, w_mod):

    def _A_ref(u, A_re, A_im, B_re, B_im, C_re, C_im, log_dt, Dskip):
        Bsz, L, D = u.shape
        dt = torch.exp(log_dt)[:, None]
        A = A_re + 1j * A_im
        Bc = B_re + 1j * B_im
        Cc = C_re + 1j * C_im
        logdA = dt * A
        dB = dt * Bc
        l = (lambda x, dt: x.to(dt))(torch.arange(L), A_re.dtype)
        P = torch.exp(logdA[:, :, None] * l[None, None, :])
        K = torch.einsum('dn,dnl,dn->dl', Cc, P, dB).real
        L2 = 2 * L
        Kp = _pad(K, ((0, 0), (0, L)))
        up = (lambda x, *d: x.permute(*d))(u, 0, 2, 1)
        up = _pad(up, ((0, 0), (0, 0), (0, L)))
        Kf = (lambda x, axes=(-2, -1): torch.fft.rfft2(x.float(), dim=axes))(Kp[:, None, :], axes=(-2, -1))
        Uf = (lambda x, axes=(-2, -1): torch.fft.rfft2(x.float(), dim=axes))(up[:, :, None, :], axes=(-2, -1))
        Yf = Uf * Kf[None, :, :, :]
        y = (lambda x, s, axes=(-2, -1): torch.fft.irfft2(x, s=s, dim=axes).to(torch.bfloat16))(Yf, (1, L2), axes=(-2, -1))[:, :, 0, :L]
        y = (lambda x, *d: x.permute(*d))(y, 0, 2, 1) + u * Dskip[None, None, :]
        return y

    def _B_ref(x, w1, w2):
        h = torch.einsum('bsd,df->bsf', x, w1)
        h = _silu(h)
        out = torch.einsum('bsf,fd->bsd', h, w2)
        return out
    x = A_u
    mod = (_silu(c) @ w_mod).reshape(c.shape[0], 6, 1, -1)
    sa, ca, ga, sb, cb, gb = (mod[:, 0].clone(), mod[:, 1].clone(), mod[:, 2].clone(), mod[:, 3].clone(), mod[:, 4].clone(), mod[:, 5].clone())
    h = _layernorm(x, axis=-1) * (1 + ca) + sa
    x = x + ga * _A_ref(h, A_A_re, A_A_im, A_B_re, A_B_im, A_C_re, A_C_im, A_log_dt, A_Dskip)
    h = _layernorm(x, axis=-1) * (1 + cb) + sb
    x = x + gb * _B_ref(h, B_w1, B_w2)
    return x
```

```cpp
#include <hip/hip_runtime.h>
#include <hip/hip_bf16.h>
typedef unsigned short u16;
constexpr int L = 2048, D = 512, N = 128, F = 1792;
__device__ __forceinline__ float bf2f(u16 b) { return __uint_as_float((unsigned)b << 16); }
__device__ __forceinline__ u16 f2bf(float f) { __hip_bfloat16 h = __float2bfloat16(f); return *(u16*)&h; }
__device__ __forceinline__ float rb(float f) { return bf2f(f2bf(f)); }

__global__ void k_mod(const u16* c, const u16* w, float* mod) {
  int j = blockIdx.x * blockDim.x + threadIdx.x;
  float acc = 0.f;
  for (int k = 0; k < 512; ++k) { float cv = bf2f(c[k]); float s = rb(cv / (1.f + __expf(-cv))); acc += s * bf2f(w[k * 3072 + j]); }
  mod[j] = rb(acc);
}
__global__ void k_ln(const u16* x, const float* mod, int so, int co, u16* h) {
  int t = blockIdx.x, lane = threadIdx.x;
  float v[8]; float s = 0.f;
  for (int i = 0; i < 8; ++i) { v[i] = bf2f(x[t * D + lane * 8 + i]); s += v[i]; }
  for (int o = 32; o; o >>= 1) s += __shfl_xor(s, o);
  float m = s / D; float q = 0.f;
  for (int i = 0; i < 8; ++i) { float d = v[i] - m; q += d * d; }
  for (int o = 32; o; o >>= 1) q += __shfl_xor(q, o);
  float mb = rb(m), vb = rb(q / D); float sd = rb(sqrtf(rb(vb + 1e-5f)));
  for (int i = 0; i < 8; ++i) { int d = lane * 8 + i; float n = rb(rb(v[i] - mb) / sd); h[t * D + d] = f2bf(rb(n * rb(1.f + mod[co + d])) + mod[so + d]); }
}
__global__ void k_kgen(const u16* Are, const u16* Aim, const u16* Bre, const u16* Bim, const u16* Cre, const u16* Cim, const u16* ldt, float* K) {
  int d = blockIdx.x;
  __shared__ float a[N], b[N], cr[N], ci[N];
  float dt = rb(expf(bf2f(ldt[d])));
  if (threadIdx.x < N) { int n = threadIdx.x; a[n] = dt * bf2f(Are[d * N + n]); b[n] = dt * bf2f(Aim[d * N + n]);
    float br = dt * bf2f(Bre[d * N + n]), bi = dt * bf2f(Bim[d * N + n]), c_r = bf2f(Cre[d * N + n]), c_i = bf2f(Cim[d * N + n]);
    cr[n] = c_r * br - c_i * bi; ci[n] = c_r * bi + c_i * br; }
  __syncthreads();
  for (int l = threadIdx.x; l < L; l += blockDim.x) { float lf = rb((float)l); float acc = 0.f;
    for (int n = 0; n < N; ++n) { float e = __expf(a[n] * lf); float s, c; __sincosf(b[n] * lf, &s, &c); acc += e * (cr[n] * c - ci[n] * s); }
    K[d * L + l] = acc; }
}
__global__ void k_conv(const u16* x, const u16* h, const float* K, const float* mod, const u16* Dsk, u16* x1) {
  int d = blockIdx.x;
  __shared__ float u[L], k[L];
  for (int t = threadIdx.x; t < L; t += blockDim.x) { u[t] = bf2f(h[t * D + d]); k[t] = K[d * L + t]; }
  __syncthreads();
  float ds = bf2f(Dsk[d]), ga = mod[2 * D + d];
  for (int t = threadIdx.x; t < L; t += blockDim.x) { float acc = 0.f;
    for (int j = 0; j <= t; ++j) acc += k[t - j] * u[j];
    float y = rb(rb(acc) + rb(u[t] * ds));
    x1[t * D + d] = f2bf(bf2f(x[t * D + d]) + rb(ga * y)); }
}
__global__ void k_g1(const u16* h2, const u16* w1, u16* H) {
  int f = blockIdx.x * blockDim.x + threadIdx.x, t = blockIdx.y;
  float acc = 0.f;
  for (int k = 0; k < D; ++k) acc += bf2f(h2[t * D + k]) * bf2f(w1[k * F + f]);
  float v = rb(acc);
  H[t * F + f] = f2bf(v / (1.f + __expf(-v)));
}
__global__ void k_g2(const u16* H, const u16* w2, const u16* x1, const float* mod, u16* out) {
  int d = blockIdx.x * blockDim.x + threadIdx.x, t = blockIdx.y;
  float acc = 0.f;
  for (int k = 0; k < F; ++k) acc += bf2f(H[t * F + k]) * bf2f(w2[k * D + d]);
  out[t * D + d] = f2bf(bf2f(x1[t * D + d]) + rb(mod[5 * D + d] * rb(acc)));
}
extern "C" void kernel_launch(void* const* d_in, const int* in_sizes, int n_in, void* d_out, int out_size, void* d_ws, size_t ws_size, hipStream_t stream) {
  const u16* x = (const u16*)d_in[0];
  char* ws = (char*)d_ws;
  float* mod = (float*)ws; u16* h = (u16*)(ws + (1 << 20)); float* K = (float*)(ws + (4 << 20)); u16* x1 = (u16*)(ws + (8 << 20));
  u16* h2 = (u16*)(ws + (12 << 20)); u16* H = (u16*)(ws + (16 << 20));
  k_mod<<<12, 256, 0, stream>>>((const u16*)d_in[12], (const u16*)d_in[13], mod);
  k_ln<<<L, 64, 0, stream>>>(x, mod, 0, D, h);
  k_kgen<<<D, 256, 0, stream>>>((const u16*)d_in[1], (const u16*)d_in[2], (const u16*)d_in[3], (const u16*)d_in[4], (const u16*)d_in[5], (const u16*)d_in[6], (const u16*)d_in[7], K);
  k_conv<<<D, 256, 0, stream>>>(x, h, K, mod, (const u16*)d_in[8], x1);
  k_ln<<<L, 64, 0, stream>>>(x1, mod, 3 * D, 4 * D, h2);
  k_g1<<<dim3(F / 256, L), 256, 0, stream>>>(h2, (const u16*)d_in[10], H);
  k_g2<<<dim3(D / 256, L), 256, 0, stream>>>(H, (const u16*)d_in[11], x1, mod, (u16*)d_out);
}
```

```cpp
#include <hip/hip_runtime.h>
#include <hip/hip_bf16.h>

#define STAMP(ws, k, s)
#define ST_RANGES {{0, 0, 128, "k1 LN"}, {0, 128, 320, "k1 GEMV"}, {0, 320, 832, "k1 Kgen"}, {0, 832, 1056, "k1 W2t"}, {1, 0, 256, "k2 conv"}, {1, 256, 480, "k2 W1t"}, {2, 0, 256, "k3"}, {3, 0, 256, "k4"}}
typedef unsigned short u16;
typedef unsigned int u32;
typedef u32 u32x2 __attribute__((ext_vector_type(2)));
typedef u32 u32x4 __attribute__((ext_vector_type(4)));
typedef float f32x2 __attribute__((ext_vector_type(2)));
typedef float f32x4 __attribute__((ext_vector_type(4)));
typedef __bf16 bf16x2 __attribute__((ext_vector_type(2)));
typedef __bf16 bf16x8 __attribute__((ext_vector_type(8)));
#define GAS __attribute__((address_space(1)))
#define LAS __attribute__((address_space(3)))

constexpr int L = 2048, D = 512, NS = 128, F = 1792;
constexpr int KROW = 2112;

constexpr size_t WS_MODP = 0;
constexpr size_t WS_STAT = WS_MODP + 4 * 3072 * 4;
constexpr size_t WS_S12 = WS_STAT + 2048 * 8;
constexpr size_t WS_XT = 1 << 20;
constexpr size_t WS_KR = WS_XT + (2 << 20);
constexpr size_t WS_X1 = WS_KR + (5 << 20);
constexpr size_t WS_W1 = WS_X1 + (2 << 20);
constexpr size_t WS_W2 = WS_W1 + (2 << 20);
constexpr size_t WS_H = WS_W2 + (2 << 20);

__device__ __forceinline__ float bflo(u32 v) { return __uint_as_float(v << 16); }
__device__ __forceinline__ float bfhi(u32 v) { return __uint_as_float(v & 0xffff0000u); }
__device__ __forceinline__ float bf2f(u16 b) { return __uint_as_float((u32)b << 16); }
__device__ __forceinline__ u32 pk(float lo, float hi) {
  f32x2 v = {lo, hi};
  bf16x2 r = __builtin_convertvector(v, bf16x2);
  return __builtin_bit_cast(u32, r);
}
__device__ __forceinline__ float rb(float f) { return bflo(pk(f, f)); }
__device__ __forceinline__ f32x4 mfma(u32x4 a, u32x4 b, f32x4 c) {
  return __builtin_amdgcn_mfma_f32_16x16x32_bf16(__builtin_bit_cast(bf16x8, a), __builtin_bit_cast(bf16x8, b), c, 0, 0, 0);
}
__device__ __forceinline__ float modval(const float* modp, int j) {
  return rb((modp[j] + modp[3072 + j]) + (modp[2 * 3072 + j] + modp[3 * 3072 + j]));
}

struct P1 {
  const u16 *x, *Are, *Aim, *Bre, *Bim, *Cre, *Cim, *ldt, *c, *wmod, *w2;
  char* ws;
};

constexpr int K1_LN = 128, K1_GV = 192, K1_KG = 512, K1_W2 = 224;

__device__ __forceinline__ void cexp_pow(float a2, float bq, float l, float& re, float& im) {
  float m = __builtin_amdgcn_exp2f(a2 * l);
  float ph = __builtin_amdgcn_fractf(bq * l);
  re = m * __builtin_amdgcn_cosf(ph);
  im = m * __builtin_amdgcn_sinf(ph);
}
__device__ __forceinline__ void split(float re, float im, u32& hi, u32& lo) {
  hi = pk(re, im);
  lo = pk(re - bflo(hi), im - bfhi(hi));
}

__global__ void __launch_bounds__(256) k1_prep(P1 p) {
  __shared__ __attribute__((aligned(16))) float sm[4 * 768];
  __shared__ __attribute__((aligned(16))) u16 kd[656];
  const int tid = threadIdx.x, lane = tid & 63, w = tid >> 6;
  int b = blockIdx.x;
  STAMP(p.ws, 0, 0);
  if (b < K1_LN) {
    const int tb = lane & 3, cb = lane >> 2;
    const int t0 = b * 16 + tb * 4, ch = w * 128 + cb * 8;
    u32x4 v[4];
#pragma unroll
    for (int i = 0; i < 4; ++i) v[i] = *(const u32x4*)(p.x + (size_t)(t0 + i) * D + ch);
    u32* xt = (u32*)(p.ws + WS_XT);
#pragma unroll
    for (int q = 0; q < 4; ++q) {
      u32x4 o = {v[0][q], v[1][q], v[2][q], v[3][q]};
      *(u32x4*)(xt + (size_t)(ch / 2 + q) * L + t0) = o;
    }
    STAMP(p.ws, 0, 1);
    float mean[4], m2[4];
#pragma unroll
    for (int i = 0; i < 4; ++i) {
      float e[8];
#pragma unroll
      for (int q = 0; q < 4; ++q) { e[2 * q] = bflo(v[i][q]); e[2 * q + 1] = bfhi(v[i][q]); }
      float s = ((e[0] + e[1]) + (e[2] + e[3])) + ((e[4] + e[5]) + (e[6] + e[7]));
      s *= 0.125f;
      float q2 = 0.f;
#pragma unroll
      for (int j = 0; j < 8; ++j) { float dlt = e[j] - s; q2 += dlt * dlt; }
      mean[i] = s; m2[i] = q2;
    }
    float cnt = 8.f;
#pragma unroll
    for (int o = 4; o < 64; o <<= 1) {
#pragma unroll
      for (int i = 0; i < 4; ++i) {
        float mo = __shfl_xor(mean[i], o), qo = __shfl_xor(m2[i], o);
        float dlt = mean[i] - mo;
        m2[i] = m2[i] + qo + dlt * dlt * (cnt * 0.5f);
        mean[i] = 0.5f * (mean[i] + mo);
      }
      cnt *= 2.f;
    }
    if (lane < 4) {
#pragma unroll
      for (int i = 0; i < 4; ++i) { sm[(w * 16 + tb * 4 + i) * 2] = mean[i]; sm[(w * 16 + tb * 4 + i) * 2 + 1] = m2[i]; }
    }
    __syncthreads();
    if (tid < 16) {
      float m[4], q[4];
#pragma unroll
      for (int i = 0; i < 4; ++i) { m[i] = sm[(i * 16 + tid) * 2]; q[i] = sm[(i * 16 + tid) * 2 + 1]; }
      float mm = 0.25f * ((m[0] + m[1]) + (m[2] + m[3]));
      float qq = (q[0] + q[1]) + (q[2] + q[3]);
#pragma unroll
      for (int i = 0; i < 4; ++i) { float dlt = m[i] - mm; qq += 128.f * dlt * dlt; }
      float sd = rb(sqrtf(rb(rb(qq * (1.f / D)) + 1e-5f)));
      f32x2 st = {rb(mm), 1.f / sd};
      *(f32x2*)(p.ws + WS_STAT + (size_t)(b * 16 + tid) * 8) = st;
    }
    STAMP(p.ws, 0, 3);
    return;
  }
  b -= K1_LN;
  if (b < K1_GV) {
    const int ks = b / 48, cbk = b % 48;
    const int rg = tid >> 3, cl = tid & 7;
    u32x4 v[4]; float s[4];
#pragma unroll
    for (int i = 0; i < 4; ++i) v[i] = *(const u32x4*)(p.wmod + (size_t)(ks * 128 + i * 32 + rg) * 3072 + cbk * 64 + cl * 8);
#pragma unroll
    for (int i = 0; i < 4; ++i) { float cv = bf2f(p.c[ks * 128 + i * 32 + rg]); s[i] = rb(cv / (1.f + __expf(-cv))); }
    float acc[8];
#pragma unroll
    for (int j = 0; j < 8; ++j) acc[j] = 0.f;
#pragma unroll
    for (int i = 0; i < 4; ++i)
#pragma unroll
      for (int q = 0; q < 4; ++q) { acc[2 * q] += s[i] * bflo(v[i][q]); acc[2 * q + 1] += s[i] * bfhi(v[i][q]); }
#pragma unroll
    for (int o = 8; o < 64; o <<= 1)
#pragma unroll
      for (int j = 0; j < 8; ++j) acc[j] += __shfl_xor(acc[j], o);
    if (lane < 8) {
#pragma unroll
      for (int j = 0; j < 8; ++j) sm[w * 64 + lane * 8 + j] = acc[j];
    }
    __syncthreads();
    if (tid < 64) ((float*)(p.ws + WS_MODP))[ks * 3072 + cbk * 64 + tid] = (sm[tid] + sm[64 + tid]) + (sm[128 + tid] + sm[192 + tid]);
    STAMP(p.ws, 0, 3);
    return;
  }
  b -= K1_GV;
  if (b < K1_KG) {
    const int d = (b & 7) * 64 + (b >> 3);
    const int s = lane & 15, g = lane >> 4;
    const size_t po = (size_t)d * NS + w * 32 + g * 8;
    const u32x4 vAr = *(const u32x4*)(p.Are + po), vAi = *(const u32x4*)(p.Aim + po), vBr = *(const u32x4*)(p.Bre + po),
                vBi = *(const u32x4*)(p.Bim + po), vCr = *(const u32x4*)(p.Cre + po), vCi = *(const u32x4*)(p.Cim + po);
    const float dt = rb(expf(bf2f(p.ldt[d])));
    asm volatile("" :: "v"(vAr), "v"(vAi), "v"(vBr), "v"(vBi), "v"(vCr), "v"(vCi), "v"(dt));
    STAMP(p.ws, 0, 1);
    const float e_col = (float)(16 * s);
    const float e0 = (float)s;
    const float e1 = (float)(s < 8 ? 256 + 2 * s : 512 + 256 * ((s - 8) >> 2) + 4 * (s & 3));
    const float e2 = (float)(s < 8 ? 1024 + 256 * (s >> 1) + 8 * (s & 1) : 2048);
    f32x4 acc[3] = {{0.f, 0.f, 0.f, 0.f}, {0.f, 0.f, 0.f, 0.f}, {0.f, 0.f, 0.f, 0.f}};
#pragma unroll
    for (int ks = 0; ks < 2; ++ks) {
      u32x4 bh, bl, ah[3], al[3];
#pragma unroll
      for (int j = 0; j < 4; ++j) {
        const int q = ks * 2 + (j >> 1);
        const bool hi = j & 1;
        const float ar = hi ? bfhi(vAr[q]) : bflo(vAr[q]), ai = hi ? bfhi(vAi[q]) : bflo(vAi[q]);
        const float br = dt * (hi ? bfhi(vBr[q]) : bflo(vBr[q])), bi = dt * (hi ? bfhi(vBi[q]) : bflo(vBi[q]));
        const float cr = hi ? bfhi(vCr[q]) : bflo(vCr[q]), ci = hi ? bfhi(vCi[q]) : bflo(vCi[q]);
        const float a2 = (dt * ar) * 1.44269504088896341f, bq = (dt * ai) * 0.15915494309189535f;
        const float kr = cr * br - ci * bi, ki = cr * bi + ci * br;
        float re, im;
        u32 h, l;
        cexp_pow(a2, bq, e_col, re, im);
        split(kr * re - ki * im, -(kr * im + ki * re), h, l);
        bh[j] = h; bl[j] = l;
        cexp_pow(a2, bq, e0, re, im); split(re, im, h, l); ah[0][j] = h; al[0][j] = l;
        cexp_pow(a2, bq, e1, re, im); split(re, im, h, l); ah[1][j] = h; al[1][j] = l;
        cexp_pow(a2, bq, e2, re, im); split(re, im, h, l); ah[2][j] = h; al[2][j] = l;
      }
#pragma unroll
      for (int t = 0; t < 3; ++t) {
        acc[t] = mfma(al[t], bh, acc[t]);
        acc[t] = mfma(ah[t], bl, acc[t]);
        acc[t] = mfma(ah[t], bh, acc[t]);
      }
    }
    asm volatile("" :: "v"(acc[0]), "v"(acc[1]), "v"(acc[2]));
    STAMP(p.ws, 0, 2);
#pragma unroll
    for (int t = 0; t < 3; ++t) *(f32x4*)(sm + w * 768 + t * 256 + s * 16 + g * 4) = acc[t];
    __syncthreads();
#pragma unroll
    for (int t = 0; t < 3; ++t) {
      const int i = t * 256 + tid;
      const float v = (sm[i] + sm[768 + i]) + (sm[2 * 768 + i] + sm[3 * 768 + i]);
      const int col = tid >> 4, row = tid & 15;
      int idx;
      if (t == 0) idx = tid;
      else if (t == 1) idx = row < 8 ? 256 + 8 * col + row : 384 + 64 * ((row - 8) >> 2) + 4 * col + (row & 3);
      else idx = row < 8 ? 512 + 32 * (row >> 1) + 2 * col + (row & 1) : (tid == 8 ? 640 : 648);
      kd[idx] = (u16)pk(v, v);
    }
    __syncthreads();
    u32 e[9];
#pragma unroll
    for (int j = 0; j < 9; ++j) {
      const int l = 2047 - (tid * 8 + j);
      const int bits = (int)(pk((float)l, 0.f) & 0xffffu);
      const int idx = l < 128 ? l : bits - 0x4300 + 128;
      e[j] = l < 0 ? 0u : (u32)kd[idx < 0 ? 0 : idx];
    }
    u32x4 c0 = {e[0] | (e[1] << 16), e[2] | (e[3] << 16), e[4] | (e[5] << 16), e[6] | (e[7] << 16)};
    u32x4 c1 = {e[1] | (e[2] << 16), e[3] | (e[4] << 16), e[5] | (e[6] << 16), e[7] | (e[8] << 16)};
    u16* kr = (u16*)(p.ws + WS_KR) + (size_t)d * 2 * KROW;
    *(u32x4*)(kr + tid * 8) = c0;
    *(u32x4*)(kr + KROW + tid * 8) = c1;
    if (tid < 16) {
      u32x4 z = {0u, 0u, 0u, 0u};
      *(u32x4*)(kr + (tid >> 3) * KROW + 2048 + (tid & 7) * 8) = z;
    }
    STAMP(p.ws, 0, 3);
    return;
  }
  b -= K1_KG;
  {
    const int db = b & 7, kp = b >> 3;
    const int dp = tid & 31, g = (tid >> 5) & 3, ks = kp * 2 + (tid >> 7);
    const int j7 = ks / 7, r7 = ks % 7;
    int Ta, Tb;
    if (r7 < 6) { Ta = 14 * j7 + 7 * (r7 / 3) + 2 * (r7 % 3); Tb = Ta + 1; } else { Ta = 14 * j7 + 6; Tb = 14 * j7 + 13; }
    u32 v[8];
#pragma unroll
    for (int j = 0; j < 8; ++j) {
      const int f = 16 * (j < 4 ? Ta : Tb) + 4 * g + (j & 3);
      v[j] = *(const u32*)(p.w2 + (size_t)f * D + db * 64 + dp * 2);
    }
    u32x4 o0, o1;
#pragma unroll
    for (int q = 0; q < 4; ++q) {
      o0[q] = (v[2 * q] & 0xffffu) | (v[2 * q + 1] << 16);
      o1[q] = (v[2 * q] >> 16) | (v[2 * q + 1] & 0xffff0000u);
    }
    u32x4* dst = (u32x4*)(p.ws + WS_W2) + ((size_t)(db * 56 + ks) * 4 + (dp >> 3)) * 64 + g * 16 + (dp & 7) * 2;
    dst[0] = o0; dst[1] = o1;
    STAMP(p.ws, 0, 3);
  }
}

struct P2 {
  const u16 *dskip, *w1;
  char* ws;
};

__global__ void __launch_bounds__(256) k2_conv(P2 p) {
  __shared__ __attribute__((aligned(16))) char smem[32768 + 2 * 4608];
  const int tid = threadIdx.x, lane = tid & 63, w = tid >> 6;
  const float* modp = (const float*)(p.ws + WS_MODP);
  int b = blockIdx.x;
  STAMP(p.ws, 1, 0);
  if (b >= 256) {
    b -= 256;
    const int kb = b & 7, fb = b >> 3;
    const int fp = tid & 31, k8 = tid >> 5;
    const int k0 = kb * 64 + k8 * 8, f = fb * 64 + fp * 2;
    u32 v[8];
#pragma unroll
    for (int j = 0; j < 8; ++j) v[j] = *(const u32*)(p.w1 + (size_t)(k0 + j) * F + f);
    float* sm = (float*)smem;
    if (tid < 128) {
      const int k = kb * 64 + (tid & 63);
      sm[tid] = tid < 64 ? rb(1.f + modval(modp, 4 * D + k)) : modval(modp, 3 * D + k);
    }
    __syncthreads();
    float s1a = 0.f, s1b = 0.f, s2a = 0.f, s2b = 0.f;
    u32x4 o0, o1;
#pragma unroll
    for (int q = 0; q < 4; ++q) {
      const float g0 = sm[k8 * 8 + 2 * q], g1 = sm[k8 * 8 + 2 * q + 1], b0 = sm[64 + k8 * 8 + 2 * q], b1 = sm[64 + k8 * 8 + 2 * q + 1];
      const float a0 = bflo(v[2 * q]), a1 = bflo(v[2 * q + 1]), c0 = bfhi(v[2 * q]), c1 = bfhi(v[2 * q + 1]);
      o0[q] = pk(a0 * g0, a1 * g1);
      o1[q] = pk(c0 * g0, c1 * g1);
      s1a += bflo(o0[q]) + bfhi(o0[q]); s1b += bflo(o1[q]) + bfhi(o1[q]);
      s2a += a0 * b0 + a1 * b1; s2b += c0 * b0 + c1 * b1;
    }
    const int T = f >> 4, cs = T / 7, ft = T % 7, ks = k0 >> 5, g = (k0 >> 3) & 3;
    u32x4* dst = (u32x4*)(p.ws + WS_W1) + ((size_t)(cs * 16 + ks) * 7 + ft) * 64 + g * 16 + (f & 15);
    dst[0] = o0; dst[1] = o1;
    f32x4 sv = {s1a, s1b, s2a, s2b};
    *(f32x4*)(sm + 128 + tid * 4) = sv;
    __syncthreads();
    if (tid < 128) {
      float a = 0.f;
#pragma unroll
      for (int i = 0; i < 8; ++i) a += sm[128 + i * 128 + tid];
      const int which = (tid >> 1) & 1, ff = fb * 64 + (tid >> 2) * 2 + (tid & 1);
      ((float*)(p.ws + WS_S12))[(kb * 2 + which) * F + ff] = a;
    }
    STAMP(p.ws, 1, 5);
    return;
  }
  const int pair = ((b & 7) * 8 + (b >> 5)) * 4 + ((b >> 3) & 3);
  const int r = lane & 15, g = lane >> 4;
  const int ch = w >> 1, half = w & 1;
  const u32* xt = (const u32*)(p.ws + WS_XT) + (size_t)pair * L;
  const f32x2* st = (const f32x2*)(p.ws + WS_STAT);
  const int tb = 512 * w + 64 * g + r;
  u32 xv[8]; f32x2 sv[8];
#pragma unroll
  for (int i = 0; i < 8; ++i) { const int t = tb + 256 * (i >> 2) + 16 * (i & 3); xv[i] = xt[t]; sv[i] = st[t]; }
  const int d0 = pair * 2;
  const float sa0 = modval(modp, d0), sa1 = modval(modp, d0 + 1);
  const float g0 = rb(1.f + modval(modp, D + d0)), g1 = rb(1.f + modval(modp, D + d0 + 1));
  const float ga0 = modval(modp, 2 * D + d0), ga1 = modval(modp, 2 * D + d0 + 1);
  const u32 dsk = *(const u32*)(p.dskip + d0);
  u32x4 T[4][8];
  {
    const int x0 = 2031 - r + 8 * g - 128 * half;
    const int cp = x0 & 1;
    const u16* kr = (const u16*)(p.ws + WS_KR) + (size_t)(d0 + ch) * 2 * KROW + cp * KROW + (x0 - cp);
    typedef u32x4 u32x4a __attribute__((aligned(4)));
#pragma unroll
    for (int e = 0; e < 4; ++e)
#pragma unroll
      for (int a = 0; a < 8; ++a) T[e][a] = *(const u32x4a*)(kr - 32 * e - 256 * a);
  }
  STAMP(p.ws, 1, 1);
  u16* U = (u16*)(smem + 32768);
  if (tid < 64) { u32x4 z = {0u, 0u, 0u, 0u}; *(u32x4*)(U + (tid >> 5) * 2304 + (tid & 31) * 8) = z; }
  u32 hv[8];
#pragma unroll
  for (int i = 0; i < 8; ++i) {
    const int t = tb + 256 * (i >> 2) + 16 * (i & 3);
    const float n0 = (bflo(xv[i]) - sv[i][0]) * sv[i][1], n1 = (bfhi(xv[i]) - sv[i][0]) * sv[i][1];
    hv[i] = pk(n0 * g0 + sa0, n1 * g1 + sa1);
    U[256 + t] = (u16)hv[i];
    U[2304 + 256 + t] = (u16)(hv[i] >> 16);
  }
  STAMP(p.ws, 1, 2);
  __syncthreads();
  f32x4 acc[8];
#pragma unroll
  for (int n = 0; n < 8; ++n) acc[n] = f32x4{0.f, 0.f, 0.f, 0.f};
  const u16* Ub = U + ch * 2304 + 256 + 16 * r - 16 + 8 * g - 128 * half;
#pragma unroll
  for (int e = 0; e < 4; ++e) {
    u32x4 uf[8];
#pragma unroll
    for (int n = 0; n < 8; ++n) uf[n] = *(const u32x4*)(Ub + 256 * n - 32 * e);
#pragma unroll
    for (int a = 0; a < 8; ++a)
#pragma unroll
      for (int n = a; n < 8; ++n) acc[n] = mfma(uf[n - a], T[e][a], acc[n]);
  }
  asm volatile("" :: "v"(acc[0]), "v"(acc[7]));
  STAMP(p.ws, 1, 3);
  f32x4* red = (f32x4*)smem;
#pragma unroll
  for (int n = 0; n < 8; ++n) red[(w * 8 + n) * 64 + lane] = acc[n];
  __syncthreads();
  u32* x1 = (u32*)(p.ws + WS_X1) + (size_t)(pair >> 2) * L * 4 + (pair & 3);
  const float ds0 = bflo(dsk), ds1 = bfhi(dsk);
#pragma unroll
  for (int tl = 0; tl < 2; ++tl) {
    const int n = 2 * w + tl;
    const f32x4 y0 = red[(0 * 8 + n) * 64 + lane] + red[(1 * 8 + n) * 64 + lane];
    const f32x4 y1 = red[(2 * 8 + n) * 64 + lane] + red[(3 * 8 + n) * 64 + lane];
#pragma unroll
    for (int q = 0; q < 4; ++q) {
      const int i = tl * 4 + q;
      const int t = tb + 256 * tl + 16 * q;
      const u32 yc = pk(y0[q], y1[q]);
      const u32 hd = pk(bflo(hv[i]) * ds0, bfhi(hv[i]) * ds1);
      const u32 yy = pk(bflo(yc) + bflo(hd), bfhi(yc) + bfhi(hd));
      const u32 gy = pk(ga0 * bflo(yy), ga1 * bfhi(yy));
      x1[t * 4] = pk(bflo(xv[i]) + bflo(gy), bfhi(xv[i]) + bfhi(gy));
    }
  }
  STAMP(p.ws, 1, 4);
  asm volatile("s_waitcnt vmcnt(0)" ::: "memory");
  STAMP(p.ws, 1, 5);
}

__global__ void __launch_bounds__(256) k3_gemm1(char* ws) {
  extern __shared__ __attribute__((aligned(16))) char smem[];
  const int tid = threadIdx.x, lane = tid & 63, w = tid >> 6;
  const int b = blockIdx.x, j = b >> 3;
  STAMP(ws, 2, 0);
  const int rbk = (b & 7) * 2 + (j & 1), cs = j >> 1;
  const int tl = lane & 15, g = lane >> 4;
  const int t0 = rbk * 128 + w * 32;
  const u32x4* w1 = (const u32x4*)(ws + WS_W1) + (size_t)cs * 112 * 64;
#pragma unroll
  for (int i = 0; i < 28; ++i) {
    const int fr = i * 4 + w;
    __builtin_amdgcn_global_load_lds((const GAS void*)(w1 + fr * 64 + lane), (LAS void*)(smem + fr * 1024), 16, 0, 0);
  }
  const u32x4* x1 = (const u32x4*)(ws + WS_X1);
  u32x4 X[16][2];
#pragma unroll
  for (int ks = 0; ks < 16; ++ks)
#pragma unroll
    for (int tt = 0; tt < 2; ++tt) X[ks][tt] = x1[(size_t)(4 * ks + g) * L + t0 + 16 * tt + tl];
  STAMP(ws, 2, 1);
  float* s12 = (float*)(smem + 112 * 1024);
  if (tid < 224) {
    const int which = tid / 112, f = cs * 112 + tid % 112;
    const float* sp = (const float*)(ws + WS_S12) + which * F + f;
    float a = 0.f;
#pragma unroll
    for (int i = 0; i < 8; ++i) a += sp[i * 2 * F];
    s12[tid] = a;
  }
  float sum[2] = {0.f, 0.f}, sq[2] = {0.f, 0.f};
  const bf16x2 ones = {(__bf16)1.f, (__bf16)1.f};
#pragma unroll
  for (int ks = 0; ks < 16; ++ks)
#pragma unroll
    for (int tt = 0; tt < 2; ++tt)
#pragma unroll
      for (int q = 0; q < 4; ++q) {
        const u32 xw = X[ks][tt][q];
        const bf16x2 xv = __builtin_bit_cast(bf16x2, xw);
        sum[tt] = __builtin_amdgcn_fdot2_f32_bf16(xv, ones, sum[tt], false);
        sq[tt] = __builtin_amdgcn_fdot2_f32_bf16(xv, xv, sq[tt], false);
      }
  asm volatile("s_waitcnt vmcnt(0)" ::: "memory");
  STAMP(ws, 2, 2);
  __syncthreads();
  f32x4 acc[7][2];
#pragma unroll
  for (int ft = 0; ft < 7; ++ft) { acc[ft][0] = f32x4{0.f, 0.f, 0.f, 0.f}; acc[ft][1] = f32x4{0.f, 0.f, 0.f, 0.f}; }
  const u32x4* wl = (const u32x4*)smem + lane;
#pragma unroll
  for (int ks = 0; ks < 16; ++ks)
#pragma unroll
    for (int ft = 0; ft < 7; ++ft) {
      const u32x4 wf = wl[(ks * 7 + ft) * 64];
      acc[ft][0] = mfma(wf, X[ks][0], acc[ft][0]);
      acc[ft][1] = mfma(wf, X[ks][1], acc[ft][1]);
    }
  asm volatile("" :: "v"(acc[6][1]), "v"(acc[6][0]));
  STAMP(ws, 2, 3);
  float mean[2], rstd[2];
#pragma unroll
  for (int tt = 0; tt < 2; ++tt) {
    float s = sum[tt], q = sq[tt];
    s += __shfl_xor(s, 16); q += __shfl_xor(q, 16);
    s += __shfl_xor(s, 32); q += __shfl_xor(q, 32);
    const float m = s * (1.f / D);
    const float var = fmaxf(q * (1.f / D) - m * m, 0.f);
    mean[tt] = rb(m);
    rstd[tt] = 1.f / rb(sqrtf(rb(rb(var) + 1e-5f)));
  }
  u32x2 o[7][2];
#pragma unroll
  for (int ft = 0; ft < 7; ++ft) {
    const f32x4 s1 = *(const f32x4*)(s12 + ft * 16 + g * 4), s2 = *(const f32x4*)(s12 + 112 + ft * 16 + g * 4);
#pragma unroll
    for (int tt = 0; tt < 2; ++tt) {
      float h[4];
#pragma unroll
      for (int q = 0; q < 4; ++q) {
        const float v = rb((acc[ft][tt][q] - mean[tt] * s1[q]) * rstd[tt] + s2[q]);
        h[q] = v * __builtin_amdgcn_rcpf(1.f + __builtin_amdgcn_exp2f(v * -1.44269504088896341f));
      }
      o[ft][tt] = u32x2{pk(h[0], h[1]), pk(h[2], h[3])};
    }
  }
  char* H = ws + WS_H;
#pragma unroll
  for (int tt = 0; tt < 2; ++tt) {
    char* hb = H + ((size_t)(t0 / 16 + tt) * 56 + 7 * (cs >> 1)) * 1024 + lane * 16;
#pragma unroll
    for (int q = 0; q < 3; ++q) {
      u32x4 v = {o[2 * q][tt][0], o[2 * q][tt][1], o[2 * q + 1][tt][0], o[2 * q + 1][tt][1]};
      *(u32x4*)(hb + (3 * (cs & 1) + q) * 1024) = v;
    }
    *(u32x2*)(hb + 6 * 1024 + (cs & 1) * 8) = o[6][tt];
  }
  STAMP(ws, 2, 4);
  asm volatile("s_waitcnt vmcnt(0)" ::: "memory");
  STAMP(ws, 2, 5);
}

__global__ void __launch_bounds__(256) k4_gemm2(char* ws, u16* out) {
  extern __shared__ __attribute__((aligned(16))) char smem[];
  const int tid = threadIdx.x, lane = tid & 63, w = tid >> 6;
  const int b = blockIdx.x, j = b >> 3;
  STAMP(ws, 3, 0);
  const int tbk = (b & 7) * 4 + (j & 3), db = j >> 2;
  const u32x4* hf = (const u32x4*)(ws + WS_H) + ((size_t)tbk * 4 * 56 + 14 * w) * 64 + lane;
  const u32x4* wf = (const u32x4*)(ws + WS_W2) + ((size_t)db * 56 + 14 * w) * 4 * 64 + lane;
  const int er = tid >> 3, ec = tid & 7;
  u32x4 xr[2];
#pragma unroll
  for (int i = 0; i < 2; ++i) xr[i] = ((const u32x4*)(ws + WS_X1))[(size_t)(db * 8 + ec) * L + tbk * 64 + er + 32 * i];
  float gb[8];
#pragma unroll
  for (int q = 0; q < 8; ++q) gb[q] = modval((const float*)(ws + WS_MODP), 5 * D + db * 64 + ec * 8 + q);
  f32x4 acc[4][4];
#pragma unroll
  for (int a = 0; a < 4; ++a)
#pragma unroll
    for (int c = 0; c < 4; ++c) acc[a][c] = f32x4{0.f, 0.f, 0.f, 0.f};
  constexpr int WIN = 6;
  u32x4 A[WIN][4], B[WIN][4];
#pragma unroll
  for (int ks = 0; ks < WIN; ++ks)
#pragma unroll
    for (int q = 0; q < 4; ++q) { A[ks][q] = wf[(ks * 4 + q) * 64]; B[ks][q] = hf[(q * 56 + ks) * 64]; }
  __builtin_amdgcn_sched_barrier(0);
#pragma unroll
  for (int ks = 0; ks < 14; ++ks) {
#pragma unroll
    for (int a = 0; a < 4; ++a)
#pragma unroll
      for (int c = 0; c < 4; ++c) acc[a][c] = mfma(A[ks % WIN][a], B[ks % WIN][c], acc[a][c]);
    __builtin_amdgcn_sched_barrier(0);
    if (ks + WIN < 14) {
#pragma unroll
      for (int q = 0; q < 4; ++q) { A[ks % WIN][q] = wf[((ks + WIN) * 4 + q) * 64]; B[ks % WIN][q] = hf[(q * 56 + ks + WIN) * 64]; }
    }
    __builtin_amdgcn_sched_barrier(0);
  }
  asm volatile("" :: "v"(acc[3][3]));
  STAMP(ws, 3, 3);
  float* red = (float*)smem;
  const int tl = lane & 15, g = lane >> 4;
#pragma unroll
  for (int a = 0; a < 4; ++a)
#pragma unroll
    for (int c = 0; c < 4; ++c) *(f32x4*)(red + (w * 64 + c * 16 + tl) * 68 + a * 16 + g * 4) = acc[a][c];
  __syncthreads();
#pragma unroll
  for (int i = 0; i < 2; ++i) {
    const int row = er + 32 * i;
    f32x4 v0 = {0.f, 0.f, 0.f, 0.f}, v1 = {0.f, 0.f, 0.f, 0.f};
#pragma unroll
    for (int ww = 0; ww < 4; ++ww) {
      v0 += *(const f32x4*)(red + (ww * 64 + row) * 68 + ec * 8);
      v1 += *(const f32x4*)(red + (ww * 64 + row) * 68 + ec * 8 + 4);
    }
    u32x4 o;
#pragma unroll
    for (int q = 0; q < 4; ++q) {
      const float a0 = q < 2 ? v0[2 * q] : v1[2 * q - 4], a1 = q < 2 ? v0[2 * q + 1] : v1[2 * q - 3];
      const u32 oc = pk(a0, a1);
      const u32 gy = pk(gb[2 * q] * bflo(oc), gb[2 * q + 1] * bfhi(oc));
      o[q] = pk(bflo(xr[i][q]) + bflo(gy), bfhi(xr[i][q]) + bfhi(gy));
    }
    *(u32x4*)(out + (size_t)(tbk * 64 + row) * D + db * 64 + ec * 8) = o;
  }
  STAMP(ws, 3, 4);
  asm volatile("s_waitcnt vmcnt(0)" ::: "memory");
  STAMP(ws, 3, 5);
}

extern "C" void kernel_launch(void* const* d_in, const int* in_sizes, int n_in, void* d_out, int out_size, void* d_ws, size_t ws_size,
                              hipStream_t stream) {
  char* ws = (char*)d_ws;
  P1 p1;
  p1.x = (const u16*)d_in[0]; p1.Are = (const u16*)d_in[1]; p1.Aim = (const u16*)d_in[2]; p1.Bre = (const u16*)d_in[3];
  p1.Bim = (const u16*)d_in[4]; p1.Cre = (const u16*)d_in[5]; p1.Cim = (const u16*)d_in[6]; p1.ldt = (const u16*)d_in[7];
  p1.c = (const u16*)d_in[12]; p1.wmod = (const u16*)d_in[13]; p1.w2 = (const u16*)d_in[11]; p1.ws = ws;
  P2 p2;
  p2.dskip = (const u16*)d_in[8]; p2.w1 = (const u16*)d_in[10]; p2.ws = ws;
  constexpr int LDS3 = 112 * 1024 + 1024, LDS4 = 4 * 64 * 68 * 4;
  (void)hipFuncSetAttribute((const void*)k3_gemm1, hipFuncAttributeMaxDynamicSharedMemorySize, LDS3);
  (void)hipFuncSetAttribute((const void*)k4_gemm2, hipFuncAttributeMaxDynamicSharedMemorySize, LDS4);
  k1_prep<<<K1_LN + K1_GV + K1_KG + K1_W2, 256, 0, stream>>>(p1);
  k2_conv<<<256 + 224, 256, 0, stream>>>(p2);
  k3_gemm1<<<256, 256, LDS3, stream>>>(ws);
  k4_gemm2<<<256, 256, LDS4, stream>>>(ws, (u16*)d_out);
}
```

```cpp
#include <hip/hip_runtime.h>
#include <hip/hip_bf16.h>

#define STAMP_DECL
#define STAMP(s)
#define STAMP_FLUSH(ws, k)
#define ST_LDS_EXTRA 0
#define ST_RANGES {{0, 32, 256, "compute waves"}, {1, 32, 256, "loader waves"}, {0, 0, 32, "helper WGs, waves 0-3"}, {1, 0, 32, "helper WGs, waves 4-7"}}

typedef unsigned short u16;
typedef unsigned int u32;
typedef unsigned long long u64;
typedef u32 u32x2 __attribute__((ext_vector_type(2)));
typedef u32 u32x4 __attribute__((ext_vector_type(4)));
typedef float f32x2 __attribute__((ext_vector_type(2)));
typedef float f32x4 __attribute__((ext_vector_type(4)));
typedef float f32x16 __attribute__((ext_vector_type(16)));
typedef short s16x4 __attribute__((ext_vector_type(4)));
typedef __bf16 bf16x2 __attribute__((ext_vector_type(2)));
typedef __bf16 bf16x8 __attribute__((ext_vector_type(8)));
typedef __amdgpu_buffer_rsrc_t rsrc_t;
#define GAS __attribute__((address_space(1)))
#define LAS __attribute__((address_space(3)))
#define CAS __attribute__((address_space(4)))

constexpr int L = 2048, D = 512, NS = 128, F = 1792;

constexpr int WS_EPOCH = 0;
constexpr int WS_SG = 0x1000;
constexpr int WS_MG = 0x8000;
constexpr int WS_F2 = 0x40000;
constexpr int WS_F3 = 0x50000;
constexpr int WS_G2 = 0x70000;
constexpr int WS_S12 = 0x74000;
constexpr int WS_MF = 0x7c000;
constexpr int WS_XG = 1 << 20;
constexpr int WS_X1 = 5 << 20;
constexpr int WS_X1S = 7 << 20;
constexpr int WS_H = 9 << 20;
constexpr int WS_END = 16 << 20;

constexpr int UNIT = 16384;
constexpr int LD_XR = 0, LD_WR = 4 * UNIT, LD_MISC = 8 * UNIT, LDS_BYTES = 9 * UNIT;
constexpr int TCOPY = 4160, PROW = 132;
constexpr int LD_T = 0, LD_U = 8 * TCOPY, LD_KD = LD_U + 2 * 4608, LD_P = LD_WR, LD_RED0 = 3 * UNIT, LD_RED1 = LD_MISC;
static_assert(LD_KD + 772 * 4 <= LD_RED0, "LDS map");
constexpr int SPIN_CAP = 1 << 15;
constexpr float LOG2E = 1.44269504088896341f;

__device__ __forceinline__ float bflo(u32 v) { return __uint_as_float(v << 16); }
__device__ __forceinline__ float bfhi(u32 v) { return __uint_as_float(v & 0xffff0000u); }
__device__ __forceinline__ u32 pk(float lo, float hi) {
  f32x2 v = {lo, hi};
  bf16x2 r = __builtin_convertvector(v, bf16x2);
  return __builtin_bit_cast(u32, r);
}
__device__ __forceinline__ float rb(float f) { return bflo(pk(f, f)); }
__device__ __forceinline__ float dot2(u32 a, u32 b, float c) {
  return __builtin_amdgcn_fdot2_f32_bf16(__builtin_bit_cast(bf16x2, a), __builtin_bit_cast(bf16x2, b), c, false);
}
__device__ __forceinline__ f32x4 mfma16(u32x4 a, u32x4 b, f32x4 c) {
  return __builtin_amdgcn_mfma_f32_16x16x32_bf16(__builtin_bit_cast(bf16x8, a), __builtin_bit_cast(bf16x8, b), c, 0, 0, 0);
}
__device__ __forceinline__ f32x16 mfma32(u32x4 a, u32x4 b, f32x16 c) {
  return __builtin_amdgcn_mfma_f32_32x32x16_bf16(__builtin_bit_cast(bf16x8, a), __builtin_bit_cast(bf16x8, b), c, 0, 0, 0);
}
template <int CTRL> __device__ __forceinline__ float dpp(float x) {
  return __builtin_bit_cast(float, __builtin_amdgcn_mov_dpp(__builtin_bit_cast(int, x), CTRL, 0xf, 0xf, true));
}
__device__ __forceinline__ float row_sum(float x) {
  x += dpp<0xB1>(x); x += dpp<0x4E>(x); x += dpp<0x124>(x); x += dpp<0x128>(x);
  return x;
}
__device__ __forceinline__ float fold32(float a, float b) {
  auto s = __builtin_amdgcn_permlane32_swap(__float_as_uint(a), __float_as_uint(b), false, false);
  return __uint_as_float(s[0]) + __uint_as_float(s[1]);
}
__device__ __forceinline__ float fold16(float a, float b) {
  auto s = __builtin_amdgcn_permlane16_swap(__float_as_uint(a), __float_as_uint(b), false, false);
  return __uint_as_float(s[0]) + __uint_as_float(s[1]);
}
__device__ __forceinline__ u32x4 mkrs(const void* p, u32 bytes) {
  const u64 a = (u64)p;
  return u32x4{(u32)a, (u32)(a >> 32) & 0xffffu, bytes, 0x00020000u};
}
__device__ __forceinline__ void dma16(u32x4 rs, int lds, int voff, int soff) {
  asm volatile("s_mov_b32 m0, %0\n\ts_nop 0\n\tbuffer_load_dwordx4 %1, %2, %3 offen lds" :: "s"(lds), "v"(voff), "s"(rs), "s"(soff) : "memory");
}
__device__ __forceinline__ void dma16_sc1(u32x4 rs, int lds, int voff, int soff) {
  asm volatile("s_mov_b32 m0, %0\n\ts_nop 0\n\tbuffer_load_dwordx4 %1, %2, %3 offen sc1 lds" :: "s"(lds), "v"(voff), "s"(rs), "s"(soff) : "memory");
}
__device__ __forceinline__ u32x4 ld_sc1(rsrc_t rs, int voff, int soff) { return __builtin_amdgcn_raw_buffer_load_b128(rs, voff, soff, 16); }
__device__ __forceinline__ void st_sc1(rsrc_t rs, int voff, int soff, u32x4 v) { __builtin_amdgcn_raw_buffer_store_b128(v, rs, voff + soff, 0, 16); }
__device__ __forceinline__ u32x4 tr_frag(LAS char* a0, LAS char* a1) {
  const s16x4 lo = __builtin_amdgcn_ds_read_tr16_b64_v4i16((LAS s16x4*)a0), hi = __builtin_amdgcn_ds_read_tr16_b64_v4i16((LAS s16x4*)a1);
  const u32x2 l = __builtin_bit_cast(u32x2, lo), h = __builtin_bit_cast(u32x2, hi);
  return u32x4{l[0], l[1], h[0], h[1]};
}
#define SCHED() __builtin_amdgcn_sched_barrier(0)
#define FENCE() asm volatile("" ::: "memory")
#define WAIT_VM(n) asm volatile("s_waitcnt vmcnt(" #n ")" ::: "memory")
#define BARRIER() do { asm volatile("s_waitcnt lgkmcnt(0)" ::: "memory"); __builtin_amdgcn_s_barrier(); FENCE(); } while (0)

__device__ __forceinline__ f32x2 cmul(f32x2 a, f32x2 b) {
  const f32x2 t = f32x2{a[0], a[0]} * b;
  return __builtin_elementwise_fma(f32x2{a[1], a[1]}, f32x2{-b[1], b[0]}, t);
}
__device__ __forceinline__ f32x2 cpow(float a2, float bq, float l) {
  const float m = __builtin_amdgcn_exp2f(a2 * l);
  const float ph = __builtin_amdgcn_fractf(bq * l);
  return f32x2{__builtin_amdgcn_cosf(ph), __builtin_amdgcn_sinf(ph)} * m;
}
__device__ __forceinline__ u32 pk2(f32x2 a) { return pk(a[0], a[1]); }
__device__ __forceinline__ f32x2 up(u32 v) { return f32x2{bflo(v), bfhi(v)}; }
__device__ __forceinline__ u32 ln_stat(float sum, float sq) {
  const float m = sum * (1.f / D);
  const float var = fmaxf(sq * (1.f / D) - m * m, 0.f);
  return pk(m, __builtin_amdgcn_sqrtf(rb(rb(var) + 1e-5f)));
}
__device__ __forceinline__ u32 tagdiff(u32x4 v, u32 ta, u32 tb) { return (v[1] ^ ta) | (v[3] ^ tb); }
__device__ __forceinline__ u32x4 poll16(rsrc_t rs, int voff, int soff, u32 ta, u32 tb) {
  u32x4 v;
  int spins = 0;
  do { FENCE(); v = ld_sc1(rs, voff, soff); } while (tagdiff(v, ta, tb) != 0u && ++spins < SPIN_CAP);
  FENCE();
  return v;
}
__device__ __forceinline__ void fold_cols(const float (&v)[16], LAS float* dst, int lane) {
  float u[8]; f32x4 t;
#pragma unroll
  for (int k = 0; k < 8; ++k) u[k] = fold32(v[k], v[k + 8]);
#pragma unroll
  for (int k = 0; k < 4; ++k) { const float a = fold16(u[k], u[k + 4]); t[k] = a + dpp<0x128>(a); }
  if (!(lane & 8)) *(LAS f32x4*)(dst + (lane & 7) * 16 + (lane >> 4) * 4) = t;
}
__device__ __forceinline__ f32x2 sum_cols(LAS const float* src, int c) {
  f32x2 a[8];
#pragma unroll
  for (int i = 0; i < 8; ++i) a[i] = *(LAS const f32x2*)(src + i * 128 + c * 2);
  return ((a[0] + a[1]) + (a[2] + a[3])) + ((a[4] + a[5]) + (a[6] + a[7]));
}

struct Params {
  const u16 *x, *Are, *Aim, *Bre, *Bim, *Cre, *Cim, *ldt, *dskip, *w1, *w2, *c, *wmod;
  u16* out;
  char* ws;
};

extern "C" __global__ void __launch_bounds__(512, 2) fused_block(Params p) {
  extern __shared__ __attribute__((aligned(16))) char smem_[];
  LAS char* smem = (LAS char*)smem_;
  const int tid = threadIdx.x, lane = tid & 63, w = __builtin_amdgcn_readfirstlane(tid >> 6), lw = w & 3;
  const int b = blockIdx.x, xcd = b & 7, j = b >> 3;
  const int pair = xcd * 32 + j, d0 = pair * 2;
  STAMP_DECL
  STAMP(0);
  u32 codepf;
  {
    u64 pc; int r0, r1;
    asm volatile("s_getpc_b64 %0\n\ts_mov_b32 %1, fused_block@rel32@lo+4\n\ts_mov_b32 %2, fused_block_end@rel32@lo+12" : "=s"(pc), "=s"(r0), "=s"(r1));
    asm volatile("global_load_dword %0, %1, %2" : "=v"(codepf) : "v"(min(tid * 128, r1 - r0)), "s"(pc + (long long)r0));
  }
  SCHED();
  const rsrc_t rws = __builtin_amdgcn_make_buffer_rsrc(p.ws, 0, WS_END, 0x00020000);
  const u32x4 qws = mkrs(p.ws, WS_END);
  u32 ta, tb, ldt2, dsk;
  auto scalars = [&]() {
    u64 e;
    asm volatile("s_load_dwordx2 %0, %3, 0x0\n\ts_load_dword %1, %4, 0x0\n\ts_load_dword %2, %5, 0x0\n\ts_waitcnt lgkmcnt(0)"
                 : "=&s"(e), "=&s"(ldt2), "=&s"(dsk) : "s"(p.ws + WS_EPOCH), "s"(p.ldt + d0), "s"(p.dskip + d0) : "memory");
    e += 0x9E3779B97F4A7C15ull;
    ta = (u32)e; tb = (u32)(e >> 32);
  };
  const bool helper = j < 4;
  const int rbk = (xcd >> 1) * 4 + (j & 3), cbk = (xcd & 1) * 7 + ((j - 4) >> 2);
  const int tbk = xcd * 4 + (j & 3), db = j >> 2;
  int ga[11];

  if (w >= 4) {
    __builtin_amdgcn_s_setprio(2);
    const u16* xr = p.x + (size_t)(b * 8 + 2 * (lane & 3)) * D + lw * 128 + (lane >> 2) * 8;
    const int xo = WS_XG + (lw * 64 + (lane >> 2) * 4) * 16384 + (lane & 3) * 16 + b * 64;
    if (lw < 2) {
      FENCE();
      const u32x4 v0 = *(const u32x4*)xr, v1 = *(const u32x4*)(xr + D);
      const int part = (b < 192 ? b / 48 : 0) * 2 + lw, gc = b % 48;
      const int k0 = part * 64 + (lane >> 3);
      const u32 c0 = p.c[k0 + 8 * (lane & 3)], c1 = p.c[k0 + 32 + 8 * (lane & 3)];
      u32x4 wv[8];
#pragma unroll
      for (int i = 0; i < 8; ++i) wv[i] = *(const u32x4*)(p.wmod + (size_t)(k0 + 8 * i) * 3072 + gc * 64 + (lane & 7) * 8);
      SCHED();
      scalars();
      STAMP(1);
#pragma unroll
      for (int q = 0; q < 4; ++q) st_sc1(rws, xo, q * 16384, u32x4{v0[q], ta, v1[q], tb});
      SCHED();
      asm volatile("" :: "v"(codepf));
      const float f0 = bflo(c0), f1 = bflo(c1);
      const u32 sp = pk(f0 * __builtin_amdgcn_rcpf(1.f + __builtin_amdgcn_exp2f(f0 * -LOG2E)), f1 * __builtin_amdgcn_rcpf(1.f + __builtin_amdgcn_exp2f(f1 * -LOG2E)));
      float acc[8];
#pragma unroll
      for (int e = 0; e < 8; ++e) acc[e] = 0.f;
#define GV(i, CTRL) { const u32 sb = __builtin_amdgcn_mov_dpp(sp, CTRL, 0xf, 0xf, true); const u32 al = sb & 0xffffu, ah = sb << 16, bl = sb >> 16, bh = sb & 0xffff0000u; \
        _Pragma("unroll") for (int q = 0; q < 4; ++q) { acc[2 * q] = dot2(wv[i][q], al, acc[2 * q]); acc[2 * q + 1] = dot2(wv[i][q], ah, acc[2 * q + 1]); \
          acc[2 * q] = dot2(wv[i + 4][q], bl, acc[2 * q]); acc[2 * q + 1] = dot2(wv[i + 4][q], bh, acc[2 * q + 1]); } }
      GV(0, 0x00) GV(1, 0x55) GV(2, 0xAA) GV(3, 0xFF)
      float m0 = fold16(fold32(acc[0], acc[4]), fold32(acc[2], acc[6])), m1 = fold16(fold32(acc[1], acc[5]), fold32(acc[3], acc[7]));
      m0 += dpp<0x128>(m0); m1 += dpp<0x128>(m1);
      if (b < 192 && !(lane & 8))
        st_sc1(rws, ((lane >> 4) * 8 + (lane & 7)) * 16, WS_MG + (part * 1536 + gc * 32) * 16, u32x4{__float_as_uint(m0), ta, __float_as_uint(m1), tb});
    } else {
      const u32x4 v0 = *(const u32x4*)xr, v1 = *(const u32x4*)(xr + D);
      const u16* xs = p.x + (size_t)(b * 8 + 4 * (lw & 1)) * D + lane * 8;
      u32x4 v[4];
#pragma unroll
      for (int i = 0; i < 4; ++i) v[i] = *(const u32x4*)(xs + i * D);
      SCHED();
      scalars();
      STAMP(1);
#pragma unroll
      for (int q = 0; q < 4; ++q) st_sc1(rws, xo, q * 16384, u32x4{v0[q], ta, v1[q], tb});
      SCHED();
      asm volatile("" :: "v"(codepf));
      float s[4], q[4];
#pragma unroll
      for (int i = 0; i < 4; ++i) {
        s[i] = 0.f; q[i] = 0.f;
#pragma unroll
        for (int z = 0; z < 4; ++z) { s[i] = dot2(v[i][z], 0x3f803f80u, s[i]); q[i] = dot2(v[i][z], v[i][z], q[i]); }
      }
      const u32 st = ln_stat(row_sum(fold16(fold32(s[0], s[2]), fold32(s[1], s[3]))), row_sum(fold16(fold32(q[0], q[2]), fold32(q[1], q[3]))));
      const auto sw = __builtin_amdgcn_permlane16_swap(st, st, false, false);
      if (!(lane & 31)) st_sc1(rws, (lane >> 5) * 16, WS_SG + b * 64 + (lw & 1) * 32, u32x4{sw[0], ta, sw[1], tb});
      if (lw == 3) *(LAS u32x4*)(smem + LD_U + (lane >> 5) * 4608 + (lane & 31) * 16) = u32x4{0u, 0u, 0u, 0u};
      else if (lane < 16) *(LAS u32x4*)(smem + LD_T + (lane >> 1) * TCOPY + 4096 + (lane & 1) * 16) = u32x4{0u, 0u, 0u, 0u};
      else if (lane == 16) *(LAS u32*)(smem + LD_KD + 768 * 4) = 0u;
    }
    __builtin_amdgcn_s_setprio(0);
    STAMP(2);
  } else {
    const int ch = w >> 1, n = (w & 1) * 64 + lane;
    const size_t po = (size_t)(d0 + ch) * NS + n;
    u32 rar = p.Are[po], rai = p.Aim[po], rbr = p.Bre[po], rbi = p.Bim[po], rcr = p.Cre[po], rci = p.Cim[po];
    SCHED();
#pragma unroll
    for (int i = 0; i < 11; ++i) {
      const int l = 2047 - (tid * 8 + i);
      const int bits = (int)(pk((float)l, 0.f) & 0xffffu);
      ga[i] = LD_KD + 4 * (l < 128 ? (l < 0 ? 768 : l) : bits - 0x4300 + 128);
      asm volatile("" : "+v"(ga[i]));
    }
    scalars();
    STAMP(1);
    asm volatile("" : "+v"(rar), "+v"(rai), "+v"(rbr), "+v"(rbi), "+v"(rcr), "+v"(rci) : "v"(codepf));
    STAMP(2);
    const float dt = rb(__builtin_amdgcn_exp2f((ch ? bfhi(ldt2) : bflo(ldt2)) * LOG2E));
    const float a2 = (dt * bflo(rar)) * LOG2E, bq = (dt * bflo(rai)) * 0.15915494309189535f;
    LAS u32* P = (LAS u32*)(smem + LD_P) + ch * 64 * PROW + n;
    const f32x2 z1 = cpow(a2, bq, 1.f), z8 = cpow(a2, bq, 8.f), z256 = cpow(a2, bq, 256.f);
    const f32x2 z16c = cmul(z8, z8) * f32x2{1.f, -1.f}, z512 = cmul(z256, z256), z768 = cmul(z512, z256);
    f32x2 u = cpow(a2, bq, 1024.f), q = {1.f, 0.f};
    f32x2 r = cmul(f32x2{bflo(rcr), bflo(rci)}, f32x2{bflo(rbr), bflo(rbi)} * dt) * f32x2{1.f, -1.f};
#pragma unroll
    for (int e = 0; e < 16; ++e) {
      P[e * PROW] = pk2(r);
      P[(16 + e) * PROW] = pk2(q);
      if (!(e & 1)) P[(32 + e / 2) * PROW] = pk2(cmul(q, z256));
      if (!(e & 3)) { P[(40 + e / 4) * PROW] = pk2(cmul(q, z512)); P[(44 + e / 4) * PROW] = pk2(cmul(q, z768)); }
      if ((e & 3) == 1) { P[(48 + e / 2) * PROW] = pk2(u); P[(49 + e / 2) * PROW] = pk2(cmul(u, z8)); }
      if ((e & 3) == 3) u = cmul(u, z256);
      r = cmul(r, z16c); q = cmul(q, z1);
    }
    P[56 * PROW] = pk2(u);
  }
  STAMP(3);
  BARRIER();
  STAMP(4);
  if (lw < 3) {
    const int s = lane & 15, g = lane >> 4, ch = w >> 2;
    LAS const u32* fb = (LAS const u32*)(smem + LD_P) + ch * 64 * PROW + s * PROW + g * 4;
    u32x4 fa[8], fc[8];
#pragma unroll
    for (int ks = 0; ks < 8; ++ks) { fa[ks] = *(LAS const u32x4*)(fb + (lw + 1) * 16 * PROW + ks * 16); fc[ks] = *(LAS const u32x4*)(fb + ks * 16); }
    const bool lo = g < 2;
    const int cs = lo ? (0x020810 >> (8 * lw)) & 0xff : (0x040410 >> (8 * lw)) & 0xff, cg = lo ? (0x400404 >> (8 * lw)) & 0xff : (0x404004 >> (8 * lw)) & 0xff;
    const int i0 = 256 * lw + cs * s + cg * g, i1 = i0 + (lo ? (0x200202 >> (8 * lw)) & 0xff : 2);
    SCHED();
    f32x4 acc = {0.f, 0.f, 0.f, 0.f};
#pragma unroll
    for (int ks = 0; ks < 8; ++ks) acc = mfma16(fa[ks], fc[ks], acc);
    LAS u16* kd = (LAS u16*)(smem + LD_KD) + ch;
    const u32 k01 = pk(acc[0], acc[1]), k23 = pk(acc[2], acc[3]);
    kd[2 * i0] = (u16)k01; kd[2 * i0 + 2] = (u16)(k01 >> 16); kd[2 * i1] = (u16)k23; kd[2 * i1 + 2] = (u16)(k23 >> 16);
  }
  STAMP(5);
  BARRIER();
  STAMP(6);

  const u32x4 tags = {ta, ta, tb, tb};
  const u32x4 qw1 = mkrs(p.w1, D * F * 2), qw2 = mkrs(p.w2, D * F * 2);
  const int vw2 = (lane >> 3) * 1024 + db * 128 + 16 * ((lane & 7) ^ (((lane >> 4) & 1) << 2));
  const int vchk = 16 * ((lane & 15) ^ ((lane >> 4) << 2));
  auto w2_stage = [&](int s) {
#pragma unroll
    for (int i = 0; i < 4; ++i) dma16(qw2, LD_WR + (s & 3) * UNIT + lw * 4096 + i * 1024, vw2, (s * 128 + lw * 32 + i * 8) * 1024);
  };
  auto w1_stage = [&](int s) {
#pragma unroll
    for (int i = 0; i < 4; ++i)
      dma16(qw1, LD_WR + (s & 3) * UNIT + lw * 4096 + i * 1024, (lane >> 4) * (F * 2) + (vchk ^ (16 * i)), (s * 64 + 16 * lw + 4 * i) * (F * 2) + cbk * 256);
  };

  if (w < 4) {
    {
      u32 e[11];
#pragma unroll
      for (int i = 0; i < 11; ++i) e[i] = *(LAS const u32*)(smem + ga[i]);
#pragma unroll
      for (int ch = 0; ch < 2; ++ch)
#pragma unroll
        for (int s = 0; s < 4; ++s) {
          u32x4 o;
#pragma unroll
          for (int z = 0; z < 4; ++z) o[z] = __builtin_amdgcn_perm(e[s + 2 * z + 1], e[s + 2 * z], ch ? 0x07060302u : 0x05040100u);
          *(LAS u32x4*)(smem + LD_T + (ch * 4 + s) * TCOPY + tid * 16) = o;
        }
    }
    const int r = lane & 15, g = lane >> 4, ch = w >> 1, half = w & 1;
    LAS const char* Tb = smem + LD_T + (ch * 4 + ((3 - r) & 3)) * TCOPY + 2 * (2028 - 4 * (r >> 2) + 8 * g - 128 * half) - 7 * 512 - 192;
    LAS const char* Ub = smem + LD_U + ch * 4608 + 2 * (256 + 16 * r - 16 + 8 * g - 128 * half) - 192;
    f32x4 acc[8];
#pragma unroll
    for (int n = 0; n < 8; ++n) acc[n] = f32x4{0.f, 0.f, 0.f, 0.f};
    u32x4 uf[8], tf[8];
#define LD_TF(a, e) do { const u32x2 l_ = *(volatile LAS const u32x2*)(Tb + 64 * (3 - (e)) + 512 * (7 - (a))), h_ = *(volatile LAS const u32x2*)(Tb + 64 * (3 - (e)) + 512 * (7 - (a)) + 8); \
                         tf[a] = u32x4{l_[0], l_[1], h_[0], h_[1]}; } while (0)
#define LD_UF(n, e) uf[n] = *(LAS const u32x4*)(Ub + 64 * (3 - (e)) + 512 * (n))
    STAMP(10);
    BARRIER();
    STAMP(11);
    LD_TF(0, 0);
#pragma unroll
    for (int n = 7; n >= 0; --n) LD_UF(n, 0);
    SCHED();
#pragma unroll
    for (int e = 0; e < 4; e += 2) {
#pragma unroll
      for (int a = 0; a < 8; ++a) {
#pragma unroll
        for (int m = 7 - a; m >= 0; --m) {
          SCHED();
          acc[a + m] = mfma16(tf[a], uf[m], acc[a + m]);
          SCHED();
          if (e == 0 && a == 0 && m > 0) LD_TF(8 - m, 0);
          if (m == 7 - a) LD_UF(7 - a, e + 1);
        }
        LD_TF(a, e + 1);
      }
#pragma unroll
      for (int m = 7; m >= 1; --m) {
        SCHED();
#pragma unroll
        for (int a = 0; a + m < 8; ++a) acc[a + m] = mfma16(tf[a], uf[m], acc[a + m]);
        SCHED();
        if (e == 0) LD_UF(m, 2);
      }
#pragma unroll
      for (int a = 0; a < 8; ++a) {
        SCHED();
        acc[a] = mfma16(tf[a], uf[0], acc[a]);
        SCHED();
        if (e == 0) LD_TF(a, 2);
      }
      if (e == 0) LD_UF(0, 2);
    }
    STAMP(12);
    LAS f32x4* red = (LAS f32x4*)(smem + (ch ? LD_RED1 : LD_RED0)) + half * 512 + (lane ^ ((lane >> 4) << 2));
#pragma unroll
    for (int n = 0; n < 8; ++n) red[n * 64] = acc[n];
    STAMP(13);
    BARRIER();
    STAMP(14);
  } else {
    u32x4 xg[4], sg[4], mg;
    const int go = lw * 4096 + lane * 16;
    const int mo = WS_MG + ((lane & 7) * 1536 + (lane < 48 ? lane >> 3 : 0) * 256 + (pair >> 5) * 32 + (pair & 3) * 8 + ((pair >> 2) & 7)) * 16;
    for (int spins = 0;; ++spins) {
      FENCE();
      mg = ld_sc1(rws, mo, 0);
#pragma unroll
      for (int i = 0; i < 4; ++i) { xg[i] = ld_sc1(rws, go + i * 1024, WS_XG + pair * 16384); sg[i] = ld_sc1(rws, go + i * 1024, WS_SG); }
      u32 bad = tagdiff(mg, ta, tb);
#pragma unroll
      for (int i = 0; i < 4; ++i) bad |= tagdiff(xg[i], ta, tb) | tagdiff(sg[i], ta, tb);
      if (!__any(bad != 0u) || spins >= SPIN_CAP) break;
    }
    STAMP(9);
    float m0 = __uint_as_float(mg[0]), m1 = __uint_as_float(mg[2]);
    m0 += dpp<0xB1>(m0); m1 += dpp<0xB1>(m1); m0 += dpp<0x4E>(m0); m1 += dpp<0x4E>(m1); m0 += dpp<0x141>(m0); m1 += dpp<0x141>(m1);
    const u32 mb = pk(m0, m1), mp = pk2(up(mb) + 1.f);
    const f32x2 sa = up(__builtin_amdgcn_readlane(mb, 0)), g1 = up(__builtin_amdgcn_readlane(mp, 8));
    f32x2 xf[4][2], hf[4][2];
#pragma unroll
    for (int i = 0; i < 4; ++i) {
#pragma unroll
      for (int e = 0; e < 2; ++e) {
        xf[i][e] = up(xg[i][2 * e]);
        hf[i][e] = (xf[i][e] - bflo(sg[i][2 * e])) * __builtin_amdgcn_rcpf(bfhi(sg[i][2 * e])) * g1 + sa;
      }
      LAS u32* U = (LAS u32*)(smem + LD_U + 512 + lw * 1024 + i * 256) + lane;
      U[0] = pk(hf[i][0][0], hf[i][1][0]);
      U[4608 / 4] = pk(hf[i][0][1], hf[i][1][1]);
    }
    STAMP(10);
    BARRIER();
    STAMP(11);
    const u32 gcb = __builtin_amdgcn_readlane(mp, 32), sbb = __builtin_amdgcn_readlane(mb, 24), gbb = __builtin_amdgcn_readlane(mb, 40);
    if (w == 7 && lane < 2)
      st_sc1(rws, lane * 4096, WS_MF + pair * 16, lane ? u32x4{gbb << 16, ta, gbb & 0xffff0000u, tb} : u32x4{(gcb & 0xffffu) | (sbb << 16), ta, (gcb >> 16) | (sbb & 0xffff0000u), tb});
    FENCE();
    if (helper) { w2_stage(0); w2_stage(1); w2_stage(2); w2_stage(3); }
    else { w1_stage(0); w1_stage(1); w1_stage(2); w1_stage(3); }
    const f32x2 gav = up(__builtin_amdgcn_readlane(mb, 16)), gcv = up(gcb), gd = gav * up(dsk);
#pragma unroll
    for (int i = 0; i < 4; ++i)
#pragma unroll
      for (int e = 0; e < 2; ++e) { xf[i][e] += hf[i][e] * gd; asm volatile("" : "+v"(xf[i][e])); }
    const int g = (lane >> 1) & 3;
    const int ro = (2 * lw * 64 + 16 * g + ((lane >> 3) ^ (4 * g))) * 16 + 8 * (lane & 1);
    WAIT_VM(16);
    STAMP(13);
    BARRIER();
    STAMP(14);
    u32x4 o[2], s0, s1;
    f32x2 yp[4][4];
#pragma unroll
    for (int i = 0; i < 4; ++i) {
      const int a = (ro ^ (128 * (i & 1))) + (i >> 1) * 1024;
      yp[i][0] = *(LAS const f32x2*)(smem + LD_RED0 + a); yp[i][1] = *(LAS const f32x2*)(smem + LD_RED0 + 8192 + a);
      yp[i][2] = *(LAS const f32x2*)(smem + LD_RED1 + a); yp[i][3] = *(LAS const f32x2*)(smem + LD_RED1 + 8192 + a);
    }
    SCHED();
#pragma unroll
    for (int i = 0; i < 4; ++i) {
      const f32x2 y0 = yp[i][0] + yp[i][1], y1 = yp[i][2] + yp[i][3];
      const f32x2 c0 = y0 * gav[0] + f32x2{xf[i][0][0], xf[i][1][0]}, c1 = y1 * gav[1] + f32x2{xf[i][0][1], xf[i][1][1]};
      s0[i] = pk2(c0 * gcv[0]); s1[i] = pk2(c1 * gcv[1]);
      o[i >> 1][2 * (i & 1)] = pk(c0[0], c1[0]); o[i >> 1][2 * (i & 1) + 1] = pk(c0[1], c1[1]);
    }
    const int so = WS_X1S + (lw * 4 + (lane >> 4)) * 131072 + d0 * 256 + (lane & 15) * 16;
    st_sc1(rws, so, 0, s0);
    st_sc1(rws, so, 256, s1);
    const int xo = WS_X1 + (lw * 8 + (lane >> 3)) * 65536 + pair * 128 + (lane & 7) * 16;
    st_sc1(rws, xo, 0, o[0]);
    st_sc1(rws, xo, 32768, o[1]);
    STAMP(15);
    WAIT_VM(0);
    STAMP(17);
    if (lane < 4) st_sc1(rws, lane * 16, WS_F2 + (lw * 256 + pair) * 64, tags);
  }
  FENCE();
  SCHED();

  if (helper) {
    const int hb = xcd * 4 + j;
    u32x4 v[8];
    const int rg = tid >> 3, cl = tid & 7;
#pragma unroll
    for (int i = 0; i < 8; ++i) v[i] = *(const u32x4*)(p.w1 + (size_t)(i * 64 + rg) * F + (hb < 28 ? hb : 0) * 64 + cl * 8);
    LAS float* rd = (LAS float*)(smem + 4096);
    if (w < 4) { const u32x4 m = poll16(rws, tid * 16, WS_MF, ta, tb); *(LAS u32x2*)(smem + tid * 8) = u32x2{m[0], m[2]}; }
    BARRIER();
    float a[16];
#pragma unroll
    for (int e = 0; e < 16; ++e) a[e] = 0.f;
#pragma unroll
    for (int i = 0; i < 8; ++i) {
      const u32 gs = *(LAS const u32*)(smem + (i * 64 + rg) * 4);
      const u32 gl = gs & 0xffffu, gh = gs << 16, sl = gs >> 16, sh = gs & 0xffff0000u;
#pragma unroll
      for (int z = 0; z < 4; ++z) {
        a[4 * z] = dot2(v[i][z], gl, a[4 * z]); a[4 * z + 1] = dot2(v[i][z], sl, a[4 * z + 1]);
        a[4 * z + 2] = dot2(v[i][z], gh, a[4 * z + 2]); a[4 * z + 3] = dot2(v[i][z], sh, a[4 * z + 3]);
      }
    }
    fold_cols(a, rd + w * 128, lane);
    STAMP(18);
    if (w < 4) poll16(rws, tid * 64, WS_F2 + (xcd >> 1) * 16384, ta, tb);
    BARRIER();
    STAMP(19);
#pragma unroll
    for (int i = 0; i < 8; ++i) v[i] = ld_sc1(rws, tid * 16, WS_X1 + hb * 65536 + i * 8192);
    if (w == 0 && hb < 28) {
      const f32x2 s = sum_cols(rd, lane);
      st_sc1(rws, lane * 16, WS_S12 + hb * 1024, u32x4{__float_as_uint(s[0]), ta, __float_as_uint(s[1]), tb});
    }
#pragma unroll
    for (int hi = 0; hi < 2; ++hi)
#pragma unroll
      for (int z = 0; z < 4; ++z) {
        float s = 0.f, q = 0.f;
#pragma unroll
        for (int i = 0; i < 4; ++i) { s = dot2(v[hi * 4 + i][z], 0x3f803f80u, s); q = dot2(v[hi * 4 + i][z], v[hi * 4 + i][z], q); }
        a[hi * 8 + 2 * z] = s; a[hi * 8 + 2 * z + 1] = q;
      }
    STAMP(20);
    fold_cols(a, rd + 1024 + w * 128, lane);
    BARRIER();
    if (w == 0) {
      const f32x2 s = sum_cols(rd + 1024, lane);
      const u32 st = ln_stat(s[0], s[1]);
      const u32 st2 = __builtin_amdgcn_mov_dpp(st, 0xB1, 0xf, 0xf, true);
      if (!(lane & 1)) st_sc1(rws, lane * 8, WS_G2 + hb * 512, u32x4{st, ta, st2, tb});
    }
    BARRIER();
    STAMP(22);
  } else if (w >= 4) {
    auto x_stage = [&](int s) {
#pragma unroll
      for (int i = 0; i < 4; ++i)
        dma16_sc1(qws, LD_XR + (s & 3) * UNIT + lw * 4096 + i * 1024, (lane >> 4) * 256 + (vchk ^ (16 * i)), WS_X1S + rbk * 131072 + s * 16384 + lw * 4096 + i * 1024);
    };
    poll16(rws, ((lane >> 3) * 32 + lw * 8 + (lane & 7)) * 64, WS_F2 + (xcd >> 1) * 16384, ta, tb);
    STAMP(19);
    x_stage(0); x_stage(1); WAIT_VM(4); BARRIER();
    x_stage(2); x_stage(3); WAIT_VM(8); BARRIER();
    x_stage(4); w1_stage(4); WAIT_VM(12); BARRIER();
#pragma unroll 1
    for (int s = 5; s < 8; ++s) { x_stage(s); w1_stage(s); WAIT_VM(16); BARRIER(); }
    w2_stage(0); WAIT_VM(12); BARRIER();
    w2_stage(1); WAIT_VM(8); BARRIER();
    w2_stage(2); BARRIER();
    w2_stage(3);
    STAMP(22);
  } else {
    const int wt = w & 1, wf = w >> 1;
    const int h = lane >> 5, blk = (lane >> 4) & 1, q = (lane & 15) >> 2, pp = lane & 3;
    int ax[2][2], aw[2][2];
#pragma unroll
    for (int c = 0; c < 2; ++c)
#pragma unroll
      for (int t = 0; t < 2; ++t) {
        const int base = 256 * (8 * h + 4 * t + q) + 16 * ((2 * blk + (pp >> 1)) ^ (2 * h + t)) + 8 * (pp & 1);
        ax[c][t] = LD_XR + base + 64 * ((2 * wt + c) ^ q);
        aw[c][t] = LD_WR + base + 64 * ((2 * wf + c) ^ q);
      }
    f32x16 acc[2][2];
#pragma unroll
    for (int a = 0; a < 2; ++a)
#pragma unroll
      for (int c = 0; c < 2; ++c)
#pragma unroll
        for (int r = 0; r < 16; ++r) acc[a][c][r] = 0.f;
    const int mi = LD_MISC + w * 4096;
    u32x4 xf[2][2], wfr[2][2], gq, sq;
#define RD_X(i, c) xf[(i) & 1][c] = tr_frag(smem + ax[c][0] + ((i) & 15) * 4096, smem + ax[c][1] + ((i) & 15) * 4096)
#define RD_W(i, c) wfr[(i) & 1][c] = tr_frag(smem + aw[c][0] + ((i) & 15) * 4096, smem + aw[c][1] + ((i) & 15) * 4096)
#define MM(i, a, c) do { SCHED(); acc[a][c] = mfma32(wfr[(i) & 1][a], xf[(i) & 1][c], acc[a][c]); SCHED(); } while (0)
    BARRIER();
    STAMP(19);
    RD_W(0, 0); RD_X(0, 0); RD_X(0, 1); RD_W(0, 1);
#pragma unroll
    for (int i = 0; i < 32; ++i) {
      if ((i & 3) != 3) {
        MM(i, 0, 0); RD_W(i + 1, 0); MM(i, 0, 1); RD_X(i + 1, 0); MM(i, 1, 0); RD_X(i + 1, 1); MM(i, 1, 1); RD_W(i + 1, 1);
      } else {
        MM(i, 0, 0); MM(i, 0, 1);
        BARRIER();
        if (i == 23) { gq = ld_sc1(rws, lane * 16, WS_G2 + rbk * 1024); sq = ld_sc1(rws, lane * 16, WS_S12 + (cbk * 128 + wf * 64) * 16); }
        MM(i, 1, 0);
        if (i < 31) { RD_W(i + 1, 0); RD_X(i + 1, 0); }
        MM(i, 1, 1);
        if (i < 31) { RD_X(i + 1, 1); RD_W(i + 1, 1); }
      }
    }
    STAMP(21);
    if (__any((tagdiff(gq, ta, tb) | tagdiff(sq, ta, tb)) != 0u)) {
      gq = poll16(rws, lane * 16, WS_G2 + rbk * 1024, ta, tb);
      sq = poll16(rws, lane * 16, WS_S12 + (cbk * 128 + wf * 64) * 16, ta, tb);
    }
    *(LAS u32x2*)(smem + mi + lane * 8) = u32x2{gq[0], gq[2]};
    *(LAS u32*)(smem + mi + 512 + lane * 4) = sq[0];
    *(LAS float*)(smem + mi + 768 + lane * 4) = __uint_as_float(sq[2]) * -LOG2E;
    float rstd[2], mr[2];
#pragma unroll
    for (int c = 0; c < 2; ++c) {
      const u32 st = *(LAS const u32*)(smem + mi + (wt * 64 + c * 32 + (lane & 31)) * 4);
      rstd[c] = __builtin_amdgcn_rcpf(bfhi(st)) * -LOG2E;
      mr[c] = -bflo(st) * rstd[c];
    }
    STAMP(22);
    LAS const float* s12 = (LAS const float*)(smem + mi + 512) + 4 * h;
    const int ho = WS_H + ((rbk * 4 + wt * 2) * 112 + cbk * 8 + wf * 4) * 1024 + lane * 16;
#pragma unroll
    for (int a = 0; a < 2; ++a)
#pragma unroll
      for (int sp = 0; sp < 2; ++sp) {
        f32x4 s1[2], s2[2];
#pragma unroll
        for (int z = 0; z < 2; ++z) { s1[z] = *(LAS const f32x4*)(s12 + a * 32 + sp * 16 + z * 8); s2[z] = *(LAS const f32x4*)(s12 + 64 + a * 32 + sp * 16 + z * 8); }
#pragma unroll
        for (int c = 0; c < 2; ++c) {
          u32x4 o;
#pragma unroll
          for (int e = 0; e < 4; ++e) {
            const int z = e >> 1, k = (e & 1) * 2;
            const f32x2 av = {acc[a][c][sp * 8 + 2 * e], acc[a][c][sp * 8 + 2 * e + 1]};
            const f32x2 cc = f32x2{s1[z][k], s1[z][k + 1]} * mr[c] + f32x2{s2[z][k], s2[z][k + 1]};
            const f32x2 ar = av * rstd[c] + cc;
            const f32x2 dn = f32x2{__builtin_amdgcn_exp2f(ar[0]), __builtin_amdgcn_exp2f(ar[1])} + 1.f;
            o[e] = pk2(ar * f32x2{__builtin_amdgcn_rcpf(dn[0]), __builtin_amdgcn_rcpf(dn[1])});
          }
          st_sc1(rws, ho, (c * 112 + a * 2 + sp) * 1024, o);
        }
      }
    STAMP(23);
    WAIT_VM(0);
    STAMP(24);
    if (lane < 8) st_sc1(rws, lane * 16, WS_F3 + ((rbk * 4 + w) * 16 + cbk) * 128, tags);
  }

  if (w >= 4) {
    const int sh = WS_H + ((tbk * 2 + (lw >> 1)) * 112 + 4 * (lw & 1)) * 1024;
    auto h_stage = [&](int s) {
#pragma unroll
      for (int i = 0; i < 4; ++i) dma16_sc1(qws, LD_XR + (s & 3) * UNIT + lw * 4096 + i * 1024, lane * 16, sh + s * 8192 + i * 1024);
    };
    if (lane < 14) poll16(rws, lane * 128, WS_F3 + ((tbk >> 1) * 4 + (lw & 1) * 2 + (tbk & 1)) * 2048, ta, tb);
    FENCE();
    STAMP(25);
    h_stage(0); h_stage(1); WAIT_VM(4); BARRIER();
    h_stage(2); h_stage(3); WAIT_VM(8); BARRIER();
    h_stage(4); w2_stage(4); WAIT_VM(12); BARRIER();
#pragma unroll 1
    for (int s = 5; s < 14; ++s) { h_stage(s); w2_stage(s); WAIT_VM(16); BARRIER(); }
    WAIT_VM(8); BARRIER();
    WAIT_VM(0); BARRIER();
    if (b == 0 && tid == 256) *(u32x2*)(p.ws + WS_EPOCH) = u32x2{ta, tb};
    STAMP(26);
    STAMP_FLUSH(p.ws, 1);
    return;
  }
  STAMP(25);
  {
    const int wt = w & 1, wd = w >> 1, h = lane >> 5, tl = lane & 31;
    const int mi = LD_MISC + w * 4096;
    dma16_sc1(qws, mi, (lane >> 2) * 128 + (lane & 3) * 16, WS_X1 + tbk * 65536 + (db * 32 + wd * 16) * 128 + wt * 64);
    dma16_sc1(qws, mi + 1024, (lane >> 2) * 128 + (lane & 3) * 16, WS_X1 + tbk * 65536 + 32768 + (db * 32 + wd * 16) * 128 + wt * 64);
    dma16_sc1(qws, mi + 2048, (lane & 15) * 16, WS_MF + 4096 + (db * 32 + wd * 16) * 16);
    const int blk = (lane >> 4) & 1, q = (lane & 15) >> 2, pp = lane & 3;
    const int ah = LD_XR + wt * 8192 + lane * 16;
    const int aw = LD_WR + 128 * (4 * h + q) + 64 * (wd ^ (q >> 1)) + 32 * blk + 8 * pp;
    f32x16 acc;
#pragma unroll
    for (int r = 0; r < 16; ++r) acc[r] = 0.f;
    WAIT_VM(0);
    f32x2 xr[8], gb[8];
#pragma unroll
    for (int i = 0; i < 8; ++i) {
      const int pr = 4 * (i >> 1) + 2 * h + (i & 1);
      xr[i] = up(*(LAS const u32*)(smem + mi + ((tl >> 2) & 1) * 1024 + pr * 64 + (tl >> 3) * 16 + (tl & 3) * 4));
      const u32x4 g = *(LAS const u32x4*)(smem + mi + 2048 + pr * 16);
      gb[i] = f32x2{__uint_as_float(g[0]), __uint_as_float(g[2])} * (-1.f / LOG2E);
      asm volatile("" : "+v"(xr[i]), "+v"(gb[i]));
    }
    u32x4 hf[4], wf[4];
#define RD2(sb, kk) do { wf[(kk) & 3] = tr_frag(sb + aw + 2048 * (kk), sb + aw + 2048 * (kk) + 1024); hf[(kk) & 3] = *(LAS const u32x4*)(sb + ah + (kk) * 1024); } while (0)
#define MM2(kk) do { SCHED(); acc = mfma32(wf[(kk) & 3], hf[(kk) & 3], acc); SCHED(); } while (0)
    BARRIER();
    STAMP(26);
    RD2(smem, 0); RD2(smem, 1); RD2(smem, 2);
#pragma unroll 1
    for (int s = 0; s < 13; ++s) {
      LAS char* sb = smem + (s & 3) * UNIT;
      LAS char* sn = smem + ((s + 1) & 3) * UNIT;
      RD2(sb, 3); MM2(0); RD2(sb, 4); MM2(1); RD2(sb, 5); MM2(2); RD2(sb, 6); MM2(3); RD2(sb, 7); MM2(4); MM2(5);
      BARRIER();
      RD2(sn, 0); RD2(sn, 1); MM2(6); RD2(sn, 2); MM2(7);
    }
    {
      LAS char* sb = smem + UNIT;
      RD2(sb, 3); MM2(0); RD2(sb, 4); MM2(1); RD2(sb, 5); MM2(2); RD2(sb, 6); MM2(3); RD2(sb, 7); MM2(4); MM2(5); MM2(6); MM2(7);
    }
    STAMP(27);
    LAS char* ot = smem + LD_XR + 2 * UNIT;
#pragma unroll
    for (int qq = 0; qq < 4; ++qq) {
      u32x2 o;
#pragma unroll
      for (int z = 0; z < 2; ++z) o[z] = pk2(f32x2{acc[4 * qq + 2 * z], acc[4 * qq + 2 * z + 1]} * gb[2 * qq + z] + xr[2 * qq + z]);
      *(LAS u32x2*)(ot + (wt * 32 + tl) * 144 + wd * 64 + qq * 16 + h * 8) = o;
    }
    BARRIER();
#pragma unroll
    for (int i = 0; i < 2; ++i) {
      const int row = (tid >> 3) + 32 * i, r = tbk * 64 + row;
      const int t = (r & ~511) + 128 * ((r >> 1) & 3) + 2 * ((r >> 3) & 63) + (r & 1);
      *(u32x4*)(p.out + (size_t)t * D + db * 64 + (tid & 7) * 8) = *(LAS const u32x4*)(ot + row * 144 + (tid & 7) * 16);
    }
    STAMP(28);
    STAMP_FLUSH(p.ws, 0);
  }
}

extern "C" __global__ void fused_block_end() {}

extern "C" void kernel_launch(void* const* d_in, const int* in_sizes, int n_in, void* d_out, int out_size, void* d_ws, size_t ws_size,
                              hipStream_t stream) {
  Params p;
  p.x = (const u16*)d_in[0]; p.Are = (const u16*)d_in[1]; p.Aim = (const u16*)d_in[2]; p.Bre = (const u16*)d_in[3];
  p.Bim = (const u16*)d_in[4]; p.Cre = (const u16*)d_in[5]; p.Cim = (const u16*)d_in[6]; p.ldt = (const u16*)d_in[7];
  p.dskip = (const u16*)d_in[8]; p.w1 = (const u16*)d_in[10]; p.w2 = (const u16*)d_in[11]; p.c = (const u16*)d_in[12];
  p.wmod = (const u16*)d_in[13]; p.out = (u16*)d_out; p.ws = (char*)d_ws;
  (void)hipFuncSetAttribute((const void*)fused_block, hipFuncAttributeMaxDynamicSharedMemorySize, LDS_BYTES + ST_LDS_EXTRA);
  fused_block<<<256, 512, LDS_BYTES + ST_LDS_EXTRA, stream>>>(p);
}
```

```cpp
#define PF_N 1
#include <hip/hip_runtime.h>
#include <hip/hip_bf16.h>

#define STAMP_DECL
#define STAMP(s)
#define STAMP_FLUSH(ws, k)
#define ST_LDS_EXTRA 0
#define ST_RANGES {{0, 32, 256, "compute waves"}, {1, 32, 256, "loader waves"}, {0, 0, 32, "helper WGs, waves 0-3"}, {1, 0, 32, "helper WGs, waves 4-7"}}

typedef unsigned short u16;
typedef unsigned int u32;
typedef unsigned long long u64;
typedef u32 u32x2 __attribute__((ext_vector_type(2)));
typedef u32 u32x4 __attribute__((ext_vector_type(4)));
typedef float f32x2 __attribute__((ext_vector_type(2)));
typedef float f32x4 __attribute__((ext_vector_type(4)));
typedef float f32x16 __attribute__((ext_vector_type(16)));
typedef short s16x4 __attribute__((ext_vector_type(4)));
typedef __bf16 bf16x2 __attribute__((ext_vector_type(2)));
typedef __bf16 bf16x8 __attribute__((ext_vector_type(8)));
typedef __amdgpu_buffer_rsrc_t rsrc_t;
#define GAS __attribute__((address_space(1)))
#define LAS __attribute__((address_space(3)))
#define CAS __attribute__((address_space(4)))

constexpr int L = 2048, D = 512, NS = 128, F = 1792;

constexpr int WS_EPOCH = 0;
constexpr int WS_SG = 0x1000;
constexpr int WS_MG = 0x8000;
constexpr int WS_MH = 0x80000;
constexpr int WS_F2 = 0x40000;
constexpr int WS_F3 = 0x50000;
constexpr int WS_G2 = 0x70000;
constexpr int WS_S12 = 0x74000;
constexpr int WS_MF = 0x7c000;
constexpr int WS_XG = 1 << 20;
constexpr int WS_X1 = 5 << 20;
constexpr int WS_X1S = 7 << 20;
constexpr int WS_H = 9 << 20;
constexpr int WS_END = 16 << 20;

constexpr int UNIT = 16384;
constexpr int LD_XR = 0, LD_WR = 4 * UNIT, LD_MISC = 9 * UNIT, LDS_BYTES = LD_MISC + 4 * 3072;
constexpr int TCOPY = 4160, PROW = 132;
constexpr int LD_T = 0, LD_U = 8 * TCOPY, LD_KD = LD_U + 2 * 4608, LD_P = LD_WR, LD_RED0 = 3 * UNIT, LD_RED1 = 8 * UNIT;
constexpr int KD_ZERO = 900, LD_PS = LD_KD + 904 * 4, LD_PG = LD_PS + 256, LD_CNT = LD_PG + 1024, LD_XT = LD_RED0;
static_assert(LD_CNT + 16 <= LD_RED0, "LDS map");
__host__ __device__ constexpr int w2_unit(int u) { return u % 9 == 8 ? 8 : (u % 9) / 2 + 4 * (u % 9 & 1); }
#define G2_NW 14
#define F2_DELAY 16
#define POLL_SLEEP 2
#define LDNT(p) __builtin_nontemporal_load(p)
#define AUX_NT 2
#define WF ""
#define AUX_W 0
constexpr int SPIN_CAP = 1 << 15;
constexpr float LOG2E = 1.44269504088896341f;

__device__ __forceinline__ float bflo(u32 v) { return __uint_as_float(v << 16); }
__device__ __forceinline__ float bfhi(u32 v) { return __uint_as_float(v & 0xffff0000u); }
__device__ __forceinline__ u32 pk(float lo, float hi) {
  f32x2 v = {lo, hi};
  bf16x2 r = __builtin_convertvector(v, bf16x2);
  return __builtin_bit_cast(u32, r);
}
__device__ __forceinline__ float rb(float f) { return bflo(pk(f, f)); }
__device__ __forceinline__ float dot2(u32 a, u32 b, float c) {
  return __builtin_amdgcn_fdot2_f32_bf16(__builtin_bit_cast(bf16x2, a), __builtin_bit_cast(bf16x2, b), c, false);
}
__device__ __forceinline__ f32x4 mfma16(u32x4 a, u32x4 b, f32x4 c) {
  return __builtin_amdgcn_mfma_f32_16x16x32_bf16(__builtin_bit_cast(bf16x8, a), __builtin_bit_cast(bf16x8, b), c, 0, 0, 0);
}
__device__ __forceinline__ f32x16 mfma32(u32x4 a, u32x4 b, f32x16 c) {
  return __builtin_amdgcn_mfma_f32_32x32x16_bf16(__builtin_bit_cast(bf16x8, a), __builtin_bit_cast(bf16x8, b), c, 0, 0, 0);
}
template <int CTRL> __device__ __forceinline__ float dpp(float x) {
  return __builtin_bit_cast(float, __builtin_amdgcn_mov_dpp(__builtin_bit_cast(int, x), CTRL, 0xf, 0xf, true));
}
__device__ __forceinline__ float row_sum(float x) {
  x += dpp<0xB1>(x); x += dpp<0x4E>(x); x += dpp<0x124>(x); x += dpp<0x128>(x);
  return x;
}
__device__ __forceinline__ float fold32(float a, float b) {
  auto s = __builtin_amdgcn_permlane32_swap(__float_as_uint(a), __float_as_uint(b), false, false);
  return __uint_as_float(s[0]) + __uint_as_float(s[1]);
}
__device__ __forceinline__ float fold16(float a, float b) {
  auto s = __builtin_amdgcn_permlane16_swap(__float_as_uint(a), __float_as_uint(b), false, false);
  return __uint_as_float(s[0]) + __uint_as_float(s[1]);
}
__device__ __forceinline__ u32x4 mkrs(const void* p, u32 bytes) {
  const u64 a = (u64)p;
  return u32x4{(u32)a, (u32)(a >> 32) & 0xffffu, bytes, 0x00020000u};
}
__device__ __forceinline__ void dma16_sc1(u32x4 rs, int lds, int voff, int soff) {
  asm volatile("s_mov_b32 m0, %0\n\ts_nop 0\n\tbuffer_load_dwordx4 %1, %2, %3 offen sc1 lds" :: "s"(lds), "v"(voff), "s"(rs), "s"(soff) : "memory");
}
#define DMA4_TEXT(F) "s_mov_b32 m0, %0\n\ts_nop 0\n\tbuffer_load_dwordx4 %1, %5, %6 offen" F " lds\n\tbuffer_load_dwordx4 %2, %5, %6 offen offset:1024" F " lds\n\t" \
                     "buffer_load_dwordx4 %3, %5, %6 offen offset:2048" F " lds\n\tbuffer_load_dwordx4 %4, %5, %6 offen offset:3072" F " lds"
__device__ __forceinline__ void dma4k(u32x4 rs, int lds, const int (&v)[4], int soff) {
  asm volatile(DMA4_TEXT(WF) :: "s"(lds), "v"(v[0]), "v"(v[1]), "v"(v[2]), "v"(v[3]), "s"(rs), "s"(soff) : "memory");
}
__device__ __forceinline__ void dma4k_sc1(u32x4 rs, int lds, const int (&v)[4], int soff) {
  asm volatile(DMA4_TEXT(" sc1") :: "s"(lds), "v"(v[0]), "v"(v[1]), "v"(v[2]), "v"(v[3]), "s"(rs), "s"(soff) : "memory");
}
__device__ __forceinline__ u32x4 ld_sc1(rsrc_t rs, int voff, int soff) { return __builtin_amdgcn_raw_buffer_load_b128(rs, voff, soff, 16); }
__device__ __forceinline__ void st_sc1(rsrc_t rs, int voff, int soff, u32x4 v) { __builtin_amdgcn_raw_buffer_store_b128(v, rs, voff + soff, 0, 16); }
__device__ __forceinline__ u32x4 tr_frag(LAS char* a0, LAS char* a1) {
  const s16x4 lo = __builtin_amdgcn_ds_read_tr16_b64_v4i16((LAS s16x4*)a0), hi = __builtin_amdgcn_ds_read_tr16_b64_v4i16((LAS s16x4*)a1);
  const u32x2 l = __builtin_bit_cast(u32x2, lo), h = __builtin_bit_cast(u32x2, hi);
  return u32x4{l[0], l[1], h[0], h[1]};
}
#define SCHED() __builtin_amdgcn_sched_barrier(0)
#define FENCE() asm volatile("" ::: "memory")
#define WAIT_VM(n) asm volatile("s_waitcnt vmcnt(" #n ")" ::: "memory")
#define BARRIER() do { asm volatile("s_waitcnt lgkmcnt(0)" ::: "memory"); __builtin_amdgcn_s_barrier(); FENCE(); } while (0)

__device__ __forceinline__ f32x2 cmul(f32x2 a, f32x2 b) {
  const f32x2 t = f32x2{a[0], a[0]} * b;
  return __builtin_elementwise_fma(f32x2{a[1], a[1]}, f32x2{-b[1], b[0]}, t);
}
__device__ __forceinline__ f32x2 cpow(float a2, float bq, float l) {
  const float m = __builtin_amdgcn_exp2f(a2 * l);
  const float ph = __builtin_amdgcn_fractf(bq * l);
  return f32x2{__builtin_amdgcn_cosf(ph), __builtin_amdgcn_sinf(ph)} * m;
}
__device__ __forceinline__ u32 pk2(f32x2 a) { return pk(a[0], a[1]); }
__device__ __forceinline__ f32x2 up(u32 v) { return f32x2{bflo(v), bfhi(v)}; }
__device__ __forceinline__ u32 ln_stat(float sum, float sq) {
  const float m = sum * (1.f / D);
  const float var = fmaxf(sq * (1.f / D) - m * m, 0.f);
  return pk(m, __builtin_amdgcn_sqrtf(rb(rb(var) + 1e-5f)));
}
__device__ __forceinline__ u32 tagdiff(u32x4 v, u32 ta, u32 tb) { return (v[1] ^ ta) | (v[3] ^ tb); }
__device__ __forceinline__ u32x4 poll16(rsrc_t rs, int voff, int soff, u32 ta, u32 tb) {
  u32x4 v;
  int spins = 0;
  for (;;) {
    FENCE();
    v = ld_sc1(rs, voff, soff);
    if (tagdiff(v, ta, tb) == 0u || ++spins >= SPIN_CAP) break;
    if (POLL_SLEEP) __builtin_amdgcn_s_sleep(POLL_SLEEP);
  }
  FENCE();
  return v;
}
__device__ __forceinline__ void fold_cols(const float (&v)[16], LAS float* dst, int lane) {
  float u[8]; f32x4 t;
#pragma unroll
  for (int k = 0; k < 8; ++k) u[k] = fold32(v[k], v[k + 8]);
#pragma unroll
  for (int k = 0; k < 4; ++k) { const float a = fold16(u[k], u[k + 4]); t[k] = a + dpp<0x128>(a); }
  if (!(lane & 8)) *(LAS f32x4*)(dst + (lane & 7) * 16 + (lane >> 4) * 4) = t;
}
__device__ __forceinline__ f32x2 sum4(LAS const float* src, int stride) {
  return (*(LAS const f32x2*)src + *(LAS const f32x2*)(src + stride)) + (*(LAS const f32x2*)(src + 2 * stride) + *(LAS const f32x2*)(src + 3 * stride));
}
__device__ __forceinline__ u32 silu_bf(u32 c) {
  const float f = bflo(c), s = f * __builtin_amdgcn_rcpf(1.f + __builtin_amdgcn_exp2f(f * -LOG2E));
  return pk(s, s);
}
template <int CTRL> __device__ __forceinline__ void gv_row(u32x4 v, u32 sp, float (&acc)[8]) {
  const u32 sb = __builtin_amdgcn_mov_dpp(sp, CTRL, 0xf, 0xf, true), al = sb & 0xffffu, ah = sb & 0xffff0000u;
#pragma unroll
  for (int q = 0; q < 4; ++q) { acc[2 * q] = dot2(v[q], al, acc[2 * q]); acc[2 * q + 1] = dot2(v[q], ah, acc[2 * q + 1]); }
}
__device__ __forceinline__ f32x2 gv_fold(const float (&acc)[8]) {
  const float m0 = fold16(fold32(acc[0], acc[4]), fold32(acc[2], acc[6])), m1 = fold16(fold32(acc[1], acc[5]), fold32(acc[3], acc[7]));
  return f32x2{m0 + dpp<0x128>(m0), m1 + dpp<0x128>(m1)};
}
#define TOUCH(dst, voff, base) asm volatile("global_load_dword %0, %1, %2" : "=v"(dst) : "v"(voff), "s"(base))

struct Params {
  const u16 *x, *Are, *Aim, *Bre, *Bim, *Cre, *Cim, *ldt, *dskip, *w1, *w2, *c, *wmod;
  u16* out;
  char* ws;
};

extern "C" __global__ void __launch_bounds__(512, 2) fused_block(Params p) {
  extern __shared__ __attribute__((aligned(16))) char smem_[];
  LAS char* smem = (LAS char*)smem_;
  const int tid = threadIdx.x, lane = tid & 63, w = __builtin_amdgcn_readfirstlane(tid >> 6), lw = w & 3;
  const int b = blockIdx.x, xcd = b & 7, j = b >> 3;
  const int pair = xcd * 32 + j, d0 = pair * 2;
  STAMP_DECL
  STAMP(0);
  const rsrc_t rws = __builtin_amdgcn_make_buffer_rsrc(p.ws, 0, WS_END, 0x00020000);
  const u32x4 qws = mkrs(p.ws, WS_END);
  asm volatile("" :: "s"(p.x), "s"(p.Are), "s"(p.Aim), "s"(p.Bre), "s"(p.Bim), "s"(p.Cre), "s"(p.Cim), "s"(p.w1), "s"(p.w2), "s"(p.c), "s"(p.wmod), "s"(p.out));
#define SCAL_ISSUE() u64 epoch; u32 ldt2, dsk; \
  asm volatile("s_load_dwordx2 %0, %3, 0x0\n\ts_load_dword %1, %4, 0x0\n\ts_load_dword %2, %5, 0x0" \
               : "=&s"(epoch), "=&s"(ldt2), "=&s"(dsk) : "s"(p.ws + WS_EPOCH), "s"(p.ldt + d0), "s"(p.dskip + d0)); SCHED()
#define SCAL_WAIT() asm volatile("s_waitcnt lgkmcnt(0)" : "+s"(epoch), "+s"(ldt2), "+s"(dsk)); \
  epoch += 0x9E3779B97F4A7C15ull; ta = (u32)epoch; tb = (u32)(epoch >> 32)
  u32 ta, tb;
  const int pch = lw >> 1, pn = (lw & 1) * 64 + lane;
  const int po = (d0 + pch) * NS + pn;
  volatile LAS u32* P = (LAS u32*)(smem + LD_P) + pch * 64 * PROW + pn;
  const bool helper = j < 4;
  const int rbk = (xcd >> 1) * 4 + (j & 3), cbk = (xcd & 1) * 7 + ((j - 4) >> 2);
  const int tbk = xcd * 4 + (j & 3), db = j >> 2;

  if (__builtin_expect(w >= 4, 1)) {
    SCAL_ISSUE();
    __builtin_amdgcn_s_setprio(2);
    const u16* xr = p.x + (size_t)(b * 8 + (lane >> 4)) * D + lw * 128 + (lane & 15) * 8;
    u32x4 xa = LDNT((const u32x4*)xr), xb = LDNT((const u32x4*)(xr + 4 * D));
    const int k0 = (b >> 4) * 32 + lw * 8 + (lane >> 3);
    u32 c0 = p.c[k0];
    u32x4 wm = LDNT((const u32x4*)(p.wmod + (size_t)k0 * 3072 + (b & 15) * 64 + (lane & 7) * 8));
    u32 rar = LDNT(p.Are + po), rai = LDNT(p.Aim + po);
    SCHED();
    SCAL_WAIT();
    if (lw == 0) {
      *(LAS u32x4*)(smem + LD_U + (lane >> 5) * 4608 + (lane & 31) * 16) = u32x4{0u, 0u, 0u, 0u};
      if (lane < 16) *(LAS u32x4*)(smem + LD_T + (lane >> 1) * TCOPY + 4096 + (lane & 1) * 16) = u32x4{0u, 0u, 0u, 0u};
      *(LAS u32*)(smem + LD_KD + KD_ZERO * 4) = 0u;
      *(LAS u32*)(smem + LD_CNT) = 0u;
    }
    BARRIER();
    STAMP(1);
    {
      LAS char* xt = smem + LD_XT + lw * 2176;
      *(LAS u32x4*)(xt + (lane >> 4) * 272 + (lane & 15) * 16) = xa;
      *(LAS u32x4*)(xt + (lane >> 4) * 272 + 1088 + (lane & 15) * 16) = xb;
      const u32x4 v0 = *(LAS const u32x4*)(xt + (lane & 3) * 544 + (lane >> 2) * 16), v1 = *(LAS const u32x4*)(xt + (lane & 3) * 544 + 272 + (lane >> 2) * 16);
      f32x4 ps = {0.f, 0.f, 0.f, 0.f};
#pragma unroll
      for (int z = 0; z < 4; ++z) {
        ps[0] = dot2(xa[z], 0x3f803f80u, ps[0]); ps[1] = dot2(xa[z], xa[z], ps[1]);
        ps[2] = dot2(xb[z], 0x3f803f80u, ps[2]); ps[3] = dot2(xb[z], xb[z], ps[3]);
      }
      SCHED();
      const int xo = WS_XG + (lw * 64 + (lane >> 2) * 4) * 16384 + (lane & 3) * 16 + b * 64;
#pragma unroll
      for (int q = 0; q < 4; ++q) st_sc1(rws, xo, q * 16384, u32x4{v0[q], ta, v1[q], tb});
      SCHED();
#pragma unroll
      for (int z = 0; z < 4; ++z) ps[z] = row_sum(ps[z]);
      if (!(lane & 15)) {
        *(LAS f32x2*)(smem + LD_PS + lw * 64 + (lane >> 4) * 8) = f32x2{ps[0], ps[1]};
        *(LAS f32x2*)(smem + LD_PS + lw * 64 + 32 + (lane >> 4) * 8) = f32x2{ps[2], ps[3]};
      }
      asm volatile("" : "+v"(c0), "+v"(wm), "+v"(rar), "+v"(rai));
      float acc[8] = {0.f, 0.f, 0.f, 0.f, 0.f, 0.f, 0.f, 0.f};
      gv_row<0xE4>(wm, silu_bf(c0), acc);
      const f32x2 m = gv_fold(acc);
      if (!(lane & 8)) *(LAS f32x2*)(smem + LD_PG + lw * 256 + ((lane >> 4) * 8 + (lane & 7)) * 8) = m;
      asm volatile("s_waitcnt lgkmcnt(0)" ::: "memory");
      u32 cnt = 0u;
      if (lane == 0) cnt = __hip_atomic_fetch_add((LAS u32*)(smem + LD_CNT), 1u, __ATOMIC_RELAXED, __HIP_MEMORY_SCOPE_WORKGROUP);
      if (__builtin_amdgcn_readfirstlane(cnt) == 3u) {
        FENCE();
        const f32x2 sq = sum4((LAS const float*)(smem + LD_PS) + (lane & 7) * 2, 16), g = sum4((LAS const float*)(smem + LD_PG) + (lane & 31) * 2, 64);
        const u32 st = ln_stat(sq[0], sq[1]), st2 = __builtin_amdgcn_mov_dpp(st, 0xB1, 0xf, 0xf, true);
        const bool hi = lane >= 32;
        if (lane < 40 && !(hi && (lane & 1)))
          st_sc1(rws, hi ? WS_SG + b * 64 + (lane & 7) * 8 : WS_MG + ((b >> 4) * 512 + (b & 15) * 32 + lane) * 16, 0,
                 u32x4{hi ? st : __float_as_uint(g[0]), ta, hi ? st2 : __float_as_uint(g[1]), tb});
      }
    }
    __builtin_amdgcn_s_setprio(0);
    STAMP(2);
    {
      const float dt = rb(__builtin_amdgcn_exp2f((pch ? bfhi(ldt2) : bflo(ldt2)) * LOG2E));
      const float a2 = (dt * bflo(rar)) * LOG2E, bq = (dt * bflo(rai)) * 0.15915494309189535f;
      const f32x2 z2 = cpow(a2, bq, 2.f), z256 = cpow(a2, bq, 256.f), z4 = cmul(z2, z2), z8 = cmul(z4, z4);
      f32x2 a = z256, c = cmul(z256, z256), d = cmul(c, z256), u = cpow(a2, bq, 1024.f);
#pragma unroll
      for (int e = 0; e < 8; ++e) {
        P[(32 + e) * PROW] = pk2(a);
        a = cmul(a, z2);
        if (e & 1) { P[(40 + e / 2) * PROW] = pk2(c); P[(44 + e / 2) * PROW] = pk2(d); c = cmul(c, z4); d = cmul(d, z4); }
        else { P[(48 + e) * PROW] = pk2(u); P[(49 + e) * PROW] = pk2(cmul(u, z8)); u = cmul(u, z256); }
      }
      P[56 * PROW] = pk2(u);
    }
    STAMP(3);
    BARRIER();
    u32x4 xg[4], sg[4], mg;
    const int go = lw * 4096 + lane * 16;
    const int mslot = (pair >> 5) * 32 + (pair & 3) * 8 + ((pair >> 2) & 7);
    const int mo = WS_MG + ((lane & 15) * 512 + ((lane >> 4) & 1) * 256 + mslot) * 16;
#pragma unroll
    for (int i = 0; i < 4; ++i) xg[i] = ld_sc1(rws, go + i * 1024, WS_XG + pair * 16384);
    BARRIER();
    __builtin_amdgcn_s_setprio(2);
    STAMP(6);
    for (int spins = 0;; ++spins) {
      FENCE();
      mg = ld_sc1(rws, mo, 0);
#pragma unroll
      for (int i = 0; i < 4; ++i) sg[i] = ld_sc1(rws, go + i * 1024, WS_SG);
      u32 bad = tagdiff(mg, ta, tb);
#pragma unroll
      for (int i = 0; i < 4; ++i) bad |= tagdiff(sg[i], ta, tb);
      if (!__any(bad != 0u) || spins >= SPIN_CAP) break;
    }
    STAMP(8);
#pragma unroll 1
    for (int spins = 0; spins < SPIN_CAP; ++spins) {
      u32 bad = 0u;
#pragma unroll
      for (int i = 0; i < 4; ++i) bad |= tagdiff(xg[i], ta, tb);
      if (__builtin_expect(!__any(bad != 0u), 1)) break;
      FENCE();
#pragma unroll
      for (int i = 0; i < 4; ++i) xg[i] = ld_sc1(rws, go + i * 1024, WS_XG + pair * 16384);
    }
    STAMP(9);
    const int mho = ((lane & 7) * 1024 + ((lane >> 3) & 3) * 256 + mslot) * 16;
    u32x4 mh = ld_sc1(rws, mho, WS_MH);
    SCHED();
    f32x2 xf[4][2], hf[4][2];
    {
      const u32 mb = pk(row_sum(__uint_as_float(mg[0])), row_sum(__uint_as_float(mg[2])));
      const f32x2 sa = up(__builtin_amdgcn_readlane(mb, 0)), g1 = up(__builtin_amdgcn_readlane(pk2(up(mb) + 1.f), 16));
#pragma unroll
      for (int i = 0; i < 4; ++i) {
#pragma unroll
        for (int e = 0; e < 2; ++e) {
          xf[i][e] = up(xg[i][2 * e]);
          hf[i][e] = (xf[i][e] - bflo(sg[i][2 * e])) * __builtin_amdgcn_rcpf(bfhi(sg[i][2 * e])) * g1 + sa;
        }
        LAS u32* U = (LAS u32*)(smem + LD_U + 512 + lw * 1024 + i * 256) + lane;
        U[0] = pk(hf[i][0][0], hf[i][1][0]);
        U[4608 / 4] = pk(hf[i][0][1], hf[i][1][1]);
      }
    }
    STAMP(10);
    BARRIER();
    STAMP(11);
    const u32x4 qw1 = mkrs(p.w1, D * F * 2), qw2 = mkrs(p.w2, D * F * 2);
    int vx[4], vw1[4], vw2[4];
#pragma unroll
    for (int i = 0; i < 4; ++i) {
      const int chk = 16 * ((lane & 15) ^ ((lane >> 4) << 2) ^ i);
      vx[i] = (lane >> 4) * 256 + chk;
      vw1[i] = (lane >> 4) * (F * 2) + chk + i * (4 * F * 2 - 1024);
      vw2[i] = (lane >> 3) * 1024 + db * 128 + 16 * ((lane & 7) ^ (((lane >> 4) & 1) << 2)) + i * 7168;
    }
    const int sw2 = ((lw >> 1) * 896 + (lw & 1) * 32) * 1024;
    auto w2_stage = [&](int u) { dma4k(qw2, w2_unit(u) * UNIT + lw * 4096, vw2, sw2 + u * 65536); };
    auto w1_stage = [&](int s) { dma4k(qw1, LD_WR + (s & 3) * UNIT + lw * 4096, vw1, (s * 64 + 16 * lw) * (F * 2) + cbk * 256); };
    auto x_stage = [&](int s) { dma4k_sc1(qws, LD_XR + (s & 3) * UNIT + lw * 4096, vx, WS_X1S + rbk * 131072 + s * 16384 + lw * 4096); };
    if (__any(tagdiff(mh, ta, tb) != 0u)) mh = poll16(rws, mho, WS_MH, ta, tb);
    if (!helper) { w1_stage(0); w1_stage(1); w1_stage(2); w1_stage(3); }
    f32x2 gav, gcv, xc[4][2];
    {
      float m0 = __uint_as_float(mh[0]), m1 = __uint_as_float(mh[2]);
      m0 += dpp<0xB1>(m0); m0 += dpp<0x4E>(m0); m0 += dpp<0x141>(m0);
      asm volatile("" : "+v"(m0));
      m1 += dpp<0xB1>(m1); m1 += dpp<0x4E>(m1); m1 += dpp<0x141>(m1);
      asm volatile("" : "+v"(m1));
      const u32 mb = pk(m0, m1);
      float p0 = bflo(mb) + 1.f, p1 = bfhi(mb) + 1.f;
      asm volatile("" : "+v"(p0)); asm volatile("" : "+v"(p1));
      const u32 gcb = __builtin_amdgcn_readlane(pk(p0, p1), 16), sbb = __builtin_amdgcn_readlane(mb, 8), gbb = __builtin_amdgcn_readlane(mb, 24);
      if (w == 7 && lane < 2)
        st_sc1(rws, lane * 4096, WS_MF + pair * 16, lane ? u32x4{gbb << 16, ta, gbb & 0xffff0000u, tb} : u32x4{(gcb & 0xffffu) | (sbb << 16), ta, (gcb >> 16) | (sbb & 0xffff0000u), tb});
      gav = up(__builtin_amdgcn_readlane(mb, 0)); gcv = up(gcb);
      float gd[2] = {gav[0] * bflo(dsk), gav[1] * bfhi(dsk)};
      asm volatile("" : "+v"(gd[0])); asm volatile("" : "+v"(gd[1]));
#pragma unroll
      for (int i = 0; i < 4; ++i)
#pragma unroll
        for (int c = 0; c < 2; ++c) {
          float a0 = __builtin_fmaf(hf[i][0][c], gd[c], xf[i][0][c]), a1 = __builtin_fmaf(hf[i][1][c], gd[c], xf[i][1][c]);
          asm volatile("" : "+v"(a0)); asm volatile("" : "+v"(a1));
          xc[i][c] = f32x2{a0, a1};
        }
    }
    STAMP(12);
    SCHED();
    u32x4 wr[20];
    {
      const rsrc_t rw2 = __builtin_amdgcn_make_buffer_rsrc((void*)p.w2, 0, D * F * 2, 0x00020000);
#pragma unroll
      for (int i = 0; i < 20; ++i) wr[i] = __builtin_amdgcn_raw_buffer_load_b128(rw2, vw2[i & 3] + (i & 3) * 1024, sw2 + (9 + i / 4) * 65536, AUX_W);
    }
    SCHED();
    const int g = (lane >> 1) & 3;
    const int ro = (2 * lw * 64 + 16 * g + ((lane >> 3) ^ (4 * g))) * 16 + 8 * (lane & 1);
    STAMP(13);
    BARRIER();
    STAMP(14);
    {
      u32x4 o[2], s0, s1;
      f32x2 yp[4][4];
#pragma unroll
      for (int i = 0; i < 4; ++i) {
        const int a = (ro ^ (128 * (i & 1))) + (i >> 1) * 1024;
        yp[i][0] = *(LAS const f32x2*)(smem + LD_RED0 + a); yp[i][1] = *(LAS const f32x2*)(smem + LD_RED0 + 8192 + a);
        yp[i][2] = *(LAS const f32x2*)(smem + LD_RED1 + a); yp[i][3] = *(LAS const f32x2*)(smem + LD_RED1 + 8192 + a);
      }
      SCHED();
#pragma unroll
      for (int i = 0; i < 4; ++i) {
        const f32x2 y0 = yp[i][0] + yp[i][1], y1 = yp[i][2] + yp[i][3];
        const f32x2 c0 = y0 * gav[0] + xc[i][0], c1 = y1 * gav[1] + xc[i][1];
        s0[i] = pk2(c0 * gcv[0]); s1[i] = pk2(c1 * gcv[1]);
        o[i >> 1][2 * (i & 1)] = pk(c0[0], c1[0]); o[i >> 1][2 * (i & 1) + 1] = pk(c0[1], c1[1]);
      }
      const int so = WS_X1S + (lw * 4 + (lane >> 4)) * 131072 + d0 * 256 + (lane & 15) * 16;
      st_sc1(rws, so, 0, s0);
      st_sc1(rws, so, 256, s1);
      const int xo = WS_X1 + (lw * 8 + (lane >> 3)) * 65536 + pair * 128 + (lane & 7) * 16;
      st_sc1(rws, xo, 0, o[0]);
      st_sc1(rws, xo, 32768, o[1]);
    }
    STAMP(15);
    WAIT_VM(0);
    STAMP(17);
    if (lane < 4) st_sc1(rws, lane * 16, WS_F2 + (lw * 256 + pair) * 64, u32x4{ta, ta, tb, tb});
    __builtin_amdgcn_s_setprio(0);
    if (helper) {
      BARRIER(); BARRIER(); BARRIER(); BARRIER();
      w2_stage(0); w2_stage(1); w2_stage(2); w2_stage(3); w2_stage(4); w2_stage(5);
    } else {
      if (F2_DELAY) __builtin_amdgcn_s_sleep(F2_DELAY);
      poll16(rws, ((lane >> 3) * 32 + lw * 8 + (lane & 7)) * 64, WS_F2 + (xcd >> 1) * 16384, ta, tb);
      STAMP(19);
      x_stage(0); x_stage(1); WAIT_VM(4); BARRIER();
      x_stage(2); x_stage(3); WAIT_VM(8); BARRIER();
      x_stage(4); w1_stage(4); WAIT_VM(12); BARRIER();
#pragma unroll 1
      for (int s = 5; s < 8; ++s) { x_stage(s); w1_stage(s); WAIT_VM(16); BARRIER(); }
      w2_stage(0); w2_stage(1); WAIT_VM(16); BARRIER();
      w2_stage(2); w2_stage(3); WAIT_VM(16); BARRIER();
      w2_stage(4); w2_stage(5); BARRIER();
    }
    STAMP(22);
    w2_stage(6); w2_stage(7); w2_stage(8);
    WAIT_VM(0);
    STAMP(25);
    BARRIER();
    BARRIER();
#pragma unroll
    for (int i = 0; i < 20; ++i) {
      if (i == 8) BARRIER();
      *(LAS u32x4*)(smem + w2_unit(9 + i / 4) * UNIT + lw * 4096 + (i & 3) * 1024 + lane * 16) = wr[i];
    }
    BARRIER();
    if (b == 0 && tid == 256) *(u32x2*)(p.ws + WS_EPOCH) = u32x2{ta, tb};
    STAMP(26);
    STAMP_FLUSH(p.ws, 1);
    return;
  }

  {
    SCAL_ISSUE();
    u32 rar = LDNT(p.Are + po), rai = LDNT(p.Aim + po), rbr = LDNT(p.Bre + po), rbi = LDNT(p.Bim + po), rcr = LDNT(p.Cre + po), rci = LDNT(p.Cim + po);
    SCHED();
    u32 cp0;
    {
      u64 pc; int r0, r1;
      asm volatile("s_getpc_b64 %0\n\ts_mov_b32 %1, fused_block@rel32@lo+4\n\ts_mov_b32 %2, fused_block_end@rel32@lo+12" : "=s"(pc), "=s"(r0), "=s"(r1));
      TOUCH(cp0, min(((w * 4 + (lane & 3)) * 32 + j) * 128, r1 - r0), pc + (long long)r0);
    }
    SCHED();
    SCAL_WAIT();
    BARRIER();
    int ga[11], ka[4];
#pragma unroll
    for (int i = 0; i < 11; ++i) {
      const int l = 2047 - (tid * 8 + i);
      const int bits = (int)(pk((float)l, 0.f) & 0xffffu);
      const int idx = l < 128 ? l : bits - 0x4300 + 128;
      ga[i] = LD_KD + 4 * (l < 0 ? KD_ZERO : idx + (idx >> 3));
      asm volatile("" : "+v"(ga[i]));
    }
    {
      const int e = lane & 15, g = lane >> 4;
      const u32 cfg = e < 8 ? (w == 0 ? 0x100004u : w == 1 ? 0x080004u : 0x022001u) : (w == 0 ? 0x100004u : w == 1 ? 0x044002u : 0x088003u);
      const int sh = cfg & 15, base = 256 * w + ((cfg >> 8) & 0xff) * (e >> sh) + (e & ((1 << sh) - 1));
#pragma unroll
      for (int r = 0; r < 4; ++r) { const int idx = base + (int)(cfg >> 16) * (4 * g + r); ka[r] = LD_KD + 4 * (idx + (idx >> 3)); asm volatile("" : "+v"(ka[r])); }
    }
    STAMP(1);
    asm volatile("" : "+v"(rar), "+v"(rai), "+v"(rbr), "+v"(rbi), "+v"(rcr), "+v"(rci) : "v"(cp0));
    STAMP(2);
    u32 ft;
    TOUCH(ft, ((b * 2 + (w & 1)) * 28 + min(lane, 27)) * 128, w < 2 ? p.w1 : p.w2);
    u32x4 wv[8]; u32 cv0, cv1;
    {
      const rsrc_t rm = __builtin_amdgcn_make_buffer_rsrc((void*)p.wmod, 0, w == 3 ? D * 3072 * 2 : 0, 0x00020000);
      const int k0 = (b >> 5) * 64 + (lane >> 3);
      cv0 = p.c[k0 + 8 * (lane & 3)]; cv1 = p.c[k0 + 32 + 8 * (lane & 3)];
#pragma unroll
      for (int i = 0; i < 8; ++i) wv[i] = __builtin_amdgcn_raw_buffer_load_b128(rm, k0 * 6144 + (lane & 7) * 16, 2048 + (b & 31) * 128 + i * 8 * 6144, AUX_NT);
    }
    SCHED();
    const float dt = rb(__builtin_amdgcn_exp2f((pch ? bfhi(ldt2) : bflo(ldt2)) * LOG2E));
    const float a2 = (dt * bflo(rar)) * LOG2E, bq = (dt * bflo(rai)) * 0.15915494309189535f;
    const f32x2 z1 = cpow(a2, bq, 1.f), z16c = cpow(a2, -bq, 16.f);
    f32x2 q = {1.f, 0.f};
    f32x2 r = cmul(f32x2{bflo(rcr), -bflo(rci)}, f32x2{bflo(rbr), -bflo(rbi)} * dt);
#pragma unroll
    for (int e = 0; e < 16; ++e) {
      P[e * PROW] = pk2(r);
      P[(16 + e) * PROW] = pk2(q);
      r = cmul(r, z16c); q = cmul(q, z1);
    }
    STAMP(3);
    BARRIER();
    STAMP(4);
    asm volatile("" : "+v"(cv0), "+v"(cv1), "+v"(wv[0]), "+v"(wv[1]), "+v"(wv[2]), "+v"(wv[3]), "+v"(wv[4]), "+v"(wv[5]), "+v"(wv[6]), "+v"(wv[7]) : "v"(ft));
    if (w < 3) {
      LAS const u32* fb = (LAS const u32*)(smem + LD_P) + (lane & 15) * PROW + ((lane >> 4) & 1) * 64 + (lane >> 5) * 32;
      u32x4 fa[16], fc[16];
#pragma unroll
      for (int ks = 0; ks < 16; ++ks) {
        const int o = (ks & 1) * 64 * PROW + (ks >> 1) * 4;
        fa[ks] = *(LAS const u32x4*)(fb + o); fc[ks] = *(LAS const u32x4*)(fb + (w + 1) * 16 * PROW + o);
      }
      SCHED();
      f32x4 acc[2] = {{0.f, 0.f, 0.f, 0.f}, {0.f, 0.f, 0.f, 0.f}};
#pragma unroll
      for (int ks = 0; ks < 16; ++ks) acc[ks & 1] = mfma16(fa[ks], fc[ks], acc[ks & 1]);
#pragma unroll
      for (int r4 = 0; r4 < 4; ++r4) *(LAS u32*)(smem + ka[r4]) = pk(acc[0][r4], acc[1][r4]);
    } else {
      const u32 s0 = silu_bf(cv0), s1 = silu_bf(cv1);
      float acc[8] = {0.f, 0.f, 0.f, 0.f, 0.f, 0.f, 0.f, 0.f};
      gv_row<0x00>(wv[0], s0, acc); gv_row<0x55>(wv[1], s0, acc); gv_row<0xAA>(wv[2], s0, acc); gv_row<0xFF>(wv[3], s0, acc);
      gv_row<0x00>(wv[4], s1, acc); gv_row<0x55>(wv[5], s1, acc); gv_row<0xAA>(wv[6], s1, acc); gv_row<0xFF>(wv[7], s1, acc);
      const f32x2 m = gv_fold(acc);
      if (!(lane & 8)) st_sc1(rws, ((lane >> 4) * 8 + (lane & 7)) * 16, WS_MH + ((b >> 5) * 1024 + (b & 31) * 32) * 16, u32x4{__float_as_uint(m[0]), ta, __float_as_uint(m[1]), tb});
    }
    STAMP(5);
    BARRIER();
    STAMP(6);
    u32 e[11];
#pragma unroll
    for (int i = 0; i < 11; ++i) e[i] = *(LAS const u32*)(smem + ga[i]);
#pragma unroll
    for (int c = 0; c < 2; ++c)
#pragma unroll
      for (int s = 0; s < 4; ++s) {
        u32x4 o;
#pragma unroll
        for (int z = 0; z < 4; ++z) o[z] = __builtin_amdgcn_perm(e[s + 2 * z + 1], e[s + 2 * z], c ? 0x07060302u : 0x05040100u);
        *(LAS u32x4*)(smem + LD_T + (c * 4 + s) * TCOPY + tid * 16) = o;
      }
  }
  {
    const int r = lane & 15, g = lane >> 4, ch = w >> 1, half = w & 1;
    LAS const char* Tb = smem + LD_T + (ch * 4 + ((3 - r) & 3)) * TCOPY + 2 * (2028 - 4 * (r >> 2) + 8 * g - 128 * half) - 7 * 512 - 192;
    LAS const char* Ub = smem + LD_U + ch * 4608 + 2 * (256 + 16 * r - 16 + 8 * g - 128 * half) - 192;
    LAS f32x4* red = (LAS f32x4*)(smem + (ch ? LD_RED1 : LD_RED0)) + half * 512 + (lane ^ ((lane >> 4) << 2));
    int pfo = ((j & 1) * 7168 + (j >> 1) * PF_N + min(tid, PF_N - 1)) * 128;
    asm volatile("" : "+v"(pfo));
    f32x4 acc[8];
#pragma unroll
    for (int n = 0; n < 8; ++n) acc[n] = f32x4{0.f, 0.f, 0.f, 0.f};
    u32x4 uf[8], tf[8];
#define LD_TF(a, e) do { const u32x2 l_ = *(volatile LAS const u32x2*)(Tb + 64 * (3 - (e)) + 512 * (7 - (a))), h_ = *(volatile LAS const u32x2*)(Tb + 64 * (3 - (e)) + 512 * (7 - (a)) + 8); \
                         tf[a] = u32x4{l_[0], l_[1], h_[0], h_[1]}; } while (0)
#define LD_UF(n, e) uf[n] = *(LAS const u32x4*)(Ub + 64 * (3 - (e)) + 512 * (n))
#define CM(a, m) do { SCHED(); acc[(a) + (m)] = mfma16(tf[a], uf[m], acc[(a) + (m)]); SCHED(); } while (0)
    STAMP(10);
    BARRIER();
    STAMP(11);
    LD_TF(0, 0);
#pragma unroll
    for (int n = 7; n >= 0; --n) LD_UF(n, 0);
#pragma unroll
    for (int a = 0; a < 8; ++a) {
#pragma unroll
      for (int m = 7 - a; m >= 0; --m) {
        CM(a, m);
        if (a == 0 && m > 0) LD_TF(8 - m, 0);
        if (m == 7 - a) LD_UF(7 - a, 1);
      }
      LD_TF(a, 1);
    }
#pragma unroll
    for (int m = 7; m >= 0; --m) {
#pragma unroll
      for (int a = 0; a + m < 8; ++a) { CM(a, m); if (m == 0) LD_TF(a, 2); }
      LD_UF(m, 2);
    }
#pragma unroll
    for (int z = 0; z < 4; ++z) {
#pragma unroll
      for (int m = 7 - z; m >= z; --m) { CM(z, m); if (m == 7 - z) LD_UF(7 - z, 3); }
      LD_TF(z, 3);
#pragma unroll
      for (int a = z + 1; a + z < 8; ++a) CM(a, z);
      LD_UF(z, 3); LD_TF(7 - z, 3);
    }
#pragma unroll
    for (int n = 0; n < 8; ++n)
#pragma unroll
      for (int a = 0; a <= n; ++a) { CM(a, n - a); if (a == 1) red[(n - 1) * 64] = acc[n - 1]; }
    STAMP(12);
    red[7 * 64] = acc[7];
    BARRIER();
    STAMP(14);
    u32 pf0;
    TOUCH(pf0, pfo, p.w2);
    WAIT_VM(0);
    asm volatile("" :: "v"(pf0));
    STAMP(17);
  }
  const int mi = LD_MISC + w * 3072;
  u32x4 gq2;
#define EXTRAS() do { const int xs_ = WS_X1 + tbk * 65536 + (db * 32 + (w >> 1) * 16) * 128 + (w & 1) * 64; \
    dma16_sc1(qws, mi + 1024, (lane >> 2) * 128 + (lane & 3) * 16, xs_); dma16_sc1(qws, mi + 2048, (lane >> 2) * 128 + (lane & 3) * 16, xs_ + 32768); \
    gq2 = ld_sc1(rws, (lane & 15) * 16, WS_MF + 4096 + (db * 32 + (w >> 1) * 16) * 16); } while (0)
  if (helper) {
    const int hb = xcd * 4 + j;
    u32x4 v[16];
    const int rg = tid >> 3, cl = tid & 7;
#pragma unroll
    for (int i = 0; i < 16; ++i) v[i] = *(const u32x4*)(p.w1 + (size_t)(i * 32 + rg) * F + (hb < 28 ? hb : 0) * 64 + cl * 8);
    LAS float* rd = (LAS float*)(smem + 4096);
    { const u32x4 m = poll16(rws, tid * 16, WS_MF, ta, tb); *(LAS u32x2*)(smem + tid * 8) = u32x2{m[0], m[2]}; }
    BARRIER();
    float a[16];
#pragma unroll
    for (int e = 0; e < 16; ++e) a[e] = 0.f;
#pragma unroll
    for (int i = 0; i < 16; ++i) {
      const u32 gs = *(LAS const u32*)(smem + (i * 32 + rg) * 4);
      const u32 gl = gs & 0xffffu, gh = gs << 16, sl = gs >> 16, sh = gs & 0xffff0000u;
#pragma unroll
      for (int z = 0; z < 4; ++z) {
        a[4 * z] = dot2(v[i][z], gl, a[4 * z]); a[4 * z + 1] = dot2(v[i][z], sl, a[4 * z + 1]);
        a[4 * z + 2] = dot2(v[i][z], gh, a[4 * z + 2]); a[4 * z + 3] = dot2(v[i][z], sh, a[4 * z + 3]);
      }
    }
    fold_cols(a, rd + w * 128, lane);
    STAMP(18);
    poll16(rws, tid * 64, WS_F2 + (xcd >> 1) * 16384, ta, tb);
    BARRIER();
    STAMP(19);
#pragma unroll
    for (int i = 0; i < 16; ++i) v[i] = ld_sc1(rws, tid * 16, WS_X1 + hb * 65536 + i * 4096);
    if (w == 0 && hb < 28) {
      const f32x2 s = sum4(rd + lane * 2, 128);
      st_sc1(rws, lane * 16, WS_S12 + hb * 1024, u32x4{__float_as_uint(s[0]), ta, __float_as_uint(s[1]), tb});
    }
#pragma unroll
    for (int e = 0; e < 16; ++e) a[e] = 0.f;
#pragma unroll
    for (int i = 0; i < 16; ++i)
#pragma unroll
      for (int z = 0; z < 4; ++z) {
        const int o = (i >> 3) * 8 + 2 * z;
        a[o] = dot2(v[i][z], 0x3f803f80u, a[o]); a[o + 1] = dot2(v[i][z], v[i][z], a[o + 1]);
      }
    STAMP(20);
    fold_cols(a, rd + 512 + w * 128, lane);
    BARRIER();
    if (w == 0) {
      const f32x2 s = sum4(rd + 512 + lane * 2, 128);
      const u32 st = ln_stat(s[0], s[1]);
      const u32 st2 = __builtin_amdgcn_mov_dpp(st, 0xB1, 0xf, 0xf, true);
      if (!(lane & 1)) st_sc1(rws, lane * 8, WS_G2 + hb * 512, u32x4{st, ta, st2, tb});
    }
    EXTRAS();
    BARRIER();
    STAMP(22);
  } else {
    const int wt = w & 1, wf = w >> 1;
    const int h = lane >> 5, blk = (lane >> 4) & 1, q = (lane & 15) >> 2, pp = lane & 3;
    int ax[2][2], aw[2][2];
#pragma unroll
    for (int c = 0; c < 2; ++c)
#pragma unroll
      for (int t = 0; t < 2; ++t) {
        const int base = 256 * (8 * h + 4 * t + q) + 16 * ((2 * blk + (pp >> 1)) ^ (2 * h + t)) + 8 * (pp & 1);
        ax[c][t] = LD_XR + base + 64 * ((2 * wt + c) ^ q);
        aw[c][t] = LD_WR + base + 64 * ((2 * wf + c) ^ q);
      }
    f32x16 acc[2][2];
#pragma unroll
    for (int a = 0; a < 2; ++a)
#pragma unroll
      for (int c = 0; c < 2; ++c)
#pragma unroll
        for (int r = 0; r < 16; ++r) acc[a][c][r] = 0.f;
    u32x4 xf[2][2], wfr[2][2], gq, sq;
#define RD_X(i, c) xf[(i) & 1][c] = tr_frag(smem + ax[c][0] + ((i) & 15) * 4096, smem + ax[c][1] + ((i) & 15) * 4096)
#define RD_W(i, c) wfr[(i) & 1][c] = tr_frag(smem + aw[c][0] + ((i) & 15) * 4096, smem + aw[c][1] + ((i) & 15) * 4096)
#define MM(i, a, c) do { SCHED(); acc[a][c] = mfma32(wfr[(i) & 1][a], xf[(i) & 1][c], acc[a][c]); SCHED(); } while (0)
    BARRIER();
    STAMP(19);
    RD_W(0, 0); RD_X(0, 0); RD_X(0, 1); RD_W(0, 1);
#pragma unroll
    for (int i = 0; i < 32; ++i) {
      if ((i & 3) != 3) {
        MM(i, 0, 0); RD_W(i + 1, 0); MM(i, 0, 1); RD_X(i + 1, 0); MM(i, 1, 0); RD_X(i + 1, 1); MM(i, 1, 1); RD_W(i + 1, 1);
      } else {
        MM(i, 0, 0); MM(i, 0, 1);
        BARRIER();
        if (i == 15) EXTRAS();
        if (i == 27) { gq = ld_sc1(rws, lane * 16, WS_G2 + rbk * 1024); sq = ld_sc1(rws, lane * 16, WS_S12 + (cbk * 128 + wf * 64) * 16); }
        MM(i, 1, 0);
        if (i < 31) { RD_W(i + 1, 0); RD_X(i + 1, 0); }
        MM(i, 1, 1);
        if (i < 31) { RD_X(i + 1, 1); RD_W(i + 1, 1); }
      }
    }
    STAMP(21);
    if (__any((tagdiff(gq, ta, tb) | tagdiff(sq, ta, tb)) != 0u)) {
      gq = poll16(rws, lane * 16, WS_G2 + rbk * 1024, ta, tb);
      sq = poll16(rws, lane * 16, WS_S12 + (cbk * 128 + wf * 64) * 16, ta, tb);
    }
    *(LAS u32x2*)(smem + mi + lane * 8) = u32x2{gq[0], gq[2]};
    *(LAS u32*)(smem + mi + 512 + lane * 4) = sq[0];
    *(LAS float*)(smem + mi + 768 + lane * 4) = __uint_as_float(sq[2]) * -LOG2E;
    float rstd[2], mr[2];
#pragma unroll
    for (int c = 0; c < 2; ++c) {
      const u32 st = *(LAS const u32*)(smem + mi + (wt * 64 + c * 32 + (lane & 31)) * 4);
      rstd[c] = __builtin_amdgcn_rcpf(bfhi(st)) * -LOG2E;
      mr[c] = -bflo(st) * rstd[c];
    }
    STAMP(22);
    LAS const float* s12 = (LAS const float*)(smem + mi + 512) + 4 * h;
    const int ho = WS_H + ((rbk * 4 + wt * 2) * 112 + cbk * 8 + wf * 4) * 1024 + lane * 16;
#pragma unroll
    for (int a = 0; a < 2; ++a)
#pragma unroll
      for (int sp = 0; sp < 2; ++sp) {
        f32x4 s1[2], s2[2];
#pragma unroll
        for (int z = 0; z < 2; ++z) { s1[z] = *(LAS const f32x4*)(s12 + a * 32 + sp * 16 + z * 8); s2[z] = *(LAS const f32x4*)(s12 + 64 + a * 32 + sp * 16 + z * 8); }
#pragma unroll
        for (int c = 0; c < 2; ++c) {
          u32x4 o;
#pragma unroll
          for (int e = 0; e < 4; ++e) {
            const int z = e >> 1, k = (e & 1) * 2;
            const f32x2 av = {acc[a][c][sp * 8 + 2 * e], acc[a][c][sp * 8 + 2 * e + 1]};
            const f32x2 cc = f32x2{s1[z][k], s1[z][k + 1]} * mr[c] + f32x2{s2[z][k], s2[z][k + 1]};
            const f32x2 ar = av * rstd[c] + cc;
            const f32x2 dn = f32x2{__builtin_amdgcn_exp2f(ar[0]), __builtin_amdgcn_exp2f(ar[1])} + 1.f;
            o[e] = pk2(ar * f32x2{__builtin_amdgcn_rcpf(dn[0]), __builtin_amdgcn_rcpf(dn[1])});
          }
          st_sc1(rws, ho, (c * 112 + a * 2 + sp) * 1024, o);
        }
      }
    STAMP(23);
    WAIT_VM(0);
    STAMP(24);
    if (lane < 8) st_sc1(rws, lane * 16, WS_F3 + ((rbk * 4 + w) * 16 + cbk) * 128, u32x4{ta, ta, tb, tb});
  }

  {
    const int wt = w & 1, kh = w >> 1, h = lane >> 5, tl = lane & 31;
    const int blk = (lane >> 4) & 1, q = (lane & 15) >> 2, pp = lane & 3;
    LAS char* aw[3][2];
#pragma unroll
    for (int c = 0; c < 6; ++c) {
      aw[c >> 1][c & 1] = smem + (c >> 1) * 65536 + kh * 8192 + 128 * (4 * h + q) + 64 * ((c & 1) ^ kh ^ (q >> 1)) + 32 * blk + 8 * pp;
      asm volatile("" : "+v"(aw[c >> 1][c & 1]));
    }
    f32x16 acc[2];
#pragma unroll
    for (int r = 0; r < 16; ++r) { acc[0][r] = 0.f; acc[1][r] = 0.f; }
    STAMP(25);
    BARRIER();
    constexpr int NW = G2_NW;
    u32x4 hf[NW], wf[3][2];
#define RD_W2C(i, c) do { LAS char* a_ = aw[w2_unit((i) >> 2) >> 2][c] + (w2_unit((i) >> 2) & 3) * UNIT + ((i) & 3) * 2048; wf[(i) % 3][c] = tr_frag(a_, a_ + 1024); } while (0)
#define RD_W2(i) do { RD_W2C(i, 0); RD_W2C(i, 1); } while (0)
    RD_W2(0); RD_W2(1);
    if (__any(tagdiff(gq2, ta, tb) != 0u)) gq2 = poll16(rws, (lane & 15) * 16, WS_MF + 4096 + (db * 32 + kh * 16) * 16, ta, tb);
    const int sh = WS_H + ((tbk * 2 + wt) * 112 + 56 * kh) * 1024;
    poll16(rws, ((lane & 1) * 32 + min(lane >> 1, 6)) * 128, WS_F3 + ((tbk >> 1) * 4 + (tbk & 1)) * 2048 + kh * 896, ta, tb);
    STAMP(26);
#pragma unroll
    for (int i = 0; i < NW; ++i) hf[i] = ld_sc1(rws, lane * 16 + (i & 3) * 1024, sh + (i >> 2) * 4096);
    SCHED();
    u32 xr[8]; f32x2 gb[8];
    *(LAS f32x2*)(smem + mi + lane * 8) = f32x2{__uint_as_float(gq2[0]), __uint_as_float(gq2[2])} * (-1.f / LOG2E);
#pragma unroll
    for (int i = 0; i < 8; ++i) {
      const int pr = 4 * (i >> 1) + 2 * h + (i & 1);
      xr[i] = *(LAS const u32*)(smem + mi + 1024 + ((tl >> 2) & 1) * 1024 + pr * 64 + (tl >> 3) * 16 + (tl & 3) * 4);
      gb[i] = *(LAS const f32x2*)(smem + mi + pr * 8);
    }
    SCHED();
#pragma unroll
    for (int i = 0; i < 56; ++i) {
      SCHED();
      acc[0] = mfma32(wf[i % 3][0], hf[i % NW], acc[0]);
      SCHED();
      if (i == 0) STAMP(29);
      if (i == 18) STAMP(30);
      if (i == 42) STAMP(31);
      if (i == 6 || i == 18 || i == 42) BARRIER();
      if (i + 2 < 56) RD_W2C(i + 2, 0);
      if (i > 0 && i - 1 + NW < 56) hf[(i - 1) % NW] = ld_sc1(rws, lane * 16 + ((i - 1 + NW) & 3) * 1024, sh + ((i - 1 + NW) >> 2) * 4096);
      SCHED();
      acc[1] = mfma32(wf[i % 3][1], hf[i % NW], acc[1]);
      SCHED();
      if (i + 2 < 56) RD_W2C(i + 2, 1);
    }
    STAMP(27);
    LAS f32x4* ex = (LAS f32x4*)(smem + w2_unit(5) * UNIT) + lane;
#pragma unroll
    for (int qq = 0; qq < 4; ++qq) ex[(w * 4 + qq) * 64] = f32x4{acc[1][4 * qq], acc[1][4 * qq + 1], acc[1][4 * qq + 2], acc[1][4 * qq + 3]};
    BARRIER();
    LAS char* ot = smem + w2_unit(6) * UNIT;
    f32x4 oth[4];
#pragma unroll
    for (int qq = 0; qq < 4; ++qq) oth[qq] = ex[((w ^ 2) * 4 + qq) * 64];
    SCHED();
#pragma unroll
    for (int qq = 0; qq < 4; ++qq) {
      const f32x4 o4 = oth[qq];
      u32x2 o;
#pragma unroll
      for (int z = 0; z < 2; ++z)
        o[z] = pk2((f32x2{acc[0][4 * qq + 2 * z], acc[0][4 * qq + 2 * z + 1]} + f32x2{o4[2 * z], o4[2 * z + 1]}) * gb[2 * qq + z] + up(xr[2 * qq + z]));
      *(LAS u32x2*)(ot + (wt * 32 + tl) * 144 + kh * 64 + qq * 16 + h * 8) = o;
    }
    BARRIER();
#pragma unroll
    for (int i = 0; i < 2; ++i) {
      const int row = (tid >> 3) + 32 * i, r = tbk * 64 + row;
      const int t = (r & ~511) + 128 * ((r >> 1) & 3) + 2 * ((r >> 3) & 63) + (r & 1);
      *(u32x4*)(p.out + (size_t)t * D + db * 64 + (tid & 7) * 8) = *(LAS const u32x4*)(ot + row * 144 + (tid & 7) * 16);
    }
    STAMP(28);
    STAMP_FLUSH(p.ws, 0);
  }
}

extern "C" __global__ void fused_block_end() {}

extern "C" void kernel_launch(void* const* d_in, const int* in_sizes, int n_in, void* d_out, int out_size, void* d_ws, size_t ws_size,
                              hipStream_t stream) {
  Params p;
  p.x = (const u16*)d_in[0]; p.Are = (const u16*)d_in[1]; p.Aim = (const u16*)d_in[2]; p.Bre = (const u16*)d_in[3];
  p.Bim = (const u16*)d_in[4]; p.Cre = (const u16*)d_in[5]; p.Cim = (const u16*)d_in[6]; p.ldt = (const u16*)d_in[7];
  p.dskip = (const u16*)d_in[8]; p.w1 = (const u16*)d_in[10]; p.w2 = (const u16*)d_in[11]; p.c = (const u16*)d_in[12];
  p.wmod = (const u16*)d_in[13]; p.out = (u16*)d_out; p.ws = (char*)d_ws;
  (void)hipFuncSetAttribute((const void*)fused_block, hipFuncAttributeMaxDynamicSharedMemorySize, LDS_BYTES + ST_LDS_EXTRA);
  fused_block<<<256, 512, LDS_BYTES + ST_LDS_EXTRA, stream>>>(p);
}
```

```cpp
#include <hip/hip_runtime.h>
#include <hip/hip_bf16.h>

#define STAMP_DECL
#define STAMP(s)
#define STAMP_FLUSH(ws, k)
#define ST_LDS_EXTRA 0
#define ST_RANGES {{0, 32, 256, "compute waves"}, {1, 32, 256, "loader waves"}, {0, 0, 32, "helper WGs, waves 0-3"}, {1, 0, 32, "helper WGs, waves 4-7"}}

typedef unsigned short u16;
typedef unsigned int u32;
typedef unsigned long long u64;
typedef u32 u32x2 __attribute__((ext_vector_type(2)));
typedef u32 u32x4 __attribute__((ext_vector_type(4)));
typedef float f32x2 __attribute__((ext_vector_type(2)));
typedef float f32x4 __attribute__((ext_vector_type(4)));
typedef float f32x16 __attribute__((ext_vector_type(16)));
typedef short s16x4 __attribute__((ext_vector_type(4)));
typedef __bf16 bf16x2 __attribute__((ext_vector_type(2)));
typedef __bf16 bf16x8 __attribute__((ext_vector_type(8)));
typedef __amdgpu_buffer_rsrc_t rsrc_t;
#define GAS __attribute__((address_space(1)))
#define LAS __attribute__((address_space(3)))
#define CAS __attribute__((address_space(4)))

constexpr int L = 2048, D = 512, NS = 128, F = 1792;

constexpr int WS_EPOCH = 0;
constexpr int WS_SG = 0x1000;
constexpr int WS_MG = 0x8000;
constexpr int WS_MH = 0x80000;
constexpr int WS_F2 = 0x40000;
constexpr int WS_F3 = 0x50000;
constexpr int WS_G2 = 0x70000;
constexpr int WS_S12 = 0x74000;
constexpr int WS_MF = 0x7c000;
constexpr int WS_XG = 1 << 20;
constexpr int WS_X1 = 5 << 20;
constexpr int WS_X1S = 7 << 20;
constexpr int WS_H = 9 << 20;
constexpr int WS_END = 16 << 20;

constexpr int UNIT = 16384;
constexpr int LD_XR = 0, LD_WR = 4 * UNIT, LD_MISC = 9 * UNIT, LDS_BYTES = LD_MISC + 4 * 3072;
constexpr int TCOPY = 4160, PROW = 132;
constexpr int LD_T = 0, LD_U = 8 * TCOPY, LD_KD = LD_U + 2 * 4608, LD_P = LD_WR, LD_RED0 = 3 * UNIT, LD_RED1 = 8 * UNIT;
constexpr int KD_ZERO = 900, LD_PS = LD_KD + 904 * 4, LD_PG = LD_PS + 256, LD_CNT = LD_PG + 1024, LD_XT = LD_RED0;
static_assert(LD_CNT + 16 <= LD_RED0, "LDS map");
__host__ __device__ constexpr int w2_unit(int u) { return u % 9 == 8 ? 8 : (u % 9) / 2 + 4 * (u % 9 & 1); }
#define G2_NW 16
#define PF_N 224
#define PF_M 4
#define F2_DELAY 16
#define POLL_SLEEP 2
#define LDNT(p) __builtin_nontemporal_load(p)
#define AUX_NT 2
#define WF ""
#define AUX_W 0
constexpr int SPIN_CAP = 1 << 15;
constexpr float LOG2E = 1.44269504088896341f;

__device__ __forceinline__ float bflo(u32 v) { return __uint_as_float(v << 16); }
__device__ __forceinline__ float bfhi(u32 v) { return __uint_as_float(v & 0xffff0000u); }
__device__ __forceinline__ u32 pk(float lo, float hi) {
  f32x2 v = {lo, hi};
  bf16x2 r = __builtin_convertvector(v, bf16x2);
  return __builtin_bit_cast(u32, r);
}
__device__ __forceinline__ float rb(float f) { return bflo(pk(f, f)); }
__device__ __forceinline__ float dot2(u32 a, u32 b, float c) {
  return __builtin_amdgcn_fdot2_f32_bf16(__builtin_bit_cast(bf16x2, a), __builtin_bit_cast(bf16x2, b), c, false);
}
__device__ __forceinline__ f32x4 mfma16(u32x4 a, u32x4 b, f32x4 c) {
  return __builtin_amdgcn_mfma_f32_16x16x32_bf16(__builtin_bit_cast(bf16x8, a), __builtin_bit_cast(bf16x8, b), c, 0, 0, 0);
}
__device__ __forceinline__ f32x16 mfma32(u32x4 a, u32x4 b, f32x16 c) {
  return __builtin_amdgcn_mfma_f32_32x32x16_bf16(__builtin_bit_cast(bf16x8, a), __builtin_bit_cast(bf16x8, b), c, 0, 0, 0);
}
template <int CTRL> __device__ __forceinline__ float dpp(float x) {
  return __builtin_bit_cast(float, __builtin_amdgcn_mov_dpp(__builtin_bit_cast(int, x), CTRL, 0xf, 0xf, true));
}
__device__ __forceinline__ float row_sum(float x) {
  x += dpp<0xB1>(x); x += dpp<0x4E>(x); x += dpp<0x124>(x); x += dpp<0x128>(x);
  return x;
}
__device__ __forceinline__ float fold32(float a, float b) {
  auto s = __builtin_amdgcn_permlane32_swap(__float_as_uint(a), __float_as_uint(b), false, false);
  return __uint_as_float(s[0]) + __uint_as_float(s[1]);
}
__device__ __forceinline__ float fold16(float a, float b) {
  auto s = __builtin_amdgcn_permlane16_swap(__float_as_uint(a), __float_as_uint(b), false, false);
  return __uint_as_float(s[0]) + __uint_as_float(s[1]);
}
__device__ __forceinline__ u32x4 mkrs(const void* p, u32 bytes) {
  const u64 a = (u64)p;
  return u32x4{(u32)a, (u32)(a >> 32) & 0xffffu, bytes, 0x00020000u};
}
__device__ __forceinline__ void dma16_sc1(u32x4 rs, int lds, int voff, int soff) {
  asm volatile("s_mov_b32 m0, %0\n\ts_nop 0\n\tbuffer_load_dwordx4 %1, %2, %3 offen sc1 lds" :: "s"(lds), "v"(voff), "s"(rs), "s"(soff) : "memory");
}
#define DMA4_TEXT(F) "s_mov_b32 m0, %0\n\ts_nop 0\n\tbuffer_load_dwordx4 %1, %5, %6 offen" F " lds\n\tbuffer_load_dwordx4 %2, %5, %6 offen offset:1024" F " lds\n\t" \
                     "buffer_load_dwordx4 %3, %5, %6 offen offset:2048" F " lds\n\tbuffer_load_dwordx4 %4, %5, %6 offen offset:3072" F " lds"
__device__ __forceinline__ void dma4k(u32x4 rs, int lds, const int (&v)[4], int soff) {
  asm volatile(DMA4_TEXT(WF) :: "s"(lds), "v"(v[0]), "v"(v[1]), "v"(v[2]), "v"(v[3]), "s"(rs), "s"(soff) : "memory");
}
__device__ __forceinline__ void dma4k_sc1(u32x4 rs, int lds, const int (&v)[4], int soff) {
  asm volatile(DMA4_TEXT(" sc1") :: "s"(lds), "v"(v[0]), "v"(v[1]), "v"(v[2]), "v"(v[3]), "s"(rs), "s"(soff) : "memory");
}
__device__ __forceinline__ u32x4 ld_sc1(rsrc_t rs, int voff, int soff) { return __builtin_amdgcn_raw_buffer_load_b128(rs, voff, soff, 16); }
__device__ __forceinline__ void st_sc1(rsrc_t rs, int voff, int soff, u32x4 v) { __builtin_amdgcn_raw_buffer_store_b128(v, rs, voff + soff, 0, 16); }
__device__ __forceinline__ u32x4 tr_frag(LAS char* a0, LAS char* a1) {
  const s16x4 lo = __builtin_amdgcn_ds_read_tr16_b64_v4i16((LAS s16x4*)a0), hi = __builtin_amdgcn_ds_read_tr16_b64_v4i16((LAS s16x4*)a1);
  const u32x2 l = __builtin_bit_cast(u32x2, lo), h = __builtin_bit_cast(u32x2, hi);
  return u32x4{l[0], l[1], h[0], h[1]};
}
#define SCHED() __builtin_amdgcn_sched_barrier(0)
#define FENCE() asm volatile("" ::: "memory")
#define WAIT_VM(n) asm volatile("s_waitcnt vmcnt(" #n ")" ::: "memory")
#define BARRIER() do { asm volatile("s_waitcnt lgkmcnt(0)" ::: "memory"); __builtin_amdgcn_s_barrier(); FENCE(); } while (0)

__device__ __forceinline__ f32x2 cmul(f32x2 a, f32x2 b) {
  const f32x2 t = f32x2{a[0], a[0]} * b;
  return __builtin_elementwise_fma(f32x2{a[1], a[1]}, f32x2{-b[1], b[0]}, t);
}
__device__ __forceinline__ f32x2 cpow(float a2, float bq, float l) {
  const float m = __builtin_amdgcn_exp2f(a2 * l);
  const float ph = __builtin_amdgcn_fractf(bq * l);
  return f32x2{__builtin_amdgcn_cosf(ph), __builtin_amdgcn_sinf(ph)} * m;
}
__device__ __forceinline__ u32 pk2(f32x2 a) { return pk(a[0], a[1]); }
__device__ __forceinline__ f32x2 up(u32 v) { return f32x2{bflo(v), bfhi(v)}; }
__device__ __forceinline__ u32 ln_stat(float sum, float sq) {
  const float m = sum * (1.f / D);
  const float var = fmaxf(sq * (1.f / D) - m * m, 0.f);
  return pk(m, __builtin_amdgcn_sqrtf(rb(rb(var) + 1e-5f)));
}
__device__ __forceinline__ u32 tagdiff(u32x4 v, u32 ta, u32 tb) { return (v[1] ^ ta) | (v[3] ^ tb); }
__device__ __forceinline__ u32x4 poll16(rsrc_t rs, int voff, int soff, u32 ta, u32 tb) {
  u32x4 v;
  int spins = 0;
  for (;;) {
    FENCE();
    v = ld_sc1(rs, voff, soff);
    if (tagdiff(v, ta, tb) == 0u || ++spins >= SPIN_CAP) break;
    if (POLL_SLEEP) __builtin_amdgcn_s_sleep(POLL_SLEEP);
  }
  FENCE();
  return v;
}
__device__ __forceinline__ void fold_cols(const float (&v)[16], LAS float* dst, int lane) {
  float u[8]; f32x4 t;
#pragma unroll
  for (int k = 0; k < 8; ++k) u[k] = fold32(v[k], v[k + 8]);
#pragma unroll
  for (int k = 0; k < 4; ++k) { const float a = fold16(u[k], u[k + 4]); t[k] = a + dpp<0x128>(a); }
  if (!(lane & 8)) *(LAS f32x4*)(dst + (lane & 7) * 16 + (lane >> 4) * 4) = t;
}
__device__ __forceinline__ f32x2 sum4(LAS const float* src, int stride) {
  return (*(LAS const f32x2*)src + *(LAS const f32x2*)(src + stride)) + (*(LAS const f32x2*)(src + 2 * stride) + *(LAS const f32x2*)(src + 3 * stride));
}
__device__ __forceinline__ u32 silu_bf(u32 c) {
  const float f = bflo(c), s = f * __builtin_amdgcn_rcpf(1.f + __builtin_amdgcn_exp2f(f * -LOG2E));
  return pk(s, s);
}
template <int CTRL> __device__ __forceinline__ void gv_row(u32x4 v, u32 sp, float (&acc)[8]) {
  const u32 sb = __builtin_amdgcn_mov_dpp(sp, CTRL, 0xf, 0xf, true), al = sb & 0xffffu, ah = sb & 0xffff0000u;
#pragma unroll
  for (int q = 0; q < 4; ++q) { acc[2 * q] = dot2(v[q], al, acc[2 * q]); acc[2 * q + 1] = dot2(v[q], ah, acc[2 * q + 1]); }
}
__device__ __forceinline__ f32x2 gv_fold(const float (&acc)[8]) {
  const float m0 = fold16(fold32(acc[0], acc[4]), fold32(acc[2], acc[6])), m1 = fold16(fold32(acc[1], acc[5]), fold32(acc[3], acc[7]));
  return f32x2{m0 + dpp<0x128>(m0), m1 + dpp<0x128>(m1)};
}
#define TOUCH(dst, voff, base) asm volatile("global_load_dword %0, %1, %2" : "=v"(dst) : "v"(voff), "s"(base))

struct Params {
  const u16 *x, *Are, *Aim, *Bre, *Bim, *Cre, *Cim, *ldt, *dskip, *w1, *w2, *c, *wmod;
  u16* out;
  char* ws;
};

extern "C" __global__ void __launch_bounds__(512, 2) fused_block(Params p) {
  extern __shared__ __attribute__((aligned(16))) char smem_[];
  LAS char* smem = (LAS char*)smem_;
  const int tid = threadIdx.x, lane = tid & 63, w = __builtin_amdgcn_readfirstlane(tid >> 6), lw = w & 3;
  const int b = blockIdx.x, xcd = b & 7, j = b >> 3;
  const int pair = xcd * 32 + j, d0 = pair * 2;
  STAMP_DECL
  STAMP(0);
  const rsrc_t rws = __builtin_amdgcn_make_buffer_rsrc(p.ws, 0, WS_END, 0x00020000);
  const u32x4 qws = mkrs(p.ws, WS_END);
  asm volatile("" :: "s"(p.x), "s"(p.Are), "s"(p.Aim), "s"(p.Bre), "s"(p.Bim), "s"(p.Cre), "s"(p.Cim), "s"(p.w1), "s"(p.w2), "s"(p.c), "s"(p.wmod), "s"(p.out));
#define SCAL_ISSUE() u64 epoch; u32 ldt2, dsk; \
  asm volatile("s_load_dwordx2 %0, %3, 0x0\n\ts_load_dword %1, %4, 0x0\n\ts_load_dword %2, %5, 0x0" \
               : "=&s"(epoch), "=&s"(ldt2), "=&s"(dsk) : "s"(p.ws + WS_EPOCH), "s"(p.ldt + d0), "s"(p.dskip + d0)); SCHED()
#define SCAL_WAIT() asm volatile("s_waitcnt lgkmcnt(0)" : "+s"(epoch), "+s"(ldt2), "+s"(dsk)); \
  epoch += 0x9E3779B97F4A7C15ull; ta = (u32)epoch; tb = (u32)(epoch >> 32)
  u32 ta, tb;
  const int pch = lw >> 1, pn = (lw & 1) * 64 + lane;
  const int po = (d0 + pch) * NS + pn;
  volatile LAS u32* P = (LAS u32*)(smem + LD_P) + pch * 64 * PROW + pn;
  const bool helper = j < 4;
  const int rbk = (xcd >> 1) * 4 + (j & 3), cbk = (xcd & 1) * 7 + ((j - 4) >> 2);
  const int tbk = xcd * 4 + (j & 3), db = j >> 2;

  if (__builtin_expect(w >= 4, 1)) {
    SCAL_ISSUE();
    __builtin_amdgcn_s_setprio(2);
    const u16* xr = p.x + (size_t)(b * 8 + (lane >> 4)) * D + lw * 128 + (lane & 15) * 8;
    u32x4 xa = LDNT((const u32x4*)xr), xb = LDNT((const u32x4*)(xr + 4 * D));
    const int k0 = (b >> 4) * 32 + lw * 8 + (lane >> 3);
    u32 c0 = p.c[k0];
    u32x4 wm = LDNT((const u32x4*)(p.wmod + (size_t)k0 * 3072 + (b & 15) * 64 + (lane & 7) * 8));
    u32 rar = LDNT(p.Are + po), rai = LDNT(p.Aim + po);
    SCHED();
    SCAL_WAIT();
    if (lw == 0) {
      *(LAS u32x4*)(smem + LD_U + (lane >> 5) * 4608 + (lane & 31) * 16) = u32x4{0u, 0u, 0u, 0u};
      if (lane < 16) *(LAS u32x4*)(smem + LD_T + (lane >> 1) * TCOPY + 4096 + (lane & 1) * 16) = u32x4{0u, 0u, 0u, 0u};
      *(LAS u32*)(smem + LD_KD + KD_ZERO * 4) = 0u;
      *(LAS u32*)(smem + LD_CNT) = 0u;
    }
    BARRIER();
    STAMP(1);
    {
      LAS char* xt = smem + LD_XT + lw * 2176;
      *(LAS u32x4*)(xt + (lane >> 4) * 272 + (lane & 15) * 16) = xa;
      *(LAS u32x4*)(xt + (lane >> 4) * 272 + 1088 + (lane & 15) * 16) = xb;
      const u32x4 v0 = *(LAS const u32x4*)(xt + (lane & 3) * 544 + (lane >> 2) * 16), v1 = *(LAS const u32x4*)(xt + (lane & 3) * 544 + 272 + (lane >> 2) * 16);
      f32x4 ps = {0.f, 0.f, 0.f, 0.f};
#pragma unroll
      for (int z = 0; z < 4; ++z) {
        ps[0] = dot2(xa[z], 0x3f803f80u, ps[0]); ps[1] = dot2(xa[z], xa[z], ps[1]);
        ps[2] = dot2(xb[z], 0x3f803f80u, ps[2]); ps[3] = dot2(xb[z], xb[z], ps[3]);
      }
      SCHED();
      const int xo = WS_XG + (lw * 64 + (lane >> 2) * 4) * 16384 + (lane & 3) * 16 + b * 64;
#pragma unroll
      for (int q = 0; q < 4; ++q) st_sc1(rws, xo, q * 16384, u32x4{v0[q], ta, v1[q], tb});
      SCHED();
#pragma unroll
      for (int z = 0; z < 4; ++z) ps[z] = row_sum(ps[z]);
      if (!(lane & 15)) {
        *(LAS f32x2*)(smem + LD_PS + lw * 64 + (lane >> 4) * 8) = f32x2{ps[0], ps[1]};
        *(LAS f32x2*)(smem + LD_PS + lw * 64 + 32 + (lane >> 4) * 8) = f32x2{ps[2], ps[3]};
      }
      asm volatile("" : "+v"(c0), "+v"(wm), "+v"(rar), "+v"(rai));
      float acc[8] = {0.f, 0.f, 0.f, 0.f, 0.f, 0.f, 0.f, 0.f};
      gv_row<0xE4>(wm, silu_bf(c0), acc);
      const f32x2 m = gv_fold(acc);
      if (!(lane & 8)) *(LAS f32x2*)(smem + LD_PG + lw * 256 + ((lane >> 4) * 8 + (lane & 7)) * 8) = m;
      asm volatile("s_waitcnt lgkmcnt(0)" ::: "memory");
      u32 cnt = 0u;
      if (lane == 0) cnt = __hip_atomic_fetch_add((LAS u32*)(smem + LD_CNT), 1u, __ATOMIC_RELAXED, __HIP_MEMORY_SCOPE_WORKGROUP);
      if (__builtin_amdgcn_readfirstlane(cnt) == 3u) {
        FENCE();
        const f32x2 sq = sum4((LAS const float*)(smem + LD_PS) + (lane & 7) * 2, 16), g = sum4((LAS const float*)(smem + LD_PG) + (lane & 31) * 2, 64);
        const u32 st = ln_stat(sq[0], sq[1]), st2 = __builtin_amdgcn_mov_dpp(st, 0xB1, 0xf, 0xf, true);
        const bool hi = lane >= 32;
        if (lane < 40 && !(hi && (lane & 1)))
          st_sc1(rws, hi ? WS_SG + b * 64 + (lane & 7) * 8 : WS_MG + ((b >> 4) * 512 + (b & 15) * 32 + lane) * 16, 0,
                 u32x4{hi ? st : __float_as_uint(g[0]), ta, hi ? st2 : __float_as_uint(g[1]), tb});
      }
    }
    __builtin_amdgcn_s_setprio(0);
    STAMP(2);
    {
      const float dt = rb(__builtin_amdgcn_exp2f((pch ? bfhi(ldt2) : bflo(ldt2)) * LOG2E));
      const float a2 = (dt * bflo(rar)) * LOG2E, bq = (dt * bflo(rai)) * 0.15915494309189535f;
      const f32x2 z2 = cpow(a2, bq, 2.f), z256 = cpow(a2, bq, 256.f), z4 = cmul(z2, z2), z8 = cmul(z4, z4);
      f32x2 a = z256, c = cmul(z256, z256), d = cmul(c, z256), u = cpow(a2, bq, 1024.f);
#pragma unroll
      for (int e = 0; e < 8; ++e) {
        P[(32 + e) * PROW] = pk2(a);
        a = cmul(a, z2);
        if (e & 1) { P[(40 + e / 2) * PROW] = pk2(c); P[(44 + e / 2) * PROW] = pk2(d); c = cmul(c, z4); d = cmul(d, z4); }
        else { P[(48 + e) * PROW] = pk2(u); P[(49 + e) * PROW] = pk2(cmul(u, z8)); u = cmul(u, z256); }
      }
      P[56 * PROW] = pk2(u);
    }
    STAMP(3);
    BARRIER();
    u32x4 xg[4], sg[4], mg;
    const int go = lw * 4096 + lane * 16;
    const int mslot = (pair >> 5) * 32 + (pair & 3) * 8 + ((pair >> 2) & 7);
    const int mo = WS_MG + ((lane & 15) * 512 + ((lane >> 4) & 1) * 256 + mslot) * 16;
#pragma unroll
    for (int i = 0; i < 4; ++i) xg[i] = ld_sc1(rws, go + i * 1024, WS_XG + pair * 16384);
    BARRIER();
    __builtin_amdgcn_s_setprio(2);
    STAMP(6);
    for (int spins = 0;; ++spins) {
      FENCE();
      mg = ld_sc1(rws, mo, 0);
#pragma unroll
      for (int i = 0; i < 4; ++i) sg[i] = ld_sc1(rws, go + i * 1024, WS_SG);
      u32 bad = tagdiff(mg, ta, tb);
#pragma unroll
      for (int i = 0; i < 4; ++i) bad |= tagdiff(sg[i], ta, tb);
      if (!__any(bad != 0u) || spins >= SPIN_CAP) break;
    }
    STAMP(8);
#pragma unroll 1
    for (int spins = 0; spins < SPIN_CAP; ++spins) {
      u32 bad = 0u;
#pragma unroll
      for (int i = 0; i < 4; ++i) bad |= tagdiff(xg[i], ta, tb);
      if (__builtin_expect(!__any(bad != 0u), 1)) break;
      FENCE();
#pragma unroll
      for (int i = 0; i < 4; ++i) xg[i] = ld_sc1(rws, go + i * 1024, WS_XG + pair * 16384);
    }
    STAMP(9);
    const int mho = ((lane & 7) * 1024 + ((lane >> 3) & 3) * 256 + mslot) * 16;
    u32x4 mh = ld_sc1(rws, mho, WS_MH);
    SCHED();
    f32x2 xf[4][2], hf[4][2];
    {
      const u32 mb = pk(row_sum(__uint_as_float(mg[0])), row_sum(__uint_as_float(mg[2])));
      const f32x2 sa = up(__builtin_amdgcn_readlane(mb, 0)), g1 = up(__builtin_amdgcn_readlane(pk2(up(mb) + 1.f), 16));
#pragma unroll
      for (int i = 0; i < 4; ++i) {
#pragma unroll
        for (int e = 0; e < 2; ++e) {
          xf[i][e] = up(xg[i][2 * e]);
          hf[i][e] = (xf[i][e] - bflo(sg[i][2 * e])) * __builtin_amdgcn_rcpf(bfhi(sg[i][2 * e])) * g1 + sa;
        }
        LAS u32* U = (LAS u32*)(smem + LD_U + 512 + lw * 1024 + i * 256) + lane;
        U[0] = pk(hf[i][0][0], hf[i][1][0]);
        U[4608 / 4] = pk(hf[i][0][1], hf[i][1][1]);
      }
    }
    STAMP(10);
    BARRIER();
    STAMP(11);
    const u32x4 qw1 = mkrs(p.w1, D * F * 2), qw2 = mkrs(p.w2, D * F * 2);
    int vx[4], vw1[4], vw2[4];
#pragma unroll
    for (int i = 0; i < 4; ++i) {
      const int chk = 16 * ((lane & 15) ^ ((lane >> 4) << 2) ^ i);
      vx[i] = (lane >> 4) * 256 + chk;
      vw1[i] = (lane >> 4) * (F * 2) + chk + i * (4 * F * 2 - 1024);
      vw2[i] = (lane >> 3) * 1024 + db * 128 + 16 * ((lane & 7) ^ (((lane >> 4) & 1) << 2)) + i * 7168;
    }
    const int sw2 = ((lw >> 1) * 896 + (lw & 1) * 32) * 1024;
    auto w2_stage = [&](int u) { dma4k(qw2, w2_unit(u) * UNIT + lw * 4096, vw2, sw2 + u * 65536); };
    auto w1_stage = [&](int s) { dma4k(qw1, LD_WR + (s & 3) * UNIT + lw * 4096, vw1, (s * 64 + 16 * lw) * (F * 2) + cbk * 256); };
    auto x_stage = [&](int s) { dma4k_sc1(qws, LD_XR + (s & 3) * UNIT + lw * 4096, vx, WS_X1S + rbk * 131072 + s * 16384 + lw * 4096); };
    if (__any(tagdiff(mh, ta, tb) != 0u)) mh = poll16(rws, mho, WS_MH, ta, tb);
    if (!helper) { w1_stage(0); w1_stage(1); w1_stage(2); w1_stage(3); }
    f32x2 gav, gcv, xc[4][2];
    {
      float m0 = __uint_as_float(mh[0]), m1 = __uint_as_float(mh[2]);
      m0 += dpp<0xB1>(m0); m0 += dpp<0x4E>(m0); m0 += dpp<0x141>(m0);
      asm volatile("" : "+v"(m0));
      m1 += dpp<0xB1>(m1); m1 += dpp<0x4E>(m1); m1 += dpp<0x141>(m1);
      asm volatile("" : "+v"(m1));
      const u32 mb = pk(m0, m1);
      float p0 = bflo(mb) + 1.f, p1 = bfhi(mb) + 1.f;
      asm volatile("" : "+v"(p0)); asm volatile("" : "+v"(p1));
      const u32 gcb = __builtin_amdgcn_readlane(pk(p0, p1), 16), sbb = __builtin_amdgcn_readlane(mb, 8), gbb = __builtin_amdgcn_readlane(mb, 24);
      if (w == 7 && lane < 2)
        st_sc1(rws, lane * 4096, WS_MF + pair * 16, lane ? u32x4{gbb << 16, ta, gbb & 0xffff0000u, tb} : u32x4{(gcb & 0xffffu) | (sbb << 16), ta, (gcb >> 16) | (sbb & 0xffff0000u), tb});
      gav = up(__builtin_amdgcn_readlane(mb, 0)); gcv = up(gcb);
      float gd[2] = {gav[0] * bflo(dsk), gav[1] * bfhi(dsk)};
      asm volatile("" : "+v"(gd[0])); asm volatile("" : "+v"(gd[1]));
#pragma unroll
      for (int i = 0; i < 4; ++i)
#pragma unroll
        for (int c = 0; c < 2; ++c) {
          float a0 = __builtin_fmaf(hf[i][0][c], gd[c], xf[i][0][c]), a1 = __builtin_fmaf(hf[i][1][c], gd[c], xf[i][1][c]);
          asm volatile("" : "+v"(a0)); asm volatile("" : "+v"(a1));
          xc[i][c] = f32x2{a0, a1};
        }
    }
    STAMP(12);
    SCHED();
    u32x4 wr[20];
    {
      const rsrc_t rw2 = __builtin_amdgcn_make_buffer_rsrc((void*)p.w2, 0, D * F * 2, 0x00020000);
#pragma unroll
      for (int i = 0; i < 20; ++i) wr[i] = __builtin_amdgcn_raw_buffer_load_b128(rw2, vw2[i & 3] + (i & 3) * 1024, sw2 + (9 + i / 4) * 65536, AUX_W);
    }
    SCHED();
    const int g = (lane >> 1) & 3;
    const int ro = (2 * lw * 64 + 16 * g + ((lane >> 3) ^ (4 * g))) * 16 + 8 * (lane & 1);
    STAMP(13);
    BARRIER();
    STAMP(14);
    {
      u32x4 o[2], s0, s1;
      f32x2 yp[4][4];
#pragma unroll
      for (int i = 0; i < 4; ++i) {
        const int a = (ro ^ (128 * (i & 1))) + (i >> 1) * 1024;
        yp[i][0] = *(LAS const f32x2*)(smem + LD_RED0 + a); yp[i][1] = *(LAS const f32x2*)(smem + LD_RED0 + 8192 + a);
        yp[i][2] = *(LAS const f32x2*)(smem + LD_RED1 + a); yp[i][3] = *(LAS const f32x2*)(smem + LD_RED1 + 8192 + a);
      }
      SCHED();
#pragma unroll
      for (int i = 0; i < 4; ++i) {
        const f32x2 y0 = yp[i][0] + yp[i][1], y1 = yp[i][2] + yp[i][3];
        const f32x2 c0 = y0 * gav[0] + xc[i][0], c1 = y1 * gav[1] + xc[i][1];
        s0[i] = pk2(c0 * gcv[0]); s1[i] = pk2(c1 * gcv[1]);
        o[i >> 1][2 * (i & 1)] = pk(c0[0], c1[0]); o[i >> 1][2 * (i & 1) + 1] = pk(c0[1], c1[1]);
      }
      const int so = WS_X1S + (lw * 4 + (lane >> 4)) * 131072 + d0 * 256 + (lane & 15) * 16;
      st_sc1(rws, so, 0, s0);
      st_sc1(rws, so, 256, s1);
      const int xo = WS_X1 + (lw * 8 + (lane >> 3)) * 65536 + pair * 128 + (lane & 7) * 16;
      st_sc1(rws, xo, 0, o[0]);
      st_sc1(rws, xo, 32768, o[1]);
    }
    STAMP(15);
    WAIT_VM(0);
    STAMP(17);
    if (lane < 4) st_sc1(rws, lane * 16, WS_F2 + (lw * 256 + pair) * 64, u32x4{ta, ta, tb, tb});
    __builtin_amdgcn_s_setprio(0);
    if (helper) {
      BARRIER(); BARRIER(); BARRIER(); BARRIER();
      w2_stage(0); w2_stage(1); w2_stage(2); w2_stage(3); w2_stage(4); w2_stage(5);
    } else {
      if (F2_DELAY) __builtin_amdgcn_s_sleep(F2_DELAY);
      poll16(rws, ((lane >> 3) * 32 + lw * 8 + (lane & 7)) * 64, WS_F2 + (xcd >> 1) * 16384, ta, tb);
      STAMP(19);
      x_stage(0); x_stage(1); WAIT_VM(4); BARRIER();
      x_stage(2); x_stage(3); WAIT_VM(8); BARRIER();
      x_stage(4); w1_stage(4); WAIT_VM(12); BARRIER();
#pragma unroll 1
      for (int s = 5; s < 8; ++s) { x_stage(s); w1_stage(s); WAIT_VM(16); BARRIER(); }
      w2_stage(0); w2_stage(1); WAIT_VM(16); BARRIER();
      w2_stage(2); w2_stage(3); WAIT_VM(16); BARRIER();
      w2_stage(4); w2_stage(5); BARRIER();
    }
    STAMP(22);
    w2_stage(6); w2_stage(7); w2_stage(8);
    WAIT_VM(0);
    STAMP(25);
    BARRIER();
    BARRIER();
#pragma unroll
    for (int i = 0; i < 20; ++i) *(LAS u32x4*)(smem + w2_unit(9 + i / 4) * UNIT + lw * 4096 + (i & 3) * 1024 + lane * 16) = wr[i];
    BARRIER();
    if (b == 0 && tid == 256) *(u32x2*)(p.ws + WS_EPOCH) = u32x2{ta, tb};
    STAMP(26);
    STAMP_FLUSH(p.ws, 1);
    return;
  }

  {
    SCAL_ISSUE();
    u32 rar = LDNT(p.Are + po), rai = LDNT(p.Aim + po), rbr = LDNT(p.Bre + po), rbi = LDNT(p.Bim + po), rcr = LDNT(p.Cre + po), rci = LDNT(p.Cim + po);
    SCHED();
    u32 cp0;
    {
      u64 pc; int r0, r1;
      asm volatile("s_getpc_b64 %0\n\ts_mov_b32 %1, fused_block@rel32@lo+4\n\ts_mov_b32 %2, fused_block_end@rel32@lo+12" : "=s"(pc), "=s"(r0), "=s"(r1));
      TOUCH(cp0, min(((w * 4 + (lane & 3)) * 32 + j) * 128, r1 - r0), pc + (long long)r0);
    }
    SCHED();
    SCAL_WAIT();
    BARRIER();
    int ga[11], ka[4];
#pragma unroll
    for (int i = 0; i < 11; ++i) {
      const int l = 2047 - (tid * 8 + i);
      const int bits = (int)(pk((float)l, 0.f) & 0xffffu);
      const int idx = l < 128 ? l : bits - 0x4300 + 128;
      ga[i] = LD_KD + 4 * (l < 0 ? KD_ZERO : idx + (idx >> 3));
      asm volatile("" : "+v"(ga[i]));
    }
    {
      const int e = lane & 15, g = lane >> 4;
      const u32 cfg = e < 8 ? (w == 0 ? 0x100004u : w == 1 ? 0x080004u : 0x022001u) : (w == 0 ? 0x100004u : w == 1 ? 0x044002u : 0x088003u);
      const int sh = cfg & 15, base = 256 * w + ((cfg >> 8) & 0xff) * (e >> sh) + (e & ((1 << sh) - 1));
#pragma unroll
      for (int r = 0; r < 4; ++r) { const int idx = base + (int)(cfg >> 16) * (4 * g + r); ka[r] = LD_KD + 4 * (idx + (idx >> 3)); asm volatile("" : "+v"(ka[r])); }
    }
    STAMP(1);
    asm volatile("" : "+v"(rar), "+v"(rai), "+v"(rbr), "+v"(rbi), "+v"(rcr), "+v"(rci) : "v"(cp0));
    STAMP(2);
    u32x4 wv[8]; u32 cv0, cv1;
    {
      const rsrc_t rm = __builtin_amdgcn_make_buffer_rsrc((void*)p.wmod, 0, w == 3 ? D * 3072 * 2 : 0, 0x00020000);
      const int k0 = (b >> 5) * 64 + (lane >> 3);
      cv0 = p.c[k0 + 8 * (lane & 3)]; cv1 = p.c[k0 + 32 + 8 * (lane & 3)];
#pragma unroll
      for (int i = 0; i < 8; ++i) wv[i] = __builtin_amdgcn_raw_buffer_load_b128(rm, k0 * 6144 + (lane & 7) * 16, 2048 + (b & 31) * 128 + i * 8 * 6144, AUX_NT);
    }
    SCHED();
    const float dt = rb(__builtin_amdgcn_exp2f((pch ? bfhi(ldt2) : bflo(ldt2)) * LOG2E));
    const float a2 = (dt * bflo(rar)) * LOG2E, bq = (dt * bflo(rai)) * 0.15915494309189535f;
    const f32x2 z1 = cpow(a2, bq, 1.f), z16c = cpow(a2, -bq, 16.f);
    f32x2 q = {1.f, 0.f};
    f32x2 r = cmul(f32x2{bflo(rcr), -bflo(rci)}, f32x2{bflo(rbr), -bflo(rbi)} * dt);
#pragma unroll
    for (int e = 0; e < 16; ++e) {
      P[e * PROW] = pk2(r);
      P[(16 + e) * PROW] = pk2(q);
      r = cmul(r, z16c); q = cmul(q, z1);
    }
    STAMP(3);
    BARRIER();
    STAMP(4);
    asm volatile("" : "+v"(cv0), "+v"(cv1), "+v"(wv[0]), "+v"(wv[1]), "+v"(wv[2]), "+v"(wv[3]), "+v"(wv[4]), "+v"(wv[5]), "+v"(wv[6]), "+v"(wv[7]));
    if (w < 3) {
      LAS const u32* fb = (LAS const u32*)(smem + LD_P) + (lane & 15) * PROW + ((lane >> 4) & 1) * 64 + (lane >> 5) * 32;
      u32x4 fa[16], fc[16];
#pragma unroll
      for (int ks = 0; ks < 16; ++ks) {
        const int o = (ks & 1) * 64 * PROW + (ks >> 1) * 4;
        fa[ks] = *(LAS const u32x4*)(fb + o); fc[ks] = *(LAS const u32x4*)(fb + (w + 1) * 16 * PROW + o);
      }
      SCHED();
      f32x4 acc[2] = {{0.f, 0.f, 0.f, 0.f}, {0.f, 0.f, 0.f, 0.f}};
#pragma unroll
      for (int ks = 0; ks < 16; ++ks) acc[ks & 1] = mfma16(fa[ks], fc[ks], acc[ks & 1]);
#pragma unroll
      for (int r4 = 0; r4 < 4; ++r4) *(LAS u32*)(smem + ka[r4]) = pk(acc[0][r4], acc[1][r4]);
    } else {
      const u32 s0 = silu_bf(cv0), s1 = silu_bf(cv1);
      float acc[8] = {0.f, 0.f, 0.f, 0.f, 0.f, 0.f, 0.f, 0.f};
      gv_row<0x00>(wv[0], s0, acc); gv_row<0x55>(wv[1], s0, acc); gv_row<0xAA>(wv[2], s0, acc); gv_row<0xFF>(wv[3], s0, acc);
      gv_row<0x00>(wv[4], s1, acc); gv_row<0x55>(wv[5], s1, acc); gv_row<0xAA>(wv[6], s1, acc); gv_row<0xFF>(wv[7], s1, acc);
      const f32x2 m = gv_fold(acc);
      if (!(lane & 8)) st_sc1(rws, ((lane >> 4) * 8 + (lane & 7)) * 16, WS_MH + ((b >> 5) * 1024 + (b & 31) * 32) * 16, u32x4{__float_as_uint(m[0]), ta, __float_as_uint(m[1]), tb});
    }
    STAMP(5);
    BARRIER();
    STAMP(6);
    u32 e[11];
#pragma unroll
    for (int i = 0; i < 11; ++i) e[i] = *(LAS const u32*)(smem + ga[i]);
#pragma unroll
    for (int c = 0; c < 2; ++c)
#pragma unroll
      for (int s = 0; s < 4; ++s) {
        u32x4 o;
#pragma unroll
        for (int z = 0; z < 4; ++z) o[z] = __builtin_amdgcn_perm(e[s + 2 * z + 1], e[s + 2 * z], c ? 0x07060302u : 0x05040100u);
        *(LAS u32x4*)(smem + LD_T + (c * 4 + s) * TCOPY + tid * 16) = o;
      }
  }
  {
    const int r = lane & 15, g = lane >> 4, ch = w >> 1, half = w & 1;
    LAS const char* Tb = smem + LD_T + (ch * 4 + ((3 - r) & 3)) * TCOPY + 2 * (2028 - 4 * (r >> 2) + 8 * g - 128 * half) - 7 * 512 - 192;
    LAS const char* Ub = smem + LD_U + ch * 4608 + 2 * (256 + 16 * r - 16 + 8 * g - 128 * half) - 192;
    LAS f32x4* red = (LAS f32x4*)(smem + (ch ? LD_RED1 : LD_RED0)) + half * 512 + (lane ^ ((lane >> 4) << 2));
    int pfo = w < 2 ? (w * 7168 + b * 18 + min(lane, 17)) * 128 : (7168 + (b * 2 + (w & 1)) * 14 + min(lane, 13)) * 128;
    asm volatile("" : "+v"(pfo));
    f32x4 acc[8];
#pragma unroll
    for (int n = 0; n < 8; ++n) acc[n] = f32x4{0.f, 0.f, 0.f, 0.f};
    u32x4 uf[8], tf[8];
#define LD_TF(a, e) do { const u32x2 l_ = *(volatile LAS const u32x2*)(Tb + 64 * (3 - (e)) + 512 * (7 - (a))), h_ = *(volatile LAS const u32x2*)(Tb + 64 * (3 - (e)) + 512 * (7 - (a)) + 8); \
                         tf[a] = u32x4{l_[0], l_[1], h_[0], h_[1]}; } while (0)
#define LD_UF(n, e) uf[n] = *(LAS const u32x4*)(Ub + 64 * (3 - (e)) + 512 * (n))
#define CM(a, m) do { SCHED(); acc[(a) + (m)] = mfma16(tf[a], uf[m], acc[(a) + (m)]); SCHED(); } while (0)
    STAMP(10);
    BARRIER();
    STAMP(11);
    LD_TF(0, 0);
#pragma unroll
    for (int n = 7; n >= 0; --n) LD_UF(n, 0);
    u32 pf0;
    TOUCH(pf0, pfo, w < 2 ? p.w2 : p.w1);
#pragma unroll
    for (int a = 0; a < 8; ++a) {
#pragma unroll
      for (int m = 7 - a; m >= 0; --m) {
        CM(a, m);
        if (a == 0 && m > 0) LD_TF(8 - m, 0);
        if (m == 7 - a) LD_UF(7 - a, 1);
      }
      LD_TF(a, 1);
    }
#pragma unroll
    for (int m = 7; m >= 0; --m) {
#pragma unroll
      for (int a = 0; a + m < 8; ++a) { CM(a, m); if (m == 0) LD_TF(a, 2); }
      LD_UF(m, 2);
    }
#pragma unroll
    for (int z = 0; z < 4; ++z) {
#pragma unroll
      for (int m = 7 - z; m >= z; --m) { CM(z, m); if (m == 7 - z) LD_UF(7 - z, 3); }
      LD_TF(z, 3);
#pragma unroll
      for (int a = z + 1; a + z < 8; ++a) CM(a, z);
      LD_UF(z, 3); LD_TF(7 - z, 3);
    }
#pragma unroll
    for (int n = 0; n < 8; ++n)
#pragma unroll
      for (int a = 0; a <= n; ++a) { CM(a, n - a); if (a == 1) red[(n - 1) * 64] = acc[n - 1]; }
    STAMP(12);
    red[7 * 64] = acc[7];
    BARRIER();
    STAMP(14);
    WAIT_VM(0);
    asm volatile("" :: "v"(pf0));
    STAMP(17);
  }
  const int mi = LD_MISC + w * 3072;
  u32x4 gq2;
#define EXTRAS() do { const int xs_ = WS_X1 + tbk * 65536 + (db * 32 + (w >> 1) * 16) * 128 + (w & 1) * 64; \
    dma16_sc1(qws, mi + 1024, (lane >> 2) * 128 + (lane & 3) * 16, xs_); dma16_sc1(qws, mi + 2048, (lane >> 2) * 128 + (lane & 3) * 16, xs_ + 32768); \
    gq2 = ld_sc1(rws, (lane & 15) * 16, WS_MF + 4096 + (db * 32 + (w >> 1) * 16) * 16); } while (0)
  if (helper) {
    const int hb = xcd * 4 + j;
    u32x4 v[16];
    const int rg = tid >> 3, cl = tid & 7;
#pragma unroll
    for (int i = 0; i < 16; ++i) v[i] = *(const u32x4*)(p.w1 + (size_t)(i * 32 + rg) * F + (hb < 28 ? hb : 0) * 64 + cl * 8);
    LAS float* rd = (LAS float*)(smem + 4096);
    { const u32x4 m = poll16(rws, tid * 16, WS_MF, ta, tb); *(LAS u32x2*)(smem + tid * 8) = u32x2{m[0], m[2]}; }
    BARRIER();
    float a[16];
#pragma unroll
    for (int e = 0; e < 16; ++e) a[e] = 0.f;
#pragma unroll
    for (int i = 0; i < 16; ++i) {
      const u32 gs = *(LAS const u32*)(smem + (i * 32 + rg) * 4);
      const u32 gl = gs & 0xffffu, gh = gs << 16, sl = gs >> 16, sh = gs & 0xffff0000u;
#pragma unroll
      for (int z = 0; z < 4; ++z) {
        a[4 * z] = dot2(v[i][z], gl, a[4 * z]); a[4 * z + 1] = dot2(v[i][z], sl, a[4 * z + 1]);
        a[4 * z + 2] = dot2(v[i][z], gh, a[4 * z + 2]); a[4 * z + 3] = dot2(v[i][z], sh, a[4 * z + 3]);
      }
    }
    fold_cols(a, rd + w * 128, lane);
    STAMP(18);
    poll16(rws, tid * 64, WS_F2 + (xcd >> 1) * 16384, ta, tb);
    BARRIER();
    STAMP(19);
#pragma unroll
    for (int i = 0; i < 16; ++i) v[i] = ld_sc1(rws, tid * 16, WS_X1 + hb * 65536 + i * 4096);
    if (w == 0 && hb < 28) {
      const f32x2 s = sum4(rd + lane * 2, 128);
      st_sc1(rws, lane * 16, WS_S12 + hb * 1024, u32x4{__float_as_uint(s[0]), ta, __float_as_uint(s[1]), tb});
    }
#pragma unroll
    for (int e = 0; e < 16; ++e) a[e] = 0.f;
#pragma unroll
    for (int i = 0; i < 16; ++i)
#pragma unroll
      for (int z = 0; z < 4; ++z) {
        const int o = (i >> 3) * 8 + 2 * z;
        a[o] = dot2(v[i][z], 0x3f803f80u, a[o]); a[o + 1] = dot2(v[i][z], v[i][z], a[o + 1]);
      }
    STAMP(20);
    fold_cols(a, rd + 512 + w * 128, lane);
    BARRIER();
    if (w == 0) {
      const f32x2 s = sum4(rd + 512 + lane * 2, 128);
      const u32 st = ln_stat(s[0], s[1]);
      const u32 st2 = __builtin_amdgcn_mov_dpp(st, 0xB1, 0xf, 0xf, true);
      if (!(lane & 1)) st_sc1(rws, lane * 8, WS_G2 + hb * 512, u32x4{st, ta, st2, tb});
    }
    EXTRAS();
    BARRIER();
    STAMP(22);
  } else {
    const int wt = w & 1, wf = w >> 1;
    const int h = lane >> 5, blk = (lane >> 4) & 1, q = (lane & 15) >> 2, pp = lane & 3;
    int ax[2][2], aw[2][2];
#pragma unroll
    for (int c = 0; c < 2; ++c)
#pragma unroll
      for (int t = 0; t < 2; ++t) {
        const int base = 256 * (8 * h + 4 * t + q) + 16 * ((2 * blk + (pp >> 1)) ^ (2 * h + t)) + 8 * (pp & 1);
        ax[c][t] = LD_XR + base + 64 * ((2 * wt + c) ^ q);
        aw[c][t] = LD_WR + base + 64 * ((2 * wf + c) ^ q);
      }
    f32x16 acc[2][2];
#pragma unroll
    for (int a = 0; a < 2; ++a)
#pragma unroll
      for (int c = 0; c < 2; ++c)
#pragma unroll
        for (int r = 0; r < 16; ++r) acc[a][c][r] = 0.f;
    u32x4 xf[2][2], wfr[2][2], gq, sq;
#define RD_X(i, c) xf[(i) & 1][c] = tr_frag(smem + ax[c][0] + ((i) & 15) * 4096, smem + ax[c][1] + ((i) & 15) * 4096)
#define RD_W(i, c) wfr[(i) & 1][c] = tr_frag(smem + aw[c][0] + ((i) & 15) * 4096, smem + aw[c][1] + ((i) & 15) * 4096)
#define MM(i, a, c) do { SCHED(); acc[a][c] = mfma32(wfr[(i) & 1][a], xf[(i) & 1][c], acc[a][c]); SCHED(); } while (0)
    BARRIER();
    STAMP(19);
    RD_W(0, 0); RD_X(0, 0); RD_X(0, 1); RD_W(0, 1);
#pragma unroll
    for (int i = 0; i < 32; ++i) {
      if ((i & 3) != 3) {
        MM(i, 0, 0); RD_W(i + 1, 0); MM(i, 0, 1); RD_X(i + 1, 0); MM(i, 1, 0); RD_X(i + 1, 1); MM(i, 1, 1); RD_W(i + 1, 1);
      } else {
        MM(i, 0, 0); MM(i, 0, 1);
        BARRIER();
        if (i == 15) EXTRAS();
        if (i == 27) { gq = ld_sc1(rws, lane * 16, WS_G2 + rbk * 1024); sq = ld_sc1(rws, lane * 16, WS_S12 + (cbk * 128 + wf * 64) * 16); }
        MM(i, 1, 0);
        if (i < 31) { RD_W(i + 1, 0); RD_X(i + 1, 0); }
        MM(i, 1, 1);
        if (i < 31) { RD_X(i + 1, 1); RD_W(i + 1, 1); }
      }
    }
    STAMP(21);
    if (__any((tagdiff(gq, ta, tb) | tagdiff(sq, ta, tb)) != 0u)) {
      gq = poll16(rws, lane * 16, WS_G2 + rbk * 1024, ta, tb);
      sq = poll16(rws, lane * 16, WS_S12 + (cbk * 128 + wf * 64) * 16, ta, tb);
    }
    *(LAS u32x2*)(smem + mi + lane * 8) = u32x2{gq[0], gq[2]};
    *(LAS u32*)(smem + mi + 512 + lane * 4) = sq[0];
    *(LAS float*)(smem + mi + 768 + lane * 4) = __uint_as_float(sq[2]) * -LOG2E;
    float rstd[2], mr[2];
#pragma unroll
    for (int c = 0; c < 2; ++c) {
      const u32 st = *(LAS const u32*)(smem + mi + (wt * 64 + c * 32 + (lane & 31)) * 4);
      rstd[c] = __builtin_amdgcn_rcpf(bfhi(st)) * -LOG2E;
      mr[c] = -bflo(st) * rstd[c];
    }
    STAMP(22);
    LAS const float* s12 = (LAS const float*)(smem + mi + 512) + 4 * h;
    const int ho = WS_H + ((rbk * 4 + wt * 2) * 112 + cbk * 8 + wf * 4) * 1024 + lane * 16;
#pragma unroll
    for (int a = 0; a < 2; ++a)
#pragma unroll
      for (int sp = 0; sp < 2; ++sp) {
        f32x4 s1[2], s2[2];
#pragma unroll
        for (int z = 0; z < 2; ++z) { s1[z] = *(LAS const f32x4*)(s12 + a * 32 + sp * 16 + z * 8); s2[z] = *(LAS const f32x4*)(s12 + 64 + a * 32 + sp * 16 + z * 8); }
#pragma unroll
        for (int c = 0; c < 2; ++c) {
          u32x4 o;
#pragma unroll
          for (int e = 0; e < 4; ++e) {
            const int z = e >> 1, k = (e & 1) * 2;
            const f32x2 av = {acc[a][c][sp * 8 + 2 * e], acc[a][c][sp * 8 + 2 * e + 1]};
            const f32x2 cc = f32x2{s1[z][k], s1[z][k + 1]} * mr[c] + f32x2{s2[z][k], s2[z][k + 1]};
            const f32x2 ar = av * rstd[c] + cc;
            const f32x2 dn = f32x2{__builtin_amdgcn_exp2f(ar[0]), __builtin_amdgcn_exp2f(ar[1])} + 1.f;
            o[e] = pk2(ar * f32x2{__builtin_amdgcn_rcpf(dn[0]), __builtin_amdgcn_rcpf(dn[1])});
          }
          st_sc1(rws, ho, (c * 112 + a * 2 + sp) * 1024, o);
        }
      }
    STAMP(23);
    WAIT_VM(0);
    STAMP(24);
    if (lane < 8) st_sc1(rws, lane * 16, WS_F3 + ((rbk * 4 + w) * 16 + cbk) * 128, u32x4{ta, ta, tb, tb});
  }

  {
    const int wt = w & 1, kh = w >> 1, h = lane >> 5, tl = lane & 31;
    const int blk = (lane >> 4) & 1, q = (lane & 15) >> 2, pp = lane & 3;
    int aw[3][2];
#pragma unroll
    for (int c = 0; c < 6; ++c) {
      aw[c >> 1][c & 1] = (c >> 1) * 65536 + kh * 8192 + 128 * (4 * h + q) + 64 * ((c & 1) ^ kh ^ (q >> 1)) + 32 * blk + 8 * pp;
      asm volatile("" : "+v"(aw[c >> 1][c & 1]));
    }
    f32x16 acc[2];
#pragma unroll
    for (int r = 0; r < 16; ++r) { acc[0][r] = 0.f; acc[1][r] = 0.f; }
    STAMP(25);
    BARRIER();
    constexpr int NW = G2_NW;
    u32x4 hf[NW], wf[3][2];
#define RD_W2C(i, c) do { LAS char* a_ = (LAS char*)aw[w2_unit((i) >> 2) >> 2][c] + (w2_unit((i) >> 2) & 3) * UNIT + ((i) & 3) * 2048; wf[(i) % 3][c] = tr_frag(a_, a_ + 1024); } while (0)
#define RD_W2(i) do { RD_W2C(i, 0); RD_W2C(i, 1); } while (0)
    RD_W2(0); RD_W2(1);
    if (__any(tagdiff(gq2, ta, tb) != 0u)) gq2 = poll16(rws, (lane & 15) * 16, WS_MF + 4096 + (db * 32 + kh * 16) * 16, ta, tb);
    const int sh = WS_H + ((tbk * 2 + wt) * 112 + 56 * kh) * 1024;
    poll16(rws, ((lane & 1) * 32 + min(lane >> 1, 6)) * 128, WS_F3 + ((tbk >> 1) * 4 + (tbk & 1)) * 2048 + kh * 896, ta, tb);
    STAMP(26);
#pragma unroll
    for (int i = 0; i < NW; ++i) hf[i] = ld_sc1(rws, lane * 16 + (i & 3) * 1024, sh + (i >> 2) * 4096);
    SCHED();
    u32 xr[8]; f32x2 gb[8];
    *(LAS f32x2*)(smem + mi + lane * 8) = f32x2{__uint_as_float(gq2[0]), __uint_as_float(gq2[2])} * (-1.f / LOG2E);
#pragma unroll
    for (int i = 0; i < 8; ++i) {
      const int pr = 4 * (i >> 1) + 2 * h + (i & 1);
      xr[i] = *(LAS const u32*)(smem + mi + 1024 + ((tl >> 2) & 1) * 1024 + pr * 64 + (tl >> 3) * 16 + (tl & 3) * 4);
      gb[i] = *(LAS const f32x2*)(smem + mi + pr * 8);
    }
    SCHED();
#pragma unroll
    for (int i = 0; i < 56; ++i) {
      SCHED();
      acc[0] = mfma32(wf[i % 3][0], hf[i % NW], acc[0]);
      SCHED();
      if (i == 0) STAMP(29);
      if (i == 18) STAMP(30);
      if (i == 34) STAMP(31);
      if (i == 18 || i == 34) BARRIER();
      if (i + 2 < 56) RD_W2C(i + 2, 0);
      if (i > 0 && i - 1 + NW < 56) hf[(i - 1) % NW] = ld_sc1(rws, lane * 16 + ((i - 1 + NW) & 3) * 1024, sh + ((i - 1 + NW) >> 2) * 4096);
      SCHED();
      acc[1] = mfma32(wf[i % 3][1], hf[i % NW], acc[1]);
      SCHED();
      if (i + 2 < 56) RD_W2C(i + 2, 1);
    }
    STAMP(27);
    LAS f32x4* ex = (LAS f32x4*)(smem + w2_unit(5) * UNIT) + lane;
#pragma unroll
    for (int qq = 0; qq < 4; ++qq) ex[(w * 4 + qq) * 64] = f32x4{acc[1][4 * qq], acc[1][4 * qq + 1], acc[1][4 * qq + 2], acc[1][4 * qq + 3]};
    BARRIER();
    LAS char* ot = smem + w2_unit(6) * UNIT;
    f32x4 oth[4];
#pragma unroll
    for (int qq = 0; qq < 4; ++qq) oth[qq] = ex[((w ^ 2) * 4 + qq) * 64];
    SCHED();
#pragma unroll
    for (int qq = 0; qq < 4; ++qq) {
      const f32x4 o4 = oth[qq];
      u32x2 o;
#pragma unroll
      for (int z = 0; z < 2; ++z)
        o[z] = pk2((f32x2{acc[0][4 * qq + 2 * z], acc[0][4 * qq + 2 * z + 1]} + f32x2{o4[2 * z], o4[2 * z + 1]}) * gb[2 * qq + z] + up(xr[2 * qq + z]));
      *(LAS u32x2*)(ot + (wt * 32 + tl) * 144 + kh * 64 + qq * 16 + h * 8) = o;
    }
    BARRIER();
#pragma unroll
    for (int i = 0; i < 2; ++i) {
      const int row = (tid >> 3) + 32 * i, r = tbk * 64 + row;
      const int t = (r & ~511) + 128 * ((r >> 1) & 3) + 2 * ((r >> 3) & 63) + (r & 1);
      *(u32x4*)(p.out + (size_t)t * D + db * 64 + (tid & 7) * 8) = *(LAS const u32x4*)(ot + row * 144 + (tid & 7) * 16);
    }
    STAMP(28);
    STAMP_FLUSH(p.ws, 0);
  }
}

extern "C" __global__ void fused_block_end() {}

extern "C" void kernel_launch(void* const* d_in, const int* in_sizes, int n_in, void* d_out, int out_size, void* d_ws, size_t ws_size,
                              hipStream_t stream) {
  Params p;
  p.x = (const u16*)d_in[0]; p.Are = (const u16*)d_in[1]; p.Aim = (const u16*)d_in[2]; p.Bre = (const u16*)d_in[3];
  p.Bim = (const u16*)d_in[4]; p.Cre = (const u16*)d_in[5]; p.Cim = (const u16*)d_in[6]; p.ldt = (const u16*)d_in[7];
  p.dskip = (const u16*)d_in[8]; p.w1 = (const u16*)d_in[10]; p.w2 = (const u16*)d_in[11]; p.c = (const u16*)d_in[12];
  p.wmod = (const u16*)d_in[13]; p.out = (u16*)d_out; p.ws = (char*)d_ws;
  (void)hipFuncSetAttribute((const void*)fused_block, hipFuncAttributeMaxDynamicSharedMemorySize, LDS_BYTES + ST_LDS_EXTRA);
  fused_block<<<256, 512, LDS_BYTES + ST_LDS_EXTRA, stream>>>(p);
}
```

```cpp
#define G_TH 2
#include <hip/hip_runtime.h>
#define ST_OFF ((size_t)128 << 20)
#define ST_SLOTS 32
#define ST_MAXW 8
#define ST_MAXB 512
#define ST_LDS_EXTRA 2048
typedef unsigned st_u2 __attribute__((ext_vector_type(2)));
#define STAMP_DECL __attribute__((address_space(3))) st_u2* st_ = (__attribute__((address_space(3))) st_u2*)(smem + LDS_BYTES) + (threadIdx.x >> 6) * 32; \
  if ((threadIdx.x & 63) < 32) st_[threadIdx.x & 31] = st_u2{0u, 0u};
#define STAMP(s) do { unsigned long long rt_, cy_; __builtin_amdgcn_sched_barrier(0); \
  asm volatile("s_memrealtime %0\n\ts_memtime %1\n\ts_waitcnt lgkmcnt(0)" : "=&s"(rt_), "=&s"(cy_) :: "memory"); \
  st_[s] = st_u2{(unsigned)rt_ | 0x80000000u, (unsigned)cy_}; __builtin_amdgcn_sched_barrier(0); } while (0)
#define STAMP_FLUSH(ws, kern) do { if ((threadIdx.x & 63) < 32) { \
  unsigned long long* p_ = (unsigned long long*)((char*)(ws) + ST_OFF) + ((((size_t)(kern) * ST_MAXB + blockIdx.x) * ST_MAXW + (threadIdx.x >> 6)) * ST_SLOTS + (threadIdx.x & 31)) * 2; \
  st_u2 v_ = st_[threadIdx.x & 31]; p_[0] = v_[0]; p_[1] = v_[1]; } } while (0)
#include <hip/hip_runtime.h>
#include <hip/hip_bf16.h>

#define ST_RANGES {{0, 32, 256, "compute waves"}, {1, 32, 256, "loader waves"}, {0, 0, 32, "helper WGs, waves 0-3"}, {1, 0, 32, "helper WGs, waves 4-7"}}

typedef unsigned short u16;
typedef unsigned int u32;
typedef unsigned long long u64;
typedef u32 u32x2 __attribute__((ext_vector_type(2)));
typedef u32 u32x4 __attribute__((ext_vector_type(4)));
typedef float f32x2 __attribute__((ext_vector_type(2)));
typedef float f32x4 __attribute__((ext_vector_type(4)));
typedef float f32x16 __attribute__((ext_vector_type(16)));
typedef short s16x4 __attribute__((ext_vector_type(4)));
typedef __bf16 bf16x2 __attribute__((ext_vector_type(2)));
typedef __bf16 bf16x8 __attribute__((ext_vector_type(8)));
typedef __amdgpu_buffer_rsrc_t rsrc_t;
#define GAS __attribute__((address_space(1)))
#define LAS __attribute__((address_space(3)))
#define CAS __attribute__((address_space(4)))

constexpr int L = 2048, D = 512, NS = 128, F = 1792;

constexpr int WS_EPOCH = 0;
constexpr int WS_SG = 0x1000;
constexpr int WS_MG = 0x80000;
constexpr int WS_F2 = 0x40000;
constexpr int WS_F3 = 0x50000;
constexpr int WS_G2 = 0x70000;
constexpr int WS_S12 = 0x74000;
constexpr int WS_MF = 0x7c000;
constexpr int WS_X1 = 5 << 20;
constexpr int WS_X1S = 7 << 20;
constexpr int WS_H = 9 << 20;
constexpr int WS_END = 16 << 20;
constexpr int OOB = 0x7ff00000;

constexpr int UNIT = 16384;
constexpr int LD_XR = 0, LD_WR = 4 * UNIT, LD_MISC = 9 * UNIT, LDS_BYTES = LD_MISC + 10240;
constexpr int TCOPY = 4160, PROW = 132;
constexpr int LD_T = 0, LD_U = 8 * TCOPY, LD_KD = LD_U + 2 * 4608, LD_P = LD_WR, LD_RED0 = 3 * UNIT, LD_RED1 = 8 * UNIT;
constexpr int KD_ZERO = 900, LD_FLG = LD_KD + 904 * 4, LD_XT = LD_RED0;
static_assert(LD_FLG + 32 <= LD_RED0, "LDS map");
__host__ __device__ constexpr int w2_unit(int u) { return u % 9 == 8 ? 8 : (u % 9) / 2 + 4 * (u % 9 & 1); }
#define G2_NW 14
#define TF_PRE 2
#define G_DELAY 4
#define S1_SLEEP 2
#define F2_DELAY 16
#define POLL_SLEEP 2
#define LDNT(p) __builtin_nontemporal_load(p)
#define AUX_NT 2
#define WF ""
#define AUX_W 0
constexpr int SPIN_CAP = 1 << 15;
constexpr float LOG2E = 1.44269504088896341f;

__device__ __forceinline__ float bflo(u32 v) { return __uint_as_float(v << 16); }
__device__ __forceinline__ float bfhi(u32 v) { return __uint_as_float(v & 0xffff0000u); }
__device__ __forceinline__ u32 pk(float lo, float hi) {
  f32x2 v = {lo, hi};
  bf16x2 r = __builtin_convertvector(v, bf16x2);
  return __builtin_bit_cast(u32, r);
}
__device__ __forceinline__ float rb(float f) { return bflo(pk(f, f)); }
__device__ __forceinline__ float dot2(u32 a, u32 b, float c) {
  return __builtin_amdgcn_fdot2_f32_bf16(__builtin_bit_cast(bf16x2, a), __builtin_bit_cast(bf16x2, b), c, false);
}
__device__ __forceinline__ f32x4 mfma16(u32x4 a, u32x4 b, f32x4 c) {
  return __builtin_amdgcn_mfma_f32_16x16x32_bf16(__builtin_bit_cast(bf16x8, a), __builtin_bit_cast(bf16x8, b), c, 0, 0, 0);
}
__device__ __forceinline__ f32x16 mfma32(u32x4 a, u32x4 b, f32x16 c) {
  return __builtin_amdgcn_mfma_f32_32x32x16_bf16(__builtin_bit_cast(bf16x8, a), __builtin_bit_cast(bf16x8, b), c, 0, 0, 0);
}
template <int CTRL> __device__ __forceinline__ float dpp(float x) {
  return __builtin_bit_cast(float, __builtin_amdgcn_mov_dpp(__builtin_bit_cast(int, x), CTRL, 0xf, 0xf, true));
}
__device__ __forceinline__ float row_sum(float x) {
  x += dpp<0xB1>(x); x += dpp<0x4E>(x); x += dpp<0x124>(x); x += dpp<0x128>(x);
  return x;
}
__device__ __forceinline__ float fold32(float a, float b) {
  auto s = __builtin_amdgcn_permlane32_swap(__float_as_uint(a), __float_as_uint(b), false, false);
  return __uint_as_float(s[0]) + __uint_as_float(s[1]);
}
__device__ __forceinline__ float fold16(float a, float b) {
  auto s = __builtin_amdgcn_permlane16_swap(__float_as_uint(a), __float_as_uint(b), false, false);
  return __uint_as_float(s[0]) + __uint_as_float(s[1]);
}
__device__ __forceinline__ u32x4 mkrs(const void* p, u32 bytes) {
  const u64 a = (u64)p;
  return u32x4{(u32)a, (u32)(a >> 32) & 0xffffu, bytes, 0x00020000u};
}
__device__ __forceinline__ void dma4(u32x4 rs, int lds, int voff, int soff) {
  asm volatile("s_mov_b32 m0, %0\n\ts_nop 0\n\tbuffer_load_dword %1, %2, %3 offen lds" :: "s"(lds), "v"(voff), "s"(rs), "s"(soff) : "memory");
}
__device__ __forceinline__ void dma16_sc1(u32x4 rs, int lds, int voff, int soff) {
  asm volatile("s_mov_b32 m0, %0\n\ts_nop 0\n\tbuffer_load_dwordx4 %1, %2, %3 offen sc1 lds" :: "s"(lds), "v"(voff), "s"(rs), "s"(soff) : "memory");
}
#define DMA4_TEXT(F) "s_mov_b32 m0, %0\n\ts_nop 0\n\tbuffer_load_dwordx4 %1, %5, %6 offen" F " lds\n\tbuffer_load_dwordx4 %2, %5, %6 offen offset:1024" F " lds\n\t" \
                     "buffer_load_dwordx4 %3, %5, %6 offen offset:2048" F " lds\n\tbuffer_load_dwordx4 %4, %5, %6 offen offset:3072" F " lds"
__device__ __forceinline__ void dma4k(u32x4 rs, int lds, const int (&v)[4], int soff) {
  asm volatile(DMA4_TEXT(WF) :: "s"(lds), "v"(v[0]), "v"(v[1]), "v"(v[2]), "v"(v[3]), "s"(rs), "s"(soff) : "memory");
}
__device__ __forceinline__ void dma4k_sc1(u32x4 rs, int lds, const int (&v)[4], int soff) {
  asm volatile(DMA4_TEXT(" sc1") :: "s"(lds), "v"(v[0]), "v"(v[1]), "v"(v[2]), "v"(v[3]), "s"(rs), "s"(soff) : "memory");
}
__device__ __forceinline__ u32x4 ld_sc1(rsrc_t rs, int voff, int soff) { return __builtin_amdgcn_raw_buffer_load_b128(rs, voff, soff, 16); }
__device__ __forceinline__ void st_sc1(rsrc_t rs, int voff, int soff, u32x4 v) { __builtin_amdgcn_raw_buffer_store_b128(v, rs, voff + soff, 0, 16); }
__device__ __forceinline__ u32x4 tr_frag(LAS char* a0, LAS char* a1) {
  const s16x4 lo = __builtin_amdgcn_ds_read_tr16_b64_v4i16((LAS s16x4*)a0), hi = __builtin_amdgcn_ds_read_tr16_b64_v4i16((LAS s16x4*)a1);
  const u32x2 l = __builtin_bit_cast(u32x2, lo), h = __builtin_bit_cast(u32x2, hi);
  return u32x4{l[0], l[1], h[0], h[1]};
}
#define SCHED() __builtin_amdgcn_sched_barrier(0)
#define FENCE() asm volatile("" ::: "memory")
#define WAIT_VM(n) asm volatile("s_waitcnt vmcnt(" #n ")" ::: "memory")
#define BARRIER() do { asm volatile("s_waitcnt lgkmcnt(0)" ::: "memory"); __builtin_amdgcn_s_barrier(); FENCE(); } while (0)

__device__ __forceinline__ f32x2 cmul(f32x2 a, f32x2 b) {
  const f32x2 t = f32x2{a[0], a[0]} * b;
  return __builtin_elementwise_fma(f32x2{a[1], a[1]}, f32x2{-b[1], b[0]}, t);
}
__device__ __forceinline__ f32x2 cpow(float a2, float bq, float l) {
  const float m = __builtin_amdgcn_exp2f(a2 * l);
  const float ph = __builtin_amdgcn_fractf(bq * l);
  return f32x2{__builtin_amdgcn_cosf(ph), __builtin_amdgcn_sinf(ph)} * m;
}
__device__ __forceinline__ u32 pk2(f32x2 a) { return pk(a[0], a[1]); }
__device__ __forceinline__ f32x2 up(u32 v) { return f32x2{bflo(v), bfhi(v)}; }
__device__ __forceinline__ u32 ln_stat(float sum, float sq) {
  const float m = sum * (1.f / D);
  const float var = fmaxf(sq * (1.f / D) - m * m, 0.f);
  return pk(m, __builtin_amdgcn_sqrtf(rb(rb(var) + 1e-5f)));
}
__device__ __forceinline__ u32 tagdiff(u32x4 v, u32 ta, u32 tb) { return (v[1] ^ ta) | (v[3] ^ tb); }
__device__ __forceinline__ u32x4 poll16(rsrc_t rs, int voff, int soff, u32 ta, u32 tb) {
  u32x4 v;
  int spins = 0;
  for (;;) {
    FENCE();
    v = ld_sc1(rs, voff, soff);
    if (tagdiff(v, ta, tb) == 0u || ++spins >= SPIN_CAP) break;
    if (POLL_SLEEP) __builtin_amdgcn_s_sleep(POLL_SLEEP);
  }
  FENCE();
  return v;
}
__device__ __forceinline__ void fold_cols(const float (&v)[16], LAS float* dst, int lane) {
  float u[8]; f32x4 t;
#pragma unroll
  for (int k = 0; k < 8; ++k) u[k] = fold32(v[k], v[k + 8]);
#pragma unroll
  for (int k = 0; k < 4; ++k) { const float a = fold16(u[k], u[k + 4]); t[k] = a + dpp<0x128>(a); }
  if (!(lane & 8)) *(LAS f32x4*)(dst + (lane & 7) * 16 + (lane >> 4) * 4) = t;
}
__device__ __forceinline__ f32x2 sum4(LAS const float* src, int stride) {
  return (*(LAS const f32x2*)src + *(LAS const f32x2*)(src + stride)) + (*(LAS const f32x2*)(src + 2 * stride) + *(LAS const f32x2*)(src + 3 * stride));
}
__device__ __forceinline__ u32 silu_bf(u32 c) {
  const float f = bflo(c), s = f * __builtin_amdgcn_rcpf(1.f + __builtin_amdgcn_exp2f(f * -LOG2E));
  return pk(s, s);
}
template <int CTRL> __device__ __forceinline__ void gv_row(u32x4 v, u32 sp, float (&acc)[8]) {
  const u32 sb = __builtin_amdgcn_mov_dpp(sp, CTRL, 0xf, 0xf, true), al = sb & 0xffffu, ah = sb & 0xffff0000u;
#pragma unroll
  for (int q = 0; q < 4; ++q) { acc[2 * q] = dot2(v[q], al, acc[2 * q]); acc[2 * q + 1] = dot2(v[q], ah, acc[2 * q + 1]); }
}
__device__ __forceinline__ f32x2 gv_fold(const float (&acc)[8]) {
  const float m0 = fold16(fold32(acc[0], acc[4]), fold32(acc[2], acc[6])), m1 = fold16(fold32(acc[1], acc[5]), fold32(acc[3], acc[7]));
  return f32x2{m0 + dpp<0x128>(m0), m1 + dpp<0x128>(m1)};
}
#define TOUCH(dst, voff, base) asm volatile("global_load_dword %0, %1, %2" : "=v"(dst) : "v"(voff), "s"(base))

__device__ __forceinline__ void quad_arrive(LAS char* f, int i, u32 n) {
  asm volatile("s_waitcnt lgkmcnt(0)" ::: "memory");
  *(volatile LAS u32*)(f + i * 4) = n;
}
__device__ __forceinline__ void quad_wait(LAS char* f, u32 n) {
  for (;;) {
    const u32x4 v = *(volatile LAS u32x4*)f;
    if (__builtin_amdgcn_readfirstlane(min(min(v[0], v[1]), min(v[2], v[3]))) >= n) break;
    __builtin_amdgcn_s_sleep(1);
  }
  FENCE();
}
__device__ __forceinline__ int only(bool c, int off) { return off | (c ? 0 : OOB); }
#define PIN(x) asm volatile("" : "+v"(x))

struct Params {
  const u16 *x, *Are, *Aim, *Bre, *Bim, *Cre, *Cim, *ldt, *dskip, *w1, *w2, *c, *wmod;
  u16* out;
  char* ws;
};

extern "C" __global__ void __launch_bounds__(512, 2) fused_block(Params p) {
  extern __shared__ __attribute__((aligned(16))) char smem_[];
  LAS char* smem = (LAS char*)smem_;
  const int tid = threadIdx.x, lane = tid & 63, w = __builtin_amdgcn_readfirstlane(tid >> 6), lw = w & 3;
  STAMP_DECL
  STAMP(0);
  *(LAS u32*)(smem + LD_FLG + w * 4) = 0u;
  BARRIER();
  asm volatile("" :: "s"(p.x), "s"(p.Are), "s"(p.Aim), "s"(p.Bre), "s"(p.Bim), "s"(p.Cre), "s"(p.Cim), "s"(p.ldt), "s"(p.dskip), "s"(p.w1), "s"(p.w2), "s"(p.c), "s"(p.wmod),
               "s"(p.out), "s"(p.ws));
#define SCAL_ISSUE(q) u64 epoch; u32 aux; \
  asm volatile("s_load_dwordx2 %0, %2, 0x0\n\ts_load_dword %1, %3, 0x0" : "=&s"(epoch), "=&s"(aux) : "s"(p.ws + WS_EPOCH), "s"(q)); SCHED()
#define SCAL_WAIT() asm volatile("s_waitcnt lgkmcnt(0)" : "+s"(epoch), "+s"(aux)); \
  epoch += 0x9E3779B97F4A7C15ull; ta = (u32)epoch; tb = (u32)(epoch >> 32); dsk = aux
#define ROLES() int b = blockIdx.x; asm volatile("" : "+s"(b)); \
  const int xcd = b & 7, j = b >> 3, pair = xcd * 32 + j, d0 = pair * 2; \
  const bool helper = j < 4; \
  const int rbk = (xcd >> 1) * 4 + (j & 3), cbk = (xcd & 1) * 7 + ((j - 4) >> 2), tbk = xcd * 4 + (j & 3), db = j >> 2; \
  const rsrc_t rws = __builtin_amdgcn_make_buffer_rsrc(p.ws, 0, WS_END, 0x00020000)

  if (__builtin_expect(w >= 4, 1)) {
    LAS char* flg = smem + LD_FLG + 16;
    u32x4 sg[4], mg, mh;
    u32x2 xg[4];
    ROLES();
    const int go = lw * 4096 + lane * 16;
    const int mo = WS_MG + ((lane & 15) * 2048 + (lane >> 4) * 256 + (pair >> 5) * 32 + (pair & 3) * 8 + ((pair >> 2) & 7)) * 16;
    u32 ta, tb, dsk;
    if (lw >= 2) {
      u32 xt, ft;
      TOUCH(xt, (j * 64 + (lw & 1) * 32 + (lane & 31)) * 1024 + xcd * 128, p.x);
      TOUCH(ft, (b * 56 + min(lane, 55)) * 128, lw & 1 ? p.w1 : p.w2);
      SCAL_ISSUE(p.dskip + d0);
      __builtin_amdgcn_s_sleep(G_DELAY);
      const u32x4 qx = mkrs(p.x, L * D * 2);
#pragma unroll
      for (int i = 0; i < 16; ++i) {
        dma4(qx, LD_XT + (lw & 1) * 4096 + i * 256, lane * 1024 + pair * 4, ((lw & 1) * 16 + i) * 65536);
        asm volatile("s_waitcnt vmcnt(%0)" :: "n"(G_TH) : "memory");
      }
      SCAL_WAIT();
      quad_arrive(flg, lw, 1u);
      STAMP(3);
#pragma unroll
      for (int i = 0; i < 4; ++i) sg[i] = ld_sc1(rws, go + i * 1024, WS_SG);
      mg = ld_sc1(rws, mo & ~0x2000, 0);
      WAIT_VM(5);
      asm volatile("" :: "v"(xt), "v"(ft));
    } else {
      u32x4 xv[4], wm[8];
#pragma unroll
      for (int i = 0; i < 4; ++i) xv[i] = LDNT((const u32x4*)(p.x + (size_t)(b * 8 + lw * 4 + i) * D) + lane);
      SCHED();
      SCAL_ISSUE(p.dskip + d0);
      const u32 c0 = p.c[(b >> 4) * 32 + (lane >> 3) + 8 * (lane & 3)];
      SCHED();
      {
        const rsrc_t ra = __builtin_amdgcn_make_buffer_rsrc((void*)p.wmod, 0, D * 3072 * 2, 0x00020000);
        const rsrc_t rb_ = __builtin_amdgcn_make_buffer_rsrc((void*)p.wmod, 0, lw == 1 ? D * 3072 * 2 : 0, 0x00020000);
#pragma unroll
        for (int i = 0; i < 8; ++i)
          wm[i] = __builtin_amdgcn_raw_buffer_load_b128(i < 4 ? ra : rb_, (lane >> 3) * 6144 + (lane & 7) * 16,
                                                        ((b >> 4) * 32 + 8 * (i & 3)) * 6144 + ((lw + (i >> 2)) * 16 + (b & 15)) * 128, AUX_NT);
      }
      SCHED();
      *(LAS u32x4*)(smem + LD_U + (lane >> 5) * 4608 + (lane & 31) * 16) = u32x4{0u, 0u, 0u, 0u};
      int so_ = only(!(lane & 31), WS_SG + b * 64 + lw * 32 + (lane >> 5) * 16);
      int mo_ = only(!(lane & 8), WS_MG + ((b >> 4) * 2048 + (lw * 16 + (b & 15)) * 32 + (lane >> 4) * 8 + (lane & 7)) * 16);
      PIN(so_); PIN(mo_);
      SCAL_WAIT();
      STAMP(1);
      float s[4], q[4];
#pragma unroll
      for (int i = 0; i < 4; ++i) {
        s[i] = 0.f; q[i] = 0.f;
#pragma unroll
        for (int z = 0; z < 4; ++z) { s[i] = dot2(xv[i][z], 0x3f803f80u, s[i]); q[i] = dot2(xv[i][z], xv[i][z], q[i]); }
      }
      const u32 st = ln_stat(row_sum(fold16(fold32(s[0], s[2]), fold32(s[1], s[3]))), row_sum(fold16(fold32(q[0], q[2]), fold32(q[1], q[3]))));
      const auto sw = __builtin_amdgcn_permlane16_swap(st, st, false, false);
      st_sc1(rws, so_, 0, u32x4{sw[0], ta, sw[1], tb});
      STAMP(2);
      const u32 sp = silu_bf(c0);
#pragma unroll
      for (int z = 0; z < 2; ++z) {
        float acc[8] = {0.f, 0.f, 0.f, 0.f, 0.f, 0.f, 0.f, 0.f};
        gv_row<0x00>(wm[4 * z], sp, acc); gv_row<0x55>(wm[4 * z + 1], sp, acc); gv_row<0xAA>(wm[4 * z + 2], sp, acc); gv_row<0xFF>(wm[4 * z + 3], sp, acc);
        const f32x2 m = gv_fold(acc);
        st_sc1(rws, z ? (mo_ + 8192) | (lw ? 0 : OOB) : mo_, 0, u32x4{__float_as_uint(m[0]), ta, __float_as_uint(m[1]), tb});
        SCHED();
      }
      quad_arrive(flg, lw, 2u);
      STAMP(4);
      quad_wait(flg, 1u);
#pragma unroll
      for (int i = 0; i < 4; ++i) sg[i] = ld_sc1(rws, go + i * 1024, WS_SG);
      mg = ld_sc1(rws, mo & ~0x2000, 0);
    }
    __builtin_amdgcn_s_setprio(2);
    quad_arrive(flg, lw, 2u);
    quad_wait(flg, 2u);
    STAMP(8);
#pragma unroll
    for (int i = 0; i < 4; ++i) xg[i] = *(LAS const u32x2*)(smem + LD_XT + lw * 2048 + i * 512 + lane * 8);
#pragma unroll 1
    for (int spins = 0; spins < SPIN_CAP; ++spins) {
      u32 bs[4];
      const u32 bm = tagdiff(mg, ta, tb);
      u32 bad = bm;
#pragma unroll
      for (int i = 0; i < 4; ++i) { bs[i] = tagdiff(sg[i], ta, tb); bad |= bs[i]; }
      if (__builtin_expect(!__any(bad != 0u), 1)) break;
      __builtin_amdgcn_s_sleep(S1_SLEEP);
      FENCE();
#pragma unroll
      for (int i = 0; i < 4; ++i) if (bs[i]) sg[i] = ld_sc1(rws, go + i * 1024, WS_SG);
      if (bm) mg = ld_sc1(rws, mo & ~0x2000, 0);
    }
    STAMP(9);
    mh = ld_sc1(rws, mo, 8192);
    SCHED();
    f32x2 xf[4][2], hf[4][2];
    {
      const u32 mb = pk(row_sum(__uint_as_float(mg[0])), row_sum(__uint_as_float(mg[2])));
      const f32x2 sa = up(__builtin_amdgcn_readlane(mb, 0)), g1 = up(__builtin_amdgcn_readlane(pk2(up(mb) + 1.f), 16));
#pragma unroll
      for (int i = 0; i < 4; ++i) {
#pragma unroll
        for (int e = 0; e < 2; ++e) {
          xf[i][e] = up(xg[i][e]);
          hf[i][e] = (xf[i][e] - bflo(sg[i][2 * e])) * __builtin_amdgcn_rcpf(bfhi(sg[i][2 * e])) * g1 + sa;
        }
        LAS u32* U = (LAS u32*)(smem + LD_U + 512 + lw * 1024 + i * 256) + lane;
        U[0] = pk(hf[i][0][0], hf[i][1][0]);
        U[4608 / 4] = pk(hf[i][0][1], hf[i][1][1]);
      }
    }
    STAMP(10);
    BARRIER();
    STAMP(11);
    const u32x4 qws = mkrs(p.ws, WS_END), qw1 = mkrs(p.w1, D * F * 2), qw2 = mkrs(p.w2, D * F * 2);
    int vx[4], vw1[4], vw2[4];
#pragma unroll
    for (int i = 0; i < 4; ++i) {
      const int chk = 16 * ((lane & 15) ^ ((lane >> 4) << 2) ^ i);
      vx[i] = (lane >> 4) * 256 + chk;
      vw1[i] = (lane >> 4) * (F * 2) + chk + i * (4 * F * 2 - 1024);
      vw2[i] = (lane >> 3) * 1024 + db * 128 + 16 * ((lane & 7) ^ (((lane >> 4) & 1) << 2)) + i * 7168;
    }
    const int sw2 = ((lw >> 1) * 896 + (lw & 1) * 32) * 1024;
    auto w2_stage = [&](int u) { dma4k(qw2, w2_unit(u) * UNIT + lw * 4096, vw2, sw2 + u * 65536); };
    auto w1_stage = [&](int s) { dma4k(qw1, LD_WR + (s & 3) * UNIT + lw * 4096, vw1, (s * 64 + 16 * lw) * (F * 2) + cbk * 256); };
    auto x_stage = [&](int s) { dma4k_sc1(qws, LD_XR + (s & 3) * UNIT + lw * 4096, vx, WS_X1S + rbk * 131072 + s * 16384 + lw * 4096); };
    if (__any(tagdiff(mh, ta, tb) != 0u)) mh = poll16(rws, mo, 8192, ta, tb);
    if (!helper) { w1_stage(0); w1_stage(1); w1_stage(2); w1_stage(3); }
    f32x2 gav, gcv, xc[4][2];
    {
      float m0 = row_sum(__uint_as_float(mh[0]));
      PIN(m0);
      float m1 = row_sum(__uint_as_float(mh[2]));
      PIN(m1);
      const u32 mb = pk(m0, m1);
      float p0 = bflo(mb) + 1.f, p1 = bfhi(mb) + 1.f, n0 = bflo(mb) * (-1.f / LOG2E), n1 = bfhi(mb) * (-1.f / LOG2E);
      PIN(p0); PIN(p1); PIN(n0); PIN(n1);
      const u32 gcb = __builtin_amdgcn_readlane(pk(p0, p1), 32), sbb = __builtin_amdgcn_readlane(mb, 16);
      u32 k0 = (gcb & 0xffffu) | (sbb << 16), k1 = (gcb >> 16) | (sbb & 0xffff0000u);
      PIN(k0); PIN(k1);
      const u32 f0 = lane & 1 ? k0 : __float_as_uint(n0), f1 = lane & 1 ? k1 : __float_as_uint(n1);
      st_sc1(rws, only((tid >> 1) == 248, WS_MF + (~lane & 1) * 4096 + pair * 16), 0, u32x4{f0, ta, f1, tb});
      gav = up(__builtin_amdgcn_readlane(mb, 0)); gcv = up(gcb);
      float gd[2] = {gav[0] * bflo(dsk), gav[1] * bfhi(dsk)};
      PIN(gd[0]); PIN(gd[1]);
#pragma unroll
      for (int i = 0; i < 4; ++i)
#pragma unroll
        for (int c = 0; c < 2; ++c) {
          float a0 = __builtin_fmaf(hf[i][0][c], gd[c], xf[i][0][c]), a1 = __builtin_fmaf(hf[i][1][c], gd[c], xf[i][1][c]);
          PIN(a0); PIN(a1);
          xc[i][c] = f32x2{a0, a1};
        }
    }
    const int g = (lane >> 1) & 3;
    LAS char* ra[2];
    ra[0] = smem + LD_RED0 + (2 * lw * 64 + 16 * g + ((lane >> 3) ^ (4 * g))) * 16 + 8 * (lane & 1);
    ra[1] = smem + LD_RED0 + (2 * lw * 64 + 16 * g + ((lane >> 3) ^ (4 * g) ^ 8)) * 16 + 8 * (lane & 1);
    LAS char* rc[2] = {ra[0] + (LD_RED1 - LD_RED0), ra[1] + (LD_RED1 - LD_RED0)};
    int so = WS_X1S + (lw * 4 + (lane >> 4)) * 131072 + d0 * 256 + (lane & 15) * 16;
    int xo0 = WS_X1 + (lw * 8 + (lane >> 3)) * 65536 + pair * 128 + (lane & 7) * 16, xo1 = xo0 + 32768, fo = only(lane < 4, WS_F2 + (lw * 256 + pair) * 64 + lane * 16);
    PIN(ra[0]); PIN(ra[1]); PIN(rc[0]); PIN(rc[1]); PIN(so); PIN(xo0); PIN(xo1); PIN(fo);
    STAMP(12);
    SCHED();
    u32x4 wr[20];
    {
      const rsrc_t rw2 = __builtin_amdgcn_make_buffer_rsrc((void*)p.w2, 0, D * F * 2, 0x00020000);
#pragma unroll
      for (int i = 0; i < 20; ++i) wr[i] = __builtin_amdgcn_raw_buffer_load_b128(rw2, vw2[i & 3] + (i & 3) * 1024, sw2 + (9 + i / 4) * 65536, AUX_W);
    }
    SCHED();
    STAMP(13);
    BARRIER();
    STAMP(14);
    {
      u32x4 o[2], s0, s1;
      f32x2 yp[4][4], c0[4], c1[4];
#pragma unroll
      for (int i = 0; i < 4; ++i) {
        yp[i][0] = *(volatile LAS const f32x2*)(ra[i & 1] + (i >> 1) * 1024); yp[i][1] = *(volatile LAS const f32x2*)(ra[i & 1] + (i >> 1) * 1024 + 8192);
      }
#pragma unroll
      for (int i = 0; i < 4; ++i) {
        yp[i][2] = *(volatile LAS const f32x2*)(rc[i & 1] + (i >> 1) * 1024); yp[i][3] = *(volatile LAS const f32x2*)(rc[i & 1] + (i >> 1) * 1024 + 8192);
      }
      SCHED();
#pragma unroll
      for (int i = 0; i < 4; ++i) { c0[i] = (yp[i][0] + yp[i][1]) * gav[0] + xc[i][0]; s0[i] = pk2(c0[i] * gcv[0]); }
      st_sc1(rws, so, 0, s0);
      SCHED();
#pragma unroll
      for (int i = 0; i < 4; ++i) { c1[i] = (yp[i][2] + yp[i][3]) * gav[1] + xc[i][1]; s1[i] = pk2(c1[i] * gcv[1]); }
      st_sc1(rws, so, 256, s1);
      SCHED();
#pragma unroll
      for (int i = 0; i < 4; ++i) { o[i >> 1][2 * (i & 1)] = pk(c0[i][0], c1[i][0]); o[i >> 1][2 * (i & 1) + 1] = pk(c0[i][1], c1[i][1]); }
      st_sc1(rws, xo0, 0, o[0]);
      st_sc1(rws, xo1, 0, o[1]);
    }
    STAMP(15);
    WAIT_VM(0);
    STAMP(17);
    st_sc1(rws, fo, 0, u32x4{ta, ta, tb, tb});
    __builtin_amdgcn_s_setprio(0);
    const int kind = helper ? 3 : lw;
    int e0 = WS_G2 + rbk * 1024, e1 = WS_S12 + (cbk * 128 + (kind - 1) * 64) * 16, e3 = WS_MF + 4096 + db * 512;
    asm volatile("" : "+s"(e0), "+s"(e1), "+s"(e3));
    const bool mid = (kind + 1) & 2;
    const int eo = (mid ? e1 : kind ? e3 : e0) + (lane & (kind == 3 ? 31 : 63)) * 16;
    LAS char* ea = smem + LD_MISC + (mid ? 256 + kind * 256 : kind * 512) + lane * (mid ? 4 : 8);
    u32x4 ev;
    if (helper) {
      BARRIER(); BARRIER(); BARRIER(); BARRIER();
      ev = poll16(rws, eo, 0, ta, tb);
      *(LAS u32*)ea = ev[0]; *(LAS u32*)(ea + 4) = ev[2];
      w2_stage(0); w2_stage(1); w2_stage(2); w2_stage(3); w2_stage(4); w2_stage(5);
    } else {
      __builtin_amdgcn_s_sleep(F2_DELAY);
      poll16(rws, ((lane >> 3) * 32 + lw * 8 + (lane & 7)) * 64, WS_F2 + (xcd >> 1) * 16384, ta, tb);
      STAMP(19);
      x_stage(0); x_stage(1); WAIT_VM(4); BARRIER();
      x_stage(2); x_stage(3); WAIT_VM(8); BARRIER();
      x_stage(4); w1_stage(4); WAIT_VM(12); BARRIER();
#pragma unroll 1
      for (int s = 5; s < 8; ++s) { x_stage(s); w1_stage(s); WAIT_VM(16); BARRIER(); }
      w2_stage(0); w2_stage(1); WAIT_VM(16); BARRIER();
      asm volatile("buffer_load_dwordx4 %0, %1, %2, 0 offen sc1" : "=v"(ev) : "v"(eo), "s"(qws) : "memory");
      w2_stage(2); w2_stage(3); WAIT_VM(17); BARRIER();
      asm volatile("s_waitcnt vmcnt(8)" : "+v"(ev) :: "memory");
      if (__any(tagdiff(ev, ta, tb) != 0u)) ev = poll16(rws, eo, 0, ta, tb);
      *(LAS u32*)ea = ev[0]; *(LAS u32*)(ea + (mid ? 512 : 4)) = ev[2];
      w2_stage(4); w2_stage(5); BARRIER();
    }
    STAMP(22);
    w2_stage(6); w2_stage(7); w2_stage(8);
    {
      const int xs = WS_X1 + tbk * 65536 + (db * 32 + (lw >> 1) * 16) * 128 + (lw & 1) * 64;
      dma16_sc1(qws, LD_MISC + 2048 + lw * 2048, (lane >> 2) * 128 + (lane & 3) * 16, xs);
      dma16_sc1(qws, LD_MISC + 3072 + lw * 2048, (lane >> 2) * 128 + (lane & 3) * 16, xs + 32768);
    }
    WAIT_VM(0);
    STAMP(25);
    BARRIER();
    BARRIER();
#pragma unroll
    for (int i = 0; i < 20; ++i) {
      if (i == 8) BARRIER();
      *(LAS u32x4*)(smem + w2_unit(9 + i / 4) * UNIT + lw * 4096 + (i & 3) * 1024 + lane * 16) = wr[i];
    }
    BARRIER();
    if (b == 0 && tid == 256) *(u32x2*)(p.ws + WS_EPOCH) = u32x2{ta, tb};
    STAMP(26);
    STAMP_FLUSH(p.ws, 1);
    return;
  }

  ROLES();
  const int pch = w >> 1, pn = (w & 1) * 64 + lane, po = (d0 + pch) * NS + pn;
  u32 rar = LDNT(p.Are + po), rai = LDNT(p.Aim + po), rbr = LDNT(p.Bre + po), rbi = LDNT(p.Bim + po), rcr = LDNT(p.Cre + po), rci = LDNT(p.Cim + po);
  SCHED();
  SCAL_ISSUE(p.ldt + d0);
  {
    u32 cp0;
    {
      u64 pc; int r0, r1;
      asm volatile("s_getpc_b64 %0\n\ts_mov_b32 %1, fused_block@rel32@lo+4\n\ts_mov_b32 %2, fused_block_end@rel32@lo+12" : "=s"(pc), "=s"(r0), "=s"(r1));
      TOUCH(cp0, min(((w * 4 + (lane & 3)) * 32 + j) * 128, r1 - r0), pc + (long long)r0);
    }
    LAS char* flg = smem + LD_FLG;
    *(LAS u32x4*)(smem + LD_T + ((lane >> 1) & 7) * TCOPY + 4096 + (lane & 1) * 16) = u32x4{0u, 0u, 0u, 0u};
    *(LAS u32*)(smem + LD_KD + KD_ZERO * 4) = 0u;
    asm volatile("s_waitcnt lgkmcnt(0)" : "+s"(epoch), "+s"(aux));
    int ga[11], ka[4];
#pragma unroll
    for (int i = 0; i < 11; ++i) {
      const int l = 2047 - (tid * 8 + i);
      const int bits = (int)(pk((float)l, 0.f) & 0xffffu);
      const int idx = l < 128 ? l : bits - 0x4300 + 128;
      ga[i] = LD_KD + 4 * (l < 0 ? KD_ZERO : idx + (idx >> 3));
    }
    {
      const int e = lane & 15, g = lane >> 4;
      const u32 cfg = e < 8 ? (w == 0 ? 0x100004u : w == 1 ? 0x080004u : 0x022001u) : (w == 0 ? 0x100004u : w == 1 ? 0x044002u : 0x088003u);
      const int sh = cfg & 15, base = 256 * w + ((cfg >> 8) & 0xff) * (e >> sh) + (e & ((1 << sh) - 1));
#pragma unroll
      for (int r = 0; r < 4; ++r) { const int idx = base + (int)(cfg >> 16) * (4 * g + r); ka[r] = LD_KD + 4 * (idx + (idx >> 3)); PIN(ka[r]); }
    }
    STAMP(1);
    asm volatile("" : "+v"(rar), "+v"(rai), "+v"(rbr), "+v"(rbi), "+v"(rcr), "+v"(rci) : "v"(cp0));
    STAMP(2);
    SCHED();
    volatile LAS u32* P = (LAS u32*)(smem + LD_P) + pch * 64 * PROW + pn;
    const float dt = rb(__builtin_amdgcn_exp2f((pch ? bfhi(aux) : bflo(aux)) * LOG2E));
    const float a2 = (dt * bflo(rar)) * LOG2E, bq = (dt * bflo(rai)) * 0.15915494309189535f;
    const f32x2 z1 = cpow(a2, bq, 1.f), z8 = cpow(a2, bq, 8.f), z256 = cpow(a2, bq, 256.f);
    const f32x2 z16c = cmul(z8, z8) * f32x2{1.f, -1.f}, z512 = cmul(z256, z256), z768 = cmul(z512, z256);
    f32x2 u = cpow(a2, bq, 1024.f), q = {1.f, 0.f};
    f32x2 r = cmul(f32x2{bflo(rcr), -bflo(rci)}, f32x2{bflo(rbr), -bflo(rbi)} * dt);
#pragma unroll
    for (int e = 0; e < 16; ++e) {
      const f32x2 rn = cmul(r, z16c), qn = cmul(q, z1);
      P[e * PROW] = pk2(r);
      P[(16 + e) * PROW] = pk2(q);
      if (!(e & 1)) P[(32 + e / 2) * PROW] = pk2(cmul(q, z256));
      if (!(e & 3)) { P[(40 + e / 4) * PROW] = pk2(cmul(q, z512)); P[(44 + e / 4) * PROW] = pk2(cmul(q, z768)); }
      if ((e & 3) == 1) { P[(48 + e / 2) * PROW] = pk2(u); P[(49 + e / 2) * PROW] = pk2(cmul(u, z8)); }
      if ((e & 3) == 3) u = cmul(u, z256);
      r = rn; q = qn;
    }
    P[56 * PROW] = pk2(u);
    STAMP(3);
    quad_arrive(flg, w, 1u);
#pragma unroll
    for (int i = 0; i < 11; ++i) PIN(ga[i]);
    quad_wait(flg, 1u);
    STAMP(4);
    if (w < 3) {
      LAS const u32* fb = (LAS const u32*)(smem + LD_P) + (lane & 15) * PROW + ((lane >> 4) & 1) * 64 + (lane >> 5) * 32;
      u32x4 fa[16], fc[16];
      f32x4 acc[2] = {{0.f, 0.f, 0.f, 0.f}, {0.f, 0.f, 0.f, 0.f}};
#pragma unroll
      for (int ks = 0; ks < 22; ++ks) {
        if (ks < 16) {
          const int o = (ks & 1) * 64 * PROW + (ks >> 1) * 4;
          fa[ks] = *(LAS const u32x4*)(fb + o); fc[ks] = *(LAS const u32x4*)(fb + (w + 1) * 16 * PROW + o);
        }
        SCHED();
        if (ks >= 6) acc[ks & 1] = mfma16(fa[ks - 6], fc[ks - 6], acc[ks & 1]);
        SCHED();
      }
#pragma unroll
      for (int r4 = 0; r4 < 4; ++r4) *(LAS u32*)(smem + ka[r4]) = pk(acc[0][r4], acc[1][r4]);
    }
    STAMP(5);
    quad_arrive(flg, w, 2u);
    quad_wait(flg, 2u);
    STAMP(6);
    u32 e[11];
#pragma unroll
    for (int i = 0; i < 11; ++i) e[i] = *(LAS const u32*)(smem + ga[i]);
#pragma unroll
    for (int c = 0; c < 2; ++c)
#pragma unroll
      for (int s = 0; s < 4; ++s) {
        u32x4 o;
#pragma unroll
        for (int z = 0; z < 4; ++z) o[z] = __builtin_amdgcn_perm(e[s + 2 * z + 1], e[s + 2 * z], c ? 0x07060302u : 0x05040100u);
        *(LAS u32x4*)(smem + LD_T + (c * 4 + s) * TCOPY + tid * 16) = o;
      }
    quad_arrive(flg, w, 3u);
  }
  epoch += 0x9E3779B97F4A7C15ull;
  const u32 ta = (u32)epoch, tb = (u32)(epoch >> 32);
  {
    const int r = lane & 15, g = lane >> 4, ch = w >> 1, half = w & 1;
    LAS const char* Tb = smem + LD_T + (ch * 4 + ((3 - r) & 3)) * TCOPY + 2 * (2028 - 4 * (r >> 2) + 8 * g - 128 * half) - 7 * 512 - 192;
    LAS const char* Ub = smem + LD_U + ch * 4608 + 2 * (256 + 16 * r - 16 + 8 * g - 128 * half) - 192;
    LAS f32x4* red = (LAS f32x4*)(smem + (ch ? LD_RED1 : LD_RED0)) + half * 512 + (lane ^ ((lane >> 4) << 2));
    PIN(Ub); PIN(red);
    f32x4 acc[8];
#pragma unroll
    for (int n = 0; n < 8; ++n) acc[n] = f32x4{0.f, 0.f, 0.f, 0.f};
    u32x4 uf[2][8], tf[4][8];
    quad_wait(smem + LD_FLG, 3u);
#define LD_TH(e, a, hh) do { const u32x2 l_ = *(volatile LAS const u32x2*)(Tb + 64 * (3 - (e)) + 512 * (7 - (a)) + 8 * (hh)); tf[e][a][2 * (hh)] = l_[0]; tf[e][a][2 * (hh) + 1] = l_[1]; } while (0)
#pragma unroll
    for (int e = 0; e < TF_PRE; ++e)
#pragma unroll
      for (int a = 0; a < 8; ++a) { LD_TH(e, a, 0); LD_TH(e, a, 1); }
#define LD_UF(n, e) uf[(e) & 1][n] = *(LAS const u32x4*)(Ub + 64 * (3 - (e)) + 512 * (n))
#define CM(e, a, m) do { SCHED(); acc[(a) + (m)] = mfma16(tf[e][a], uf[(e) & 1][m], acc[(a) + (m)]); SCHED(); } while (0)
    STAMP(10);
    BARRIER();
    STAMP(11);
#pragma unroll
    for (int n = 0; n < 8; ++n) LD_UF(n, 0);
#pragma unroll
    for (int e = 0; e < 3; ++e) {
      int cnt = 0;
#pragma unroll
      for (int m = 0; m < 8; ++m)
#pragma unroll
        for (int a = 0; a + m < 8; ++a) {
          CM(e, a, m);
          if (cnt >= 4 && cnt < 12) LD_UF(cnt - 4, e + 1);
          if (cnt >= 12 && cnt < 28 && e + TF_PRE < 4) LD_TH(e + TF_PRE, (cnt - 12) >> 1, (cnt - 12) & 1);
          ++cnt;
        }
    }
#pragma unroll
    for (int n = 0; n < 8; ++n)
#pragma unroll
      for (int a = 0; a <= n; ++a) { CM(3, a, n - a); if (a == 1) red[(n - 1) * 64] = acc[n - 1]; }
    STAMP(12);
    red[7 * 64] = acc[7];
    BARRIER();
    STAMP(14);
  }
  if (helper) {
    const int hb = xcd * 4 + j;
    u32x4 v[16];
    const int rg = tid >> 3, cl = tid & 7;
#pragma unroll
    for (int i = 0; i < 16; ++i) v[i] = *(const u32x4*)(p.w1 + (size_t)(i * 32 + rg) * F + (hb < 28 ? hb : 0) * 64 + cl * 8);
    LAS float* rd = (LAS float*)(smem + 4096);
    { const u32x4 m = poll16(rws, tid * 16, WS_MF, ta, tb); *(LAS u32x2*)(smem + tid * 8) = u32x2{m[0], m[2]}; }
    BARRIER();
    float a[16];
#pragma unroll
    for (int e = 0; e < 16; ++e) a[e] = 0.f;
#pragma unroll
    for (int i = 0; i < 16; ++i) {
      const u32 gs = *(LAS const u32*)(smem + (i * 32 + rg) * 4);
      const u32 gl = gs & 0xffffu, gh = gs << 16, sl = gs >> 16, sh = gs & 0xffff0000u;
#pragma unroll
      for (int z = 0; z < 4; ++z) {
        a[4 * z] = dot2(v[i][z], gl, a[4 * z]); a[4 * z + 1] = dot2(v[i][z], sl, a[4 * z + 1]);
        a[4 * z + 2] = dot2(v[i][z], gh, a[4 * z + 2]); a[4 * z + 3] = dot2(v[i][z], sh, a[4 * z + 3]);
      }
    }
    fold_cols(a, rd + w * 128, lane);
    STAMP(18);
    poll16(rws, tid * 64, WS_F2 + (xcd >> 1) * 16384, ta, tb);
    BARRIER();
    STAMP(19);
#pragma unroll
    for (int i = 0; i < 16; ++i) v[i] = ld_sc1(rws, tid * 16, WS_X1 + hb * 65536 + i * 4096);
    if (w == 0 && hb < 28) {
      const f32x2 s = sum4(rd + lane * 2, 128);
      st_sc1(rws, lane * 16, WS_S12 + hb * 1024, u32x4{__float_as_uint(s[0]), ta, __float_as_uint(s[1] * -LOG2E), tb});
    }
#pragma unroll
    for (int e = 0; e < 16; ++e) a[e] = 0.f;
#pragma unroll
    for (int i = 0; i < 16; ++i)
#pragma unroll
      for (int z = 0; z < 4; ++z) {
        const int o = (i >> 3) * 8 + 2 * z;
        a[o] = dot2(v[i][z], 0x3f803f80u, a[o]); a[o + 1] = dot2(v[i][z], v[i][z], a[o + 1]);
      }
    STAMP(20);
    fold_cols(a, rd + 512 + w * 128, lane);
    BARRIER();
    if (w == 0) {
      const f32x2 s = sum4(rd + 512 + lane * 2, 128);
      const u32 st = ln_stat(s[0], s[1]);
      const u32 st2 = __builtin_amdgcn_mov_dpp(st, 0xB1, 0xf, 0xf, true);
      if (!(lane & 1)) st_sc1(rws, lane * 8, WS_G2 + hb * 512, u32x4{st, ta, st2, tb});
    }
    BARRIER();
    STAMP(22);
  } else {
    const int wt = w & 1, wf = w >> 1;
    const int h = lane >> 5, blk = (lane >> 4) & 1, q = (lane & 15) >> 2, pp = lane & 3;
    int ax[2][2], aw[2][2];
#pragma unroll
    for (int c = 0; c < 2; ++c)
#pragma unroll
      for (int t = 0; t < 2; ++t) {
        const int base = 256 * (8 * h + 4 * t + q) + 16 * ((2 * blk + (pp >> 1)) ^ (2 * h + t)) + 8 * (pp & 1);
        ax[c][t] = LD_XR + base + 64 * ((2 * wt + c) ^ q);
        aw[c][t] = LD_WR + base + 64 * ((2 * wf + c) ^ q);
      }
    f32x16 acc[2][2];
#pragma unroll
    for (int a = 0; a < 2; ++a)
#pragma unroll
      for (int c = 0; c < 2; ++c)
#pragma unroll
        for (int r = 0; r < 16; ++r) acc[a][c][r] = 0.f;
    u32x4 xf[2][2], wfr[2][2];
    u32 st[2];
    int ho = WS_H + ((rbk * 4 + wt * 2) * 112 + cbk * 8 + wf * 4) * 1024 + lane * 16, ho1 = ho + 112 * 1024;
    int fo = only(lane < 8, WS_F3 + ((rbk * 4 + w) * 16 + cbk) * 128 + lane * 16);
    PIN(ho); PIN(ho1); PIN(fo);
#define RD_X(i, c) xf[(i) & 1][c] = tr_frag(smem + ax[c][0] + ((i) & 15) * 4096, smem + ax[c][1] + ((i) & 15) * 4096)
#define RD_W(i, c) wfr[(i) & 1][c] = tr_frag(smem + aw[c][0] + ((i) & 15) * 4096, smem + aw[c][1] + ((i) & 15) * 4096)
#define MM(i, a, c) do { SCHED(); acc[a][c] = mfma32(wfr[(i) & 1][a], xf[(i) & 1][c], acc[a][c]); SCHED(); } while (0)
    BARRIER();
    STAMP(19);
    RD_W(0, 0); RD_X(0, 0); RD_X(0, 1); RD_W(0, 1);
#pragma unroll
    for (int i = 0; i < 32; ++i) {
      if ((i & 3) != 3) {
        MM(i, 0, 0); RD_W(i + 1, 0); MM(i, 0, 1); RD_X(i + 1, 0); MM(i, 1, 0); RD_X(i + 1, 1); MM(i, 1, 1); RD_W(i + 1, 1);
      } else {
        MM(i, 0, 0); MM(i, 0, 1);
        BARRIER();
        if (i == 31) { st[0] = *(LAS const u32*)(smem + LD_MISC + (wt * 64 + (lane & 31)) * 4); st[1] = *(LAS const u32*)(smem + LD_MISC + (wt * 64 + 32 + (lane & 31)) * 4); }
        MM(i, 1, 0);
        if (i < 31) { RD_W(i + 1, 0); RD_X(i + 1, 0); }
        MM(i, 1, 1);
        if (i < 31) { RD_X(i + 1, 1); RD_W(i + 1, 1); }
      }
    }
    STAMP(21);
    float rstd[2], mr[2];
#pragma unroll
    for (int c = 0; c < 2; ++c) {
      rstd[c] = __builtin_amdgcn_rcpf(bfhi(st[c])) * -LOG2E;
      mr[c] = -bflo(st[c]) * rstd[c];
    }
    STAMP(22);
    LAS const float* s12 = (LAS const float*)(smem + LD_MISC + 512) + wf * 64 + 4 * h;
#pragma unroll
    for (int a = 0; a < 2; ++a)
#pragma unroll
      for (int sp = 0; sp < 2; ++sp) {
        f32x4 s1[2], s2[2];
#pragma unroll
        for (int z = 0; z < 2; ++z) { s1[z] = *(LAS const f32x4*)(s12 + a * 32 + sp * 16 + z * 8); s2[z] = *(LAS const f32x4*)(s12 + 128 + a * 32 + sp * 16 + z * 8); }
#pragma unroll
        for (int c = 0; c < 2; ++c) {
          u32x4 o;
#pragma unroll
          for (int e = 0; e < 4; ++e) {
            const int z = e >> 1, k = (e & 1) * 2;
            const f32x2 av = {acc[a][c][sp * 8 + 2 * e], acc[a][c][sp * 8 + 2 * e + 1]};
            const f32x2 cc = f32x2{s1[z][k], s1[z][k + 1]} * mr[c] + f32x2{s2[z][k], s2[z][k + 1]};
            const f32x2 ar = av * rstd[c] + cc;
            const f32x2 dn = f32x2{__builtin_amdgcn_exp2f(ar[0]), __builtin_amdgcn_exp2f(ar[1])} + 1.f;
            o[e] = pk2(ar * f32x2{__builtin_amdgcn_rcpf(dn[0]), __builtin_amdgcn_rcpf(dn[1])});
          }
          st_sc1(rws, c ? ho1 : ho, (a * 2 + sp) * 1024, o);
        }
      }
    STAMP(23);
    WAIT_VM(0);
    STAMP(24);
    st_sc1(rws, fo, 0, u32x4{ta, ta, tb, tb});
  }

  {
    const int wt = w & 1, kh = w >> 1, h = lane >> 5, tl = lane & 31;
    const int blk = (lane >> 4) & 1, q = (lane & 15) >> 2, pp = lane & 3;
    LAS char* aw[3][2];
#pragma unroll
    for (int c = 0; c < 6; ++c) {
      aw[c >> 1][c & 1] = smem + (c >> 1) * 65536 + kh * 8192 + 128 * (4 * h + q) + 64 * ((c & 1) ^ kh ^ (q >> 1)) + 32 * blk + 8 * pp;
      asm volatile("" : "+v"(aw[c >> 1][c & 1]));
    }
    f32x16 acc[2];
#pragma unroll
    for (int r = 0; r < 16; ++r) { acc[0][r] = 0.f; acc[1][r] = 0.f; }
    STAMP(25);
    BARRIER();
    constexpr int NW = G2_NW;
    u32x4 hf[NW], wf[3][2];
#define RD_W2C(i, c) do { LAS char* a_ = aw[w2_unit((i) >> 2) >> 2][c] + (w2_unit((i) >> 2) & 3) * UNIT + ((i) & 3) * 2048; wf[(i) % 3][c] = tr_frag(a_, a_ + 1024); } while (0)
#define RD_W2(i) do { RD_W2C(i, 0); RD_W2C(i, 1); } while (0)
    RD_W2(0); RD_W2(1);
    const int sh = WS_H + ((tbk * 2 + wt) * 112 + 56 * kh) * 1024;
    poll16(rws, ((lane & 1) * 32 + min(lane >> 1, 6)) * 128, WS_F3 + ((tbk >> 1) * 4 + (tbk & 1)) * 2048 + kh * 896, ta, tb);
    STAMP(26);
#pragma unroll
    for (int i = 0; i < NW; ++i) hf[i] = ld_sc1(rws, lane * 16 + (i & 3) * 1024, sh + (i >> 2) * 4096);
    SCHED();
    u32 xr[8]; f32x2 gb[8];
#pragma unroll
    for (int i = 0; i < 8; ++i) {
      const int pr = 4 * (i >> 1) + 2 * h + (i & 1);
      xr[i] = *(LAS const u32*)(smem + LD_MISC + 2048 + w * 2048 + ((tl >> 2) & 1) * 1024 + pr * 64 + (tl >> 3) * 16 + (tl & 3) * 4);
      gb[i] = *(LAS const f32x2*)(smem + LD_MISC + 1536 + (kh * 16 + pr) * 8);
    }
    SCHED();
#pragma unroll
    for (int i = 0; i < 56; ++i) {
      SCHED();
      acc[0] = mfma32(wf[i % 3][0], hf[i % NW], acc[0]);
      SCHED();
      if (i == 0) STAMP(29);
      if (i == 18) STAMP(30);
      if (i == 42) STAMP(31);
      if (i == 6 || i == 18 || i == 42) BARRIER();
      if (i + 2 < 56) RD_W2C(i + 2, 0);
      if (i > 0 && i - 1 + NW < 56) hf[(i - 1) % NW] = ld_sc1(rws, lane * 16 + ((i - 1 + NW) & 3) * 1024, sh + ((i - 1 + NW) >> 2) * 4096);
      SCHED();
      acc[1] = mfma32(wf[i % 3][1], hf[i % NW], acc[1]);
      SCHED();
      if (i + 2 < 56) RD_W2C(i + 2, 1);
    }
    STAMP(27);
    LAS f32x4* ex = (LAS f32x4*)(smem + w2_unit(5) * UNIT) + lane;
#pragma unroll
    for (int qq = 0; qq < 4; ++qq) ex[(w * 4 + qq) * 64] = f32x4{acc[1][4 * qq], acc[1][4 * qq + 1], acc[1][4 * qq + 2], acc[1][4 * qq + 3]};
    BARRIER();
    LAS char* ot = smem + w2_unit(6) * UNIT;
    f32x4 oth[4];
#pragma unroll
    for (int qq = 0; qq < 4; ++qq) oth[qq] = ex[((w ^ 2) * 4 + qq) * 64];
    SCHED();
#pragma unroll
    for (int qq = 0; qq < 4; ++qq) {
      const f32x4 o4 = oth[qq];
      u32x2 o;
#pragma unroll
      for (int z = 0; z < 2; ++z)
        o[z] = pk2((f32x2{acc[0][4 * qq + 2 * z], acc[0][4 * qq + 2 * z + 1]} + f32x2{o4[2 * z], o4[2 * z + 1]}) * gb[2 * qq + z] + up(xr[2 * qq + z]));
      *(LAS u32x2*)(ot + (wt * 32 + tl) * 144 + kh * 64 + qq * 16 + h * 8) = o;
    }
    BARRIER();
#pragma unroll
    for (int i = 0; i < 2; ++i) {
      const int row = (tid >> 3) + 32 * i, r = tbk * 64 + row;
      const int t = (r & ~511) + 128 * ((r >> 1) & 3) + 2 * ((r >> 3) & 63) + (r & 1);
      *(u32x4*)(p.out + (size_t)t * D + db * 64 + (tid & 7) * 8) = *(LAS const u32x4*)(ot + row * 144 + (tid & 7) * 16);
    }
    STAMP(28);
    STAMP_FLUSH(p.ws, 0);
  }
}

extern "C" __global__ void fused_block_end() {}

extern "C" __global__ void __launch_bounds__(1024) probe_k(char* ws) {
  __shared__ unsigned mn, mx; __shared__ unsigned long long sum; __shared__ unsigned cnt;
  if (threadIdx.x == 0) { mn = 0xffffffffu; mx = 0u; sum = 0ull; cnt = 0u; }
  __syncthreads();
  const unsigned long long* p = (const unsigned long long*)(ws + ST_OFF);
  const int b = threadIdx.x >> 2, w = (threadIdx.x & 3) + 0 * 4;
  const size_t base = (((size_t)0 * ST_MAXB + b) * ST_MAXW + w) * ST_SLOTS * 2;
  const unsigned t0 = (unsigned)p[base], ts = (unsigned)p[base + 2 * 28];
  atomicMin(&mn, t0);
  if (ts) { atomicMax(&mx, ts); atomicAdd(&sum, (unsigned long long)ts); atomicAdd(&cnt, 1u); }
  __syncthreads();
  unsigned d = 0u;
  if (d > 10000u) d = 10000u;
  const unsigned long long a = __builtin_amdgcn_s_memrealtime();
  while (__builtin_amdgcn_s_memrealtime() - a < d + 300u) {}
}
extern "C" void kernel_launch(void* const* d_in, const int* in_sizes, int n_in, void* d_out, int out_size, void* d_ws, size_t ws_size,
                              hipStream_t stream) {
  Params p;
  p.x = (const u16*)d_in[0]; p.Are = (const u16*)d_in[1]; p.Aim = (const u16*)d_in[2]; p.Bre = (const u16*)d_in[3];
  p.Bim = (const u16*)d_in[4]; p.Cre = (const u16*)d_in[5]; p.Cim = (const u16*)d_in[6]; p.ldt = (const u16*)d_in[7];
  p.dskip = (const u16*)d_in[8]; p.w1 = (const u16*)d_in[10]; p.w2 = (const u16*)d_in[11]; p.c = (const u16*)d_in[12];
  p.wmod = (const u16*)d_in[13]; p.out = (u16*)d_out; p.ws = (char*)d_ws;
  (void)hipFuncSetAttribute((const void*)fused_block, hipFuncAttributeMaxDynamicSharedMemorySize, LDS_BYTES + ST_LDS_EXTRA);
  fused_block<<<256, 512, LDS_BYTES + ST_LDS_EXTRA, stream>>>(p);
  probe_k<<<1, 1024, 0, stream>>>(p.ws);
}
```

```cpp
#define XG_DELAY 20
#include <hip/hip_runtime.h>
#include <hip/hip_bf16.h>

#define STAMP_DECL
#define STAMP(s) __builtin_amdgcn_sched_barrier(0)
#define STAMP_FLUSH(ws, k)
#define ST_LDS_EXTRA 0
#define ST_RANGES {{0, 32, 256, "compute waves"}, {1, 32, 256, "loader waves"}, {0, 0, 32, "helper WGs, waves 0-3"}, {1, 0, 32, "helper WGs, waves 4-7"}}

typedef unsigned short u16;
typedef unsigned int u32;
typedef unsigned long long u64;
typedef u32 u32x2 __attribute__((ext_vector_type(2)));
typedef u32 u32x4 __attribute__((ext_vector_type(4)));
typedef float f32x2 __attribute__((ext_vector_type(2)));
typedef float f32x4 __attribute__((ext_vector_type(4)));
typedef float f32x16 __attribute__((ext_vector_type(16)));
typedef short s16x4 __attribute__((ext_vector_type(4)));
typedef __bf16 bf16x2 __attribute__((ext_vector_type(2)));
typedef __bf16 bf16x8 __attribute__((ext_vector_type(8)));
typedef __amdgpu_buffer_rsrc_t rsrc_t;
#define GAS __attribute__((address_space(1)))
#define LAS __attribute__((address_space(3)))
#define CAS __attribute__((address_space(4)))

constexpr int L = 2048, D = 512, NS = 128, F = 1792;

constexpr int WS_EPOCH = 0;
constexpr int WS_SG = 0x1000;
constexpr int WS_MG = 0x80000;
constexpr int WS_F2 = 0x40000;
constexpr int WS_F3 = 0x50000;
constexpr int WS_G2 = 0x70000;
constexpr int WS_S12 = 0x74000;
constexpr int WS_MF = 0x7c000;
constexpr int WS_XG = 2 << 20;
constexpr int WS_X1 = 5 << 20;
constexpr int WS_X1S = 7 << 20;
constexpr int WS_H = 9 << 20;
constexpr int WS_END = 16 << 20;
constexpr int OOB = 0x7ff00000;

constexpr int UNIT = 16384;
constexpr int LD_XR = 0, LD_WR = 4 * UNIT, LD_MISC = 9 * UNIT, LD_FLG = LD_MISC + 10240, LD_MV = LD_FLG + 32, LDS_BYTES = LD_MV + 64;
constexpr int TCOPY = 4160, PROW = 132;
constexpr int LD_T = 0, LD_U = 8 * TCOPY, LD_KD = LD_U + 2 * 4608, LD_P = LD_WR, LD_RED0 = 3 * UNIT, LD_RED1 = 8 * UNIT;
constexpr int KD_ZERO = 900, LD_XT = LD_RED0;
static_assert(LD_KD + 904 * 4 <= LD_RED0, "LDS map");
__host__ __device__ constexpr int w2_phys(int u) { return (u + 5) % 14; }
__host__ __device__ constexpr int w2_unit(int u) { return u % 9 == 8 ? 8 : (u % 9) / 2 + 4 * (u % 9 & 1); }
#define G2_NW 14
#define G2_N0 14
__host__ __device__ constexpr int h_next(int i) { int a = G2_N0 + 2 * i, b = i > 0 ? i - 1 + G2_NW : G2_NW; a = a < b ? a : b; return a < 56 ? a : 56; }
#define TF_PRE 4
#define H_SLEEP 80
#define S1_SLEEP 2
#define F2_DELAY 16
#define POLL_SLEEP 2
#define LDNT(p) __builtin_nontemporal_load(p)
#define AUX_NT 2
#define WF ""
#define AUX_W 0
constexpr int SPIN_CAP = 1 << 15;
constexpr float LOG2E = 1.44269504088896341f;

__device__ __forceinline__ float bflo(u32 v) { return __uint_as_float(v << 16); }
__device__ __forceinline__ float bfhi(u32 v) { return __uint_as_float(v & 0xffff0000u); }
__device__ __forceinline__ u32 pk(float lo, float hi) {
  f32x2 v = {lo, hi};
  bf16x2 r = __builtin_convertvector(v, bf16x2);
  return __builtin_bit_cast(u32, r);
}
__device__ __forceinline__ float rb(float f) { return bflo(pk(f, f)); }
__device__ __forceinline__ float dot2(u32 a, u32 b, float c) {
  return __builtin_amdgcn_fdot2_f32_bf16(__builtin_bit_cast(bf16x2, a), __builtin_bit_cast(bf16x2, b), c, false);
}
__device__ __forceinline__ f32x4 mfma16(u32x4 a, u32x4 b, f32x4 c) {
  return __builtin_amdgcn_mfma_f32_16x16x32_bf16(__builtin_bit_cast(bf16x8, a), __builtin_bit_cast(bf16x8, b), c, 0, 0, 0);
}
__device__ __forceinline__ f32x16 mfma32(u32x4 a, u32x4 b, f32x16 c) {
  return __builtin_amdgcn_mfma_f32_32x32x16_bf16(__builtin_bit_cast(bf16x8, a), __builtin_bit_cast(bf16x8, b), c, 0, 0, 0);
}
template <int CTRL> __device__ __forceinline__ float dpp(float x) {
  return __builtin_bit_cast(float, __builtin_amdgcn_mov_dpp(__builtin_bit_cast(int, x), CTRL, 0xf, 0xf, true));
}
__device__ __forceinline__ float row_sum(float x) {
  x += dpp<0xB1>(x); x += dpp<0x4E>(x); x += dpp<0x124>(x); x += dpp<0x128>(x);
  return x;
}
__device__ __forceinline__ float fold32(float a, float b) {
  auto s = __builtin_amdgcn_permlane32_swap(__float_as_uint(a), __float_as_uint(b), false, false);
  return __uint_as_float(s[0]) + __uint_as_float(s[1]);
}
__device__ __forceinline__ float fold16(float a, float b) {
  auto s = __builtin_amdgcn_permlane16_swap(__float_as_uint(a), __float_as_uint(b), false, false);
  return __uint_as_float(s[0]) + __uint_as_float(s[1]);
}
__device__ __forceinline__ u32x4 mkrs(const void* p, u32 bytes) {
  const u64 a = (u64)p;
  return u32x4{(u32)a, (u32)(a >> 32) & 0xffffu, bytes, 0x00020000u};
}
__device__ __forceinline__ void dma4(u32x4 rs, int lds, int voff, int soff) {
  asm volatile("s_mov_b32 m0, %0\n\ts_nop 0\n\tbuffer_load_dword %1, %2, %3 offen lds" :: "s"(lds), "v"(voff), "s"(rs), "s"(soff) : "memory");
}
__device__ __forceinline__ void dma16_sc1(u32x4 rs, int lds, int voff, int soff) {
  asm volatile("s_mov_b32 m0, %0\n\ts_nop 0\n\tbuffer_load_dwordx4 %1, %2, %3 offen sc1 lds" :: "s"(lds), "v"(voff), "s"(rs), "s"(soff) : "memory");
}
#define DMA4_TEXT(F) "s_mov_b32 m0, %0\n\ts_nop 0\n\tbuffer_load_dwordx4 %1, %5, %6 offen" F " lds\n\tbuffer_load_dwordx4 %2, %5, %6 offen offset:1024" F " lds\n\t" \
                     "buffer_load_dwordx4 %3, %5, %6 offen offset:2048" F " lds\n\tbuffer_load_dwordx4 %4, %5, %6 offen offset:3072" F " lds"
__device__ __forceinline__ void dma4k(u32x4 rs, int lds, const int (&v)[4], int soff) {
  asm volatile(DMA4_TEXT(WF) :: "s"(lds), "v"(v[0]), "v"(v[1]), "v"(v[2]), "v"(v[3]), "s"(rs), "s"(soff) : "memory");
}
__device__ __forceinline__ void dma4k_sc1(u32x4 rs, int lds, const int (&v)[4], int soff) {
  asm volatile(DMA4_TEXT(" sc1") :: "s"(lds), "v"(v[0]), "v"(v[1]), "v"(v[2]), "v"(v[3]), "s"(rs), "s"(soff) : "memory");
}
__device__ __forceinline__ u32x4 ld_sc1(rsrc_t rs, int voff, int soff) { return __builtin_amdgcn_raw_buffer_load_b128(rs, voff, soff, 16); }
__device__ __forceinline__ void st_sc1(rsrc_t rs, int voff, int soff, u32x4 v) { __builtin_amdgcn_raw_buffer_store_b128(v, rs, voff + soff, 0, 16); }
__device__ __forceinline__ u32x4 tr_frag(LAS char* a0, LAS char* a1) {
  const s16x4 lo = __builtin_amdgcn_ds_read_tr16_b64_v4i16((LAS s16x4*)a0), hi = __builtin_amdgcn_ds_read_tr16_b64_v4i16((LAS s16x4*)a1);
  const u32x2 l = __builtin_bit_cast(u32x2, lo), h = __builtin_bit_cast(u32x2, hi);
  return u32x4{l[0], l[1], h[0], h[1]};
}
#define SCHED() __builtin_amdgcn_sched_barrier(0)
#define FENCE() asm volatile("" ::: "memory")
#define WAIT_VM(n) asm volatile("s_waitcnt vmcnt(" #n ")" ::: "memory")
#define BARRIER() do { asm volatile("s_waitcnt lgkmcnt(0)" ::: "memory"); __builtin_amdgcn_s_barrier(); FENCE(); } while (0)

__device__ __forceinline__ f32x2 cmul(f32x2 a, f32x2 b) {
  const f32x2 t = f32x2{a[0], a[0]} * b;
  return __builtin_elementwise_fma(f32x2{a[1], a[1]}, f32x2{-b[1], b[0]}, t);
}
__device__ __forceinline__ f32x2 cpow(float a2, float bq, float l) {
  const float m = __builtin_amdgcn_exp2f(a2 * l);
  const float ph = __builtin_amdgcn_fractf(bq * l);
  return f32x2{__builtin_amdgcn_cosf(ph), __builtin_amdgcn_sinf(ph)} * m;
}
__device__ __forceinline__ u32 pk2(f32x2 a) { return pk(a[0], a[1]); }
__device__ __forceinline__ f32x2 up(u32 v) { return f32x2{bflo(v), bfhi(v)}; }
__device__ __forceinline__ u32 ln_stat(float sum, float sq) {
  const float m = sum * (1.f / D);
  const float var = fmaxf(sq * (1.f / D) - m * m, 0.f);
  return pk(m, __builtin_amdgcn_sqrtf(rb(rb(var) + 1e-5f)));
}
__device__ __forceinline__ u32 tagdiff(u32x4 v, u32 ta, u32 tb) { return (v[1] ^ ta) | (v[3] ^ tb); }
__device__ __forceinline__ u32x4 poll16(rsrc_t rs, int voff, int soff, u32 ta, u32 tb) {
  u32x4 v;
  int spins = 0;
  for (;;) {
    FENCE();
    v = ld_sc1(rs, voff, soff);
    if (tagdiff(v, ta, tb) == 0u || ++spins >= SPIN_CAP) break;
    if (POLL_SLEEP) __builtin_amdgcn_s_sleep(POLL_SLEEP);
  }
  FENCE();
  return v;
}
__device__ __forceinline__ void fold_cols(const float (&v)[16], LAS float* dst, int lane) {
  float u[8]; f32x4 t;
#pragma unroll
  for (int k = 0; k < 8; ++k) u[k] = fold32(v[k], v[k + 8]);
#pragma unroll
  for (int k = 0; k < 4; ++k) { const float a = fold16(u[k], u[k + 4]); t[k] = a + dpp<0x128>(a); }
  if (!(lane & 8)) *(LAS f32x4*)(dst + (lane & 7) * 16 + (lane >> 4) * 4) = t;
}
__device__ __forceinline__ f32x2 sum4(LAS const float* src, int stride) {
  return (*(LAS const f32x2*)src + *(LAS const f32x2*)(src + stride)) + (*(LAS const f32x2*)(src + 2 * stride) + *(LAS const f32x2*)(src + 3 * stride));
}
__device__ __forceinline__ u32 silu_bf(u32 c) {
  const float f = bflo(c), s = f * __builtin_amdgcn_rcpf(1.f + __builtin_amdgcn_exp2f(f * -LOG2E));
  return pk(s, s);
}
template <int CTRL> __device__ __forceinline__ void gv_row(u32x4 v, u32 sp, float (&acc)[8]) {
  const u32 sb = __builtin_amdgcn_mov_dpp(sp, CTRL, 0xf, 0xf, true), al = sb & 0xffffu, ah = sb & 0xffff0000u;
#pragma unroll
  for (int q = 0; q < 4; ++q) { acc[2 * q] = dot2(v[q], al, acc[2 * q]); acc[2 * q + 1] = dot2(v[q], ah, acc[2 * q + 1]); }
}
__device__ __forceinline__ f32x2 gv_fold(const float (&acc)[8]) {
  const float m0 = fold16(fold32(acc[0], acc[4]), fold32(acc[2], acc[6])), m1 = fold16(fold32(acc[1], acc[5]), fold32(acc[3], acc[7]));
  return f32x2{m0 + dpp<0x128>(m0), m1 + dpp<0x128>(m1)};
}
#define TOUCH(dst, voff, base) asm volatile("global_load_dword %0, %1, %2" : "=v"(dst) : "v"(voff), "s"(base))

__device__ __forceinline__ void quad_arrive(LAS char* f, int i, u32 n) {
  asm volatile("s_waitcnt lgkmcnt(0)" ::: "memory");
  *(volatile LAS u32*)(f + i * 4) = n;
}
__device__ __forceinline__ void quad_wait(LAS char* f, u32 n) {
  for (;;) {
    const u32x4 v = *(volatile LAS u32x4*)f;
    if (__builtin_amdgcn_readfirstlane(min(min(v[0], v[1]), min(v[2], v[3]))) >= n) break;
    __builtin_amdgcn_s_sleep(1);
  }
  FENCE();
}
__device__ __forceinline__ int only(bool c, int off) { return off | (c ? 0 : OOB); }
#define PIN(x) asm volatile("" : "+v"(x))

struct Params {
  const u16 *x, *Are, *Aim, *Bre, *Bim, *Cre, *Cim, *ldt, *dskip, *w1, *w2, *c, *wmod;
  u16* out;
  char* ws;
};

extern "C" __global__ void __launch_bounds__(512, 2) fused_block(Params p) {
  extern __shared__ __attribute__((aligned(16))) char smem_[];
  LAS char* smem = (LAS char*)smem_;
  const int tid = threadIdx.x, lane = tid & 63, w = __builtin_amdgcn_readfirstlane(tid >> 6), lw = w & 3;
  STAMP_DECL
  STAMP(0);
  *(LAS u32*)(smem + LD_FLG + w * 4) = 0u;
  BARRIER();
  asm volatile("" :: "s"(p.x), "s"(p.Are), "s"(p.Aim), "s"(p.Bre), "s"(p.Bim), "s"(p.Cre), "s"(p.Cim), "s"(p.ldt), "s"(p.dskip), "s"(p.w1), "s"(p.w2), "s"(p.c), "s"(p.wmod),
               "s"(p.out), "s"(p.ws));
#define SCAL_ISSUE(q) u64 epoch; u32 aux; \
  asm volatile("s_load_dwordx2 %0, %2, 0x0\n\ts_load_dword %1, %3, 0x0" : "=&s"(epoch), "=&s"(aux) : "s"(p.ws + WS_EPOCH), "s"(q)); SCHED()
#define SCAL_WAIT() asm volatile("s_waitcnt lgkmcnt(0)" : "+s"(epoch), "+s"(aux)); \
  epoch += 0x9E3779B97F4A7C15ull; ta = (u32)epoch; tb = (u32)(epoch >> 32); dsk = aux
#define ROLES() int b = blockIdx.x; asm volatile("" : "+s"(b)); \
  const int xcd = b & 7, j = b >> 3, pair = xcd * 32 + j, d0 = pair * 2; \
  const bool helper = j < 4; \
  const int rbk = (xcd >> 1) * 4 + (j & 3), cbk = (xcd & 1) * 7 + ((j - 4) >> 2), tbk = xcd * 4 + (j & 3), db = ((j >> 2) + 3 - (int)((u64)p.w2 >> 7)) & 7; \
  const rsrc_t rws = __builtin_amdgcn_make_buffer_rsrc(p.ws, 0, WS_END, 0x00020000)

  if (__builtin_expect(w >= 4, 1)) {
    LAS char* flg = smem + LD_FLG + 16;
    u32x4 sg[4];
    u32x2 xg[4];
    ROLES();
    const int go = lw * 4096 + lane * 16;
    u32 ta, tb, dsk;
    const u32x4 qws = mkrs(p.ws, WS_END);
#define POLL_ISSUE() asm volatile("buffer_load_dwordx4 %0, %4, %5, 0 offen sc1\n\tbuffer_load_dwordx4 %1, %4, %5, 0 offen offset:1024 sc1\n\t" \
      "buffer_load_dwordx4 %2, %4, %5, 0 offen offset:2048 sc1\n\tbuffer_load_dwordx4 %3, %4, %5, 0 offen offset:3072 sc1" \
      : "=&v"(sg[0]), "=&v"(sg[1]), "=&v"(sg[2]), "=&v"(sg[3]) : "v"(go + WS_SG), "s"(qws) : "memory")
#define POLL_BAD() (tagdiff(sg[0], ta, tb) | tagdiff(sg[1], ta, tb) | tagdiff(sg[2], ta, tb) | tagdiff(sg[3], ta, tb))
    int vx[4], vw1[4], vw2[4], so, xo0, xo1, fo, mfo, fto;
    LAS char *ra[2], *rc[2];
    auto addresses = [&]() {
      const int hm = (j - 4) >> 31;
#pragma unroll
      for (int i = 0; i < 4; ++i) {
        const int chk = 16 * ((lane & 15) ^ ((lane >> 4) << 2) ^ i);
        vx[i] = (lane >> 4) * 256 + chk;
        vw1[i] = (lane >> (4 + hm)) * (F * 2) + ((chk & ~hm) | ((lane & 7) * 16 & hm)) + i * ((4 + (4 & hm)) * F * 2 - 1024);
        vw2[i] = (lane >> 3) * 1024 + db * 128 + 16 * ((lane & 7) ^ (((lane >> 4) & 1) << 2)) + i * 7168;
        PIN(vx[i]); PIN(vw1[i]); PIN(vw2[i]);
      }
      const int g = (lane >> 1) & 3;
      ra[0] = smem + LD_RED0 + (2 * lw * 64 + 16 * g + ((lane >> 3) ^ (4 * g))) * 16 + 8 * (lane & 1);
      ra[1] = smem + LD_RED0 + (2 * lw * 64 + 16 * g + ((lane >> 3) ^ (4 * g) ^ 8)) * 16 + 8 * (lane & 1);
      rc[0] = ra[0] + (LD_RED1 - LD_RED0); rc[1] = ra[1] + (LD_RED1 - LD_RED0);
      so = WS_X1S + (lw * 4 + (lane >> 4)) * 131072 + d0 * 256 + (lane & 15) * 16;
      xo0 = WS_X1 + (lw * 8 + (lane >> 3)) * 65536 + pair * 128 + (lane & 7) * 16; xo1 = xo0 + 32768;
      fo = only(lane < 4, WS_F2 + (lw * 256 + pair) * 64 + lane * 16);
      mfo = only((tid >> 1) == 248, WS_MF + (~lane & 1) * 4096 + pair * 16);
      const int fn = lw < 2 ? 28 : 14;
      fto = ((lw < 2 ? lw * 7168 : lw * 3584) + b * fn + min(lane, fn - 1)) * 128;
      PIN(fto); PIN(mfo); PIN(ra[0]); PIN(ra[1]); PIN(rc[0]); PIN(rc[1]); PIN(so); PIN(xo0); PIN(xo1); PIN(fo);
      SCHED();
    };
    if (lw >= 2) {
      u32 xt;
      TOUCH(xt, (j * 32 + (lw & 1) * 16 + (lane & 15)) * 1024 + xcd * 128, p.x);
      SCAL_ISSUE(p.dskip + d0);
      addresses();
      const u32x4 qx = mkrs(p.x, L * D * 2);
#pragma unroll
      for (int i = 0; i < 8; ++i) {
        dma4(qx, LD_XT + (lw & 1) * 2048 + i * 256, lane * 1024 + pair * 4, ((lw & 1) * 8 + i) * 65536);
        WAIT_VM(2);
      }
      SCAL_WAIT();
      if (XG_DELAY) __builtin_amdgcn_s_sleep(XG_DELAY);
      quad_arrive(flg, lw, 1u);
      STAMP(3);
      POLL_ISSUE();
      const int xo = WS_XG + ((lw & 1) * 128 + (lane >> 1)) * 8192 + pair * 32 + (lane & 1) * 16;
      u32x4 xe[4];
#pragma unroll
      for (int i = 0; i < 4; ++i) xe[i] = ld_sc1(rws, xo, i * 262144);
      asm volatile("s_waitcnt vmcnt(8)" :: "v"(xt) : "memory");
      quad_arrive(flg, lw, 2u);
      asm volatile("s_waitcnt vmcnt(4)" : "+v"(sg[0]), "+v"(sg[1]), "+v"(sg[2]), "+v"(sg[3]) :: "memory");
      if (__builtin_expect(__any((tagdiff(xe[0], ta, tb) | tagdiff(xe[1], ta, tb) | tagdiff(xe[2], ta, tb) | tagdiff(xe[3], ta, tb)) != 0u), 0)) {
#pragma unroll
        for (int i = 0; i < 4; ++i) if (tagdiff(xe[i], ta, tb)) xe[i] = poll16(rws, xo, i * 262144, ta, tb);
      }
#pragma unroll
      for (int i = 0; i < 4; ++i) xg[i] = u32x2{xe[i][0], xe[i][2]};
    } else {
      u32x4 xv[4], wm[8];
#pragma unroll
      for (int i = 0; i < 4; ++i) xv[i] = LDNT((const u32x4*)(p.x + (size_t)(lw * 1024 + b * 4 + i) * D) + lane);
      SCHED();
      SCAL_ISSUE(p.dskip + d0);
      const u32 c0 = p.c[(b >> 4) * 32 + (lane >> 3) + 8 * (lane & 3)];
      SCHED();
      {
        const rsrc_t ra = __builtin_amdgcn_make_buffer_rsrc((void*)p.wmod, 0, D * 3072 * 2, 0x00020000);
        const rsrc_t rb_ = __builtin_amdgcn_make_buffer_rsrc((void*)p.wmod, 0, lw == 0 ? D * 3072 * 2 : 0, 0x00020000);
#pragma unroll
        for (int i = 0; i < 8; ++i)
          wm[i] = __builtin_amdgcn_raw_buffer_load_b128(i < 4 ? ra : rb_, (lane >> 3) * 6144 + (lane & 7) * 16,
                                                        ((b >> 4) * 32 + 8 * (i & 3)) * 6144 + ((lw + (i >> 2) * 2) * 16 + (b & 15)) * 128, AUX_NT);
      }
      SCHED();
      *(LAS u32x4*)(smem + LD_U + (lane >> 5) * 4608 + (lane & 31) * 16) = u32x4{0u, 0u, 0u, 0u};
      int so_ = only(!(lane & 31), WS_SG + lw * 8192 + b * 32 + (lane >> 5) * 16);
      int mo_ = only(!(lane & 8), WS_MG + ((b >> 4) * 2048 + (lw * 16 + (b & 15)) * 32 + (lane >> 4) * 8 + (lane & 7)) * 16);
      PIN(so_); PIN(mo_);
      SCAL_WAIT();
      STAMP(1);
      LAS char* xw = smem + LD_XT + 4096;
      if (lw) {
#pragma unroll
        for (int i = 0; i < 4; ++i) *(LAS u32x4*)(xw + i * 1056 + lane * 16) = xv[i];
      }
      float s[4], q[4];
#pragma unroll
      for (int i = 0; i < 4; ++i) {
        s[i] = 0.f; q[i] = 0.f;
#pragma unroll
        for (int z = 0; z < 4; ++z) { s[i] = dot2(xv[i][z], 0x3f803f80u, s[i]); q[i] = dot2(xv[i][z], xv[i][z], q[i]); }
      }
      const u32 st = ln_stat(row_sum(fold16(fold32(s[0], s[2]), fold32(s[1], s[3]))), row_sum(fold16(fold32(q[0], q[2]), fold32(q[1], q[3]))));
      const auto sw = __builtin_amdgcn_permlane16_swap(st, st, false, false);
      st_sc1(rws, so_, 0, u32x4{sw[0], ta, sw[1], tb});
      STAMP(2);
      {
        u32 a[8][2];
#pragma unroll
        for (int i = 0; i < 8; ++i) {
          LAS const u32* src = (LAS const u32*)(xw + (lane & 1) * 2112 + i * 128 + (lane >> 1) * 4);
          a[i][0] = src[0]; a[i][1] = src[264];
        }
#pragma unroll
        for (int i = 0; i < 8; ++i) st_sc1(rws, (lw ? WS_XG + b * 8192 : OOB) + lane * 16 + (i >> 2) * 4096, (i & 3) * 1024, u32x4{a[i][0], ta, a[i][1], tb});
      }
      SCHED();
      const u32 sp = silu_bf(c0);
#pragma unroll
      for (int z = 0; z < 2; ++z) {
        float acc[8] = {0.f, 0.f, 0.f, 0.f, 0.f, 0.f, 0.f, 0.f};
        gv_row<0x00>(wm[4 * z], sp, acc); gv_row<0x55>(wm[4 * z + 1], sp, acc); gv_row<0xAA>(wm[4 * z + 2], sp, acc); gv_row<0xFF>(wm[4 * z + 3], sp, acc);
        const f32x2 m = gv_fold(acc);
        st_sc1(rws, z ? (mo_ + 16384) | (lw ? OOB : 0) : mo_, 0, u32x4{__float_as_uint(m[0]), ta, __float_as_uint(m[1]), tb});
        SCHED();
      }
      quad_arrive(flg, lw, 1u);
      STAMP(4);
      addresses();
      quad_wait(flg, 1u);
      const int mo = (WS_MG + ((lane & 15) * 2048 + (lane >> 4) * 256 + (pair >> 5) * 32 + (pair & 3) * 8 + ((pair >> 2) & 7)) * 16 & (lw ? -1 : ~0x2000)) + lw * 8192;
      u32x4 mg;
      asm volatile("buffer_load_dwordx4 %0, %1, %2, 0 offen sc1" : "=v"(mg) : "v"(mo), "s"(qws) : "memory");
      POLL_ISSUE();
      asm volatile("s_waitcnt vmcnt(4)" : "+v"(mg) :: "memory");
      asm volatile("s_waitcnt vmcnt(0)" : "+v"(sg[0]), "+v"(sg[1]), "+v"(sg[2]), "+v"(sg[3]) :: "memory");
      if (__builtin_expect(__any(tagdiff(mg, ta, tb) != 0u), 0)) mg = poll16(rws, mo, 0, ta, tb);
      const u32 mb = pk(row_sum(__uint_as_float(mg[0])), row_sum(__uint_as_float(mg[2])));
      *(LAS u32x2*)(smem + LD_MV + lw * 32 + (lane >> 4) * 8) = u32x2{mb, pk2(up(mb) + 1.f)};
      quad_arrive(flg, lw, 2u);
    }
    __builtin_amdgcn_s_setprio(2);
    STAMP(7);
    if (__builtin_expect(__any(POLL_BAD() != 0u), 0)) {
#pragma unroll 1
      for (int spins = 0; spins < SPIN_CAP; ++spins) {
        FENCE();
#pragma unroll
        for (int i = 0; i < 4; ++i) if (tagdiff(sg[i], ta, tb)) sg[i] = ld_sc1(rws, go + i * 1024, WS_SG);
        if (!__any(POLL_BAD() != 0u)) break;
        __builtin_amdgcn_s_sleep(S1_SLEEP);
      }
    }
    STAMP(8);
    f32x2 xf[4][2], hf[4][2];
    float rs[4][2], nm[4][2];
#pragma unroll
    for (int i = 0; i < 4; ++i)
#pragma unroll
      for (int e = 0; e < 2; ++e) {
        rs[i][e] = __builtin_amdgcn_rcpf(bfhi(sg[i][2 * e]));
        nm[i][e] = -bflo(sg[i][2 * e]) * rs[i][e];
        PIN(rs[i][e]); PIN(nm[i][e]);
      }
    const u32x4 qw1 = mkrs(p.w1, D * F * 2), qw2 = mkrs(p.w2, D * F * 2);
    const int sw2 = ((lw >> 1) * 896 + (lw & 1) * 32) * 1024;
    auto w2_stage = [&](int u) { dma4k(qw2, w2_unit(u) * UNIT + lw * 4096, vw2, sw2 + w2_phys(u) * 65536); };
    const int wstep = (helper ? 32 : 16) * F * 2, wbase = helper ? min(tbk, 27) * 128 : cbk * 256;
    auto w1_stage = [&](int s) { dma4k(qw1, LD_WR + (s & 3) * UNIT + lw * 4096, vw1, (s * 4 + lw) * wstep + wbase); };
    auto x_stage = [&](int s) { dma4k_sc1(qws, LD_XR + (s & 3) * UNIT + lw * 4096, vx, WS_X1S + rbk * 131072 + s * 16384 + lw * 4096); };
    SCHED();
    quad_wait(flg, 2u);
    STAMP(9);
    if (lw < 2) {
#pragma unroll
      for (int i = 0; i < 4; ++i) xg[i] = *(LAS const u32x2*)(smem + LD_XT + lw * 2048 + i * 512 + lane * 8);
    }
    const u32x4 mv0 = *(LAS const u32x4*)(smem + LD_MV), mv1 = *(LAS const u32x4*)(smem + LD_MV + 32), mv2 = *(LAS const u32x4*)(smem + LD_MV + 48);
    const f32x2 sa = up(mv0[0]), g1 = up(mv0[3]);
#pragma unroll
    for (int i = 0; i < 4; ++i) {
#pragma unroll
      for (int e = 0; e < 2; ++e) { xf[i][e] = up(xg[i][e]); hf[i][e] = (xf[i][e] * rs[i][e] + nm[i][e]) * g1 + sa; }
      LAS u32* U = (LAS u32*)(smem + LD_U + 512 + lw * 1024 + i * 256) + lane;
      U[0] = pk(hf[i][0][0], hf[i][1][0]);
      U[4608 / 4] = pk(hf[i][0][1], hf[i][1][1]);
    }
    STAMP(10);
    BARRIER();
    STAMP(11);
    u32 ft;
    TOUCH(ft, fto, lw < 2 ? p.w2 : p.w1);
    w1_stage(0); w1_stage(1); w1_stage(2); w1_stage(3);
    const f32x2 gav = up(mv1[0]), gcv = up(mv2[1]);
    f32x2 xc[4][2];
    float gd[2] = {gav[0] * bflo(dsk), gav[1] * bfhi(dsk)}, n0 = bflo(mv2[2]) * (-1.f / LOG2E), n1 = bfhi(mv2[2]) * (-1.f / LOG2E);
    u32 k0 = __builtin_amdgcn_perm(mv1[2], mv2[1], 0x05040100u), k1 = __builtin_amdgcn_perm(mv1[2], mv2[1], 0x07060302u);
    PIN(gd[0]); PIN(gd[1]); PIN(n0); PIN(n1); PIN(k0); PIN(k1);
    const u32x4 mfv = {lane & 1 ? k0 : __float_as_uint(n0), ta, lane & 1 ? k1 : __float_as_uint(n1), tb};
    st_sc1(rws, mfo, 0, mfv);
#pragma unroll
    for (int i = 0; i < 4; ++i)
#pragma unroll
      for (int c = 0; c < 2; ++c) {
        float a0 = __builtin_fmaf(hf[i][0][c], gd[c], xf[i][0][c]), a1 = __builtin_fmaf(hf[i][1][c], gd[c], xf[i][1][c]);
        PIN(a0); PIN(a1);
        xc[i][c] = f32x2{a0, a1};
      }
    STAMP(12);
    SCHED();
    u32x4 wr[20];
    auto wr_load = [&](bool on) {
      const rsrc_t rw2 = __builtin_amdgcn_make_buffer_rsrc((void*)p.w2, 0, on ? D * F * 2 : 0, 0x00020000);
#pragma unroll
      for (int i = 0; i < 20; ++i) wr[i] = __builtin_amdgcn_raw_buffer_load_b128(rw2, vw2[i & 3] + (i & 3) * 1024, sw2 + (i / 4) * 65536, AUX_W);
    };
    wr_load(!helper);
    SCHED();
    STAMP(13);
    BARRIER();
    STAMP(14);
    {
      u32x4 o[2], s0, s1;
      f32x2 yp[4][4], c0[4], c1[4];
#pragma unroll
      for (int i = 0; i < 4; ++i) {
        yp[i][0] = *(volatile LAS const f32x2*)(ra[i & 1] + (i >> 1) * 1024); yp[i][1] = *(volatile LAS const f32x2*)(ra[i & 1] + (i >> 1) * 1024 + 8192);
      }
#pragma unroll
      for (int i = 0; i < 4; ++i) {
        yp[i][2] = *(volatile LAS const f32x2*)(rc[i & 1] + (i >> 1) * 1024); yp[i][3] = *(volatile LAS const f32x2*)(rc[i & 1] + (i >> 1) * 1024 + 8192);
      }
      SCHED();
#pragma unroll
      for (int i = 0; i < 4; ++i) { c0[i] = (yp[i][0] + yp[i][1]) * gav[0] + xc[i][0]; s0[i] = pk2(c0[i] * gcv[0]); }
      st_sc1(rws, so, 0, s0);
      SCHED();
#pragma unroll
      for (int i = 0; i < 4; ++i) { c1[i] = (yp[i][2] + yp[i][3]) * gav[1] + xc[i][1]; s1[i] = pk2(c1[i] * gcv[1]); }
      st_sc1(rws, so, 256, s1);
      SCHED();
#pragma unroll
      for (int i = 0; i < 4; ++i) { o[i >> 1][2 * (i & 1)] = pk(c0[i][0], c1[i][0]); o[i >> 1][2 * (i & 1) + 1] = pk(c0[i][1], c1[i][1]); }
      st_sc1(rws, xo0, 0, o[0]);
      st_sc1(rws, xo1, 0, o[1]);
    }
    STAMP(15);
    WAIT_VM(0);
    asm volatile("" :: "v"(ft));
    STAMP(17);
    st_sc1(rws, fo, 0, u32x4{ta, ta, tb, tb});
    __builtin_amdgcn_s_setprio(0);
    const int kind = helper ? 3 : lw;
    int e0 = WS_G2 + rbk * 1024, e1 = WS_S12 + (cbk * 128 + (kind - 1) * 64) * 16, e3 = WS_MF + 4096 + db * 512;
    asm volatile("" : "+s"(e0), "+s"(e1), "+s"(e3));
    const bool mid = (kind + 1) & 2;
    const int eo = (mid ? e1 : kind ? e3 : e0) + (lane & (kind == 3 ? 31 : 63)) * 16;
    LAS char* ea = smem + LD_MISC + (mid ? 256 + kind * 256 : kind * 512) + lane * (mid ? 4 : 8);
    u32x4 ev;
    if (helper) {
      BARRIER();
      ev = poll16(rws, eo, 0, ta, tb);
      *(LAS u32*)ea = ev[0]; *(LAS u32*)(ea + 4) = ev[2];
      BARRIER();
      __builtin_amdgcn_s_sleep(20);
      w2_stage(1); w2_stage(2); w2_stage(3);
      BARRIER(); BARRIER();
      w2_stage(0); w2_stage(4); w2_stage(5);
      wr_load(true);
    } else {
      __builtin_amdgcn_s_sleep(F2_DELAY);
      poll16(rws, ((lane >> 3) * 32 + lw * 8 + (lane & 7)) * 64, WS_F2 + (xcd >> 1) * 16384, ta, tb);
      STAMP(19);
      x_stage(0); x_stage(1); WAIT_VM(4); BARRIER();
      x_stage(2); x_stage(3); WAIT_VM(8); BARRIER();
      x_stage(4); w1_stage(4); WAIT_VM(12); BARRIER();
#pragma unroll 1
      for (int s = 5; s < 8; ++s) { x_stage(s); w1_stage(s); WAIT_VM(16); BARRIER(); }
      w2_stage(0); w2_stage(1); WAIT_VM(16); BARRIER();
      asm volatile("buffer_load_dwordx4 %0, %1, %2, 0 offen sc1" : "=v"(ev) : "v"(eo), "s"(qws) : "memory");
      w2_stage(2); w2_stage(3); WAIT_VM(17); BARRIER();
      asm volatile("s_waitcnt vmcnt(8)" : "+v"(ev) :: "memory");
      if (__any(tagdiff(ev, ta, tb) != 0u)) ev = poll16(rws, eo, 0, ta, tb);
      *(LAS u32*)ea = ev[0]; *(LAS u32*)(ea + (mid ? 512 : 4)) = ev[2];
      w2_stage(4); w2_stage(5); BARRIER();
    }
    STAMP(22);
    w2_stage(6); w2_stage(7); w2_stage(8);
    {
      const int xs = WS_X1 + tbk * 65536 + (db * 32 + (lw >> 1) * 16) * 128 + (lw & 1) * 64;
      dma16_sc1(qws, LD_MISC + 2048 + lw * 2048, (lane >> 2) * 128 + (lane & 3) * 16, xs);
      dma16_sc1(qws, LD_MISC + 3072 + lw * 2048, (lane >> 2) * 128 + (lane & 3) * 16, xs + 32768);
    }
    WAIT_VM(0);
    STAMP(25);
    BARRIER();
    BARRIER();
#pragma unroll
    for (int i = 0; i < 20; ++i) {
      if (i == 8) BARRIER();
      *(LAS u32x4*)(smem + w2_unit(9 + i / 4) * UNIT + lw * 4096 + (i & 3) * 1024 + lane * 16) = wr[i];
    }
    BARRIER();
    if (b == 0 && tid == 256) *(u32x2*)(p.ws + WS_EPOCH) = u32x2{ta, tb};
    STAMP(26);
    STAMP_FLUSH(p.ws, 1);
    return;
  }

  ROLES();
  const int pch = w >> 1, pn = (w & 1) * 64 + lane, po = (d0 + pch) * NS + pn;
  u32 rar = LDNT(p.Are + po), rai = LDNT(p.Aim + po), rbr = LDNT(p.Bre + po), rbi = LDNT(p.Bim + po), rcr = LDNT(p.Cre + po), rci = LDNT(p.Cim + po);
  SCHED();
  SCAL_ISSUE(p.ldt + d0);
  {
    u32 cp0;
    {
      u64 pc; int r0, r1;
      asm volatile("s_getpc_b64 %0\n\ts_mov_b32 %1, fused_block@rel32@lo+4\n\ts_mov_b32 %2, fused_block_end@rel32@lo+12" : "=s"(pc), "=s"(r0), "=s"(r1));
      TOUCH(cp0, min(((w * 4 + (lane & 3)) * 32 + j) * 128, r1 - r0), pc + (long long)r0);
    }
    LAS char* flg = smem + LD_FLG;
    *(LAS u32x4*)(smem + LD_T + ((lane >> 1) & 7) * TCOPY + 4096 + (lane & 1) * 16) = u32x4{0u, 0u, 0u, 0u};
    *(LAS u32*)(smem + LD_KD + KD_ZERO * 4) = 0u;
    asm volatile("s_waitcnt lgkmcnt(0)" : "+s"(epoch), "+s"(aux));
    int ga[11], ka[4];
#pragma unroll
    for (int i = 0; i < 11; ++i) {
      const int l = 2047 - (tid * 8 + i);
      const int bits = (int)(pk((float)l, 0.f) & 0xffffu);
      const int idx = l < 128 ? l : bits - 0x4300 + 128;
      ga[i] = LD_KD + 4 * (l < 0 ? KD_ZERO : idx + (idx >> 3));
    }
    {
      const int e = lane & 15, g = lane >> 4;
      const u32 cfg = e < 8 ? (w == 0 ? 0x100004u : w == 1 ? 0x080004u : 0x022001u) : (w == 0 ? 0x100004u : w == 1 ? 0x044002u : 0x088003u);
      const int sh = cfg & 15, base = 256 * w + ((cfg >> 8) & 0xff) * (e >> sh) + (e & ((1 << sh) - 1));
#pragma unroll
      for (int r = 0; r < 4; ++r) { const int idx = base + (int)(cfg >> 16) * (4 * g + r); ka[r] = LD_KD + 4 * (idx + (idx >> 3)); PIN(ka[r]); }
    }
    STAMP(1);
    asm volatile("" : "+v"(rar), "+v"(rai), "+v"(rbr), "+v"(rbi), "+v"(rcr), "+v"(rci) : "v"(cp0));
    STAMP(2);
    SCHED();
    volatile LAS u32* P = (LAS u32*)(smem + LD_P) + pch * 64 * PROW + pn;
    const float dt = rb(__builtin_amdgcn_exp2f((pch ? bfhi(aux) : bflo(aux)) * LOG2E));
    const float a2 = (dt * bflo(rar)) * LOG2E, bq = (dt * bflo(rai)) * 0.15915494309189535f;
    const f32x2 z1 = cpow(a2, bq, 1.f), z8 = cpow(a2, bq, 8.f), z256 = cpow(a2, bq, 256.f);
    const f32x2 z16c = cmul(z8, z8) * f32x2{1.f, -1.f}, z512 = cmul(z256, z256), z768 = cmul(z512, z256);
    f32x2 u = cpow(a2, bq, 1024.f), q = {1.f, 0.f};
    f32x2 r = cmul(f32x2{bflo(rcr), -bflo(rci)}, f32x2{bflo(rbr), -bflo(rbi)} * dt);
#pragma unroll
    for (int e = 0; e < 16; ++e) {
      const f32x2 rn = cmul(r, z16c), qn = cmul(q, z1);
      P[e * PROW] = pk2(r);
      P[(16 + e) * PROW] = pk2(q);
      if (!(e & 1)) P[(32 + e / 2) * PROW] = pk2(cmul(q, z256));
      if (!(e & 3)) { P[(40 + e / 4) * PROW] = pk2(cmul(q, z512)); P[(44 + e / 4) * PROW] = pk2(cmul(q, z768)); }
      if ((e & 3) == 1) { P[(48 + e / 2) * PROW] = pk2(u); P[(49 + e / 2) * PROW] = pk2(cmul(u, z8)); }
      if ((e & 3) == 3) u = cmul(u, z256);
      r = rn; q = qn;
    }
    P[56 * PROW] = pk2(u);
    STAMP(3);
    quad_arrive(flg, w, 1u);
#pragma unroll
    for (int i = 0; i < 11; ++i) PIN(ga[i]);
    quad_wait(flg, 1u);
    STAMP(4);
    if (w < 3) {
      LAS const u32* fb = (LAS const u32*)(smem + LD_P) + (lane & 15) * PROW + ((lane >> 4) & 1) * 64 + (lane >> 5) * 32;
      u32x4 fa[16], fc[16];
      f32x4 acc[2] = {{0.f, 0.f, 0.f, 0.f}, {0.f, 0.f, 0.f, 0.f}};
#pragma unroll
      for (int ks = 0; ks < 22; ++ks) {
        if (ks < 16) {
          const int o = (ks & 1) * 64 * PROW + (ks >> 1) * 4;
          fa[ks] = *(LAS const u32x4*)(fb + o); fc[ks] = *(LAS const u32x4*)(fb + (w + 1) * 16 * PROW + o);
        }
        SCHED();
        if (ks >= 6) acc[ks & 1] = mfma16(fa[ks - 6], fc[ks - 6], acc[ks & 1]);
        SCHED();
      }
#pragma unroll
      for (int r4 = 0; r4 < 4; ++r4) *(LAS u32*)(smem + ka[r4]) = pk(acc[0][r4], acc[1][r4]);
    }
    STAMP(5);
    quad_arrive(flg, w, 2u);
    quad_wait(flg, 2u);
    STAMP(6);
    u32 e[11];
#pragma unroll
    for (int i = 0; i < 11; ++i) e[i] = *(LAS const u32*)(smem + ga[i]);
#pragma unroll
    for (int c = 0; c < 2; ++c)
#pragma unroll
      for (int s = 0; s < 4; ++s) {
        u32x4 o;
#pragma unroll
        for (int z = 0; z < 4; ++z) o[z] = __builtin_amdgcn_perm(e[s + 2 * z + 1], e[s + 2 * z], c ? 0x07060302u : 0x05040100u);
        *(LAS u32x4*)(smem + LD_T + (c * 4 + s) * TCOPY + tid * 16) = o;
      }
    quad_arrive(flg, w, 3u);
  }
  epoch += 0x9E3779B97F4A7C15ull;
  const u32 ta = (u32)epoch, tb = (u32)(epoch >> 32);
  {
    const int r = lane & 15, g = lane >> 4, ch = w >> 1, half = w & 1;
    LAS const char* Tb = smem + LD_T + (ch * 4 + ((3 - r) & 3)) * TCOPY + 2 * (2028 - 4 * (r >> 2) + 8 * g - 128 * half) - 7 * 512 - 192;
    LAS const char* Ub = smem + LD_U + ch * 4608 + 2 * (256 + 16 * r - 16 + 8 * g - 128 * half) - 192;
    LAS f32x4* red = (LAS f32x4*)(smem + (ch ? LD_RED1 : LD_RED0)) + half * 512 + (lane ^ ((lane >> 4) << 2));
    PIN(Ub); PIN(red);
    f32x4 acc[8];
#pragma unroll
    for (int n = 0; n < 8; ++n) acc[n] = f32x4{0.f, 0.f, 0.f, 0.f};
    u32x4 uf[2][8], tf[4][8];
    quad_wait(smem + LD_FLG, 3u);
#define LD_TH(e, a, hh) do { const u32x2 l_ = *(volatile LAS const u32x2*)(Tb + 64 * (3 - (e)) + 512 * (7 - (a)) + 8 * (hh)); tf[e][a][2 * (hh)] = l_[0]; tf[e][a][2 * (hh) + 1] = l_[1]; } while (0)
#pragma unroll
    for (int e = 0; e < TF_PRE; ++e)
#pragma unroll
      for (int a = 0; a < 8; ++a) { LD_TH(e, a, 0); LD_TH(e, a, 1); }
#define LD_UF(n, e) uf[(e) & 1][n] = *(LAS const u32x4*)(Ub + 64 * (3 - (e)) + 512 * (n))
#define CM(e, a, m) do { SCHED(); acc[(a) + (m)] = mfma16(tf[e][a], uf[(e) & 1][m], acc[(a) + (m)]); SCHED(); } while (0)
    STAMP(10);
    BARRIER();
    STAMP(11);
#pragma unroll
    for (int n = 0; n < 8; ++n) LD_UF(n, 0);
#pragma unroll
    for (int e = 0; e < 3; ++e) {
      int cnt = 0;
#pragma unroll
      for (int m = 0; m < 8; ++m)
#pragma unroll
        for (int a = 0; a + m < 8; ++a) {
          CM(e, a, m);
          if (cnt >= 4 && cnt < 12) LD_UF(cnt - 4, e + 1);
          if (cnt >= 12 && cnt < 28 && e + TF_PRE < 4) LD_TH(e + TF_PRE, (cnt - 12) >> 1, (cnt - 12) & 1);
          ++cnt;
        }
    }
#pragma unroll
    for (int n = 0; n < 8; ++n)
#pragma unroll
      for (int a = 0; a <= n; ++a) { CM(3, a, n - a); if (a == 1) red[(n - 1) * 64] = acc[n - 1]; }
    STAMP(12);
    red[7 * 64] = acc[7];
    BARRIER();
    STAMP(14);
    STAMP(17);
  }
  if (helper) {
    const int hb = xcd * 4 + j;
    u32x4 v[16];
    const int rg = tid >> 3;
    LAS float* rd = (LAS float*)(smem + 4096);
    { const u32x4 m = poll16(rws, tid * 16, WS_MF, ta, tb); *(LAS u32x2*)(smem + tid * 8) = u32x2{m[0], m[2]}; }
    BARRIER();
#pragma unroll
    for (int i = 0; i < 16; ++i) v[i] = *(LAS const u32x4*)(smem + LD_WR + i * 4096 + tid * 16);
    float a[16];
#pragma unroll
    for (int e = 0; e < 16; ++e) a[e] = 0.f;
#pragma unroll
    for (int i = 0; i < 16; ++i) {
      const u32 gs = *(LAS const u32*)(smem + (i * 32 + rg) * 4);
      const u32 gl = gs & 0xffffu, gh = gs << 16, sl = gs >> 16, sh = gs & 0xffff0000u;
#pragma unroll
      for (int z = 0; z < 4; ++z) {
        a[4 * z] = dot2(v[i][z], gl, a[4 * z]); a[4 * z + 1] = dot2(v[i][z], sl, a[4 * z + 1]);
        a[4 * z + 2] = dot2(v[i][z], gh, a[4 * z + 2]); a[4 * z + 3] = dot2(v[i][z], sh, a[4 * z + 3]);
      }
    }
    fold_cols(a, rd + w * 128, lane);
    STAMP(18);
    poll16(rws, tid * 64, WS_F2 + (xcd >> 1) * 16384, ta, tb);
    BARRIER();
    STAMP(19);
#pragma unroll
    for (int i = 0; i < 16; ++i) v[i] = ld_sc1(rws, tid * 16, WS_X1 + hb * 65536 + i * 4096);
    if (w == 0 && hb < 28) {
      const f32x2 s = sum4(rd + lane * 2, 128);
      st_sc1(rws, lane * 16, WS_S12 + hb * 1024, u32x4{__float_as_uint(s[0]), ta, __float_as_uint(s[1] * -LOG2E), tb});
    }
#pragma unroll
    for (int e = 0; e < 16; ++e) a[e] = 0.f;
#pragma unroll
    for (int i = 0; i < 16; ++i)
#pragma unroll
      for (int z = 0; z < 4; ++z) {
        const int o = (i >> 3) * 8 + 2 * z;
        a[o] = dot2(v[i][z], 0x3f803f80u, a[o]); a[o + 1] = dot2(v[i][z], v[i][z], a[o + 1]);
      }
    STAMP(20);
    fold_cols(a, rd + 512 + w * 128, lane);
    BARRIER();
    if (w == 0) {
      const f32x2 s = sum4(rd + 512 + lane * 2, 128);
      const u32 st = ln_stat(s[0], s[1]);
      const u32 st2 = __builtin_amdgcn_mov_dpp(st, 0xB1, 0xf, 0xf, true);
      if (!(lane & 1)) st_sc1(rws, lane * 8, WS_G2 + hb * 512, u32x4{st, ta, st2, tb});
    }
    BARRIER();
    STAMP(22);
    __builtin_amdgcn_s_sleep(H_SLEEP);
  } else {
    const int wt = w & 1, wf = w >> 1;
    const int h = lane >> 5, blk = (lane >> 4) & 1, q = (lane & 15) >> 2, pp = lane & 3;
    int ax[2][2], aw[2][2];
#pragma unroll
    for (int c = 0; c < 2; ++c)
#pragma unroll
      for (int t = 0; t < 2; ++t) {
        const int base = 256 * (8 * h + 4 * t + q) + 16 * ((2 * blk + (pp >> 1)) ^ (2 * h + t)) + 8 * (pp & 1);
        ax[c][t] = LD_XR + base + 64 * ((2 * wt + c) ^ q);
        aw[c][t] = LD_WR + base + 64 * ((2 * wf + c) ^ q);
        PIN(ax[c][t]); PIN(aw[c][t]);
      }
    f32x16 acc[2][2];
#pragma unroll
    for (int a = 0; a < 2; ++a)
#pragma unroll
      for (int c = 0; c < 2; ++c)
#pragma unroll
        for (int r = 0; r < 16; ++r) acc[a][c][r] = 0.f;
    u32x4 xf[2][2], wfr[2][2];
    u32 st[2];
    int ho = WS_H + ((rbk * 4 + wt * 2) * 112 + cbk * 8 + wf * 4) * 1024 + lane * 16, ho1 = ho + 112 * 1024;
    int fo = only(lane < 8, WS_F3 + ((rbk * 4 + w) * 16 + cbk) * 128 + lane * 16);
    PIN(ho); PIN(ho1); PIN(fo);
#define RD_X(i, c) xf[(i) & 1][c] = tr_frag(smem + ax[c][0] + ((i) & 15) * 4096, smem + ax[c][1] + ((i) & 15) * 4096)
#define RD_W(i, c) wfr[(i) & 1][c] = tr_frag(smem + aw[c][0] + ((i) & 15) * 4096, smem + aw[c][1] + ((i) & 15) * 4096)
#define MM(i, a, c) do { SCHED(); acc[a][c] = mfma32(wfr[(i) & 1][a], xf[(i) & 1][c], acc[a][c]); SCHED(); } while (0)
    BARRIER();
    STAMP(19);
    RD_W(0, 0); RD_X(0, 0); RD_X(0, 1); RD_W(0, 1);
#pragma unroll
    for (int i = 0; i < 32; ++i) {
      if ((i & 3) != 3) {
        MM(i, 0, 0); RD_W(i + 1, 0); MM(i, 0, 1); RD_X(i + 1, 0); MM(i, 1, 0); RD_X(i + 1, 1); MM(i, 1, 1); RD_W(i + 1, 1);
      } else {
        MM(i, 0, 0); MM(i, 0, 1);
        BARRIER();
        if (i == 31) { st[0] = *(LAS const u32*)(smem + LD_MISC + (wt * 64 + (lane & 31)) * 4); st[1] = *(LAS const u32*)(smem + LD_MISC + (wt * 64 + 32 + (lane & 31)) * 4); }
        MM(i, 1, 0);
        if (i < 31) { RD_W(i + 1, 0); RD_X(i + 1, 0); }
        MM(i, 1, 1);
        if (i < 31) { RD_X(i + 1, 1); RD_W(i + 1, 1); }
      }
    }
    STAMP(21);
    float rstd[2], mr[2];
#pragma unroll
    for (int c = 0; c < 2; ++c) {
      rstd[c] = __builtin_amdgcn_rcpf(bfhi(st[c])) * -LOG2E;
      mr[c] = -bflo(st[c]) * rstd[c];
    }
    STAMP(22);
    LAS const float* s12 = (LAS const float*)(smem + LD_MISC + 512) + wf * 64 + 4 * h;
#pragma unroll
    for (int a = 0; a < 2; ++a)
#pragma unroll
      for (int sp = 0; sp < 2; ++sp) {
        f32x4 s1[2], s2[2];
#pragma unroll
        for (int z = 0; z < 2; ++z) { s1[z] = *(LAS const f32x4*)(s12 + a * 32 + sp * 16 + z * 8); s2[z] = *(LAS const f32x4*)(s12 + 128 + a * 32 + sp * 16 + z * 8); }
#pragma unroll
        for (int c = 0; c < 2; ++c) {
          u32x4 o;
#pragma unroll
          for (int e = 0; e < 4; ++e) {
            const int z = e >> 1, k = (e & 1) * 2;
            const f32x2 av = {acc[a][c][sp * 8 + 2 * e], acc[a][c][sp * 8 + 2 * e + 1]};
            const f32x2 cc = f32x2{s1[z][k], s1[z][k + 1]} * mr[c] + f32x2{s2[z][k], s2[z][k + 1]};
            const f32x2 ar = av * rstd[c] + cc;
            const f32x2 dn = f32x2{__builtin_amdgcn_exp2f(ar[0]), __builtin_amdgcn_exp2f(ar[1])} + 1.f;
            o[e] = pk2(ar * f32x2{__builtin_amdgcn_rcpf(dn[0]), __builtin_amdgcn_rcpf(dn[1])});
          }
          st_sc1(rws, c ? ho1 : ho, (a * 2 + sp) * 1024, o);
        }
      }
    STAMP(23);
    WAIT_VM(0);
    STAMP(24);
    st_sc1(rws, fo, 0, u32x4{ta, ta, tb, tb});
  }

  {
    const int wt = w & 1, kh = w >> 1, h = lane >> 5, tl = lane & 31;
    const int blk = (lane >> 4) & 1, q = (lane & 15) >> 2, pp = lane & 3;
    LAS char* aw[3][2];
#pragma unroll
    for (int c = 0; c < 6; ++c) {
      aw[c >> 1][c & 1] = smem + (c >> 1) * 65536 + kh * 8192 + 128 * (4 * h + q) + 64 * ((c & 1) ^ kh ^ (q >> 1)) + 32 * blk + 8 * pp;
      asm volatile("" : "+v"(aw[c >> 1][c & 1]));
    }
    f32x16 acc[2];
#pragma unroll
    for (int r = 0; r < 16; ++r) { acc[0][r] = 0.f; acc[1][r] = 0.f; }
    STAMP(25);
    BARRIER();
    constexpr int NW = G2_NW;
    u32x4 hf[NW], wf[3][2];
#define RD_W2C(i, c) do { LAS char* a_ = aw[w2_unit((i) >> 2) >> 2][c] + (w2_unit((i) >> 2) & 3) * UNIT + ((i) & 3) * 2048; wf[(i) % 3][c] = tr_frag(a_, a_ + 1024); } while (0)
#define RD_W2(i) do { RD_W2C(i, 0); RD_W2C(i, 1); } while (0)
    RD_W2(0); RD_W2(1);
    const int sh = WS_H + ((tbk * 2 + wt) * 112 + 56 * kh) * 1024;
#define LD_H(i) hf[(i) % NW] = ld_sc1(rws, lane * 16 + ((i) & 3) * 1024, sh + w2_phys((i) >> 2) * 4096)
    poll16(rws, ((lane & 1) * 32 + min(lane >> 1, 6)) * 128, WS_F3 + ((tbk >> 1) * 4 + (tbk & 1)) * 2048 + kh * 896, ta, tb);
    STAMP(26);
#pragma unroll
    for (int i = 0; i < G2_N0; ++i) LD_H(i);
    SCHED();
    u32 xr[8]; f32x2 gb[8];
#pragma unroll
    for (int i = 0; i < 8; ++i) {
      const int pr = 4 * (i >> 1) + 2 * h + (i & 1);
      xr[i] = *(LAS const u32*)(smem + LD_MISC + 2048 + w * 2048 + ((tl >> 2) & 1) * 1024 + pr * 64 + (tl >> 3) * 16 + (tl & 3) * 4);
      gb[i] = *(LAS const f32x2*)(smem + LD_MISC + 1536 + (kh * 16 + pr) * 8);
    }
    SCHED();
#pragma unroll
    for (int i = 0; i < 56; ++i) {
      SCHED();
      acc[0] = mfma32(wf[i % 3][0], hf[i % NW], acc[0]);
      SCHED();
      if (i == 0) STAMP(29);
      if (i == 18) STAMP(30);
      if (i == 42) STAMP(31);
      if (i == 6 || i == 18 || i == 42) BARRIER();
      if (i + 2 < 56) RD_W2C(i + 2, 0);
      if (h_next(i) < h_next(i + 1)) LD_H(h_next(i));
      SCHED();
      acc[1] = mfma32(wf[i % 3][1], hf[i % NW], acc[1]);
      SCHED();
      if (i + 2 < 56) RD_W2C(i + 2, 1);
      if (h_next(i) + 1 < h_next(i + 1)) LD_H(h_next(i) + 1);
      asm volatile("" :: "v"(wf[i % 3][0]), "v"(wf[i % 3][1]), "v"(hf[i % NW]));
    }
    STAMP(27);
    LAS f32x4* ex = (LAS f32x4*)(smem + w2_unit(5) * UNIT) + lane;
#pragma unroll
    for (int qq = 0; qq < 4; ++qq) ex[(w * 4 + qq) * 64] = f32x4{acc[1][4 * qq], acc[1][4 * qq + 1], acc[1][4 * qq + 2], acc[1][4 * qq + 3]};
    BARRIER();
    LAS char* ot = smem + w2_unit(6) * UNIT;
    f32x4 oth[4];
#pragma unroll
    for (int qq = 0; qq < 4; ++qq) oth[qq] = ex[((w ^ 2) * 4 + qq) * 64];
    SCHED();
#pragma unroll
    for (int qq = 0; qq < 4; ++qq) {
      const f32x4 o4 = oth[qq];
      u32x2 o;
#pragma unroll
      for (int z = 0; z < 2; ++z)
        o[z] = pk2((f32x2{acc[0][4 * qq + 2 * z], acc[0][4 * qq + 2 * z + 1]} + f32x2{o4[2 * z], o4[2 * z + 1]}) * gb[2 * qq + z] + up(xr[2 * qq + z]));
      *(LAS u32x2*)(ot + (wt * 32 + tl) * 144 + kh * 64 + qq * 16 + h * 8) = o;
    }
    BARRIER();
#pragma unroll
    for (int i = 0; i < 2; ++i) {
      const int row = (tid >> 3) + 32 * i, r = tbk * 64 + row;
      const int t = (r & ~511) + 128 * ((r >> 1) & 3) + 2 * ((r >> 3) & 63) + (r & 1);
      *(u32x4*)(p.out + (size_t)t * D + db * 64 + (tid & 7) * 8) = *(LAS const u32x4*)(ot + row * 144 + (tid & 7) * 16);
    }
    STAMP(28);
    STAMP_FLUSH(p.ws, 0);
  }
}

extern "C" __global__ void fused_block_end() {}

extern "C" void kernel_launch(void* const* d_in, const int* in_sizes, int n_in, void* d_out, int out_size, void* d_ws, size_t ws_size,
                              hipStream_t stream) {
  Params p;
  p.x = (const u16*)d_in[0]; p.Are = (const u16*)d_in[1]; p.Aim = (const u16*)d_in[2]; p.Bre = (const u16*)d_in[3];
  p.Bim = (const u16*)d_in[4]; p.Cre = (const u16*)d_in[5]; p.Cim = (const u16*)d_in[6]; p.ldt = (const u16*)d_in[7];
  p.dskip = (const u16*)d_in[8]; p.w1 = (const u16*)d_in[10]; p.w2 = (const u16*)d_in[11]; p.c = (const u16*)d_in[12];
  p.wmod = (const u16*)d_in[13]; p.out = (u16*)d_out; p.ws = (char*)d_ws;
  (void)hipFuncSetAttribute((const void*)fused_block, hipFuncAttributeMaxDynamicSharedMemorySize, LDS_BYTES + ST_LDS_EXTRA);
  fused_block<<<256, 512, LDS_BYTES + ST_LDS_EXTRA, stream>>>(p);
}
```

```cpp
#define OUT_AUX 2
#include <hip/hip_runtime.h>
#include <hip/hip_bf16.h>

#define STAMP_DECL
#define STAMP(s) __builtin_amdgcn_sched_barrier(0)
#define STAMP_FLUSH(ws, k)
#define ST_LDS_EXTRA 0
#define ST_RANGES {{0, 32, 256, "compute waves"}, {1, 32, 256, "loader waves"}, {0, 0, 32, "helper WGs, waves 0-3"}, {1, 0, 32, "helper WGs, waves 4-7"}}

typedef unsigned short u16;
typedef unsigned int u32;
typedef unsigned long long u64;
typedef u32 u32x2 __attribute__((ext_vector_type(2)));
typedef u32 u32x4 __attribute__((ext_vector_type(4)));
typedef float f32x2 __attribute__((ext_vector_type(2)));
typedef float f32x4 __attribute__((ext_vector_type(4)));
typedef float f32x16 __attribute__((ext_vector_type(16)));
typedef short s16x4 __attribute__((ext_vector_type(4)));
typedef __bf16 bf16x2 __attribute__((ext_vector_type(2)));
typedef __bf16 bf16x8 __attribute__((ext_vector_type(8)));
typedef __amdgpu_buffer_rsrc_t rsrc_t;
#define GAS __attribute__((address_space(1)))
#define LAS __attribute__((address_space(3)))
#define CAS __attribute__((address_space(4)))

constexpr int L = 2048, D = 512, NS = 128, F = 1792;

constexpr int WS_EPOCH = 0;
constexpr int WS_SG = 0x1000;
constexpr int WS_MG = 0x80000;
constexpr int WS_F2 = 0x40000;
constexpr int WS_F3 = 0x50000;
constexpr int WS_G2 = 0x70000;
constexpr int WS_S12 = 0x74000;
constexpr int WS_MF = 0x7c000;
constexpr int WS_XG = 2 << 20;
constexpr int WS_X1 = 0x440000;
constexpr int WS_X1S = 0x680000;
constexpr int WS_H = 9 << 20;
constexpr int WS_END = 16 << 20;
#define PADS 1
constexpr int ST_XG = 8192 + 128 * PADS, ST_MG = 1536 * 16 + 128 * PADS, ST_X1H = 32768 + 128 * PADS, ST_X1 = 2 * ST_X1H, ST_X1S = 131072 + 384 * PADS;
constexpr int OOB = 0x7ff00000;

constexpr int UNIT = 16384;
constexpr int LD_XR = 0, LD_WR = 4 * UNIT, LD_MISC = 9 * UNIT, LD_FLG = LD_MISC + 10240, LD_MV = LD_FLG + 32, LDS_BYTES = LD_MV + 64;
constexpr int TCOPY = 4160, PROW = 132;
constexpr int LD_T = 0, LD_U = 8 * TCOPY, LD_KD = LD_U + 2 * 4608, LD_P = LD_WR, LD_RED0 = 3 * UNIT, LD_RED1 = 8 * UNIT;
constexpr int KD_ZERO = 900, LD_XT = LD_RED0;
static_assert(LD_KD + 904 * 4 <= LD_RED0, "LDS map");
__host__ __device__ constexpr int w2_phys(int u) { return (u + 5) % 14; }
__host__ __device__ constexpr int w2_unit(int u) { return u % 9 == 8 ? 8 : (u % 9) / 2 + 4 * (u % 9 & 1); }
#define G2_NW 14
#define G2_N0 14
__host__ __device__ constexpr int h_next(int i) { int a = G2_N0 + 2 * i, b = i > 0 ? i - 1 + G2_NW : G2_NW; a = a < b ? a : b; return a < 56 ? a : 56; }
#define TF_PRE 4
#define F3_DELAY 10
#define XG_DELAY 0
#define G_EARLY 1
#define G_SIG 5
#define FT_AT 3
#define FT_DELAY 0
#define H_SLEEP 110
#define S1_SLEEP 2
#define F2_DELAY 16
#define POLL_SLEEP 2
#define LDNT(p) __builtin_nontemporal_load(p)
#define AUX_NT 2
#define WF ""
#define AUX_W 0
constexpr int SPIN_CAP = 1 << 15;
constexpr float LOG2E = 1.44269504088896341f;

__device__ __forceinline__ float bflo(u32 v) { return __uint_as_float(v << 16); }
__device__ __forceinline__ float bfhi(u32 v) { return __uint_as_float(v & 0xffff0000u); }
__device__ __forceinline__ u32 pk(float lo, float hi) {
  f32x2 v = {lo, hi};
  bf16x2 r = __builtin_convertvector(v, bf16x2);
  return __builtin_bit_cast(u32, r);
}
__device__ __forceinline__ float rb(float f) { return bflo(pk(f, f)); }
__device__ __forceinline__ float dot2(u32 a, u32 b, float c) {
  return __builtin_amdgcn_fdot2_f32_bf16(__builtin_bit_cast(bf16x2, a), __builtin_bit_cast(bf16x2, b), c, false);
}
__device__ __forceinline__ f32x4 mfma16(u32x4 a, u32x4 b, f32x4 c) {
  return __builtin_amdgcn_mfma_f32_16x16x32_bf16(__builtin_bit_cast(bf16x8, a), __builtin_bit_cast(bf16x8, b), c, 0, 0, 0);
}
__device__ __forceinline__ f32x16 mfma32(u32x4 a, u32x4 b, f32x16 c) {
  return __builtin_amdgcn_mfma_f32_32x32x16_bf16(__builtin_bit_cast(bf16x8, a), __builtin_bit_cast(bf16x8, b), c, 0, 0, 0);
}
template <int CTRL> __device__ __forceinline__ float dpp(float x) {
  return __builtin_bit_cast(float, __builtin_amdgcn_mov_dpp(__builtin_bit_cast(int, x), CTRL, 0xf, 0xf, true));
}
__device__ __forceinline__ float row_sum(float x) {
  x += dpp<0xB1>(x); x += dpp<0x4E>(x); x += dpp<0x124>(x); x += dpp<0x128>(x);
  return x;
}
__device__ __forceinline__ float fold32(float a, float b) {
  auto s = __builtin_amdgcn_permlane32_swap(__float_as_uint(a), __float_as_uint(b), false, false);
  return __uint_as_float(s[0]) + __uint_as_float(s[1]);
}
__device__ __forceinline__ float fold16(float a, float b) {
  auto s = __builtin_amdgcn_permlane16_swap(__float_as_uint(a), __float_as_uint(b), false, false);
  return __uint_as_float(s[0]) + __uint_as_float(s[1]);
}
__device__ __forceinline__ u32x4 mkrs(const void* p, u32 bytes) {
  const u64 a = (u64)p;
  return u32x4{(u32)a, (u32)(a >> 32) & 0xffffu, bytes, 0x00020000u};
}
__device__ __forceinline__ void dma4(u32x4 rs, int lds, int voff, int soff) {
  asm volatile("s_mov_b32 m0, %0\n\ts_nop 0\n\tbuffer_load_dword %1, %2, %3 offen lds" :: "s"(lds), "v"(voff), "s"(rs), "s"(soff) : "memory");
}
__device__ __forceinline__ void dma16_sc1(u32x4 rs, int lds, int voff, int soff) {
  asm volatile("s_mov_b32 m0, %0\n\ts_nop 0\n\tbuffer_load_dwordx4 %1, %2, %3 offen sc1 lds" :: "s"(lds), "v"(voff), "s"(rs), "s"(soff) : "memory");
}
#define DMA4_TEXT(F) "s_mov_b32 m0, %0\n\ts_nop 0\n\tbuffer_load_dwordx4 %1, %5, %6 offen" F " lds\n\tbuffer_load_dwordx4 %2, %5, %6 offen offset:1024" F " lds\n\t" \
                     "buffer_load_dwordx4 %3, %5, %6 offen offset:2048" F " lds\n\tbuffer_load_dwordx4 %4, %5, %6 offen offset:3072" F " lds"
__device__ __forceinline__ void dma4k(u32x4 rs, int lds, const int (&v)[4], int soff) {
  asm volatile(DMA4_TEXT(WF) :: "s"(lds), "v"(v[0]), "v"(v[1]), "v"(v[2]), "v"(v[3]), "s"(rs), "s"(soff) : "memory");
}
__device__ __forceinline__ void dma4k_sc1(u32x4 rs, int lds, const int (&v)[4], int soff) {
  asm volatile(DMA4_TEXT(" sc1") :: "s"(lds), "v"(v[0]), "v"(v[1]), "v"(v[2]), "v"(v[3]), "s"(rs), "s"(soff) : "memory");
}
__device__ __forceinline__ u32x4 ld_sc1(rsrc_t rs, int voff, int soff) { return __builtin_amdgcn_raw_buffer_load_b128(rs, voff, soff, 16); }
__device__ __forceinline__ void st_sc1(rsrc_t rs, int voff, int soff, u32x4 v) { __builtin_amdgcn_raw_buffer_store_b128(v, rs, voff + soff, 0, 16); }
__device__ __forceinline__ u32x4 tr_frag(LAS char* a0, LAS char* a1) {
  const s16x4 lo = __builtin_amdgcn_ds_read_tr16_b64_v4i16((LAS s16x4*)a0), hi = __builtin_amdgcn_ds_read_tr16_b64_v4i16((LAS s16x4*)a1);
  const u32x2 l = __builtin_bit_cast(u32x2, lo), h = __builtin_bit_cast(u32x2, hi);
  return u32x4{l[0], l[1], h[0], h[1]};
}
#define SCHED() __builtin_amdgcn_sched_barrier(0)
#define FENCE() asm volatile("" ::: "memory")
#define WAIT_VM(n) asm volatile("s_waitcnt vmcnt(" #n ")" ::: "memory")
#define BARRIER() do { asm volatile("s_waitcnt lgkmcnt(0)" ::: "memory"); __builtin_amdgcn_s_barrier(); FENCE(); } while (0)

__device__ __forceinline__ f32x2 cmul(f32x2 a, f32x2 b) {
  const f32x2 t = f32x2{a[0], a[0]} * b;
  return __builtin_elementwise_fma(f32x2{a[1], a[1]}, f32x2{-b[1], b[0]}, t);
}
__device__ __forceinline__ f32x2 cpow(float a2, float bq, float l) {
  const float m = __builtin_amdgcn_exp2f(a2 * l);
  const float ph = __builtin_amdgcn_fractf(bq * l);
  return f32x2{__builtin_amdgcn_cosf(ph), __builtin_amdgcn_sinf(ph)} * m;
}
__device__ __forceinline__ u32 pk2(f32x2 a) { return pk(a[0], a[1]); }
__device__ __forceinline__ f32x2 up(u32 v) { return f32x2{bflo(v), bfhi(v)}; }
__device__ __forceinline__ u32 ln_stat(float sum, float sq) {
  const float m = sum * (1.f / D);
  const float var = fmaxf(sq * (1.f / D) - m * m, 0.f);
  return pk(m, __builtin_amdgcn_sqrtf(rb(rb(var) + 1e-5f)));
}
__device__ __forceinline__ u32 tagdiff(u32x4 v, u32 ta, u32 tb) { return (v[1] ^ ta) | (v[3] ^ tb); }
__device__ __forceinline__ u32x4 poll16(rsrc_t rs, int voff, int soff, u32 ta, u32 tb) {
  u32x4 v;
  int spins = 0;
  for (;;) {
    FENCE();
    v = ld_sc1(rs, voff, soff);
    if (tagdiff(v, ta, tb) == 0u || ++spins >= SPIN_CAP) break;
    if (POLL_SLEEP) __builtin_amdgcn_s_sleep(POLL_SLEEP);
  }
  FENCE();
  return v;
}
__device__ __forceinline__ bool bad4(const u32x4 (&v)[4], u32 ta, u32 tb) {
  return (tagdiff(v[0], ta, tb) | tagdiff(v[1], ta, tb) | tagdiff(v[2], ta, tb) | tagdiff(v[3], ta, tb)) != 0u;
}
__device__ __forceinline__ void retry4(u32x4 (&v)[4], rsrc_t rs, int voff, int step, u32 ta, u32 tb) {
#pragma unroll 1
  for (int spins = 0; spins < SPIN_CAP; ++spins) {
    __builtin_amdgcn_s_sleep(S1_SLEEP);
    FENCE();
#pragma unroll
    for (int i = 0; i < 4; ++i) if (tagdiff(v[i], ta, tb)) v[i] = ld_sc1(rs, voff, i * step);
    if (!__any(bad4(v, ta, tb))) break;
  }
}
__device__ __forceinline__ void fold_cols(const float (&v)[16], LAS float* dst, int lane) {
  float u[8]; f32x4 t;
#pragma unroll
  for (int k = 0; k < 8; ++k) u[k] = fold32(v[k], v[k + 8]);
#pragma unroll
  for (int k = 0; k < 4; ++k) { const float a = fold16(u[k], u[k + 4]); t[k] = a + dpp<0x128>(a); }
  if (!(lane & 8)) *(LAS f32x4*)(dst + (lane & 7) * 16 + (lane >> 4) * 4) = t;
}
__device__ __forceinline__ f32x2 sum4(LAS const float* src, int stride) {
  return (*(LAS const f32x2*)src + *(LAS const f32x2*)(src + stride)) + (*(LAS const f32x2*)(src + 2 * stride) + *(LAS const f32x2*)(src + 3 * stride));
}
__device__ __forceinline__ u32 silu_bf(u32 c) {
  const float f = bflo(c), s = f * __builtin_amdgcn_rcpf(1.f + __builtin_amdgcn_exp2f(f * -LOG2E));
  return pk(s, s);
}
template <int CTRL> __device__ __forceinline__ void gv_row(u32x4 v, u32 sp, float (&acc)[8]) {
  const u32 sb = __builtin_amdgcn_mov_dpp(sp, CTRL, 0xf, 0xf, true), al = sb & 0xffffu, ah = sb & 0xffff0000u;
#pragma unroll
  for (int q = 0; q < 4; ++q) { acc[2 * q] = dot2(v[q], al, acc[2 * q]); acc[2 * q + 1] = dot2(v[q], ah, acc[2 * q + 1]); }
}
__device__ __forceinline__ f32x2 gv_fold(const float (&acc)[8]) {
  const float m0 = fold16(fold32(acc[0], acc[4]), fold32(acc[2], acc[6])), m1 = fold16(fold32(acc[1], acc[5]), fold32(acc[3], acc[7]));
  return f32x2{m0 + dpp<0x128>(m0), m1 + dpp<0x128>(m1)};
}
#define TOUCH(dst, voff, base) asm volatile("global_load_dword %0, %1, %2" : "=v"(dst) : "v"(voff), "s"(base))

__device__ __forceinline__ void quad_arrive(LAS char* f, int i, u32 n) {
  asm volatile("s_waitcnt lgkmcnt(0)" ::: "memory");
  *(volatile LAS u32*)(f + i * 4) = n;
}
__device__ __forceinline__ void quad_wait(LAS char* f, u32 n) {
  for (;;) {
    const u32x4 v = *(volatile LAS u32x4*)f;
    if (__builtin_amdgcn_readfirstlane(min(min(v[0], v[1]), min(v[2], v[3]))) >= n) break;
    __builtin_amdgcn_s_sleep(1);
  }
  FENCE();
}
__device__ __forceinline__ int only(bool c, int off) { return off | (c ? 0 : OOB); }
#define PIN(x) asm volatile("" : "+v"(x))

struct Params {
  const u16 *x, *Are, *Aim, *Bre, *Bim, *Cre, *Cim, *ldt, *dskip, *w1, *w2, *c, *wmod;
  u16* out;
  char* ws;
};

extern "C" __global__ void __launch_bounds__(512, 2) fused_block(Params p) {
  extern __shared__ __attribute__((aligned(16))) char smem_[];
  LAS char* smem = (LAS char*)smem_;
  const int tid = threadIdx.x, lane = tid & 63, w = __builtin_amdgcn_readfirstlane(tid >> 6), lw = w & 3;
  STAMP_DECL
  STAMP(0);
  *(LAS u32*)(smem + LD_FLG + w * 4) = 0u;
  BARRIER();
  asm volatile("" :: "s"(p.x), "s"(p.Are), "s"(p.Aim), "s"(p.Bre), "s"(p.Bim), "s"(p.Cre), "s"(p.Cim), "s"(p.ldt), "s"(p.dskip), "s"(p.w1), "s"(p.w2), "s"(p.c), "s"(p.wmod),
               "s"(p.out), "s"(p.ws));
#define SCAL_ISSUE(q) u64 epoch; u32 aux; \
  asm volatile("s_load_dwordx2 %0, %2, 0x0\n\ts_load_dword %1, %3, 0x0" : "=&s"(epoch), "=&s"(aux) : "s"(p.ws + WS_EPOCH), "s"(q)); SCHED()
#define SCAL_WAIT() asm volatile("s_waitcnt lgkmcnt(0)" : "+s"(epoch), "+s"(aux)); \
  epoch += 0x9E3779B97F4A7C15ull; ta = (u32)epoch; tb = (u32)(epoch >> 32); dsk = aux
#define ROLES() int b = blockIdx.x; asm volatile("" : "+s"(b)); \
  const int xcd = b & 7, j = b >> 3, pair = xcd * 32 + j, d0 = pair * 2; \
  const bool helper = j < 4; \
  const int rbk = (xcd >> 1) * 4 + (j & 3), cbk = (xcd & 1) * 7 + ((j - 4) >> 2), tbk = xcd * 4 + (j & 3), db = ((j >> 2) + 3 - (int)((u64)p.w2 >> 7)) & 7; \
  const rsrc_t rws = __builtin_amdgcn_make_buffer_rsrc(p.ws, 0, WS_END, 0x00020000)

  if (__builtin_expect(w >= 4, 1)) {
    LAS char* flg = smem + LD_FLG + 16;
    u32x4 sg[4];
    u32x2 xg[4];
    ROLES();
    const int go = lw * 4096 + lane * 16;
    u32 ta, tb, dsk;
    const u32x4 qws = mkrs(p.ws, WS_END);
#define POLL_ISSUE() asm volatile("buffer_load_dwordx4 %0, %4, %5, 0 offen sc1\n\tbuffer_load_dwordx4 %1, %4, %5, 0 offen offset:1024 sc1\n\t" \
      "buffer_load_dwordx4 %2, %4, %5, 0 offen offset:2048 sc1\n\tbuffer_load_dwordx4 %3, %4, %5, 0 offen offset:3072 sc1" \
      : "=&v"(sg[0]), "=&v"(sg[1]), "=&v"(sg[2]), "=&v"(sg[3]) : "v"(go + WS_SG), "s"(qws) : "memory")
    int vx[4], vw1[4], vw2[4], so, xo0, xo1, fo, mfo;
    LAS char *ra[2], *rc[2];
    auto addresses = [&]() {
      const int hm = (j - 4) >> 31;
#pragma unroll
      for (int i = 0; i < 4; ++i) {
        const int chk = 16 * ((lane & 15) ^ ((lane >> 4) << 2) ^ i);
        vx[i] = (lane >> 4) * 256 + chk;
        vw1[i] = (lane >> (4 + hm)) * (F * 2) + ((chk & ~hm) | ((lane & 7) * 16 & hm)) + i * ((4 + (4 & hm)) * F * 2 - 1024);
        vw2[i] = (lane >> 3) * 1024 + db * 128 + 16 * ((lane & 7) ^ (((lane >> 4) & 1) << 2)) + i * 7168;
        PIN(vx[i]); PIN(vw1[i]); PIN(vw2[i]);
      }
      const int g = (lane >> 1) & 3;
      ra[0] = smem + LD_RED0 + (2 * lw * 64 + 16 * g + ((lane >> 3) ^ (4 * g))) * 16 + 8 * (lane & 1);
      ra[1] = smem + LD_RED0 + (2 * lw * 64 + 16 * g + ((lane >> 3) ^ (4 * g) ^ 8)) * 16 + 8 * (lane & 1);
      rc[0] = ra[0] + (LD_RED1 - LD_RED0); rc[1] = ra[1] + (LD_RED1 - LD_RED0);
      so = WS_X1S + (lw * 4 + (lane >> 4)) * ST_X1S + d0 * 256 + (lane & 15) * 16;
      xo0 = WS_X1 + (lw * 8 + (lane >> 3)) * ST_X1 + pair * 128 + (lane & 7) * 16; xo1 = xo0 + ST_X1H;
      fo = only(lane < 4, WS_F2 + (lw * 256 + pair) * 64 + lane * 16);
      mfo = only((tid >> 1) == 248, WS_MF + (~lane & 1) * 4096 + pair * 16);
      PIN(mfo); PIN(ra[0]); PIN(ra[1]); PIN(rc[0]); PIN(rc[1]); PIN(so); PIN(xo0); PIN(xo1); PIN(fo);
      SCHED();
    };
    f32x2 xf[4][2], hf[4][2], ca[4][2];
    u32x4 mv1, mv2;
    auto coefficients = [&]() {
      __builtin_amdgcn_s_setprio(2);
      STAMP(7);
      if (__builtin_expect(__any(bad4(sg, ta, tb)), 0)) retry4(sg, rws, go + WS_SG, 1024, ta, tb);
      float rs[4][2];
#pragma unroll
      for (int i = 0; i < 4; ++i)
#pragma unroll
        for (int e = 0; e < 2; ++e) { rs[i][e] = __builtin_amdgcn_rcpf(bfhi(sg[i][2 * e])); PIN(rs[i][e]); }
      quad_wait(flg, 3u);
      const u32x4 mv0 = *(LAS const u32x4*)(smem + LD_MV);
      mv1 = *(LAS const u32x4*)(smem + LD_MV + 32); mv2 = *(LAS const u32x4*)(smem + LD_MV + 48);
      if (lw < 2) {
#pragma unroll
        for (int i = 0; i < 4; ++i) xg[i] = *(LAS const u32x2*)(smem + LD_XT + lw * 2048 + i * 512 + lane * 8);
      }
      const f32x2 sa = up(mv0[0]), g1 = up(mv0[3]);
#pragma unroll
      for (int i = 0; i < 4; ++i)
#pragma unroll
        for (int e = 0; e < 2; ++e) {
          ca[i][e] = g1 * rs[i][e];
          hf[i][e] = sa - ca[i][e] * bflo(sg[i][2 * e]);
          PIN(ca[i][e]); PIN(hf[i][e]);
        }
      STAMP(8);
    };
    if (lw >= 2) {
      u32 xt;
      TOUCH(xt, (j * 32 + (lw & 1) * 16 + (lane & 15)) * 1024 + xcd * 128, p.x);
      SCAL_ISSUE(p.dskip + d0);
      addresses();
      const u32x4 qx = mkrs(p.x, L * D * 2);
      SCAL_WAIT();
      quad_arrive(flg, lw, 1u);
#pragma unroll
      for (int i = 0; i < 8; ++i) {
        if (i == G_EARLY) quad_wait(flg, 1u);
        if (i == G_SIG) *(volatile LAS u32*)(flg + lw * 4) = 2u;
        dma4(qx, LD_XT + (lw & 1) * 2048 + i * 256, lane * 1024 + pair * 4, ((lw & 1) * 8 + i) * 65536);
        WAIT_VM(2);
      }
      if (XG_DELAY) __builtin_amdgcn_s_sleep(XG_DELAY);
      STAMP(3);
      POLL_ISSUE();
      const int xo = WS_XG + ((lw & 1) * 128 + (lane >> 1)) * ST_XG + pair * 32 + (lane & 1) * 16;
      u32x4 xe[4];
#pragma unroll
      for (int i = 0; i < 4; ++i) xe[i] = ld_sc1(rws, xo, i * 32 * ST_XG);
      asm volatile("s_waitcnt vmcnt(8)" :: "v"(xt) : "memory");
      quad_arrive(flg, lw, 3u);
      asm volatile("s_waitcnt vmcnt(4)" : "+v"(sg[0]), "+v"(sg[1]), "+v"(sg[2]), "+v"(sg[3]) :: "memory");
      coefficients();
      if (__builtin_expect(__any(bad4(xe, ta, tb)), 0)) retry4(xe, rws, xo, 32 * ST_XG, ta, tb);
#pragma unroll
      for (int i = 0; i < 4; ++i) xg[i] = u32x2{xe[i][0], xe[i][2]};
    } else {
      u32x4 xv[4], wm[8];
      const int gcb = (b + (b >> 4)) & 15;
#pragma unroll
      for (int i = 0; i < 4; ++i) xv[i] = LDNT((const u32x4*)(p.x + (size_t)(lw * 1024 + b * 4 + i) * D) + lane);
      SCHED();
      SCAL_ISSUE(p.dskip + d0);
      const u32 c0 = p.c[(b >> 4) * 32 + (lane >> 3) + 8 * (lane & 3)];
      SCHED();
      {
        const rsrc_t ra = __builtin_amdgcn_make_buffer_rsrc((void*)p.wmod, 0, D * 3072 * 2, 0x00020000);
        const rsrc_t rb_ = __builtin_amdgcn_make_buffer_rsrc((void*)p.wmod, 0, lw == 0 ? D * 3072 * 2 : 0, 0x00020000);
#pragma unroll
        for (int i = 0; i < 8; ++i)
          wm[i] = __builtin_amdgcn_raw_buffer_load_b128(i < 4 ? ra : rb_, (lane >> 3) * 6144 + (lane & 7) * 16,
                                                        ((b >> 4) * 32 + 8 * (i & 3)) * 6144 + ((lw + (i >> 2) * 2) * 16 + gcb) * 128, AUX_NT);
      }
      SCHED();
      *(LAS u32x4*)(smem + LD_U + (lane >> 5) * 4608 + (lane & 31) * 16) = u32x4{0u, 0u, 0u, 0u};
      int so_ = only(!(lane & 31), WS_SG + lw * 8192 + b * 32 + (lane >> 5) * 16);
      int mo_ = only(!(lane & 8), WS_MG + (b >> 4) * ST_MG + ((lw * 16 + gcb) * 32 + (lane >> 4) * 8 + (lane & 7)) * 16);
      PIN(so_); PIN(mo_);
      SCAL_WAIT();
      __builtin_amdgcn_s_setprio(3);
      STAMP(1);
      LAS char* xw = smem + LD_XT + 4096;
      if (lw) {
#pragma unroll
        for (int i = 0; i < 4; ++i) *(LAS u32x4*)(xw + i * 1056 + lane * 16) = xv[i];
      }
      float s[4], q[4];
#pragma unroll
      for (int i = 0; i < 4; ++i) {
        s[i] = 0.f; q[i] = 0.f;
#pragma unroll
        for (int z = 0; z < 4; ++z) { s[i] = dot2(xv[i][z], 0x3f803f80u, s[i]); q[i] = dot2(xv[i][z], xv[i][z], q[i]); }
      }
      const u32 st = ln_stat(row_sum(fold16(fold32(s[0], s[2]), fold32(s[1], s[3]))), row_sum(fold16(fold32(q[0], q[2]), fold32(q[1], q[3]))));
      const auto sw = __builtin_amdgcn_permlane16_swap(st, st, false, false);
      st_sc1(rws, so_, 0, u32x4{sw[0], ta, sw[1], tb});
      STAMP(2);
      auto gv_tile = [&](int z, u32 sp) {
        float acc[8] = {0.f, 0.f, 0.f, 0.f, 0.f, 0.f, 0.f, 0.f};
        gv_row<0x00>(wm[4 * z], sp, acc); gv_row<0x55>(wm[4 * z + 1], sp, acc); gv_row<0xAA>(wm[4 * z + 2], sp, acc); gv_row<0xFF>(wm[4 * z + 3], sp, acc);
        const f32x2 m = gv_fold(acc);
        st_sc1(rws, mo_ + z * 16384, 0, u32x4{__float_as_uint(m[0]), ta, __float_as_uint(m[1]), tb});
        SCHED();
      };
      if (lw) {
        u32 a[8][2];
#pragma unroll
        for (int i = 0; i < 8; ++i) {
          LAS const u32* src = (LAS const u32*)(xw + (lane & 1) * 2112 + i * 128 + (lane >> 1) * 4);
          a[i][0] = src[0]; a[i][1] = src[264];
        }
#pragma unroll
        for (int i = 0; i < 8; ++i) st_sc1(rws, WS_XG + b * ST_XG + lane * 16 + (i >> 2) * 4096, (i & 3) * 1024, u32x4{a[i][0], ta, a[i][1], tb});
        SCHED();
        gv_tile(0, silu_bf(c0));
        PIN(wm[4]); PIN(wm[5]); PIN(wm[6]); PIN(wm[7]);
      } else {
        const u32 sp = silu_bf(c0);
        gv_tile(0, sp);
        gv_tile(1, sp);
      }
      __builtin_amdgcn_s_setprio(0);
      quad_arrive(flg, lw, 2u);
      STAMP(4);
      addresses();
      quad_wait(flg, 2u);
      const int mo = WS_MG + (lane & 15) * ST_MG + ((((lane >> 4) & (2 * lw + 1)) + 2 * lw) * 256 + (pair >> 5) * 32 + (pair & 3) * 8 + ((pair >> 2) & 7)) * 16;
      u32x4 mg;
      asm volatile("buffer_load_dwordx4 %0, %1, %2, 0 offen sc1" : "=v"(mg) : "v"(mo), "s"(qws) : "memory");
      POLL_ISSUE();
      asm volatile("s_waitcnt vmcnt(4)" : "+v"(mg) :: "memory");
      if (__builtin_expect(__any(tagdiff(mg, ta, tb) != 0u), 0)) {
        asm volatile("s_waitcnt vmcnt(0)" : "+v"(sg[0]), "+v"(sg[1]), "+v"(sg[2]), "+v"(sg[3]) :: "memory");
        mg = poll16(rws, mo, 0, ta, tb);
      }
      const u32 mb = pk(row_sum(__uint_as_float(mg[0])), row_sum(__uint_as_float(mg[2])));
      *(LAS u32x2*)(smem + LD_MV + lw * 32 + (lane >> 4) * 8) = u32x2{mb, pk2(up(mb) + 1.f)};
      quad_arrive(flg, lw, 3u);
      asm volatile("s_waitcnt vmcnt(0)" : "+v"(sg[0]), "+v"(sg[1]), "+v"(sg[2]), "+v"(sg[3]) :: "memory");
      coefficients();
    }
    const u32x4 qw1 = mkrs(p.w1, D * F * 2), qw2 = mkrs(p.w2, D * F * 2);
    const int sw2 = ((lw >> 1) * 896 + (lw & 1) * 32) * 1024;
    auto w2_stage = [&](int u) { dma4k(qw2, w2_unit(u) * UNIT + lw * 4096, vw2, sw2 + w2_phys(u) * 65536); };
    const int wstep = (helper ? 32 : 16) * F * 2, wbase = helper ? min(tbk, 27) * 128 : cbk * 256;
    auto w1_stage = [&](int s) { dma4k(qw1, LD_WR + (s & 3) * UNIT + lw * 4096, vw1, (s * 4 + lw) * wstep + wbase); };
    auto x_stage = [&](int s) { dma4k_sc1(qws, LD_XR + (s & 3) * UNIT + lw * 4096, vx, WS_X1S + rbk * ST_X1S + s * 16384 + lw * 4096); };
    *(volatile LAS u32*)(flg + lw * 4) = 4u;
    STAMP(9);
#pragma unroll
    for (int i = 0; i < 4; ++i) {
#pragma unroll
      for (int e = 0; e < 2; ++e) { xf[i][e] = up(xg[i][e]); hf[i][e] += xf[i][e] * ca[i][e]; }
      LAS u32* U = (LAS u32*)(smem + LD_U + 512 + lw * 1024 + i * 256) + lane;
      U[0] = pk(hf[i][0][0], hf[i][1][0]);
      U[4608 / 4] = pk(hf[i][0][1], hf[i][1][1]);
    }
    STAMP(10);
    BARRIER();
    STAMP(11);
    w1_stage(0); w1_stage(1); w1_stage(2); w1_stage(3);
    const f32x2 gav = up(mv1[0]), gcv = up(mv2[1]);
    f32x2 xc[4][2];
    float gd[2] = {gav[0] * bflo(dsk), gav[1] * bfhi(dsk)}, n0 = bflo(mv2[2]) * (-1.f / LOG2E), n1 = bfhi(mv2[2]) * (-1.f / LOG2E);
    u32 k0 = __builtin_amdgcn_perm(mv1[2], mv2[1], 0x05040100u), k1 = __builtin_amdgcn_perm(mv1[2], mv2[1], 0x07060302u);
    PIN(gd[0]); PIN(gd[1]); PIN(n0); PIN(n1); PIN(k0); PIN(k1);
    const u32x4 mfv = {lane & 1 ? k0 : __float_as_uint(n0), ta, lane & 1 ? k1 : __float_as_uint(n1), tb};
    st_sc1(rws, mfo, 0, mfv);
#pragma unroll
    for (int i = 0; i < 4; ++i)
#pragma unroll
      for (int c = 0; c < 2; ++c) {
        float a0 = __builtin_fmaf(hf[i][0][c], gd[c], xf[i][0][c]), a1 = __builtin_fmaf(hf[i][1][c], gd[c], xf[i][1][c]);
        PIN(a0); PIN(a1);
        xc[i][c] = f32x2{a0, a1};
      }
    STAMP(12);
    SCHED();
    u32x4 wr[20];
    auto wr_load = [&](bool on) {
      const rsrc_t rw2 = __builtin_amdgcn_make_buffer_rsrc((void*)p.w2, 0, on ? D * F * 2 : 0, 0x00020000);
#pragma unroll
      for (int i = 0; i < 20; ++i) wr[i] = __builtin_amdgcn_raw_buffer_load_b128(rw2, vw2[i & 3] + (i & 3) * 1024, sw2 + (i / 4) * 65536, AUX_W);
    };
    wr_load(!helper);
    SCHED();
    STAMP(13);
    BARRIER();
    STAMP(14);
    {
      u32x4 o[2], s0, s1;
      f32x2 yp[4][4], c0[4], c1[4];
#pragma unroll
      for (int i = 0; i < 4; ++i) {
        yp[i][0] = *(volatile LAS const f32x2*)(ra[i & 1] + (i >> 1) * 1024); yp[i][1] = *(volatile LAS const f32x2*)(ra[i & 1] + (i >> 1) * 1024 + 8192);
      }
#pragma unroll
      for (int i = 0; i < 4; ++i) {
        yp[i][2] = *(volatile LAS const f32x2*)(rc[i & 1] + (i >> 1) * 1024); yp[i][3] = *(volatile LAS const f32x2*)(rc[i & 1] + (i >> 1) * 1024 + 8192);
      }
      SCHED();
#pragma unroll
      for (int i = 0; i < 4; ++i) { c0[i] = (yp[i][0] + yp[i][1]) * gav[0] + xc[i][0]; s0[i] = pk2(c0[i] * gcv[0]); }
      st_sc1(rws, so, 0, s0);
      SCHED();
#pragma unroll
      for (int i = 0; i < 4; ++i) { c1[i] = (yp[i][2] + yp[i][3]) * gav[1] + xc[i][1]; s1[i] = pk2(c1[i] * gcv[1]); }
      st_sc1(rws, so, 256, s1);
      SCHED();
#pragma unroll
      for (int i = 0; i < 4; ++i) { o[i >> 1][2 * (i & 1)] = pk(c0[i][0], c1[i][0]); o[i >> 1][2 * (i & 1) + 1] = pk(c0[i][1], c1[i][1]); }
      st_sc1(rws, xo0, 0, o[0]);
      st_sc1(rws, xo1, 0, o[1]);
    }
    STAMP(15);
    WAIT_VM(0);
    STAMP(17);
    st_sc1(rws, fo, 0, u32x4{ta, ta, tb, tb});
    __builtin_amdgcn_s_setprio(0);
    const int kind = helper ? 3 : lw;
    int e0 = WS_G2 + rbk * 1024, e1 = WS_S12 + (cbk * 128 + (kind - 1) * 64) * 16, e3 = WS_MF + 4096 + db * 512;
    asm volatile("" : "+s"(e0), "+s"(e1), "+s"(e3));
    const bool mid = (kind + 1) & 2;
    const int eo = (mid ? e1 : kind ? e3 : e0) + (lane & (kind == 3 ? 31 : 63)) * 16;
    LAS char* ea = smem + LD_MISC + (mid ? 256 + kind * 256 : kind * 512) + lane * (mid ? 4 : 8);
    u32x4 ev;
    if (helper) {
      BARRIER();
      ev = poll16(rws, eo, 0, ta, tb);
      *(LAS u32*)ea = ev[0]; *(LAS u32*)(ea + 4) = ev[2];
      BARRIER();
      __builtin_amdgcn_s_sleep(20);
      w2_stage(1); w2_stage(2); w2_stage(3);
      BARRIER(); BARRIER();
      w2_stage(0); w2_stage(4); w2_stage(5);
      wr_load(true);
    } else {
      __builtin_amdgcn_s_sleep(F2_DELAY);
      poll16(rws, ((lane >> 3) * 32 + lw * 8 + (lane & 7)) * 64, WS_F2 + (xcd >> 1) * 16384, ta, tb);
      STAMP(19);
      x_stage(0); x_stage(1); WAIT_VM(4); BARRIER();
      x_stage(2); x_stage(3); WAIT_VM(8); BARRIER();
      x_stage(4); w1_stage(4); WAIT_VM(12); BARRIER();
#pragma unroll 1
      for (int s = 5; s < 8; ++s) { x_stage(s); w1_stage(s); WAIT_VM(16); BARRIER(); }
      w2_stage(0); w2_stage(1); WAIT_VM(16); BARRIER();
      asm volatile("buffer_load_dwordx4 %0, %1, %2, 0 offen sc1" : "=v"(ev) : "v"(eo), "s"(qws) : "memory");
      w2_stage(2); w2_stage(3); WAIT_VM(17); BARRIER();
      asm volatile("s_waitcnt vmcnt(8)" : "+v"(ev) :: "memory");
      if (__any(tagdiff(ev, ta, tb) != 0u)) ev = poll16(rws, eo, 0, ta, tb);
      *(LAS u32*)ea = ev[0]; *(LAS u32*)(ea + (mid ? 512 : 4)) = ev[2];
      w2_stage(4); w2_stage(5); BARRIER();
    }
    STAMP(22);
    w2_stage(6); w2_stage(7); w2_stage(8);
    {
      const int xs = WS_X1 + tbk * ST_X1 + (db * 32 + (lw >> 1) * 16) * 128 + (lw & 1) * 64;
      dma16_sc1(qws, LD_MISC + 2048 + lw * 2048, (lane >> 2) * 128 + (lane & 3) * 16, xs);
      dma16_sc1(qws, LD_MISC + 3072 + lw * 2048, (lane >> 2) * 128 + (lane & 3) * 16, xs + ST_X1H);
    }
    WAIT_VM(0);
    STAMP(25);
    BARRIER();
    BARRIER();
#pragma unroll
    for (int i = 0; i < 20; ++i) {
      if (i == 8) BARRIER();
      *(LAS u32x4*)(smem + w2_unit(9 + i / 4) * UNIT + lw * 4096 + (i & 3) * 1024 + lane * 16) = wr[i];
    }
    BARRIER();
    if (b == 0 && tid == 256) *(u32x2*)(p.ws + WS_EPOCH) = u32x2{ta, tb};
    STAMP(26);
    STAMP_FLUSH(p.ws, 1);
    return;
  }

  ROLES();
  const int pch = w >> 1, pn = (w & 1) * 64 + lane, po = (d0 + pch) * NS + pn;
  u32 rar = LDNT(p.Are + po), rai = LDNT(p.Aim + po), rbr = LDNT(p.Bre + po), rbi = LDNT(p.Bim + po), rcr = LDNT(p.Cre + po), rci = LDNT(p.Cim + po);
  SCHED();
  SCAL_ISSUE(p.ldt + d0);
  {
    u32 cp0;
    {
      u64 pc; int r0, r1;
      asm volatile("s_getpc_b64 %0\n\ts_mov_b32 %1, fused_block@rel32@lo+4\n\ts_mov_b32 %2, fused_block_end@rel32@lo+12" : "=s"(pc), "=s"(r0), "=s"(r1));
      TOUCH(cp0, min(((w * 4 + (lane & 3)) * 32 + j) * 128, r1 - r0), pc + (long long)r0);
    }
    LAS char* flg = smem + LD_FLG;
    *(LAS u32x4*)(smem + LD_T + ((lane >> 1) & 7) * TCOPY + 4096 + (lane & 1) * 16) = u32x4{0u, 0u, 0u, 0u};
    *(LAS u32*)(smem + LD_KD + KD_ZERO * 4) = 0u;
    asm volatile("s_waitcnt lgkmcnt(0)" : "+s"(epoch), "+s"(aux));
    int ga[11], ka[4];
#pragma unroll
    for (int i = 0; i < 11; ++i) {
      const int l = 2047 - (tid * 8 + i);
      const int bits = (int)(pk((float)l, 0.f) & 0xffffu);
      const int idx = l < 128 ? l : bits - 0x4300 + 128;
      ga[i] = LD_KD + 4 * (l < 0 ? KD_ZERO : idx + (idx >> 3));
    }
    {
      const int e = lane & 15, g = lane >> 4;
      const u32 cfg = e < 8 ? (w == 0 ? 0x100004u : w == 1 ? 0x080004u : 0x022001u) : (w == 0 ? 0x100004u : w == 1 ? 0x044002u : 0x088003u);
      const int sh = cfg & 15, base = 256 * w + ((cfg >> 8) & 0xff) * (e >> sh) + (e & ((1 << sh) - 1));
#pragma unroll
      for (int r = 0; r < 4; ++r) { const int idx = base + (int)(cfg >> 16) * (4 * g + r); ka[r] = LD_KD + 4 * (idx + (idx >> 3)); PIN(ka[r]); }
    }
    STAMP(1);
    asm volatile("" : "+v"(rar), "+v"(rai), "+v"(rbr), "+v"(rbi), "+v"(rcr), "+v"(rci) : "v"(cp0));
    STAMP(2);
    SCHED();
    volatile LAS u32* P = (LAS u32*)(smem + LD_P) + pch * 64 * PROW + pn;
    const float dt = rb(__builtin_amdgcn_exp2f((pch ? bfhi(aux) : bflo(aux)) * LOG2E));
    const float a2 = (dt * bflo(rar)) * LOG2E, bq = (dt * bflo(rai)) * 0.15915494309189535f;
    const f32x2 z1 = cpow(a2, bq, 1.f), z8 = cpow(a2, bq, 8.f), z256 = cpow(a2, bq, 256.f);
    const f32x2 z16c = cmul(z8, z8) * f32x2{1.f, -1.f}, z512 = cmul(z256, z256), z768 = cmul(z512, z256);
    f32x2 u = cpow(a2, bq, 1024.f), q = {1.f, 0.f};
    f32x2 r = cmul(f32x2{bflo(rcr), -bflo(rci)}, f32x2{bflo(rbr), -bflo(rbi)} * dt);
#pragma unroll
    for (int e = 0; e < 16; ++e) {
      const f32x2 rn = cmul(r, z16c), qn = cmul(q, z1);
      P[e * PROW] = pk2(r);
      P[(16 + e) * PROW] = pk2(q);
      if (!(e & 1)) P[(32 + e / 2) * PROW] = pk2(cmul(q, z256));
      if (!(e & 3)) { P[(40 + e / 4) * PROW] = pk2(cmul(q, z512)); P[(44 + e / 4) * PROW] = pk2(cmul(q, z768)); }
      if ((e & 3) == 1) { P[(48 + e / 2) * PROW] = pk2(u); P[(49 + e / 2) * PROW] = pk2(cmul(u, z8)); }
      if ((e & 3) == 3) u = cmul(u, z256);
      r = rn; q = qn;
    }
    P[56 * PROW] = pk2(u);
    STAMP(3);
    quad_arrive(flg, w, 1u);
#pragma unroll
    for (int i = 0; i < 11; ++i) PIN(ga[i]);
    quad_wait(flg, 1u);
    STAMP(4);
    if (w < 3) {
      LAS const u32* fb = (LAS const u32*)(smem + LD_P) + (lane & 15) * PROW + ((lane >> 4) & 1) * 64 + (lane >> 5) * 32;
      u32x4 fa[16], fc[16];
      f32x4 acc[2] = {{0.f, 0.f, 0.f, 0.f}, {0.f, 0.f, 0.f, 0.f}};
#pragma unroll
      for (int ks = 0; ks < 22; ++ks) {
        if (ks < 16) {
          const int o = (ks & 1) * 64 * PROW + (ks >> 1) * 4;
          fa[ks] = *(LAS const u32x4*)(fb + o); fc[ks] = *(LAS const u32x4*)(fb + (w + 1) * 16 * PROW + o);
        }
        SCHED();
        if (ks >= 6) acc[ks & 1] = mfma16(fa[ks - 6], fc[ks - 6], acc[ks & 1]);
        SCHED();
      }
#pragma unroll
      for (int r4 = 0; r4 < 4; ++r4) *(LAS u32*)(smem + ka[r4]) = pk(acc[0][r4], acc[1][r4]);
    }
    STAMP(5);
    quad_arrive(flg, w, 2u);
    quad_wait(flg, 2u);
    STAMP(6);
    u32 e[11];
#pragma unroll
    for (int i = 0; i < 11; ++i) e[i] = *(LAS const u32*)(smem + ga[i]);
#pragma unroll
    for (int c = 0; c < 2; ++c)
#pragma unroll
      for (int s = 0; s < 4; ++s) {
        u32x4 o;
#pragma unroll
        for (int z = 0; z < 4; ++z) o[z] = __builtin_amdgcn_perm(e[s + 2 * z + 1], e[s + 2 * z], c ? 0x07060302u : 0x05040100u);
        *(LAS u32x4*)(smem + LD_T + (c * 4 + s) * TCOPY + tid * 16) = o;
      }
    quad_arrive(flg, w, 3u);
  }
  epoch += 0x9E3779B97F4A7C15ull;
  const u32 ta = (u32)epoch, tb = (u32)(epoch >> 32);
  {
    const int r = lane & 15, g = lane >> 4, ch = w >> 1, half = w & 1;
    LAS const char* Tb = smem + LD_T + (ch * 4 + ((3 - r) & 3)) * TCOPY + 2 * (2028 - 4 * (r >> 2) + 8 * g - 128 * half) - 7 * 512 - 192;
    LAS const char* Ub = smem + LD_U + ch * 4608 + 2 * (256 + 16 * r - 16 + 8 * g - 128 * half) - 192;
    LAS f32x4* red = (LAS f32x4*)(smem + (ch ? LD_RED1 : LD_RED0)) + half * 512 + (lane ^ ((lane >> 4) << 2));
    PIN(Ub); PIN(red);
    f32x4 acc[8];
#pragma unroll
    for (int n = 0; n < 8; ++n) acc[n] = f32x4{0.f, 0.f, 0.f, 0.f};
    u32x4 uf[2][8], tf[4][8];
    quad_wait(smem + LD_FLG, 3u);
#define LD_TH(e, a, hh) do { const u32x2 l_ = *(volatile LAS const u32x2*)(Tb + 64 * (3 - (e)) + 512 * (7 - (a)) + 8 * (hh)); tf[e][a][2 * (hh)] = l_[0]; tf[e][a][2 * (hh) + 1] = l_[1]; } while (0)
#pragma unroll
    for (int e = 0; e < TF_PRE; ++e)
#pragma unroll
      for (int a = 0; a < 8; ++a) { LD_TH(e, a, 0); LD_TH(e, a, 1); }
#define LD_UF(n, e) uf[(e) & 1][n] = *(LAS const u32x4*)(Ub + 64 * (3 - (e)) + 512 * (n))
#define CM(e, a, m) do { SCHED(); acc[(a) + (m)] = mfma16(tf[e][a], uf[(e) & 1][m], acc[(a) + (m)]); SCHED(); } while (0)
    STAMP(10);
    u32 ft;
    quad_wait(smem + LD_FLG + 16, FT_AT);
    __builtin_amdgcn_s_sleep(FT_DELAY);
    TOUCH(ft, ((b * 2 + (w & 1)) * 28 + min(lane, 27)) * 128, w < 2 ? p.w1 : p.w2);
    BARRIER();
    STAMP(11);
#pragma unroll
    for (int n = 0; n < 8; ++n) LD_UF(n, 0);
#pragma unroll
    for (int e = 0; e < 3; ++e) {
      int cnt = 0;
#pragma unroll
      for (int m = 0; m < 8; ++m)
#pragma unroll
        for (int a = 0; a + m < 8; ++a) {
          CM(e, a, m);
          if (cnt >= 4 && cnt < 12) LD_UF(cnt - 4, e + 1);
          if (cnt >= 12 && cnt < 28 && e + TF_PRE < 4) LD_TH(e + TF_PRE, (cnt - 12) >> 1, (cnt - 12) & 1);
          ++cnt;
        }
    }
#pragma unroll
    for (int n = 0; n < 8; ++n)
#pragma unroll
      for (int a = 0; a <= n; ++a) { CM(3, a, n - a); if (a == 1) red[(n - 1) * 64] = acc[n - 1]; }
    STAMP(12);
    red[7 * 64] = acc[7];
    BARRIER();
    STAMP(14);
    WAIT_VM(0);
    asm volatile("" :: "v"(ft));
  }
  if (helper) {
    const int hb = xcd * 4 + j;
    u32x4 v[16];
    const int rg = tid >> 3;
    LAS float* rd = (LAS float*)(smem + 4096);
    { const u32x4 m = poll16(rws, tid * 16, WS_MF, ta, tb); *(LAS u32x2*)(smem + tid * 8) = u32x2{m[0], m[2]}; }
    BARRIER();
#pragma unroll
    for (int i = 0; i < 16; ++i) v[i] = *(LAS const u32x4*)(smem + LD_WR + i * 4096 + tid * 16);
    float a[16];
#pragma unroll
    for (int e = 0; e < 16; ++e) a[e] = 0.f;
#pragma unroll
    for (int i = 0; i < 16; ++i) {
      const u32 gs = *(LAS const u32*)(smem + (i * 32 + rg) * 4);
      const u32 gl = gs & 0xffffu, gh = gs << 16, sl = gs >> 16, sh = gs & 0xffff0000u;
#pragma unroll
      for (int z = 0; z < 4; ++z) {
        a[4 * z] = dot2(v[i][z], gl, a[4 * z]); a[4 * z + 1] = dot2(v[i][z], sl, a[4 * z + 1]);
        a[4 * z + 2] = dot2(v[i][z], gh, a[4 * z + 2]); a[4 * z + 3] = dot2(v[i][z], sh, a[4 * z + 3]);
      }
    }
    fold_cols(a, rd + w * 128, lane);
    STAMP(18);
    poll16(rws, tid * 64, WS_F2 + (xcd >> 1) * 16384, ta, tb);
    BARRIER();
    STAMP(19);
#pragma unroll
    for (int i = 0; i < 16; ++i) v[i] = ld_sc1(rws, tid * 16, WS_X1 + hb * ST_X1 + (i >> 3) * ST_X1H + (i & 7) * 4096);
    if (w == 0 && hb < 28) {
      const f32x2 s = sum4(rd + lane * 2, 128);
      st_sc1(rws, lane * 16, WS_S12 + hb * 1024, u32x4{__float_as_uint(s[0]), ta, __float_as_uint(s[1] * -LOG2E), tb});
    }
#pragma unroll
    for (int e = 0; e < 16; ++e) a[e] = 0.f;
#pragma unroll
    for (int i = 0; i < 16; ++i)
#pragma unroll
      for (int z = 0; z < 4; ++z) {
        const int o = (i >> 3) * 8 + 2 * z;
        a[o] = dot2(v[i][z], 0x3f803f80u, a[o]); a[o + 1] = dot2(v[i][z], v[i][z], a[o + 1]);
      }
    STAMP(20);
    fold_cols(a, rd + 512 + w * 128, lane);
    BARRIER();
    if (w == 0) {
      const f32x2 s = sum4(rd + 512 + lane * 2, 128);
      const u32 st = ln_stat(s[0], s[1]);
      const u32 st2 = __builtin_amdgcn_mov_dpp(st, 0xB1, 0xf, 0xf, true);
      if (!(lane & 1)) st_sc1(rws, lane * 8, WS_G2 + hb * 512, u32x4{st, ta, st2, tb});
    }
    BARRIER();
    STAMP(22);
    __builtin_amdgcn_s_sleep(H_SLEEP);
  } else {
    const int wt = w & 1, wf = w >> 1;
    const int h = lane >> 5, blk = (lane >> 4) & 1, q = (lane & 15) >> 2, pp = lane & 3;
    int ax[2][2], aw[2][2];
#pragma unroll
    for (int c = 0; c < 2; ++c)
#pragma unroll
      for (int t = 0; t < 2; ++t) {
        const int base = 256 * (8 * h + 4 * t + q) + 16 * ((2 * blk + (pp >> 1)) ^ (2 * h + t)) + 8 * (pp & 1);
        ax[c][t] = LD_XR + base + 64 * ((2 * wt + c) ^ q);
        aw[c][t] = LD_WR + base + 64 * ((2 * wf + c) ^ q);
        PIN(ax[c][t]); PIN(aw[c][t]);
      }
    f32x16 acc[2][2];
#pragma unroll
    for (int a = 0; a < 2; ++a)
#pragma unroll
      for (int c = 0; c < 2; ++c)
#pragma unroll
        for (int r = 0; r < 16; ++r) acc[a][c][r] = 0.f;
    u32x4 xf[2][2], wfr[2][2];
    u32 st[2];
    int ho = WS_H + ((rbk * 4 + wt * 2) * 112 + cbk * 8 + wf * 4) * 1024 + lane * 16, ho1 = ho + 112 * 1024;
    int fo = only(lane < 8, WS_F3 + ((rbk * 4 + w) * 16 + cbk) * 128 + lane * 16);
    PIN(ho); PIN(ho1); PIN(fo);
#define RD_X(i, c) xf[(i) & 1][c] = tr_frag(smem + ax[c][0] + ((i) & 15) * 4096, smem + ax[c][1] + ((i) & 15) * 4096)
#define RD_W(i, c) wfr[(i) & 1][c] = tr_frag(smem + aw[c][0] + ((i) & 15) * 4096, smem + aw[c][1] + ((i) & 15) * 4096)
#define MM(i, a, c) do { SCHED(); acc[a][c] = mfma32(wfr[(i) & 1][a], xf[(i) & 1][c], acc[a][c]); SCHED(); } while (0)
    BARRIER();
    STAMP(19);
    RD_W(0, 0); RD_X(0, 0); RD_X(0, 1); RD_W(0, 1);
#pragma unroll
    for (int i = 0; i < 32; ++i) {
      if ((i & 3) != 3) {
        MM(i, 0, 0); RD_W(i + 1, 0); MM(i, 0, 1); RD_X(i + 1, 0); MM(i, 1, 0); RD_X(i + 1, 1); MM(i, 1, 1); RD_W(i + 1, 1);
      } else {
        MM(i, 0, 0); MM(i, 0, 1);
        BARRIER();
        if (i == 31) { st[0] = *(LAS const u32*)(smem + LD_MISC + (wt * 64 + (lane & 31)) * 4); st[1] = *(LAS const u32*)(smem + LD_MISC + (wt * 64 + 32 + (lane & 31)) * 4); }
        MM(i, 1, 0);
        if (i < 31) { RD_W(i + 1, 0); RD_X(i + 1, 0); }
        MM(i, 1, 1);
        if (i < 31) { RD_X(i + 1, 1); RD_W(i + 1, 1); }
      }
    }
    STAMP(21);
    float rstd[2], mr[2];
#pragma unroll
    for (int c = 0; c < 2; ++c) {
      rstd[c] = __builtin_amdgcn_rcpf(bfhi(st[c])) * -LOG2E;
      mr[c] = -bflo(st[c]) * rstd[c];
    }
    STAMP(22);
    LAS const float* s12 = (LAS const float*)(smem + LD_MISC + 512) + wf * 64 + 4 * h;
#pragma unroll
    for (int a = 0; a < 2; ++a)
#pragma unroll
      for (int sp = 0; sp < 2; ++sp) {
        f32x4 s1[2], s2[2];
#pragma unroll
        for (int z = 0; z < 2; ++z) { s1[z] = *(LAS const f32x4*)(s12 + a * 32 + sp * 16 + z * 8); s2[z] = *(LAS const f32x4*)(s12 + 128 + a * 32 + sp * 16 + z * 8); }
#pragma unroll
        for (int c = 0; c < 2; ++c) {
          u32x4 o;
#pragma unroll
          for (int e = 0; e < 4; ++e) {
            const int z = e >> 1, k = (e & 1) * 2;
            const f32x2 av = {acc[a][c][sp * 8 + 2 * e], acc[a][c][sp * 8 + 2 * e + 1]};
            const f32x2 cc = f32x2{s1[z][k], s1[z][k + 1]} * mr[c] + f32x2{s2[z][k], s2[z][k + 1]};
            const f32x2 ar = av * rstd[c] + cc;
            const f32x2 dn = f32x2{__builtin_amdgcn_exp2f(ar[0]), __builtin_amdgcn_exp2f(ar[1])} + 1.f;
            o[e] = pk2(ar * f32x2{__builtin_amdgcn_rcpf(dn[0]), __builtin_amdgcn_rcpf(dn[1])});
          }
          st_sc1(rws, c ? ho1 : ho, (a * 2 + sp) * 1024, o);
        }
      }
    STAMP(23);
    WAIT_VM(0);
    STAMP(24);
    st_sc1(rws, fo, 0, u32x4{ta, ta, tb, tb});
  }

  {
    const int wt = w & 1, kh = w >> 1, h = lane >> 5, tl = lane & 31;
    const int blk = (lane >> 4) & 1, q = (lane & 15) >> 2, pp = lane & 3;
    LAS char* aw[3][2];
#pragma unroll
    for (int c = 0; c < 6; ++c) {
      aw[c >> 1][c & 1] = smem + (c >> 1) * 65536 + kh * 8192 + 128 * (4 * h + q) + 64 * ((c & 1) ^ kh ^ (q >> 1)) + 32 * blk + 8 * pp;
      asm volatile("" : "+v"(aw[c >> 1][c & 1]));
    }
    const rsrc_t ro = __builtin_amdgcn_make_buffer_rsrc(p.out, 0, L * D * 2, 0x00020000);
    int oo[2];
#pragma unroll
    for (int i = 0; i < 2; ++i) {
      const int r = tbk * 64 + (tid >> 3) + 32 * i;
      oo[i] = ((r & ~511) + 128 * ((r >> 1) & 3) + 2 * ((r >> 3) & 63) + (r & 1)) * (D * 2) + db * 128 + (tid & 7) * 16;
      PIN(oo[i]);
    }
    f32x16 acc[2];
#pragma unroll
    for (int r = 0; r < 16; ++r) { acc[0][r] = 0.f; acc[1][r] = 0.f; }
    STAMP(25);
    BARRIER();
    constexpr int NW = G2_NW;
    u32x4 hf[NW], wf[3][2];
#define RD_W2C(i, c) do { LAS char* a_ = aw[w2_unit((i) >> 2) >> 2][c] + (w2_unit((i) >> 2) & 3) * UNIT + ((i) & 3) * 2048; wf[(i) % 3][c] = tr_frag(a_, a_ + 1024); } while (0)
#define RD_W2(i) do { RD_W2C(i, 0); RD_W2C(i, 1); } while (0)
    RD_W2(0); RD_W2(1);
    const int sh = WS_H + ((tbk * 2 + wt) * 112 + 56 * kh) * 1024;
#define LD_H(i) hf[(i) % NW] = ld_sc1(rws, lane * 16 + ((i) & 3) * 1024, sh + w2_phys((i) >> 2) * 4096)
    if (F3_DELAY) __builtin_amdgcn_s_sleep(F3_DELAY);
    poll16(rws, ((lane & 1) * 32 + min(lane >> 1, 6)) * 128, WS_F3 + ((tbk >> 1) * 4 + (tbk & 1)) * 2048 + kh * 896, ta, tb);
    STAMP(26);
#pragma unroll
    for (int i = 0; i < G2_N0; ++i) LD_H(i);
    SCHED();
    u32 xr[8]; f32x2 gb[8];
#pragma unroll
    for (int i = 0; i < 8; ++i) {
      const int pr = 4 * (i >> 1) + 2 * h + (i & 1);
      xr[i] = *(LAS const u32*)(smem + LD_MISC + 2048 + w * 2048 + ((tl >> 2) & 1) * 1024 + pr * 64 + (tl >> 3) * 16 + (tl & 3) * 4);
      gb[i] = *(LAS const f32x2*)(smem + LD_MISC + 1536 + (kh * 16 + pr) * 8);
    }
    SCHED();
#pragma unroll
    for (int i = 0; i < 56; ++i) {
      SCHED();
      acc[0] = mfma32(wf[i % 3][0], hf[i % NW], acc[0]);
      SCHED();
      if (i == 0) STAMP(29);
      if (i == 18) STAMP(30);
      if (i == 42) STAMP(31);
      if (i == 6 || i == 18 || i == 42) BARRIER();
      if (i + 2 < 56) RD_W2C(i + 2, 0);
      if (h_next(i) < h_next(i + 1)) LD_H(h_next(i));
      SCHED();
      acc[1] = mfma32(wf[i % 3][1], hf[i % NW], acc[1]);
      SCHED();
      if (i + 2 < 56) RD_W2C(i + 2, 1);
      if (h_next(i) + 1 < h_next(i + 1)) LD_H(h_next(i) + 1);
      asm volatile("" :: "v"(wf[i % 3][0]), "v"(wf[i % 3][1]), "v"(hf[i % NW]));
    }
    STAMP(27);
    LAS f32x4* ex = (LAS f32x4*)(smem + w2_unit(5) * UNIT) + lane;
#pragma unroll
    for (int qq = 0; qq < 4; ++qq) ex[(w * 4 + qq) * 64] = f32x4{acc[1][4 * qq], acc[1][4 * qq + 1], acc[1][4 * qq + 2], acc[1][4 * qq + 3]};
    BARRIER();
    LAS char* ot = smem + w2_unit(6) * UNIT;
    f32x4 oth[4];
#pragma unroll
    for (int qq = 0; qq < 4; ++qq) oth[qq] = ex[((w ^ 2) * 4 + qq) * 64];
    SCHED();
#pragma unroll
    for (int qq = 0; qq < 4; ++qq) {
      const f32x4 o4 = oth[qq];
      u32x2 o;
#pragma unroll
      for (int z = 0; z < 2; ++z)
        o[z] = pk2((f32x2{acc[0][4 * qq + 2 * z], acc[0][4 * qq + 2 * z + 1]} + f32x2{o4[2 * z], o4[2 * z + 1]}) * gb[2 * qq + z] + up(xr[2 * qq + z]));
      *(LAS u32x2*)(ot + (wt * 32 + tl) * 144 + kh * 64 + qq * 16 + h * 8) = o;
    }
    BARRIER();
#pragma unroll
    for (int i = 0; i < 2; ++i) {
      const int row = (tid >> 3) + 32 * i;
      __builtin_amdgcn_raw_buffer_store_b128(*(LAS const u32x4*)(ot + row * 144 + (tid & 7) * 16), ro, oo[i], 0, OUT_AUX);
    }
    STAMP(28);
    STAMP_FLUSH(p.ws, 0);
  }
}

extern "C" __global__ void fused_block_end() {}

extern "C" void kernel_launch(void* const* d_in, const int* in_sizes, int n_in, void* d_out, int out_size, void* d_ws, size_t ws_size,
                              hipStream_t stream) {
  Params p;
  p.x = (const u16*)d_in[0]; p.Are = (const u16*)d_in[1]; p.Aim = (const u16*)d_in[2]; p.Bre = (const u16*)d_in[3];
  p.Bim = (const u16*)d_in[4]; p.Cre = (const u16*)d_in[5]; p.Cim = (const u16*)d_in[6]; p.ldt = (const u16*)d_in[7];
  p.dskip = (const u16*)d_in[8]; p.w1 = (const u16*)d_in[10]; p.w2 = (const u16*)d_in[11]; p.c = (const u16*)d_in[12];
  p.wmod = (const u16*)d_in[13]; p.out = (u16*)d_out; p.ws = (char*)d_ws;
  (void)hipFuncSetAttribute((const void*)fused_block, hipFuncAttributeMaxDynamicSharedMemorySize, LDS_BYTES + ST_LDS_EXTRA);
  fused_block<<<256, 512, LDS_BYTES + ST_LDS_EXTRA, stream>>>(p);
}
```

```cpp
#define LIGHT 11
#include <hip/hip_runtime.h>
#define ST_OFF ((size_t)128 << 20)
#define ST_SLOTS 32
#define ST_MAXW 8
#define ST_MAXB 512
#define ST_LDS_EXTRA 2048
typedef unsigned st_u2 __attribute__((ext_vector_type(2)));
#define STAMP_DECL __attribute__((address_space(3))) st_u2* st_ = (__attribute__((address_space(3))) st_u2*)(smem + LDS_BYTES) + (threadIdx.x >> 6) * 32; \
  if ((threadIdx.x & 63) < 32) st_[threadIdx.x & 31] = st_u2{0u, 0u};
#define ST_ON(s) ((s) == 0 || (s) == 28 || (s) == LIGHT)
#define STAMP(s) do { unsigned long long rt_, cy_; __builtin_amdgcn_sched_barrier(0); if (!ST_ON(s)) break; \
  asm volatile("s_memrealtime %0\n\ts_memtime %1\n\ts_waitcnt lgkmcnt(0)" : "=&s"(rt_), "=&s"(cy_) :: "memory"); \
  st_[s] = st_u2{(unsigned)rt_ | 0x80000000u, (unsigned)cy_}; __builtin_amdgcn_sched_barrier(0); } while (0)
#define STAMP_FLUSH(ws, kern) do { if ((threadIdx.x & 63) < 32) { \
  unsigned long long* p_ = (unsigned long long*)((char*)(ws) + ST_OFF) + ((((size_t)(kern) * ST_MAXB + blockIdx.x) * ST_MAXW + (threadIdx.x >> 6)) * ST_SLOTS + (threadIdx.x & 31)) * 2; \
  st_u2 v_ = st_[threadIdx.x & 31]; p_[0] = v_[0]; p_[1] = v_[1]; } } while (0)
#include <hip/hip_runtime.h>
#include <hip/hip_bf16.h>

#define ST_RANGES {{0, 32, 256, "compute waves"}, {1, 32, 256, "loader waves"}, {0, 0, 32, "helper WGs, waves 0-3"}, {1, 0, 32, "helper WGs, waves 4-7"}}

typedef unsigned short u16;
typedef unsigned int u32;
typedef unsigned long long u64;
typedef u32 u32x2 __attribute__((ext_vector_type(2)));
typedef u32 u32x4 __attribute__((ext_vector_type(4)));
typedef float f32x2 __attribute__((ext_vector_type(2)));
typedef float f32x4 __attribute__((ext_vector_type(4)));
typedef float f32x16 __attribute__((ext_vector_type(16)));
typedef short s16x4 __attribute__((ext_vector_type(4)));
typedef __bf16 bf16x2 __attribute__((ext_vector_type(2)));
typedef __bf16 bf16x8 __attribute__((ext_vector_type(8)));
typedef __amdgpu_buffer_rsrc_t rsrc_t;
#define GAS __attribute__((address_space(1)))
#define LAS __attribute__((address_space(3)))
#define CAS __attribute__((address_space(4)))

constexpr int L = 2048, D = 512, NS = 128, F = 1792;

constexpr int WS_EPOCH = 0;
constexpr int WS_SG = 0x1000;
constexpr int WS_MG = 0x80000;
constexpr int WS_F2 = 0x40000;
constexpr int WS_F3 = 0x50000;
constexpr int WS_G2 = 0x70000;
constexpr int WS_S12 = 0x74000;
constexpr int WS_MF = 0x7c000;
constexpr int WS_XG = 2 << 20;
constexpr int WS_X1 = 0x440000;
constexpr int WS_X1S = 0x680000;
constexpr int WS_H = 9 << 20;
constexpr int WS_END = 16 << 20;
#define PADS 1
constexpr int ST_XG = 8192 + 128 * PADS, ST_MG = 1536 * 16 + 128 * PADS, ST_X1H = 32768 + 128 * PADS, ST_X1 = 2 * ST_X1H, ST_X1S = 131072 + 384 * PADS;
constexpr int OOB = 0x7ff00000;

constexpr int UNIT = 16384;
constexpr int LD_XR = 0, LD_WR = 4 * UNIT, LD_MISC = 9 * UNIT, LD_FLG = LD_MISC + 10240, LD_MV = LD_FLG + 32, LDS_BYTES = LD_MV + 64;
constexpr int TCOPY = 4160, PROW = 132;
constexpr int LD_T = 0, LD_U = 8 * TCOPY, LD_KD = LD_U + 2 * 4608, LD_P = LD_WR, LD_RED0 = 3 * UNIT, LD_RED1 = 8 * UNIT;
constexpr int KD_ZERO = 900, LD_XT = LD_RED0;
static_assert(LD_KD + 904 * 4 <= LD_RED0, "LDS map");
__host__ __device__ constexpr int w2_phys(int u) { return (u + 5) % 14; }
__host__ __device__ constexpr int w2_unit(int u) { return u % 9 == 8 ? 8 : (u % 9) / 2 + 4 * (u % 9 & 1); }
#define G2_NW 14
#define G2_N0 14
__host__ __device__ constexpr int h_next(int i) { int a = G2_N0 + 2 * i, b = i > 0 ? i - 1 + G2_NW : G2_NW; a = a < b ? a : b; return a < 56 ? a : 56; }
#define TF_PRE 4
#define F3_DELAY 10
#define XG_DELAY 0
#define OUT_AUX 0
#define G_EARLY 1
#define G_SIG 5
#define FT_AT 3
#define FT_DELAY 0
#define H_SLEEP 110
#define S1_SLEEP 2
#define F2_DELAY 16
#define POLL_SLEEP 2
#define LDNT(p) __builtin_nontemporal_load(p)
#define AUX_NT 2
#define WF ""
#define AUX_W 0
constexpr int SPIN_CAP = 1 << 15;
constexpr float LOG2E = 1.44269504088896341f;

__device__ __forceinline__ float bflo(u32 v) { return __uint_as_float(v << 16); }
__device__ __forceinline__ float bfhi(u32 v) { return __uint_as_float(v & 0xffff0000u); }
__device__ __forceinline__ u32 pk(float lo, float hi) {
  f32x2 v = {lo, hi};
  bf16x2 r = __builtin_convertvector(v, bf16x2);
  return __builtin_bit_cast(u32, r);
}
__device__ __forceinline__ float rb(float f) { return bflo(pk(f, f)); }
__device__ __forceinline__ float dot2(u32 a, u32 b, float c) {
  return __builtin_amdgcn_fdot2_f32_bf16(__builtin_bit_cast(bf16x2, a), __builtin_bit_cast(bf16x2, b), c, false);
}
__device__ __forceinline__ f32x4 mfma16(u32x4 a, u32x4 b, f32x4 c) {
  return __builtin_amdgcn_mfma_f32_16x16x32_bf16(__builtin_bit_cast(bf16x8, a), __builtin_bit_cast(bf16x8, b), c, 0, 0, 0);
}
__device__ __forceinline__ f32x16 mfma32(u32x4 a, u32x4 b, f32x16 c) {
  return __builtin_amdgcn_mfma_f32_32x32x16_bf16(__builtin_bit_cast(bf16x8, a), __builtin_bit_cast(bf16x8, b), c, 0, 0, 0);
}
template <int CTRL> __device__ __forceinline__ float dpp(float x) {
  return __builtin_bit_cast(float, __builtin_amdgcn_mov_dpp(__builtin_bit_cast(int, x), CTRL, 0xf, 0xf, true));
}
__device__ __forceinline__ float row_sum(float x) {
  x += dpp<0xB1>(x); x += dpp<0x4E>(x); x += dpp<0x124>(x); x += dpp<0x128>(x);
  return x;
}
__device__ __forceinline__ float fold32(float a, float b) {
  auto s = __builtin_amdgcn_permlane32_swap(__float_as_uint(a), __float_as_uint(b), false, false);
  return __uint_as_float(s[0]) + __uint_as_float(s[1]);
}
__device__ __forceinline__ float fold16(float a, float b) {
  auto s = __builtin_amdgcn_permlane16_swap(__float_as_uint(a), __float_as_uint(b), false, false);
  return __uint_as_float(s[0]) + __uint_as_float(s[1]);
}
__device__ __forceinline__ u32x4 mkrs(const void* p, u32 bytes) {
  const u64 a = (u64)p;
  return u32x4{(u32)a, (u32)(a >> 32) & 0xffffu, bytes, 0x00020000u};
}
__device__ __forceinline__ void dma4(u32x4 rs, int lds, int voff, int soff) {
  asm volatile("s_mov_b32 m0, %0\n\ts_nop 0\n\tbuffer_load_dword %1, %2, %3 offen lds" :: "s"(lds), "v"(voff), "s"(rs), "s"(soff) : "memory");
}
__device__ __forceinline__ void dma16_sc1(u32x4 rs, int lds, int voff, int soff) {
  asm volatile("s_mov_b32 m0, %0\n\ts_nop 0\n\tbuffer_load_dwordx4 %1, %2, %3 offen sc1 lds" :: "s"(lds), "v"(voff), "s"(rs), "s"(soff) : "memory");
}
#define DMA4_TEXT(F) "s_mov_b32 m0, %0\n\ts_nop 0\n\tbuffer_load_dwordx4 %1, %5, %6 offen" F " lds\n\tbuffer_load_dwordx4 %2, %5, %6 offen offset:1024" F " lds\n\t" \
                     "buffer_load_dwordx4 %3, %5, %6 offen offset:2048" F " lds\n\tbuffer_load_dwordx4 %4, %5, %6 offen offset:3072" F " lds"
__device__ __forceinline__ void dma4k(u32x4 rs, int lds, const int (&v)[4], int soff) {
  asm volatile(DMA4_TEXT(WF) :: "s"(lds), "v"(v[0]), "v"(v[1]), "v"(v[2]), "v"(v[3]), "s"(rs), "s"(soff) : "memory");
}
__device__ __forceinline__ void dma4k_sc1(u32x4 rs, int lds, const int (&v)[4], int soff) {
  asm volatile(DMA4_TEXT(" sc1") :: "s"(lds), "v"(v[0]), "v"(v[1]), "v"(v[2]), "v"(v[3]), "s"(rs), "s"(soff) : "memory");
}
__device__ __forceinline__ u32x4 ld_sc1(rsrc_t rs, int voff, int soff) { return __builtin_amdgcn_raw_buffer_load_b128(rs, voff, soff, 16); }
__device__ __forceinline__ void st_sc1(rsrc_t rs, int voff, int soff, u32x4 v) { __builtin_amdgcn_raw_buffer_store_b128(v, rs, voff + soff, 0, 16); }
__device__ __forceinline__ u32x4 tr_frag(LAS char* a0, LAS char* a1) {
  const s16x4 lo = __builtin_amdgcn_ds_read_tr16_b64_v4i16((LAS s16x4*)a0), hi = __builtin_amdgcn_ds_read_tr16_b64_v4i16((LAS s16x4*)a1);
  const u32x2 l = __builtin_bit_cast(u32x2, lo), h = __builtin_bit_cast(u32x2, hi);
  return u32x4{l[0], l[1], h[0], h[1]};
}
#define SCHED() __builtin_amdgcn_sched_barrier(0)
#define FENCE() asm volatile("" ::: "memory")
#define WAIT_VM(n) asm volatile("s_waitcnt vmcnt(" #n ")" ::: "memory")
#define BARRIER() do { asm volatile("s_waitcnt lgkmcnt(0)" ::: "memory"); __builtin_amdgcn_s_barrier(); FENCE(); } while (0)

__device__ __forceinline__ f32x2 cmul(f32x2 a, f32x2 b) {
  const f32x2 t = f32x2{a[0], a[0]} * b;
  return __builtin_elementwise_fma(f32x2{a[1], a[1]}, f32x2{-b[1], b[0]}, t);
}
__device__ __forceinline__ f32x2 cpow(float a2, float bq, float l) {
  const float m = __builtin_amdgcn_exp2f(a2 * l);
  const float ph = __builtin_amdgcn_fractf(bq * l);
  return f32x2{__builtin_amdgcn_cosf(ph), __builtin_amdgcn_sinf(ph)} * m;
}
__device__ __forceinline__ u32 pk2(f32x2 a) { return pk(a[0], a[1]); }
__device__ __forceinline__ f32x2 up(u32 v) { return f32x2{bflo(v), bfhi(v)}; }
__device__ __forceinline__ u32 ln_stat(float sum, float sq) {
  const float m = sum * (1.f / D);
  const float var = fmaxf(sq * (1.f / D) - m * m, 0.f);
  return pk(m, __builtin_amdgcn_sqrtf(rb(rb(var) + 1e-5f)));
}
__device__ __forceinline__ u32 tagdiff(u32x4 v, u32 ta, u32 tb) { return (v[1] ^ ta) | (v[3] ^ tb); }
__device__ __forceinline__ u32x4 poll16(rsrc_t rs, int voff, int soff, u32 ta, u32 tb) {
  u32x4 v;
  int spins = 0;
  for (;;) {
    FENCE();
    v = ld_sc1(rs, voff, soff);
    if (tagdiff(v, ta, tb) == 0u || ++spins >= SPIN_CAP) break;
    if (POLL_SLEEP) __builtin_amdgcn_s_sleep(POLL_SLEEP);
  }
  FENCE();
  return v;
}
__device__ __forceinline__ bool bad4(const u32x4 (&v)[4], u32 ta, u32 tb) {
  return (tagdiff(v[0], ta, tb) | tagdiff(v[1], ta, tb) | tagdiff(v[2], ta, tb) | tagdiff(v[3], ta, tb)) != 0u;
}
__device__ __forceinline__ void retry4(u32x4 (&v)[4], rsrc_t rs, int voff, int step, u32 ta, u32 tb) {
#pragma unroll 1
  for (int spins = 0; spins < SPIN_CAP; ++spins) {
    __builtin_amdgcn_s_sleep(S1_SLEEP);
    FENCE();
#pragma unroll
    for (int i = 0; i < 4; ++i) if (tagdiff(v[i], ta, tb)) v[i] = ld_sc1(rs, voff, i * step);
    if (!__any(bad4(v, ta, tb))) break;
  }
}
__device__ __forceinline__ void fold_cols(const float (&v)[16], LAS float* dst, int lane) {
  float u[8]; f32x4 t;
#pragma unroll
  for (int k = 0; k < 8; ++k) u[k] = fold32(v[k], v[k + 8]);
#pragma unroll
  for (int k = 0; k < 4; ++k) { const float a = fold16(u[k], u[k + 4]); t[k] = a + dpp<0x128>(a); }
  if (!(lane & 8)) *(LAS f32x4*)(dst + (lane & 7) * 16 + (lane >> 4) * 4) = t;
}
__device__ __forceinline__ f32x2 sum4(LAS const float* src, int stride) {
  return (*(LAS const f32x2*)src + *(LAS const f32x2*)(src + stride)) + (*(LAS const f32x2*)(src + 2 * stride) + *(LAS const f32x2*)(src + 3 * stride));
}
__device__ __forceinline__ u32 silu_bf(u32 c) {
  const float f = bflo(c), s = f * __builtin_amdgcn_rcpf(1.f + __builtin_amdgcn_exp2f(f * -LOG2E));
  return pk(s, s);
}
template <int CTRL> __device__ __forceinline__ void gv_row(u32x4 v, u32 sp, float (&acc)[8]) {
  const u32 sb = __builtin_amdgcn_mov_dpp(sp, CTRL, 0xf, 0xf, true), al = sb & 0xffffu, ah = sb & 0xffff0000u;
#pragma unroll
  for (int q = 0; q < 4; ++q) { acc[2 * q] = dot2(v[q], al, acc[2 * q]); acc[2 * q + 1] = dot2(v[q], ah, acc[2 * q + 1]); }
}
__device__ __forceinline__ f32x2 gv_fold(const float (&acc)[8]) {
  const float m0 = fold16(fold32(acc[0], acc[4]), fold32(acc[2], acc[6])), m1 = fold16(fold32(acc[1], acc[5]), fold32(acc[3], acc[7]));
  return f32x2{m0 + dpp<0x128>(m0), m1 + dpp<0x128>(m1)};
}
#define TOUCH(dst, voff, base) asm volatile("global_load_dword %0, %1, %2" : "=v"(dst) : "v"(voff), "s"(base))

__device__ __forceinline__ void quad_arrive(LAS char* f, int i, u32 n) {
  asm volatile("s_waitcnt lgkmcnt(0)" ::: "memory");
  *(volatile LAS u32*)(f + i * 4) = n;
}
__device__ __forceinline__ void quad_wait(LAS char* f, u32 n) {
  for (;;) {
    const u32x4 v = *(volatile LAS u32x4*)f;
    if (__builtin_amdgcn_readfirstlane(min(min(v[0], v[1]), min(v[2], v[3]))) >= n) break;
    __builtin_amdgcn_s_sleep(1);
  }
  FENCE();
}
__device__ __forceinline__ int only(bool c, int off) { return off | (c ? 0 : OOB); }
#define PIN(x) asm volatile("" : "+v"(x))

struct Params {
  const u16 *x, *Are, *Aim, *Bre, *Bim, *Cre, *Cim, *ldt, *dskip, *w1, *w2, *c, *wmod;
  u16* out;
  char* ws;
};

extern "C" __global__ void __launch_bounds__(512, 2) fused_block(Params p) {
  extern __shared__ __attribute__((aligned(16))) char smem_[];
  LAS char* smem = (LAS char*)smem_;
  const int tid = threadIdx.x, lane = tid & 63, w = __builtin_amdgcn_readfirstlane(tid >> 6), lw = w & 3;
  STAMP_DECL
  STAMP(0);
  *(LAS u32*)(smem + LD_FLG + w * 4) = 0u;
  BARRIER();
  asm volatile("" :: "s"(p.x), "s"(p.Are), "s"(p.Aim), "s"(p.Bre), "s"(p.Bim), "s"(p.Cre), "s"(p.Cim), "s"(p.ldt), "s"(p.dskip), "s"(p.w1), "s"(p.w2), "s"(p.c), "s"(p.wmod),
               "s"(p.out), "s"(p.ws));
#define SCAL_ISSUE(q) u64 epoch; u32 aux; \
  asm volatile("s_load_dwordx2 %0, %2, 0x0\n\ts_load_dword %1, %3, 0x0" : "=&s"(epoch), "=&s"(aux) : "s"(p.ws + WS_EPOCH), "s"(q)); SCHED()
#define SCAL_WAIT() asm volatile("s_waitcnt lgkmcnt(0)" : "+s"(epoch), "+s"(aux)); \
  epoch += 0x9E3779B97F4A7C15ull; ta = (u32)epoch; tb = (u32)(epoch >> 32); dsk = aux
#define ROLES() int b = blockIdx.x; asm volatile("" : "+s"(b)); \
  const int xcd = b & 7, j = b >> 3, pair = xcd * 32 + j, d0 = pair * 2; \
  const bool helper = j < 4; \
  const int rbk = (xcd >> 1) * 4 + (j & 3), cbk = (xcd & 1) * 7 + ((j - 4) >> 2), tbk = xcd * 4 + (j & 3), db = ((j >> 2) + 3 - (int)((u64)p.w2 >> 7)) & 7; \
  const rsrc_t rws = __builtin_amdgcn_make_buffer_rsrc(p.ws, 0, WS_END, 0x00020000)

  if (__builtin_expect(w >= 4, 1)) {
    LAS char* flg = smem + LD_FLG + 16;
    u32x4 sg[4];
    u32x2 xg[4];
    ROLES();
    const int go = lw * 4096 + lane * 16;
    u32 ta, tb, dsk;
    const u32x4 qws = mkrs(p.ws, WS_END);
#define POLL_ISSUE() asm volatile("buffer_load_dwordx4 %0, %4, %5, 0 offen sc1\n\tbuffer_load_dwordx4 %1, %4, %5, 0 offen offset:1024 sc1\n\t" \
      "buffer_load_dwordx4 %2, %4, %5, 0 offen offset:2048 sc1\n\tbuffer_load_dwordx4 %3, %4, %5, 0 offen offset:3072 sc1" \
      : "=&v"(sg[0]), "=&v"(sg[1]), "=&v"(sg[2]), "=&v"(sg[3]) : "v"(go + WS_SG), "s"(qws) : "memory")
    int vx[4], vw1[4], vw2[4], so, xo0, xo1, fo, mfo;
    LAS char *ra[2], *rc[2];
    auto addresses = [&]() {
      const int hm = (j - 4) >> 31;
#pragma unroll
      for (int i = 0; i < 4; ++i) {
        const int chk = 16 * ((lane & 15) ^ ((lane >> 4) << 2) ^ i);
        vx[i] = (lane >> 4) * 256 + chk;
        vw1[i] = (lane >> (4 + hm)) * (F * 2) + ((chk & ~hm) | ((lane & 7) * 16 & hm)) + i * ((4 + (4 & hm)) * F * 2 - 1024);
        vw2[i] = (lane >> 3) * 1024 + db * 128 + 16 * ((lane & 7) ^ (((lane >> 4) & 1) << 2)) + i * 7168;
        PIN(vx[i]); PIN(vw1[i]); PIN(vw2[i]);
      }
      const int g = (lane >> 1) & 3;
      ra[0] = smem + LD_RED0 + (2 * lw * 64 + 16 * g + ((lane >> 3) ^ (4 * g))) * 16 + 8 * (lane & 1);
      ra[1] = smem + LD_RED0 + (2 * lw * 64 + 16 * g + ((lane >> 3) ^ (4 * g) ^ 8)) * 16 + 8 * (lane & 1);
      rc[0] = ra[0] + (LD_RED1 - LD_RED0); rc[1] = ra[1] + (LD_RED1 - LD_RED0);
      so = WS_X1S + (lw * 4 + (lane >> 4)) * ST_X1S + d0 * 256 + (lane & 15) * 16;
      xo0 = WS_X1 + (lw * 8 + (lane >> 3)) * ST_X1 + pair * 128 + (lane & 7) * 16; xo1 = xo0 + ST_X1H;
      fo = only(lane < 4, WS_F2 + (lw * 256 + pair) * 64 + lane * 16);
      mfo = only((tid >> 1) == 248, WS_MF + (~lane & 1) * 4096 + pair * 16);
      PIN(mfo); PIN(ra[0]); PIN(ra[1]); PIN(rc[0]); PIN(rc[1]); PIN(so); PIN(xo0); PIN(xo1); PIN(fo);
      SCHED();
    };
    f32x2 xf[4][2], hf[4][2], ca[4][2];
    u32x4 mv1, mv2;
    auto coefficients = [&]() {
      __builtin_amdgcn_s_setprio(2);
      STAMP(7);
      if (__builtin_expect(__any(bad4(sg, ta, tb)), 0)) retry4(sg, rws, go + WS_SG, 1024, ta, tb);
      float rs[4][2];
#pragma unroll
      for (int i = 0; i < 4; ++i)
#pragma unroll
        for (int e = 0; e < 2; ++e) { rs[i][e] = __builtin_amdgcn_rcpf(bfhi(sg[i][2 * e])); PIN(rs[i][e]); }
      quad_wait(flg, 3u);
      const u32x4 mv0 = *(LAS const u32x4*)(smem + LD_MV);
      mv1 = *(LAS const u32x4*)(smem + LD_MV + 32); mv2 = *(LAS const u32x4*)(smem + LD_MV + 48);
      if (lw < 2) {
#pragma unroll
        for (int i = 0; i < 4; ++i) xg[i] = *(LAS const u32x2*)(smem + LD_XT + lw * 2048 + i * 512 + lane * 8);
      }
      const f32x2 sa = up(mv0[0]), g1 = up(mv0[3]);
#pragma unroll
      for (int i = 0; i < 4; ++i)
#pragma unroll
        for (int e = 0; e < 2; ++e) {
          ca[i][e] = g1 * rs[i][e];
          hf[i][e] = sa - ca[i][e] * bflo(sg[i][2 * e]);
          PIN(ca[i][e]); PIN(hf[i][e]);
        }
      STAMP(8);
    };
    if (lw >= 2) {
      u32 xt;
      TOUCH(xt, (j * 32 + (lw & 1) * 16 + (lane & 15)) * 1024 + xcd * 128, p.x);
      SCAL_ISSUE(p.dskip + d0);
      addresses();
      const u32x4 qx = mkrs(p.x, L * D * 2);
      SCAL_WAIT();
      quad_arrive(flg, lw, 1u);
#pragma unroll
      for (int i = 0; i < 8; ++i) {
        if (i == G_EARLY) quad_wait(flg, 1u);
        if (i == G_SIG) *(volatile LAS u32*)(flg + lw * 4) = 2u;
        dma4(qx, LD_XT + (lw & 1) * 2048 + i * 256, lane * 1024 + pair * 4, ((lw & 1) * 8 + i) * 65536);
        WAIT_VM(2);
      }
      if (XG_DELAY) __builtin_amdgcn_s_sleep(XG_DELAY);
      STAMP(3);
      POLL_ISSUE();
      const int xo = WS_XG + ((lw & 1) * 128 + (lane >> 1)) * ST_XG + pair * 32 + (lane & 1) * 16;
      u32x4 xe[4];
#pragma unroll
      for (int i = 0; i < 4; ++i) xe[i] = ld_sc1(rws, xo, i * 32 * ST_XG);
      asm volatile("s_waitcnt vmcnt(8)" :: "v"(xt) : "memory");
      quad_arrive(flg, lw, 3u);
      asm volatile("s_waitcnt vmcnt(4)" : "+v"(sg[0]), "+v"(sg[1]), "+v"(sg[2]), "+v"(sg[3]) :: "memory");
      coefficients();
      if (__builtin_expect(__any(bad4(xe, ta, tb)), 0)) retry4(xe, rws, xo, 32 * ST_XG, ta, tb);
#pragma unroll
      for (int i = 0; i < 4; ++i) xg[i] = u32x2{xe[i][0], xe[i][2]};
    } else {
      u32x4 xv[4], wm[8];
      const int gcb = (b + (b >> 4)) & 15;
#pragma unroll
      for (int i = 0; i < 4; ++i) xv[i] = LDNT((const u32x4*)(p.x + (size_t)(lw * 1024 + b * 4 + i) * D) + lane);
      SCHED();
      SCAL_ISSUE(p.dskip + d0);
      const u32 c0 = p.c[(b >> 4) * 32 + (lane >> 3) + 8 * (lane & 3)];
      SCHED();
      {
        const rsrc_t ra = __builtin_amdgcn_make_buffer_rsrc((void*)p.wmod, 0, D * 3072 * 2, 0x00020000);
        const rsrc_t rb_ = __builtin_amdgcn_make_buffer_rsrc((void*)p.wmod, 0, lw == 0 ? D * 3072 * 2 : 0, 0x00020000);
#pragma unroll
        for (int i = 0; i < 8; ++i)
          wm[i] = __builtin_amdgcn_raw_buffer_load_b128(i < 4 ? ra : rb_, (lane >> 3) * 6144 + (lane & 7) * 16,
                                                        ((b >> 4) * 32 + 8 * (i & 3)) * 6144 + ((lw + (i >> 2) * 2) * 16 + gcb) * 128, AUX_NT);
      }
      SCHED();
      *(LAS u32x4*)(smem + LD_U + (lane >> 5) * 4608 + (lane & 31) * 16) = u32x4{0u, 0u, 0u, 0u};
      int so_ = only(!(lane & 31), WS_SG + lw * 8192 + b * 32 + (lane >> 5) * 16);
      int mo_ = only(!(lane & 8), WS_MG + (b >> 4) * ST_MG + ((lw * 16 + gcb) * 32 + (lane >> 4) * 8 + (lane & 7)) * 16);
      PIN(so_); PIN(mo_);
      SCAL_WAIT();
      __builtin_amdgcn_s_setprio(3);
      STAMP(1);
      LAS char* xw = smem + LD_XT + 4096;
      if (lw) {
#pragma unroll
        for (int i = 0; i < 4; ++i) *(LAS u32x4*)(xw + i * 1056 + lane * 16) = xv[i];
      }
      float s[4], q[4];
#pragma unroll
      for (int i = 0; i < 4; ++i) {
        s[i] = 0.f; q[i] = 0.f;
#pragma unroll
        for (int z = 0; z < 4; ++z) { s[i] = dot2(xv[i][z], 0x3f803f80u, s[i]); q[i] = dot2(xv[i][z], xv[i][z], q[i]); }
      }
      const u32 st = ln_stat(row_sum(fold16(fold32(s[0], s[2]), fold32(s[1], s[3]))), row_sum(fold16(fold32(q[0], q[2]), fold32(q[1], q[3]))));
      const auto sw = __builtin_amdgcn_permlane16_swap(st, st, false, false);
      st_sc1(rws, so_, 0, u32x4{sw[0], ta, sw[1], tb});
      STAMP(2);
      auto gv_tile = [&](int z, u32 sp) {
        float acc[8] = {0.f, 0.f, 0.f, 0.f, 0.f, 0.f, 0.f, 0.f};
        gv_row<0x00>(wm[4 * z], sp, acc); gv_row<0x55>(wm[4 * z + 1], sp, acc); gv_row<0xAA>(wm[4 * z + 2], sp, acc); gv_row<0xFF>(wm[4 * z + 3], sp, acc);
        const f32x2 m = gv_fold(acc);
        st_sc1(rws, mo_ + z * 16384, 0, u32x4{__float_as_uint(m[0]), ta, __float_as_uint(m[1]), tb});
        SCHED();
      };
      if (lw) {
        u32 a[8][2];
#pragma unroll
        for (int i = 0; i < 8; ++i) {
          LAS const u32* src = (LAS const u32*)(xw + (lane & 1) * 2112 + i * 128 + (lane >> 1) * 4);
          a[i][0] = src[0]; a[i][1] = src[264];
        }
#pragma unroll
        for (int i = 0; i < 8; ++i) st_sc1(rws, WS_XG + b * ST_XG + lane * 16 + (i >> 2) * 4096, (i & 3) * 1024, u32x4{a[i][0], ta, a[i][1], tb});
        SCHED();
        gv_tile(0, silu_bf(c0));
        PIN(wm[4]); PIN(wm[5]); PIN(wm[6]); PIN(wm[7]);
      } else {
        const u32 sp = silu_bf(c0);
        gv_tile(0, sp);
        gv_tile(1, sp);
      }
      __builtin_amdgcn_s_setprio(0);
      quad_arrive(flg, lw, 2u);
      STAMP(4);
      addresses();
      quad_wait(flg, 2u);
      const int mo = WS_MG + (lane & 15) * ST_MG + ((((lane >> 4) & (2 * lw + 1)) + 2 * lw) * 256 + (pair >> 5) * 32 + (pair & 3) * 8 + ((pair >> 2) & 7)) * 16;
      u32x4 mg;
      asm volatile("buffer_load_dwordx4 %0, %1, %2, 0 offen sc1" : "=v"(mg) : "v"(mo), "s"(qws) : "memory");
      POLL_ISSUE();
      asm volatile("s_waitcnt vmcnt(4)" : "+v"(mg) :: "memory");
      if (__builtin_expect(__any(tagdiff(mg, ta, tb) != 0u), 0)) {
        asm volatile("s_waitcnt vmcnt(0)" : "+v"(sg[0]), "+v"(sg[1]), "+v"(sg[2]), "+v"(sg[3]) :: "memory");
        mg = poll16(rws, mo, 0, ta, tb);
      }
      const u32 mb = pk(row_sum(__uint_as_float(mg[0])), row_sum(__uint_as_float(mg[2])));
      *(LAS u32x2*)(smem + LD_MV + lw * 32 + (lane >> 4) * 8) = u32x2{mb, pk2(up(mb) + 1.f)};
      quad_arrive(flg, lw, 3u);
      asm volatile("s_waitcnt vmcnt(0)" : "+v"(sg[0]), "+v"(sg[1]), "+v"(sg[2]), "+v"(sg[3]) :: "memory");
      coefficients();
    }
    const u32x4 qw1 = mkrs(p.w1, D * F * 2), qw2 = mkrs(p.w2, D * F * 2);
    const int sw2 = ((lw >> 1) * 896 + (lw & 1) * 32) * 1024;
    auto w2_stage = [&](int u) { dma4k(qw2, w2_unit(u) * UNIT + lw * 4096, vw2, sw2 + w2_phys(u) * 65536); };
    const int wstep = (helper ? 32 : 16) * F * 2, wbase = helper ? min(tbk, 27) * 128 : cbk * 256;
    auto w1_stage = [&](int s) { dma4k(qw1, LD_WR + (s & 3) * UNIT + lw * 4096, vw1, (s * 4 + lw) * wstep + wbase); };
    auto x_stage = [&](int s) { dma4k_sc1(qws, LD_XR + (s & 3) * UNIT + lw * 4096, vx, WS_X1S + rbk * ST_X1S + s * 16384 + lw * 4096); };
    *(volatile LAS u32*)(flg + lw * 4) = 4u;
    STAMP(9);
#pragma unroll
    for (int i = 0; i < 4; ++i) {
#pragma unroll
      for (int e = 0; e < 2; ++e) { xf[i][e] = up(xg[i][e]); hf[i][e] += xf[i][e] * ca[i][e]; }
      LAS u32* U = (LAS u32*)(smem + LD_U + 512 + lw * 1024 + i * 256) + lane;
      U[0] = pk(hf[i][0][0], hf[i][1][0]);
      U[4608 / 4] = pk(hf[i][0][1], hf[i][1][1]);
    }
    STAMP(10);
    BARRIER();
    STAMP(11);
    w1_stage(0); w1_stage(1); w1_stage(2); w1_stage(3);
    const f32x2 gav = up(mv1[0]), gcv = up(mv2[1]);
    f32x2 xc[4][2];
    float gd[2] = {gav[0] * bflo(dsk), gav[1] * bfhi(dsk)}, n0 = bflo(mv2[2]) * (-1.f / LOG2E), n1 = bfhi(mv2[2]) * (-1.f / LOG2E);
    u32 k0 = __builtin_amdgcn_perm(mv1[2], mv2[1], 0x05040100u), k1 = __builtin_amdgcn_perm(mv1[2], mv2[1], 0x07060302u);
    PIN(gd[0]); PIN(gd[1]); PIN(n0); PIN(n1); PIN(k0); PIN(k1);
    const u32x4 mfv = {lane & 1 ? k0 : __float_as_uint(n0), ta, lane & 1 ? k1 : __float_as_uint(n1), tb};
    st_sc1(rws, mfo, 0, mfv);
#pragma unroll
    for (int i = 0; i < 4; ++i)
#pragma unroll
      for (int c = 0; c < 2; ++c) {
        float a0 = __builtin_fmaf(hf[i][0][c], gd[c], xf[i][0][c]), a1 = __builtin_fmaf(hf[i][1][c], gd[c], xf[i][1][c]);
        PIN(a0); PIN(a1);
        xc[i][c] = f32x2{a0, a1};
      }
    STAMP(12);
    SCHED();
    u32x4 wr[20];
    auto wr_load = [&](bool on) {
      const rsrc_t rw2 = __builtin_amdgcn_make_buffer_rsrc((void*)p.w2, 0, on ? D * F * 2 : 0, 0x00020000);
#pragma unroll
      for (int i = 0; i < 20; ++i) wr[i] = __builtin_amdgcn_raw_buffer_load_b128(rw2, vw2[i & 3] + (i & 3) * 1024, sw2 + (i / 4) * 65536, AUX_W);
    };
    wr_load(!helper);
    SCHED();
    STAMP(13);
    BARRIER();
    STAMP(14);
    {
      u32x4 o[2], s0, s1;
      f32x2 yp[4][4], c0[4], c1[4];
#pragma unroll
      for (int i = 0; i < 4; ++i) {
        yp[i][0] = *(volatile LAS const f32x2*)(ra[i & 1] + (i >> 1) * 1024); yp[i][1] = *(volatile LAS const f32x2*)(ra[i & 1] + (i >> 1) * 1024 + 8192);
      }
#pragma unroll
      for (int i = 0; i < 4; ++i) {
        yp[i][2] = *(volatile LAS const f32x2*)(rc[i & 1] + (i >> 1) * 1024); yp[i][3] = *(volatile LAS const f32x2*)(rc[i & 1] + (i >> 1) * 1024 + 8192);
      }
      SCHED();
#pragma unroll
      for (int i = 0; i < 4; ++i) { c0[i] = (yp[i][0] + yp[i][1]) * gav[0] + xc[i][0]; s0[i] = pk2(c0[i] * gcv[0]); }
      st_sc1(rws, so, 0, s0);
      SCHED();
#pragma unroll
      for (int i = 0; i < 4; ++i) { c1[i] = (yp[i][2] + yp[i][3]) * gav[1] + xc[i][1]; s1[i] = pk2(c1[i] * gcv[1]); }
      st_sc1(rws, so, 256, s1);
      SCHED();
#pragma unroll
      for (int i = 0; i < 4; ++i) { o[i >> 1][2 * (i & 1)] = pk(c0[i][0], c1[i][0]); o[i >> 1][2 * (i & 1) + 1] = pk(c0[i][1], c1[i][1]); }
      st_sc1(rws, xo0, 0, o[0]);
      st_sc1(rws, xo1, 0, o[1]);
    }
    STAMP(15);
    WAIT_VM(0);
    STAMP(17);
    st_sc1(rws, fo, 0, u32x4{ta, ta, tb, tb});
    __builtin_amdgcn_s_setprio(0);
    const int kind = helper ? 3 : lw;
    int e0 = WS_G2 + rbk * 1024, e1 = WS_S12 + (cbk * 128 + (kind - 1) * 64) * 16, e3 = WS_MF + 4096 + db * 512;
    asm volatile("" : "+s"(e0), "+s"(e1), "+s"(e3));
    const bool mid = (kind + 1) & 2;
    const int eo = (mid ? e1 : kind ? e3 : e0) + (lane & (kind == 3 ? 31 : 63)) * 16;
    LAS char* ea = smem + LD_MISC + (mid ? 256 + kind * 256 : kind * 512) + lane * (mid ? 4 : 8);
    u32x4 ev;
    if (helper) {
      BARRIER();
      ev = poll16(rws, eo, 0, ta, tb);
      *(LAS u32*)ea = ev[0]; *(LAS u32*)(ea + 4) = ev[2];
      BARRIER();
      __builtin_amdgcn_s_sleep(20);
      w2_stage(1); w2_stage(2); w2_stage(3);
      BARRIER(); BARRIER();
      w2_stage(0); w2_stage(4); w2_stage(5);
      wr_load(true);
    } else {
      __builtin_amdgcn_s_sleep(F2_DELAY);
      poll16(rws, ((lane >> 3) * 32 + lw * 8 + (lane & 7)) * 64, WS_F2 + (xcd >> 1) * 16384, ta, tb);
      STAMP(19);
      x_stage(0); x_stage(1); WAIT_VM(4); BARRIER();
      x_stage(2); x_stage(3); WAIT_VM(8); BARRIER();
      x_stage(4); w1_stage(4); WAIT_VM(12); BARRIER();
#pragma unroll 1
      for (int s = 5; s < 8; ++s) { x_stage(s); w1_stage(s); WAIT_VM(16); BARRIER(); }
      w2_stage(0); w2_stage(1); WAIT_VM(16); BARRIER();
      asm volatile("buffer_load_dwordx4 %0, %1, %2, 0 offen sc1" : "=v"(ev) : "v"(eo), "s"(qws) : "memory");
      w2_stage(2); w2_stage(3); WAIT_VM(17); BARRIER();
      asm volatile("s_waitcnt vmcnt(8)" : "+v"(ev) :: "memory");
      if (__any(tagdiff(ev, ta, tb) != 0u)) ev = poll16(rws, eo, 0, ta, tb);
      *(LAS u32*)ea = ev[0]; *(LAS u32*)(ea + (mid ? 512 : 4)) = ev[2];
      w2_stage(4); w2_stage(5); BARRIER();
    }
    STAMP(22);
    w2_stage(6); w2_stage(7); w2_stage(8);
    {
      const int xs = WS_X1 + tbk * ST_X1 + (db * 32 + (lw >> 1) * 16) * 128 + (lw & 1) * 64;
      dma16_sc1(qws, LD_MISC + 2048 + lw * 2048, (lane >> 2) * 128 + (lane & 3) * 16, xs);
      dma16_sc1(qws, LD_MISC + 3072 + lw * 2048, (lane >> 2) * 128 + (lane & 3) * 16, xs + ST_X1H);
    }
    WAIT_VM(0);
    STAMP(25);
    BARRIER();
    BARRIER();
#pragma unroll
    for (int i = 0; i < 20; ++i) {
      if (i == 8) BARRIER();
      *(LAS u32x4*)(smem + w2_unit(9 + i / 4) * UNIT + lw * 4096 + (i & 3) * 1024 + lane * 16) = wr[i];
    }
    BARRIER();
    if (b == 0 && tid == 256) *(u32x2*)(p.ws + WS_EPOCH) = u32x2{ta, tb};
    STAMP(26);
    STAMP_FLUSH(p.ws, 1);
    return;
  }

  ROLES();
  const int pch = w >> 1, pn = (w & 1) * 64 + lane, po = (d0 + pch) * NS + pn;
  u32 rar = LDNT(p.Are + po), rai = LDNT(p.Aim + po), rbr = LDNT(p.Bre + po), rbi = LDNT(p.Bim + po), rcr = LDNT(p.Cre + po), rci = LDNT(p.Cim + po);
  SCHED();
  SCAL_ISSUE(p.ldt + d0);
  {
    u32 cp0;
    {
      u64 pc; int r0, r1;
      asm volatile("s_getpc_b64 %0\n\ts_mov_b32 %1, fused_block@rel32@lo+4\n\ts_mov_b32 %2, fused_block_end@rel32@lo+12" : "=s"(pc), "=s"(r0), "=s"(r1));
      TOUCH(cp0, min(((w * 4 + (lane & 3)) * 32 + j) * 128, r1 - r0), pc + (long long)r0);
    }
    LAS char* flg = smem + LD_FLG;
    *(LAS u32x4*)(smem + LD_T + ((lane >> 1) & 7) * TCOPY + 4096 + (lane & 1) * 16) = u32x4{0u, 0u, 0u, 0u};
    *(LAS u32*)(smem + LD_KD + KD_ZERO * 4) = 0u;
    asm volatile("s_waitcnt lgkmcnt(0)" : "+s"(epoch), "+s"(aux));
    int ga[11], ka[4];
#pragma unroll
    for (int i = 0; i < 11; ++i) {
      const int l = 2047 - (tid * 8 + i);
      const int bits = (int)(pk((float)l, 0.f) & 0xffffu);
      const int idx = l < 128 ? l : bits - 0x4300 + 128;
      ga[i] = LD_KD + 4 * (l < 0 ? KD_ZERO : idx + (idx >> 3));
    }
    {
      const int e = lane & 15, g = lane >> 4;
      const u32 cfg = e < 8 ? (w == 0 ? 0x100004u : w == 1 ? 0x080004u : 0x022001u) : (w == 0 ? 0x100004u : w == 1 ? 0x044002u : 0x088003u);
      const int sh = cfg & 15, base = 256 * w + ((cfg >> 8) & 0xff) * (e >> sh) + (e & ((1 << sh) - 1));
#pragma unroll
      for (int r = 0; r < 4; ++r) { const int idx = base + (int)(cfg >> 16) * (4 * g + r); ka[r] = LD_KD + 4 * (idx + (idx >> 3)); PIN(ka[r]); }
    }
    STAMP(1);
    asm volatile("" : "+v"(rar), "+v"(rai), "+v"(rbr), "+v"(rbi), "+v"(rcr), "+v"(rci) : "v"(cp0));
    STAMP(2);
    SCHED();
    volatile LAS u32* P = (LAS u32*)(smem + LD_P) + pch * 64 * PROW + pn;
    const float dt = rb(__builtin_amdgcn_exp2f((pch ? bfhi(aux) : bflo(aux)) * LOG2E));
    const float a2 = (dt * bflo(rar)) * LOG2E, bq = (dt * bflo(rai)) * 0.15915494309189535f;
    const f32x2 z1 = cpow(a2, bq, 1.f), z8 = cpow(a2, bq, 8.f), z256 = cpow(a2, bq, 256.f);
    const f32x2 z16c = cmul(z8, z8) * f32x2{1.f, -1.f}, z512 = cmul(z256, z256), z768 = cmul(z512, z256);
    f32x2 u = cpow(a2, bq, 1024.f), q = {1.f, 0.f};
    f32x2 r = cmul(f32x2{bflo(rcr), -bflo(rci)}, f32x2{bflo(rbr), -bflo(rbi)} * dt);
#pragma unroll
    for (int e = 0; e < 16; ++e) {
      const f32x2 rn = cmul(r, z16c), qn = cmul(q, z1);
      P[e * PROW] = pk2(r);
      P[(16 + e) * PROW] = pk2(q);
      if (!(e & 1)) P[(32 + e / 2) * PROW] = pk2(cmul(q, z256));
      if (!(e & 3)) { P[(40 + e / 4) * PROW] = pk2(cmul(q, z512)); P[(44 + e / 4) * PROW] = pk2(cmul(q, z768)); }
      if ((e & 3) == 1) { P[(48 + e / 2) * PROW] = pk2(u); P[(49 + e / 2) * PROW] = pk2(cmul(u, z8)); }
      if ((e & 3) == 3) u = cmul(u, z256);
      r = rn; q = qn;
    }
    P[56 * PROW] = pk2(u);
    STAMP(3);
    quad_arrive(flg, w, 1u);
#pragma unroll
    for (int i = 0; i < 11; ++i) PIN(ga[i]);
    quad_wait(flg, 1u);
    STAMP(4);
    if (w < 3) {
      LAS const u32* fb = (LAS const u32*)(smem + LD_P) + (lane & 15) * PROW + ((lane >> 4) & 1) * 64 + (lane >> 5) * 32;
      u32x4 fa[16], fc[16];
      f32x4 acc[2] = {{0.f, 0.f, 0.f, 0.f}, {0.f, 0.f, 0.f, 0.f}};
#pragma unroll
      for (int ks = 0; ks < 22; ++ks) {
        if (ks < 16) {
          const int o = (ks & 1) * 64 * PROW + (ks >> 1) * 4;
          fa[ks] = *(LAS const u32x4*)(fb + o); fc[ks] = *(LAS const u32x4*)(fb + (w + 1) * 16 * PROW + o);
        }
        SCHED();
        if (ks >= 6) acc[ks & 1] = mfma16(fa[ks - 6], fc[ks - 6], acc[ks & 1]);
        SCHED();
      }
#pragma unroll
      for (int r4 = 0; r4 < 4; ++r4) *(LAS u32*)(smem + ka[r4]) = pk(acc[0][r4], acc[1][r4]);
    }
    STAMP(5);
    quad_arrive(flg, w, 2u);
    quad_wait(flg, 2u);
    STAMP(6);
    u32 e[11];
#pragma unroll
    for (int i = 0; i < 11; ++i) e[i] = *(LAS const u32*)(smem + ga[i]);
#pragma unroll
    for (int c = 0; c < 2; ++c)
#pragma unroll
      for (int s = 0; s < 4; ++s) {
        u32x4 o;
#pragma unroll
        for (int z = 0; z < 4; ++z) o[z] = __builtin_amdgcn_perm(e[s + 2 * z + 1], e[s + 2 * z], c ? 0x07060302u : 0x05040100u);
        *(LAS u32x4*)(smem + LD_T + (c * 4 + s) * TCOPY + tid * 16) = o;
      }
    quad_arrive(flg, w, 3u);
  }
  epoch += 0x9E3779B97F4A7C15ull;
  const u32 ta = (u32)epoch, tb = (u32)(epoch >> 32);
  {
    const int r = lane & 15, g = lane >> 4, ch = w >> 1, half = w & 1;
    LAS const char* Tb = smem + LD_T + (ch * 4 + ((3 - r) & 3)) * TCOPY + 2 * (2028 - 4 * (r >> 2) + 8 * g - 128 * half) - 7 * 512 - 192;
    LAS const char* Ub = smem + LD_U + ch * 4608 + 2 * (256 + 16 * r - 16 + 8 * g - 128 * half) - 192;
    LAS f32x4* red = (LAS f32x4*)(smem + (ch ? LD_RED1 : LD_RED0)) + half * 512 + (lane ^ ((lane >> 4) << 2));
    PIN(Ub); PIN(red);
    f32x4 acc[8];
#pragma unroll
    for (int n = 0; n < 8; ++n) acc[n] = f32x4{0.f, 0.f, 0.f, 0.f};
    u32x4 uf[2][8], tf[4][8];
    quad_wait(smem + LD_FLG, 3u);
#define LD_TH(e, a, hh) do { const u32x2 l_ = *(volatile LAS const u32x2*)(Tb + 64 * (3 - (e)) + 512 * (7 - (a)) + 8 * (hh)); tf[e][a][2 * (hh)] = l_[0]; tf[e][a][2 * (hh) + 1] = l_[1]; } while (0)
#pragma unroll
    for (int e = 0; e < TF_PRE; ++e)
#pragma unroll
      for (int a = 0; a < 8; ++a) { LD_TH(e, a, 0); LD_TH(e, a, 1); }
#define LD_UF(n, e) uf[(e) & 1][n] = *(LAS const u32x4*)(Ub + 64 * (3 - (e)) + 512 * (n))
#define CM(e, a, m) do { SCHED(); acc[(a) + (m)] = mfma16(tf[e][a], uf[(e) & 1][m], acc[(a) + (m)]); SCHED(); } while (0)
    STAMP(10);
    u32 ft;
    quad_wait(smem + LD_FLG + 16, FT_AT);
    __builtin_amdgcn_s_sleep(FT_DELAY);
    TOUCH(ft, ((b * 2 + (w & 1)) * 28 + min(lane, 27)) * 128, w < 2 ? p.w1 : p.w2);
    BARRIER();
    STAMP(11);
#pragma unroll
    for (int n = 0; n < 8; ++n) LD_UF(n, 0);
#pragma unroll
    for (int e = 0; e < 3; ++e) {
      int cnt = 0;
#pragma unroll
      for (int m = 0; m < 8; ++m)
#pragma unroll
        for (int a = 0; a + m < 8; ++a) {
          CM(e, a, m);
          if (cnt >= 4 && cnt < 12) LD_UF(cnt - 4, e + 1);
          if (cnt >= 12 && cnt < 28 && e + TF_PRE < 4) LD_TH(e + TF_PRE, (cnt - 12) >> 1, (cnt - 12) & 1);
          ++cnt;
        }
    }
#pragma unroll
    for (int n = 0; n < 8; ++n)
#pragma unroll
      for (int a = 0; a <= n; ++a) { CM(3, a, n - a); if (a == 1) red[(n - 1) * 64] = acc[n - 1]; }
    STAMP(12);
    red[7 * 64] = acc[7];
    BARRIER();
    STAMP(14);
    WAIT_VM(0);
    asm volatile("" :: "v"(ft));
  }
  if (helper) {
    const int hb = xcd * 4 + j;
    u32x4 v[16];
    const int rg = tid >> 3;
    LAS float* rd = (LAS float*)(smem + 4096);
    { const u32x4 m = poll16(rws, tid * 16, WS_MF, ta, tb); *(LAS u32x2*)(smem + tid * 8) = u32x2{m[0], m[2]}; }
    BARRIER();
#pragma unroll
    for (int i = 0; i < 16; ++i) v[i] = *(LAS const u32x4*)(smem + LD_WR + i * 4096 + tid * 16);
    float a[16];
#pragma unroll
    for (int e = 0; e < 16; ++e) a[e] = 0.f;
#pragma unroll
    for (int i = 0; i < 16; ++i) {
      const u32 gs = *(LAS const u32*)(smem + (i * 32 + rg) * 4);
      const u32 gl = gs & 0xffffu, gh = gs << 16, sl = gs >> 16, sh = gs & 0xffff0000u;
#pragma unroll
      for (int z = 0; z < 4; ++z) {
        a[4 * z] = dot2(v[i][z], gl, a[4 * z]); a[4 * z + 1] = dot2(v[i][z], sl, a[4 * z + 1]);
        a[4 * z + 2] = dot2(v[i][z], gh, a[4 * z + 2]); a[4 * z + 3] = dot2(v[i][z], sh, a[4 * z + 3]);
      }
    }
    fold_cols(a, rd + w * 128, lane);
    STAMP(18);
    poll16(rws, tid * 64, WS_F2 + (xcd >> 1) * 16384, ta, tb);
    BARRIER();
    STAMP(19);
#pragma unroll
    for (int i = 0; i < 16; ++i) v[i] = ld_sc1(rws, tid * 16, WS_X1 + hb * ST_X1 + (i >> 3) * ST_X1H + (i & 7) * 4096);
    if (w == 0 && hb < 28) {
      const f32x2 s = sum4(rd + lane * 2, 128);
      st_sc1(rws, lane * 16, WS_S12 + hb * 1024, u32x4{__float_as_uint(s[0]), ta, __float_as_uint(s[1] * -LOG2E), tb});
    }
#pragma unroll
    for (int e = 0; e < 16; ++e) a[e] = 0.f;
#pragma unroll
    for (int i = 0; i < 16; ++i)
#pragma unroll
      for (int z = 0; z < 4; ++z) {
        const int o = (i >> 3) * 8 + 2 * z;
        a[o] = dot2(v[i][z], 0x3f803f80u, a[o]); a[o + 1] = dot2(v[i][z], v[i][z], a[o + 1]);
      }
    STAMP(20);
    fold_cols(a, rd + 512 + w * 128, lane);
    BARRIER();
    if (w == 0) {
      const f32x2 s = sum4(rd + 512 + lane * 2, 128);
      const u32 st = ln_stat(s[0], s[1]);
      const u32 st2 = __builtin_amdgcn_mov_dpp(st, 0xB1, 0xf, 0xf, true);
      if (!(lane & 1)) st_sc1(rws, lane * 8, WS_G2 + hb * 512, u32x4{st, ta, st2, tb});
    }
    BARRIER();
    STAMP(22);
    __builtin_amdgcn_s_sleep(H_SLEEP);
  } else {
    const int wt = w & 1, wf = w >> 1;
    const int h = lane >> 5, blk = (lane >> 4) & 1, q = (lane & 15) >> 2, pp = lane & 3;
    int ax[2][2], aw[2][2];
#pragma unroll
    for (int c = 0; c < 2; ++c)
#pragma unroll
      for (int t = 0; t < 2; ++t) {
        const int base = 256 * (8 * h + 4 * t + q) + 16 * ((2 * blk + (pp >> 1)) ^ (2 * h + t)) + 8 * (pp & 1);
        ax[c][t] = LD_XR + base + 64 * ((2 * wt + c) ^ q);
        aw[c][t] = LD_WR + base + 64 * ((2 * wf + c) ^ q);
        PIN(ax[c][t]); PIN(aw[c][t]);
      }
    f32x16 acc[2][2];
#pragma unroll
    for (int a = 0; a < 2; ++a)
#pragma unroll
      for (int c = 0; c < 2; ++c)
#pragma unroll
        for (int r = 0; r < 16; ++r) acc[a][c][r] = 0.f;
    u32x4 xf[2][2], wfr[2][2];
    u32 st[2];
    int ho = WS_H + ((rbk * 4 + wt * 2) * 112 + cbk * 8 + wf * 4) * 1024 + lane * 16, ho1 = ho + 112 * 1024;
    int fo = only(lane < 8, WS_F3 + ((rbk * 4 + w) * 16 + cbk) * 128 + lane * 16);
    PIN(ho); PIN(ho1); PIN(fo);
#define RD_X(i, c) xf[(i) & 1][c] = tr_frag(smem + ax[c][0] + ((i) & 15) * 4096, smem + ax[c][1] + ((i) & 15) * 4096)
#define RD_W(i, c) wfr[(i) & 1][c] = tr_frag(smem + aw[c][0] + ((i) & 15) * 4096, smem + aw[c][1] + ((i) & 15) * 4096)
#define MM(i, a, c) do { SCHED(); acc[a][c] = mfma32(wfr[(i) & 1][a], xf[(i) & 1][c], acc[a][c]); SCHED(); } while (0)
    BARRIER();
    STAMP(19);
    RD_W(0, 0); RD_X(0, 0); RD_X(0, 1); RD_W(0, 1);
#pragma unroll
    for (int i = 0; i < 32; ++i) {
      if ((i & 3) != 3) {
        MM(i, 0, 0); RD_W(i + 1, 0); MM(i, 0, 1); RD_X(i + 1, 0); MM(i, 1, 0); RD_X(i + 1, 1); MM(i, 1, 1); RD_W(i + 1, 1);
      } else {
        MM(i, 0, 0); MM(i, 0, 1);
        BARRIER();
        if (i == 31) { st[0] = *(LAS const u32*)(smem + LD_MISC + (wt * 64 + (lane & 31)) * 4); st[1] = *(LAS const u32*)(smem + LD_MISC + (wt * 64 + 32 + (lane & 31)) * 4); }
        MM(i, 1, 0);
        if (i < 31) { RD_W(i + 1, 0); RD_X(i + 1, 0); }
        MM(i, 1, 1);
        if (i < 31) { RD_X(i + 1, 1); RD_W(i + 1, 1); }
      }
    }
    STAMP(21);
    float rstd[2], mr[2];
#pragma unroll
    for (int c = 0; c < 2; ++c) {
      rstd[c] = __builtin_amdgcn_rcpf(bfhi(st[c])) * -LOG2E;
      mr[c] = -bflo(st[c]) * rstd[c];
    }
    STAMP(22);
    LAS const float* s12 = (LAS const float*)(smem + LD_MISC + 512) + wf * 64 + 4 * h;
#pragma unroll
    for (int a = 0; a < 2; ++a)
#pragma unroll
      for (int sp = 0; sp < 2; ++sp) {
        f32x4 s1[2], s2[2];
#pragma unroll
        for (int z = 0; z < 2; ++z) { s1[z] = *(LAS const f32x4*)(s12 + a * 32 + sp * 16 + z * 8); s2[z] = *(LAS const f32x4*)(s12 + 128 + a * 32 + sp * 16 + z * 8); }
#pragma unroll
        for (int c = 0; c < 2; ++c) {
          u32x4 o;
#pragma unroll
          for (int e = 0; e < 4; ++e) {
            const int z = e >> 1, k = (e & 1) * 2;
            const f32x2 av = {acc[a][c][sp * 8 + 2 * e], acc[a][c][sp * 8 + 2 * e + 1]};
            const f32x2 cc = f32x2{s1[z][k], s1[z][k + 1]} * mr[c] + f32x2{s2[z][k], s2[z][k + 1]};
            const f32x2 ar = av * rstd[c] + cc;
            const f32x2 dn = f32x2{__builtin_amdgcn_exp2f(ar[0]), __builtin_amdgcn_exp2f(ar[1])} + 1.f;
            o[e] = pk2(ar * f32x2{__builtin_amdgcn_rcpf(dn[0]), __builtin_amdgcn_rcpf(dn[1])});
          }
          st_sc1(rws, c ? ho1 : ho, (a * 2 + sp) * 1024, o);
        }
      }
    STAMP(23);
    WAIT_VM(0);
    STAMP(24);
    st_sc1(rws, fo, 0, u32x4{ta, ta, tb, tb});
  }

  {
    const int wt = w & 1, kh = w >> 1, h = lane >> 5, tl = lane & 31;
    const int blk = (lane >> 4) & 1, q = (lane & 15) >> 2, pp = lane & 3;
    LAS char* aw[3][2];
#pragma unroll
    for (int c = 0; c < 6; ++c) {
      aw[c >> 1][c & 1] = smem + (c >> 1) * 65536 + kh * 8192 + 128 * (4 * h + q) + 64 * ((c & 1) ^ kh ^ (q >> 1)) + 32 * blk + 8 * pp;
      asm volatile("" : "+v"(aw[c >> 1][c & 1]));
    }
    const rsrc_t ro = __builtin_amdgcn_make_buffer_rsrc(p.out, 0, L * D * 2, 0x00020000);
    int oo[2];
#pragma unroll
    for (int i = 0; i < 2; ++i) {
      const int r = tbk * 64 + (tid >> 3) + 32 * i;
      oo[i] = ((r & ~511) + 128 * ((r >> 1) & 3) + 2 * ((r >> 3) & 63) + (r & 1)) * (D * 2) + db * 128 + (tid & 7) * 16;
      PIN(oo[i]);
    }
    f32x16 acc[2];
#pragma unroll
    for (int r = 0; r < 16; ++r) { acc[0][r] = 0.f; acc[1][r] = 0.f; }
    STAMP(25);
    BARRIER();
    constexpr int NW = G2_NW;
    u32x4 hf[NW], wf[3][2];
#define RD_W2C(i, c) do { LAS char* a_ = aw[w2_unit((i) >> 2) >> 2][c] + (w2_unit((i) >> 2) & 3) * UNIT + ((i) & 3) * 2048; wf[(i) % 3][c] = tr_frag(a_, a_ + 1024); } while (0)
#define RD_W2(i) do { RD_W2C(i, 0); RD_W2C(i, 1); } while (0)
    RD_W2(0); RD_W2(1);
    const int sh = WS_H + ((tbk * 2 + wt) * 112 + 56 * kh) * 1024;
#define LD_H(i) hf[(i) % NW] = ld_sc1(rws, lane * 16 + ((i) & 3) * 1024, sh + w2_phys((i) >> 2) * 4096)
    if (F3_DELAY) __builtin_amdgcn_s_sleep(F3_DELAY);
    poll16(rws, ((lane & 1) * 32 + min(lane >> 1, 6)) * 128, WS_F3 + ((tbk >> 1) * 4 + (tbk & 1)) * 2048 + kh * 896, ta, tb);
    STAMP(26);
#pragma unroll
    for (int i = 0; i < G2_N0; ++i) LD_H(i);
    SCHED();
    u32 xr[8]; f32x2 gb[8];
#pragma unroll
    for (int i = 0; i < 8; ++i) {
      const int pr = 4 * (i >> 1) + 2 * h + (i & 1);
      xr[i] = *(LAS const u32*)(smem + LD_MISC + 2048 + w * 2048 + ((tl >> 2) & 1) * 1024 + pr * 64 + (tl >> 3) * 16 + (tl & 3) * 4);
      gb[i] = *(LAS const f32x2*)(smem + LD_MISC + 1536 + (kh * 16 + pr) * 8);
    }
    SCHED();
#pragma unroll
    for (int i = 0; i < 56; ++i) {
      SCHED();
      acc[0] = mfma32(wf[i % 3][0], hf[i % NW], acc[0]);
      SCHED();
      if (i == 0) STAMP(29);
      if (i == 18) STAMP(30);
      if (i == 42) STAMP(31);
      if (i == 6 || i == 18 || i == 42) BARRIER();
      if (i + 2 < 56) RD_W2C(i + 2, 0);
      if (h_next(i) < h_next(i + 1)) LD_H(h_next(i));
      SCHED();
      acc[1] = mfma32(wf[i % 3][1], hf[i % NW], acc[1]);
      SCHED();
      if (i + 2 < 56) RD_W2C(i + 2, 1);
      if (h_next(i) + 1 < h_next(i + 1)) LD_H(h_next(i) + 1);
      asm volatile("" :: "v"(wf[i % 3][0]), "v"(wf[i % 3][1]), "v"(hf[i % NW]));
    }
    STAMP(27);
    LAS f32x4* ex = (LAS f32x4*)(smem + w2_unit(5) * UNIT) + lane;
#pragma unroll
    for (int qq = 0; qq < 4; ++qq) ex[(w * 4 + qq) * 64] = f32x4{acc[1][4 * qq], acc[1][4 * qq + 1], acc[1][4 * qq + 2], acc[1][4 * qq + 3]};
    BARRIER();
    LAS char* ot = smem + w2_unit(6) * UNIT;
    f32x4 oth[4];
#pragma unroll
    for (int qq = 0; qq < 4; ++qq) oth[qq] = ex[((w ^ 2) * 4 + qq) * 64];
    SCHED();
#pragma unroll
    for (int qq = 0; qq < 4; ++qq) {
      const f32x4 o4 = oth[qq];
      u32x2 o;
#pragma unroll
      for (int z = 0; z < 2; ++z)
        o[z] = pk2((f32x2{acc[0][4 * qq + 2 * z], acc[0][4 * qq + 2 * z + 1]} + f32x2{o4[2 * z], o4[2 * z + 1]}) * gb[2 * qq + z] + up(xr[2 * qq + z]));
      *(LAS u32x2*)(ot + (wt * 32 + tl) * 144 + kh * 64 + qq * 16 + h * 8) = o;
    }
    BARRIER();
#pragma unroll
    for (int i = 0; i < 2; ++i) {
      const int row = (tid >> 3) + 32 * i;
      __builtin_amdgcn_raw_buffer_store_b128(*(LAS const u32x4*)(ot + row * 144 + (tid & 7) * 16), ro, oo[i], 0, OUT_AUX);
    }
    STAMP(28);
    STAMP_FLUSH(p.ws, 0);
  }
}

extern "C" __global__ void fused_block_end() {}

extern "C" __global__ void __launch_bounds__(1024) probe_k(char* ws) {
  __shared__ unsigned mn, mx; __shared__ unsigned long long sum; __shared__ unsigned cnt;
  if (threadIdx.x == 0) { mn = 0xffffffffu; mx = 0u; sum = 0ull; cnt = 0u; }
  __syncthreads();
  const unsigned long long* p = (const unsigned long long*)(ws + ST_OFF);
  const int b = threadIdx.x >> 2, w = (threadIdx.x & 3) + 0 * 4;
  const size_t base = (((size_t)0 * ST_MAXB + b) * ST_MAXW + w) * ST_SLOTS * 2;
  const unsigned t0 = (unsigned)p[base], ts = (unsigned)p[base + 2 * 28];
  atomicMin(&mn, t0);
  if (ts) { atomicMax(&mx, ts); atomicAdd(&sum, (unsigned long long)ts); atomicAdd(&cnt, 1u); }
  __syncthreads();
  unsigned d = 0u;
  if (d > 10000u) d = 10000u;
  for (int i = 0; i < 1000000; ++i) if ((((unsigned)__builtin_amdgcn_s_memrealtime() | 0x80000000u) - mn) >= d + 5000u) break;
}
extern "C" void kernel_launch(void* const* d_in, const int* in_sizes, int n_in, void* d_out, int out_size, void* d_ws, size_t ws_size,
                              hipStream_t stream) {
  Params p;
  p.x = (const u16*)d_in[0]; p.Are = (const u16*)d_in[1]; p.Aim = (const u16*)d_in[2]; p.Bre = (const u16*)d_in[3];
  p.Bim = (const u16*)d_in[4]; p.Cre = (const u16*)d_in[5]; p.Cim = (const u16*)d_in[6]; p.ldt = (const u16*)d_in[7];
  p.dskip = (const u16*)d_in[8]; p.w1 = (const u16*)d_in[10]; p.w2 = (const u16*)d_in[11]; p.c = (const u16*)d_in[12];
  p.wmod = (const u16*)d_in[13]; p.out = (u16*)d_out; p.ws = (char*)d_ws;
  (void)hipFuncSetAttribute((const void*)fused_block, hipFuncAttributeMaxDynamicSharedMemorySize, LDS_BYTES + ST_LDS_EXTRA);
  fused_block<<<256, 512, LDS_BYTES + ST_LDS_EXTRA, stream>>>(p);
  probe_k<<<1, 1024, 0, stream>>>(p.ws);
}
```

```cpp
#define F2_DELAY 22
#include <hip/hip_runtime.h>
#include <hip/hip_bf16.h>

#define STAMP_DECL
#define STAMP(s) __builtin_amdgcn_sched_barrier(0)
#define STAMP_FLUSH(ws, k)
#define ST_LDS_EXTRA 0
#define ST_RANGES {{0, 32, 256, "compute waves"}, {1, 32, 256, "loader waves"}, {0, 0, 32, "helper WGs, waves 0-3"}, {1, 0, 32, "helper WGs, waves 4-7"}}

typedef unsigned short u16;
typedef unsigned int u32;
typedef unsigned long long u64;
typedef u32 u32x2 __attribute__((ext_vector_type(2)));
typedef u32 u32x4 __attribute__((ext_vector_type(4)));
typedef float f32x2 __attribute__((ext_vector_type(2)));
typedef float f32x4 __attribute__((ext_vector_type(4)));
typedef float f32x16 __attribute__((ext_vector_type(16)));
typedef short s16x4 __attribute__((ext_vector_type(4)));
typedef __bf16 bf16x2 __attribute__((ext_vector_type(2)));
typedef __bf16 bf16x8 __attribute__((ext_vector_type(8)));
typedef __amdgpu_buffer_rsrc_t rsrc_t;
#define GAS __attribute__((address_space(1)))
#define LAS __attribute__((address_space(3)))
#define CAS __attribute__((address_space(4)))

constexpr int L = 2048, D = 512, NS = 128, F = 1792;

constexpr int WS_EPOCH = 0;
constexpr int WS_SG = 0x1000;
constexpr int WS_MG = 0x80000;
constexpr int WS_F2 = 0x40000;
constexpr int WS_F3 = 0x50000;
constexpr int WS_G2 = 0x70000;
constexpr int WS_S12 = 0x74000;
constexpr int WS_MF = 0x7c000;
constexpr int WS_XG = 2 << 20;
constexpr int WS_X1 = 0x440000;
constexpr int WS_X1S = 0x680000;
constexpr int WS_H = 9 << 20;
constexpr int WS_END = 16 << 20;
#define PADS 1
constexpr int ST_XG = 8192 + 128 * PADS, ST_MG = 1536 * 16 + 128 * PADS, ST_X1H = 32768 + 128 * PADS, ST_X1 = 2 * ST_X1H, ST_X1S = 131072 + 384 * PADS;
constexpr int OOB = 0x7ff00000;

constexpr int UNIT = 16384;
constexpr int LD_XR = 0, LD_WR = 4 * UNIT, LD_MISC = 9 * UNIT, LD_FLG = LD_MISC + 10240, LD_MV = LD_FLG + 32, LDS_BYTES = LD_MV + 64;
constexpr int TCOPY = 4160, PROW = 132;
constexpr int LD_T = 0, LD_U = 8 * TCOPY, LD_KD = LD_U + 2 * 4608, LD_P = LD_WR, LD_RED0 = 3 * UNIT, LD_RED1 = 8 * UNIT;
constexpr int KD_ZERO = 900, LD_XT = LD_RED0;
static_assert(LD_KD + 904 * 4 <= LD_RED0, "LDS map");
__host__ __device__ constexpr int w2_phys(int u) { return (u + 5) % 14; }
__host__ __device__ constexpr int w2_unit(int u) { return u % 9 == 8 ? 8 : (u % 9) / 2 + 4 * (u % 9 & 1); }
#define G2_NW 14
#define G2_N0 14
__host__ __device__ constexpr int h_next(int i) { int a = G2_N0 + 2 * i, b = i > 0 ? i - 1 + G2_NW : G2_NW; a = a < b ? a : b; return a < 56 ? a : 56; }
#define TF_PRE 4
#define F3_DELAY 10
#define XG_DELAY 0
#define OUT_AUX 2
#define G_EARLY 1
#define G_SIG 5
#define FT_AT 3
#define FT_DELAY 0
#define H_SLEEP 110
#define S1_SLEEP 2
#define POLL_SLEEP 2
#define LDNT(p) __builtin_nontemporal_load(p)
#define AUX_NT 2
#define WF ""
#define AUX_W 0
constexpr int SPIN_CAP = 1 << 15;
constexpr float LOG2E = 1.44269504088896341f;

__device__ __forceinline__ float bflo(u32 v) { return __uint_as_float(v << 16); }
__device__ __forceinline__ float bfhi(u32 v) { return __uint_as_float(v & 0xffff0000u); }
__device__ __forceinline__ u32 pk(float lo, float hi) {
  f32x2 v = {lo, hi};
  bf16x2 r = __builtin_convertvector(v, bf16x2);
  return __builtin_bit_cast(u32, r);
}
__device__ __forceinline__ float rb(float f) { return bflo(pk(f, f)); }
__device__ __forceinline__ float dot2(u32 a, u32 b, float c) {
  return __builtin_amdgcn_fdot2_f32_bf16(__builtin_bit_cast(bf16x2, a), __builtin_bit_cast(bf16x2, b), c, false);
}
__device__ __forceinline__ f32x4 mfma16(u32x4 a, u32x4 b, f32x4 c) {
  return __builtin_amdgcn_mfma_f32_16x16x32_bf16(__builtin_bit_cast(bf16x8, a), __builtin_bit_cast(bf16x8, b), c, 0, 0, 0);
}
__device__ __forceinline__ f32x16 mfma32(u32x4 a, u32x4 b, f32x16 c) {
  return __builtin_amdgcn_mfma_f32_32x32x16_bf16(__builtin_bit_cast(bf16x8, a), __builtin_bit_cast(bf16x8, b), c, 0, 0, 0);
}
template <int CTRL> __device__ __forceinline__ float dpp(float x) {
  return __builtin_bit_cast(float, __builtin_amdgcn_mov_dpp(__builtin_bit_cast(int, x), CTRL, 0xf, 0xf, true));
}
__device__ __forceinline__ float row_sum(float x) {
  x += dpp<0xB1>(x); x += dpp<0x4E>(x); x += dpp<0x124>(x); x += dpp<0x128>(x);
  return x;
}
__device__ __forceinline__ float fold32(float a, float b) {
  auto s = __builtin_amdgcn_permlane32_swap(__float_as_uint(a), __float_as_uint(b), false, false);
  return __uint_as_float(s[0]) + __uint_as_float(s[1]);
}
__device__ __forceinline__ float fold16(float a, float b) {
  auto s = __builtin_amdgcn_permlane16_swap(__float_as_uint(a), __float_as_uint(b), false, false);
  return __uint_as_float(s[0]) + __uint_as_float(s[1]);
}
__device__ __forceinline__ u32x4 mkrs(const void* p, u32 bytes) {
  const u64 a = (u64)p;
  return u32x4{(u32)a, (u32)(a >> 32) & 0xffffu, bytes, 0x00020000u};
}
__device__ __forceinline__ void dma4(u32x4 rs, int lds, int voff, int soff) {
  asm volatile("s_mov_b32 m0, %0\n\ts_nop 0\n\tbuffer_load_dword %1, %2, %3 offen lds" :: "s"(lds), "v"(voff), "s"(rs), "s"(soff) : "memory");
}
__device__ __forceinline__ void dma16_sc1(u32x4 rs, int lds, int voff, int soff) {
  asm volatile("s_mov_b32 m0, %0\n\ts_nop 0\n\tbuffer_load_dwordx4 %1, %2, %3 offen sc1 lds" :: "s"(lds), "v"(voff), "s"(rs), "s"(soff) : "memory");
}
#define DMA4_TEXT(F) "s_mov_b32 m0, %0\n\ts_nop 0\n\tbuffer_load_dwordx4 %1, %5, %6 offen" F " lds\n\tbuffer_load_dwordx4 %2, %5, %6 offen offset:1024" F " lds\n\t" \
                     "buffer_load_dwordx4 %3, %5, %6 offen offset:2048" F " lds\n\tbuffer_load_dwordx4 %4, %5, %6 offen offset:3072" F " lds"
__device__ __forceinline__ void dma4k(u32x4 rs, int lds, const int (&v)[4], int soff) {
  asm volatile(DMA4_TEXT(WF) :: "s"(lds), "v"(v[0]), "v"(v[1]), "v"(v[2]), "v"(v[3]), "s"(rs), "s"(soff) : "memory");
}
__device__ __forceinline__ void dma4k_sc1(u32x4 rs, int lds, const int (&v)[4], int soff) {
  asm volatile(DMA4_TEXT(" sc1") :: "s"(lds), "v"(v[0]), "v"(v[1]), "v"(v[2]), "v"(v[3]), "s"(rs), "s"(soff) : "memory");
}
__device__ __forceinline__ u32x4 ld_sc1(rsrc_t rs, int voff, int soff) { return __builtin_amdgcn_raw_buffer_load_b128(rs, voff, soff, 16); }
__device__ __forceinline__ void st_sc1(rsrc_t rs, int voff, int soff, u32x4 v) { __builtin_amdgcn_raw_buffer_store_b128(v, rs, voff + soff, 0, 16); }
__device__ __forceinline__ u32x4 tr_frag(LAS char* a0, LAS char* a1) {
  const s16x4 lo = __builtin_amdgcn_ds_read_tr16_b64_v4i16((LAS s16x4*)a0), hi = __builtin_amdgcn_ds_read_tr16_b64_v4i16((LAS s16x4*)a1);
  const u32x2 l = __builtin_bit_cast(u32x2, lo), h = __builtin_bit_cast(u32x2, hi);
  return u32x4{l[0], l[1], h[0], h[1]};
}
#define SCHED() __builtin_amdgcn_sched_barrier(0)
#define FENCE() asm volatile("" ::: "memory")
#define WAIT_VM(n) asm volatile("s_waitcnt vmcnt(" #n ")" ::: "memory")
#define BARRIER() do { asm volatile("s_waitcnt lgkmcnt(0)" ::: "memory"); __builtin_amdgcn_s_barrier(); FENCE(); } while (0)

__device__ __forceinline__ f32x2 cmul(f32x2 a, f32x2 b) {
  const f32x2 t = f32x2{a[0], a[0]} * b;
  return __builtin_elementwise_fma(f32x2{a[1], a[1]}, f32x2{-b[1], b[0]}, t);
}
__device__ __forceinline__ f32x2 cpow(float a2, float bq, float l) {
  const float m = __builtin_amdgcn_exp2f(a2 * l);
  const float ph = __builtin_amdgcn_fractf(bq * l);
  return f32x2{__builtin_amdgcn_cosf(ph), __builtin_amdgcn_sinf(ph)} * m;
}
__device__ __forceinline__ u32 pk2(f32x2 a) { return pk(a[0], a[1]); }
__device__ __forceinline__ f32x2 up(u32 v) { return f32x2{bflo(v), bfhi(v)}; }
__device__ __forceinline__ u32 ln_stat(float sum, float sq) {
  const float m = sum * (1.f / D);
  const float var = fmaxf(sq * (1.f / D) - m * m, 0.f);
  return pk(m, __builtin_amdgcn_sqrtf(rb(rb(var) + 1e-5f)));
}
__device__ __forceinline__ u32 tagdiff(u32x4 v, u32 ta, u32 tb) { return (v[1] ^ ta) | (v[3] ^ tb); }
__device__ __forceinline__ u32x4 poll16(rsrc_t rs, int voff, int soff, u32 ta, u32 tb) {
  u32x4 v;
  int spins = 0;
  for (;;) {
    FENCE();
    v = ld_sc1(rs, voff, soff);
    if (tagdiff(v, ta, tb) == 0u || ++spins >= SPIN_CAP) break;
    if (POLL_SLEEP) __builtin_amdgcn_s_sleep(POLL_SLEEP);
  }
  FENCE();
  return v;
}
__device__ __forceinline__ bool bad4(const u32x4 (&v)[4], u32 ta, u32 tb) {
  return (tagdiff(v[0], ta, tb) | tagdiff(v[1], ta, tb) | tagdiff(v[2], ta, tb) | tagdiff(v[3], ta, tb)) != 0u;
}
__device__ __forceinline__ void retry4(u32x4 (&v)[4], rsrc_t rs, int voff, int step, u32 ta, u32 tb) {
#pragma unroll 1
  for (int spins = 0; spins < SPIN_CAP; ++spins) {
    __builtin_amdgcn_s_sleep(S1_SLEEP);
    FENCE();
#pragma unroll
    for (int i = 0; i < 4; ++i) if (tagdiff(v[i], ta, tb)) v[i] = ld_sc1(rs, voff, i * step);
    if (!__any(bad4(v, ta, tb))) break;
  }
}
__device__ __forceinline__ void fold_cols(const float (&v)[16], LAS float* dst, int lane) {
  float u[8]; f32x4 t;
#pragma unroll
  for (int k = 0; k < 8; ++k) u[k] = fold32(v[k], v[k + 8]);
#pragma unroll
  for (int k = 0; k < 4; ++k) { const float a = fold16(u[k], u[k + 4]); t[k] = a + dpp<0x128>(a); }
  if (!(lane & 8)) *(LAS f32x4*)(dst + (lane & 7) * 16 + (lane >> 4) * 4) = t;
}
__device__ __forceinline__ f32x2 sum4(LAS const float* src, int stride) {
  return (*(LAS const f32x2*)src + *(LAS const f32x2*)(src + stride)) + (*(LAS const f32x2*)(src + 2 * stride) + *(LAS const f32x2*)(src + 3 * stride));
}
__device__ __forceinline__ u32 silu_bf(u32 c) {
  const float f = bflo(c), s = f * __builtin_amdgcn_rcpf(1.f + __builtin_amdgcn_exp2f(f * -LOG2E));
  return pk(s, s);
}
template <int CTRL> __device__ __forceinline__ void gv_row(u32x4 v, u32 sp, float (&acc)[8]) {
  const u32 sb = __builtin_amdgcn_mov_dpp(sp, CTRL, 0xf, 0xf, true), al = sb & 0xffffu, ah = sb & 0xffff0000u;
#pragma unroll
  for (int q = 0; q < 4; ++q) { acc[2 * q] = dot2(v[q], al, acc[2 * q]); acc[2 * q + 1] = dot2(v[q], ah, acc[2 * q + 1]); }
}
__device__ __forceinline__ f32x2 gv_fold(const float (&acc)[8]) {
  const float m0 = fold16(fold32(acc[0], acc[4]), fold32(acc[2], acc[6])), m1 = fold16(fold32(acc[1], acc[5]), fold32(acc[3], acc[7]));
  return f32x2{m0 + dpp<0x128>(m0), m1 + dpp<0x128>(m1)};
}
#define TOUCH(dst, voff, base) asm volatile("global_load_dword %0, %1, %2" : "=v"(dst) : "v"(voff), "s"(base))

__device__ __forceinline__ void quad_arrive(LAS char* f, int i, u32 n) {
  asm volatile("s_waitcnt lgkmcnt(0)" ::: "memory");
  *(volatile LAS u32*)(f + i * 4) = n;
}
__device__ __forceinline__ void quad_wait(LAS char* f, u32 n) {
  for (;;) {
    const u32x4 v = *(volatile LAS u32x4*)f;
    if (__builtin_amdgcn_readfirstlane(min(min(v[0], v[1]), min(v[2], v[3]))) >= n) break;
    __builtin_amdgcn_s_sleep(1);
  }
  FENCE();
}
__device__ __forceinline__ int only(bool c, int off) { return off | (c ? 0 : OOB); }
#define PIN(x) asm volatile("" : "+v"(x))

struct Params {
  const u16 *x, *Are, *Aim, *Bre, *Bim, *Cre, *Cim, *ldt, *dskip, *w1, *w2, *c, *wmod;
  u16* out;
  char* ws;
};

extern "C" __global__ void __launch_bounds__(512, 2) fused_block(Params p) {
  extern __shared__ __attribute__((aligned(16))) char smem_[];
  LAS char* smem = (LAS char*)smem_;
  const int tid = threadIdx.x, lane = tid & 63, w = __builtin_amdgcn_readfirstlane(tid >> 6), lw = w & 3;
  STAMP_DECL
  STAMP(0);
  *(LAS u32*)(smem + LD_FLG + w * 4) = 0u;
  BARRIER();
  asm volatile("" :: "s"(p.x), "s"(p.Are), "s"(p.Aim), "s"(p.Bre), "s"(p.Bim), "s"(p.Cre), "s"(p.Cim), "s"(p.ldt), "s"(p.dskip), "s"(p.w1), "s"(p.w2), "s"(p.c), "s"(p.wmod),
               "s"(p.out), "s"(p.ws));
#define SCAL_ISSUE(q) u64 epoch; u32 aux; \
  asm volatile("s_load_dwordx2 %0, %2, 0x0\n\ts_load_dword %1, %3, 0x0" : "=&s"(epoch), "=&s"(aux) : "s"(p.ws + WS_EPOCH), "s"(q)); SCHED()
#define SCAL_WAIT() asm volatile("s_waitcnt lgkmcnt(0)" : "+s"(epoch), "+s"(aux)); \
  epoch += 0x9E3779B97F4A7C15ull; ta = (u32)epoch; tb = (u32)(epoch >> 32); dsk = aux
#define ROLES() int b = blockIdx.x; asm volatile("" : "+s"(b)); \
  const int xcd = b & 7, j = b >> 3, pair = xcd * 32 + j, d0 = pair * 2; \
  const bool helper = j < 4; \
  const int rbk = (xcd >> 1) * 4 + (j & 3), cbk = (xcd & 1) * 7 + ((j - 4) >> 2), tbk = xcd * 4 + (j & 3), db = ((j >> 2) + 3 - (int)((u64)p.w2 >> 7)) & 7; \
  const rsrc_t rws = __builtin_amdgcn_make_buffer_rsrc(p.ws, 0, WS_END, 0x00020000)

  if (__builtin_expect(w >= 4, 1)) {
    LAS char* flg = smem + LD_FLG + 16;
    u32x4 sg[4];
    u32x2 xg[4];
    ROLES();
    const int go = lw * 4096 + lane * 16;
    u32 ta, tb, dsk;
    const u32x4 qws = mkrs(p.ws, WS_END);
#define POLL_ISSUE() asm volatile("buffer_load_dwordx4 %0, %4, %5, 0 offen sc1\n\tbuffer_load_dwordx4 %1, %4, %5, 0 offen offset:1024 sc1\n\t" \
      "buffer_load_dwordx4 %2, %4, %5, 0 offen offset:2048 sc1\n\tbuffer_load_dwordx4 %3, %4, %5, 0 offen offset:3072 sc1" \
      : "=&v"(sg[0]), "=&v"(sg[1]), "=&v"(sg[2]), "=&v"(sg[3]) : "v"(go + WS_SG), "s"(qws) : "memory")
    int vx[4], vw1[4], vw2[4], so, xo0, xo1, fo, mfo;
    LAS char *ra[2], *rc[2];
    auto addresses = [&]() {
      const int hm = (j - 4) >> 31;
#pragma unroll
      for (int i = 0; i < 4; ++i) {
        const int chk = 16 * ((lane & 15) ^ ((lane >> 4) << 2) ^ i);
        vx[i] = (lane >> 4) * 256 + chk;
        vw1[i] = (lane >> (4 + hm)) * (F * 2) + ((chk & ~hm) | ((lane & 7) * 16 & hm)) + i * ((4 + (4 & hm)) * F * 2 - 1024);
        vw2[i] = (lane >> 3) * 1024 + db * 128 + 16 * ((lane & 7) ^ (((lane >> 4) & 1) << 2)) + i * 7168;
        PIN(vx[i]); PIN(vw1[i]); PIN(vw2[i]);
      }
      const int g = (lane >> 1) & 3;
      ra[0] = smem + LD_RED0 + (2 * lw * 64 + 16 * g + ((lane >> 3) ^ (4 * g))) * 16 + 8 * (lane & 1);
      ra[1] = smem + LD_RED0 + (2 * lw * 64 + 16 * g + ((lane >> 3) ^ (4 * g) ^ 8)) * 16 + 8 * (lane & 1);
      rc[0] = ra[0] + (LD_RED1 - LD_RED0); rc[1] = ra[1] + (LD_RED1 - LD_RED0);
      so = WS_X1S + (lw * 4 + (lane >> 4)) * ST_X1S + d0 * 256 + (lane & 15) * 16;
      xo0 = WS_X1 + (lw * 8 + (lane >> 3)) * ST_X1 + pair * 128 + (lane & 7) * 16; xo1 = xo0 + ST_X1H;
      fo = only(lane < 4, WS_F2 + (lw * 256 + pair) * 64 + lane * 16);
      mfo = only((tid >> 1) == 248, WS_MF + (~lane & 1) * 4096 + pair * 16);
      PIN(mfo); PIN(ra[0]); PIN(ra[1]); PIN(rc[0]); PIN(rc[1]); PIN(so); PIN(xo0); PIN(xo1); PIN(fo);
      SCHED();
    };
    f32x2 xf[4][2], hf[4][2], ca[4][2];
    u32x4 mv1, mv2;
    auto coefficients = [&]() {
      __builtin_amdgcn_s_setprio(2);
      STAMP(7);
      if (__builtin_expect(__any(bad4(sg, ta, tb)), 0)) retry4(sg, rws, go + WS_SG, 1024, ta, tb);
      float rs[4][2];
#pragma unroll
      for (int i = 0; i < 4; ++i)
#pragma unroll
        for (int e = 0; e < 2; ++e) { rs[i][e] = __builtin_amdgcn_rcpf(bfhi(sg[i][2 * e])); PIN(rs[i][e]); }
      quad_wait(flg, 3u);
      const u32x4 mv0 = *(LAS const u32x4*)(smem + LD_MV);
      mv1 = *(LAS const u32x4*)(smem + LD_MV + 32); mv2 = *(LAS const u32x4*)(smem + LD_MV + 48);
      if (lw < 2) {
#pragma unroll
        for (int i = 0; i < 4; ++i) xg[i] = *(LAS const u32x2*)(smem + LD_XT + lw * 2048 + i * 512 + lane * 8);
      }
      const f32x2 sa = up(mv0[0]), g1 = up(mv0[3]);
#pragma unroll
      for (int i = 0; i < 4; ++i)
#pragma unroll
        for (int e = 0; e < 2; ++e) {
          ca[i][e] = g1 * rs[i][e];
          hf[i][e] = sa - ca[i][e] * bflo(sg[i][2 * e]);
          PIN(ca[i][e]); PIN(hf[i][e]);
        }
      STAMP(8);
    };
    if (lw >= 2) {
      u32 xt;
      TOUCH(xt, (j * 32 + (lw & 1) * 16 + (lane & 15)) * 1024 + xcd * 128, p.x);
      SCAL_ISSUE(p.dskip + d0);
      addresses();
      const u32x4 qx = mkrs(p.x, L * D * 2);
      SCAL_WAIT();
      quad_arrive(flg, lw, 1u);
#pragma unroll
      for (int i = 0; i < 8; ++i) {
        if (i == G_EARLY) quad_wait(flg, 1u);
        if (i == G_SIG) *(volatile LAS u32*)(flg + lw * 4) = 2u;
        dma4(qx, LD_XT + (lw & 1) * 2048 + i * 256, lane * 1024 + pair * 4, ((lw & 1) * 8 + i) * 65536);
        WAIT_VM(2);
      }
      if (XG_DELAY) __builtin_amdgcn_s_sleep(XG_DELAY);
      STAMP(3);
      POLL_ISSUE();
      const int xo = WS_XG + ((lw & 1) * 128 + (lane >> 1)) * ST_XG + pair * 32 + (lane & 1) * 16;
      u32x4 xe[4];
#pragma unroll
      for (int i = 0; i < 4; ++i) xe[i] = ld_sc1(rws, xo, i * 32 * ST_XG);
      asm volatile("s_waitcnt vmcnt(8)" :: "v"(xt) : "memory");
      quad_arrive(flg, lw, 3u);
      asm volatile("s_waitcnt vmcnt(4)" : "+v"(sg[0]), "+v"(sg[1]), "+v"(sg[2]), "+v"(sg[3]) :: "memory");
      coefficients();
      if (__builtin_expect(__any(bad4(xe, ta, tb)), 0)) retry4(xe, rws, xo, 32 * ST_XG, ta, tb);
#pragma unroll
      for (int i = 0; i < 4; ++i) xg[i] = u32x2{xe[i][0], xe[i][2]};
    } else {
      u32x4 xv[4], wm[8];
      const int gcb = (b + (b >> 4)) & 15;
#pragma unroll
      for (int i = 0; i < 4; ++i) xv[i] = LDNT((const u32x4*)(p.x + (size_t)(lw * 1024 + b * 4 + i) * D) + lane);
      SCHED();
      SCAL_ISSUE(p.dskip + d0);
      const u32 c0 = p.c[(b >> 4) * 32 + (lane >> 3) + 8 * (lane & 3)];
      SCHED();
      {
        const rsrc_t ra = __builtin_amdgcn_make_buffer_rsrc((void*)p.wmod, 0, D * 3072 * 2, 0x00020000);
        const rsrc_t rb_ = __builtin_amdgcn_make_buffer_rsrc((void*)p.wmod, 0, lw == 0 ? D * 3072 * 2 : 0, 0x00020000);
#pragma unroll
        for (int i = 0; i < 8; ++i)
          wm[i] = __builtin_amdgcn_raw_buffer_load_b128(i < 4 ? ra : rb_, (lane >> 3) * 6144 + (lane & 7) * 16,
                                                        ((b >> 4) * 32 + 8 * (i & 3)) * 6144 + ((lw + (i >> 2) * 2) * 16 + gcb) * 128, AUX_NT);
      }
      SCHED();
      *(LAS u32x4*)(smem + LD_U + (lane >> 5) * 4608 + (lane & 31) * 16) = u32x4{0u, 0u, 0u, 0u};
      int so_ = only(!(lane & 31), WS_SG + lw * 8192 + b * 32 + (lane >> 5) * 16);
      int mo_ = only(!(lane & 8), WS_MG + (b >> 4) * ST_MG + ((lw * 16 + gcb) * 32 + (lane >> 4) * 8 + (lane & 7)) * 16);
      PIN(so_); PIN(mo_);
      SCAL_WAIT();
      __builtin_amdgcn_s_setprio(3);
      STAMP(1);
      LAS char* xw = smem + LD_XT + 4096;
      if (lw) {
#pragma unroll
        for (int i = 0; i < 4; ++i) *(LAS u32x4*)(xw + i * 1056 + lane * 16) = xv[i];
      }
      float s[4], q[4];
#pragma unroll
      for (int i = 0; i < 4; ++i) {
        s[i] = 0.f; q[i] = 0.f;
#pragma unroll
        for (int z = 0; z < 4; ++z) { s[i] = dot2(xv[i][z], 0x3f803f80u, s[i]); q[i] = dot2(xv[i][z], xv[i][z], q[i]); }
      }
      const u32 st = ln_stat(row_sum(fold16(fold32(s[0], s[2]), fold32(s[1], s[3]))), row_sum(fold16(fold32(q[0], q[2]), fold32(q[1], q[3]))));
      const auto sw = __builtin_amdgcn_permlane16_swap(st, st, false, false);
      st_sc1(rws, so_, 0, u32x4{sw[0], ta, sw[1], tb});
      STAMP(2);
      auto gv_tile = [&](int z, u32 sp) {
        float acc[8] = {0.f, 0.f, 0.f, 0.f, 0.f, 0.f, 0.f, 0.f};
        gv_row<0x00>(wm[4 * z], sp, acc); gv_row<0x55>(wm[4 * z + 1], sp, acc); gv_row<0xAA>(wm[4 * z + 2], sp, acc); gv_row<0xFF>(wm[4 * z + 3], sp, acc);
        const f32x2 m = gv_fold(acc);
        st_sc1(rws, mo_ + z * 16384, 0, u32x4{__float_as_uint(m[0]), ta, __float_as_uint(m[1]), tb});
        SCHED();
      };
      if (lw) {
        u32 a[8][2];
#pragma unroll
        for (int i = 0; i < 8; ++i) {
          LAS const u32* src = (LAS const u32*)(xw + (lane & 1) * 2112 + i * 128 + (lane >> 1) * 4);
          a[i][0] = src[0]; a[i][1] = src[264];
        }
#pragma unroll
        for (int i = 0; i < 8; ++i) st_sc1(rws, WS_XG + b * ST_XG + lane * 16 + (i >> 2) * 4096, (i & 3) * 1024, u32x4{a[i][0], ta, a[i][1], tb});
        SCHED();
        gv_tile(0, silu_bf(c0));
        PIN(wm[4]); PIN(wm[5]); PIN(wm[6]); PIN(wm[7]);
      } else {
        const u32 sp = silu_bf(c0);
        gv_tile(0, sp);
        gv_tile(1, sp);
      }
      __builtin_amdgcn_s_setprio(0);
      quad_arrive(flg, lw, 2u);
      STAMP(4);
      addresses();
      quad_wait(flg, 2u);
      const int mo = WS_MG + (lane & 15) * ST_MG + ((((lane >> 4) & (2 * lw + 1)) + 2 * lw) * 256 + (pair >> 5) * 32 + (pair & 3) * 8 + ((pair >> 2) & 7)) * 16;
      u32x4 mg;
      asm volatile("buffer_load_dwordx4 %0, %1, %2, 0 offen sc1" : "=v"(mg) : "v"(mo), "s"(qws) : "memory");
      POLL_ISSUE();
      asm volatile("s_waitcnt vmcnt(4)" : "+v"(mg) :: "memory");
      if (__builtin_expect(__any(tagdiff(mg, ta, tb) != 0u), 0)) {
        asm volatile("s_waitcnt vmcnt(0)" : "+v"(sg[0]), "+v"(sg[1]), "+v"(sg[2]), "+v"(sg[3]) :: "memory");
        mg = poll16(rws, mo, 0, ta, tb);
      }
      const u32 mb = pk(row_sum(__uint_as_float(mg[0])), row_sum(__uint_as_float(mg[2])));
      *(LAS u32x2*)(smem + LD_MV + lw * 32 + (lane >> 4) * 8) = u32x2{mb, pk2(up(mb) + 1.f)};
      quad_arrive(flg, lw, 3u);
      asm volatile("s_waitcnt vmcnt(0)" : "+v"(sg[0]), "+v"(sg[1]), "+v"(sg[2]), "+v"(sg[3]) :: "memory");
      coefficients();
    }
    const u32x4 qw1 = mkrs(p.w1, D * F * 2), qw2 = mkrs(p.w2, D * F * 2);
    const int sw2 = ((lw >> 1) * 896 + (lw & 1) * 32) * 1024;
    auto w2_stage = [&](int u) { dma4k(qw2, w2_unit(u) * UNIT + lw * 4096, vw2, sw2 + w2_phys(u) * 65536); };
    const int wstep = (helper ? 32 : 16) * F * 2, wbase = helper ? min(tbk, 27) * 128 : cbk * 256;
    auto w1_stage = [&](int s) { dma4k(qw1, LD_WR + (s & 3) * UNIT + lw * 4096, vw1, (s * 4 + lw) * wstep + wbase); };
    auto x_stage = [&](int s) { dma4k_sc1(qws, LD_XR + (s & 3) * UNIT + lw * 4096, vx, WS_X1S + rbk * ST_X1S + s * 16384 + lw * 4096); };
    *(volatile LAS u32*)(flg + lw * 4) = 4u;
    STAMP(9);
#pragma unroll
    for (int i = 0; i < 4; ++i) {
#pragma unroll
      for (int e = 0; e < 2; ++e) { xf[i][e] = up(xg[i][e]); hf[i][e] += xf[i][e] * ca[i][e]; }
      LAS u32* U = (LAS u32*)(smem + LD_U + 512 + lw * 1024 + i * 256) + lane;
      U[0] = pk(hf[i][0][0], hf[i][1][0]);
      U[4608 / 4] = pk(hf[i][0][1], hf[i][1][1]);
    }
    STAMP(10);
    BARRIER();
    STAMP(11);
    w1_stage(0); w1_stage(1); w1_stage(2); w1_stage(3);
    const f32x2 gav = up(mv1[0]), gcv = up(mv2[1]);
    f32x2 xc[4][2];
    float gd[2] = {gav[0] * bflo(dsk), gav[1] * bfhi(dsk)}, n0 = bflo(mv2[2]) * (-1.f / LOG2E), n1 = bfhi(mv2[2]) * (-1.f / LOG2E);
    u32 k0 = __builtin_amdgcn_perm(mv1[2], mv2[1], 0x05040100u), k1 = __builtin_amdgcn_perm(mv1[2], mv2[1], 0x07060302u);
    PIN(gd[0]); PIN(gd[1]); PIN(n0); PIN(n1); PIN(k0); PIN(k1);
    const u32x4 mfv = {lane & 1 ? k0 : __float_as_uint(n0), ta, lane & 1 ? k1 : __float_as_uint(n1), tb};
    st_sc1(rws, mfo, 0, mfv);
#pragma unroll
    for (int i = 0; i < 4; ++i)
#pragma unroll
      for (int c = 0; c < 2; ++c) {
        float a0 = __builtin_fmaf(hf[i][0][c], gd[c], xf[i][0][c]), a1 = __builtin_fmaf(hf[i][1][c], gd[c], xf[i][1][c]);
        PIN(a0); PIN(a1);
        xc[i][c] = f32x2{a0, a1};
      }
    STAMP(12);
    SCHED();
    u32x4 wr[20];
    auto wr_load = [&](bool on) {
      const rsrc_t rw2 = __builtin_amdgcn_make_buffer_rsrc((void*)p.w2, 0, on ? D * F * 2 : 0, 0x00020000);
#pragma unroll
      for (int i = 0; i < 20; ++i) wr[i] = __builtin_amdgcn_raw_buffer_load_b128(rw2, vw2[i & 3] + (i & 3) * 1024, sw2 + (i / 4) * 65536, AUX_W);
    };
    wr_load(!helper);
    SCHED();
    STAMP(13);
    BARRIER();
    STAMP(14);
    {
      u32x4 o[2], s0, s1;
      f32x2 yp[4][4], c0[4], c1[4];
#pragma unroll
      for (int i = 0; i < 4; ++i) {
        yp[i][0] = *(volatile LAS const f32x2*)(ra[i & 1] + (i >> 1) * 1024); yp[i][1] = *(volatile LAS const f32x2*)(ra[i & 1] + (i >> 1) * 1024 + 8192);
      }
#pragma unroll
      for (int i = 0; i < 4; ++i) {
        yp[i][2] = *(volatile LAS const f32x2*)(rc[i & 1] + (i >> 1) * 1024); yp[i][3] = *(volatile LAS const f32x2*)(rc[i & 1] + (i >> 1) * 1024 + 8192);
      }
      SCHED();
#pragma unroll
      for (int i = 0; i < 4; ++i) { c0[i] = (yp[i][0] + yp[i][1]) * gav[0] + xc[i][0]; s0[i] = pk2(c0[i] * gcv[0]); }
      st_sc1(rws, so, 0, s0);
      SCHED();
#pragma unroll
      for (int i = 0; i < 4; ++i) { c1[i] = (yp[i][2] + yp[i][3]) * gav[1] + xc[i][1]; s1[i] = pk2(c1[i] * gcv[1]); }
      st_sc1(rws, so, 256, s1);
      SCHED();
#pragma unroll
      for (int i = 0; i < 4; ++i) { o[i >> 1][2 * (i & 1)] = pk(c0[i][0], c1[i][0]); o[i >> 1][2 * (i & 1) + 1] = pk(c0[i][1], c1[i][1]); }
      st_sc1(rws, xo0, 0, o[0]);
      st_sc1(rws, xo1, 0, o[1]);
    }
    STAMP(15);
    WAIT_VM(0);
    STAMP(17);
    st_sc1(rws, fo, 0, u32x4{ta, ta, tb, tb});
    __builtin_amdgcn_s_setprio(0);
    const int kind = helper ? 3 : lw;
    int e0 = WS_G2 + rbk * 1024, e1 = WS_S12 + (cbk * 128 + (kind - 1) * 64) * 16, e3 = WS_MF + 4096 + db * 512;
    asm volatile("" : "+s"(e0), "+s"(e1), "+s"(e3));
    const bool mid = (kind + 1) & 2;
    const int eo = (mid ? e1 : kind ? e3 : e0) + (lane & (kind == 3 ? 31 : 63)) * 16;
    LAS char* ea = smem + LD_MISC + (mid ? 256 + kind * 256 : kind * 512) + lane * (mid ? 4 : 8);
    u32x4 ev;
    if (helper) {
      BARRIER();
      ev = poll16(rws, eo, 0, ta, tb);
      *(LAS u32*)ea = ev[0]; *(LAS u32*)(ea + 4) = ev[2];
      BARRIER();
      __builtin_amdgcn_s_sleep(20);
      w2_stage(1); w2_stage(2); w2_stage(3);
      BARRIER(); BARRIER();
      w2_stage(0); w2_stage(4); w2_stage(5);
      wr_load(true);
    } else {
      __builtin_amdgcn_s_sleep(F2_DELAY);
      poll16(rws, ((lane >> 3) * 32 + lw * 8 + (lane & 7)) * 64, WS_F2 + (xcd >> 1) * 16384, ta, tb);
      STAMP(19);
      x_stage(0); x_stage(1); WAIT_VM(4); BARRIER();
      x_stage(2); x_stage(3); WAIT_VM(8); BARRIER();
      x_stage(4); w1_stage(4); WAIT_VM(12); BARRIER();
#pragma unroll 1
      for (int s = 5; s < 8; ++s) { x_stage(s); w1_stage(s); WAIT_VM(16); BARRIER(); }
      w2_stage(0); w2_stage(1); WAIT_VM(16); BARRIER();
      asm volatile("buffer_load_dwordx4 %0, %1, %2, 0 offen sc1" : "=v"(ev) : "v"(eo), "s"(qws) : "memory");
      w2_stage(2); w2_stage(3); WAIT_VM(17); BARRIER();
      asm volatile("s_waitcnt vmcnt(8)" : "+v"(ev) :: "memory");
      if (__any(tagdiff(ev, ta, tb) != 0u)) ev = poll16(rws, eo, 0, ta, tb);
      *(LAS u32*)ea = ev[0]; *(LAS u32*)(ea + (mid ? 512 : 4)) = ev[2];
      w2_stage(4); w2_stage(5); BARRIER();
    }
    STAMP(22);
    w2_stage(6); w2_stage(7); w2_stage(8);
    {
      const int xs = WS_X1 + tbk * ST_X1 + (db * 32 + (lw >> 1) * 16) * 128 + (lw & 1) * 64;
      dma16_sc1(qws, LD_MISC + 2048 + lw * 2048, (lane >> 2) * 128 + (lane & 3) * 16, xs);
      dma16_sc1(qws, LD_MISC + 3072 + lw * 2048, (lane >> 2) * 128 + (lane & 3) * 16, xs + ST_X1H);
    }
    WAIT_VM(0);
    STAMP(25);
    BARRIER();
    BARRIER();
#pragma unroll
    for (int i = 0; i < 20; ++i) {
      if (i == 8) BARRIER();
      *(LAS u32x4*)(smem + w2_unit(9 + i / 4) * UNIT + lw * 4096 + (i & 3) * 1024 + lane * 16) = wr[i];
    }
    BARRIER();
    if (b == 0 && tid == 256) *(u32x2*)(p.ws + WS_EPOCH) = u32x2{ta, tb};
    STAMP(26);
    STAMP_FLUSH(p.ws, 1);
    return;
  }

  ROLES();
  const int pch = w >> 1, pn = (w & 1) * 64 + lane, po = (d0 + pch) * NS + pn;
  u32 rar = LDNT(p.Are + po), rai = LDNT(p.Aim + po), rbr = LDNT(p.Bre + po), rbi = LDNT(p.Bim + po), rcr = LDNT(p.Cre + po), rci = LDNT(p.Cim + po);
  SCHED();
  SCAL_ISSUE(p.ldt + d0);
  {
    u32 cp0;
    {
      u64 pc; int r0, r1;
      asm volatile("s_getpc_b64 %0\n\ts_mov_b32 %1, fused_block@rel32@lo+4\n\ts_mov_b32 %2, fused_block_end@rel32@lo+12" : "=s"(pc), "=s"(r0), "=s"(r1));
      TOUCH(cp0, min(((w * 4 + (lane & 3)) * 32 + j) * 128, r1 - r0), pc + (long long)r0);
    }
    LAS char* flg = smem + LD_FLG;
    *(LAS u32x4*)(smem + LD_T + ((lane >> 1) & 7) * TCOPY + 4096 + (lane & 1) * 16) = u32x4{0u, 0u, 0u, 0u};
    *(LAS u32*)(smem + LD_KD + KD_ZERO * 4) = 0u;
    asm volatile("s_waitcnt lgkmcnt(0)" : "+s"(epoch), "+s"(aux));
    int ga[11], ka[4];
#pragma unroll
    for (int i = 0; i < 11; ++i) {
      const int l = 2047 - (tid * 8 + i);
      const int bits = (int)(pk((float)l, 0.f) & 0xffffu);
      const int idx = l < 128 ? l : bits - 0x4300 + 128;
      ga[i] = LD_KD + 4 * (l < 0 ? KD_ZERO : idx + (idx >> 3));
    }
    {
      const int e = lane & 15, g = lane >> 4;
      const u32 cfg = e < 8 ? (w == 0 ? 0x100004u : w == 1 ? 0x080004u : 0x022001u) : (w == 0 ? 0x100004u : w == 1 ? 0x044002u : 0x088003u);
      const int sh = cfg & 15, base = 256 * w + ((cfg >> 8) & 0xff) * (e >> sh) + (e & ((1 << sh) - 1));
#pragma unroll
      for (int r = 0; r < 4; ++r) { const int idx = base + (int)(cfg >> 16) * (4 * g + r); ka[r] = LD_KD + 4 * (idx + (idx >> 3)); PIN(ka[r]); }
    }
    STAMP(1);
    asm volatile("" : "+v"(rar), "+v"(rai), "+v"(rbr), "+v"(rbi), "+v"(rcr), "+v"(rci) : "v"(cp0));
    STAMP(2);
    SCHED();
    volatile LAS u32* P = (LAS u32*)(smem + LD_P) + pch * 64 * PROW + pn;
    const float dt = rb(__builtin_amdgcn_exp2f((pch ? bfhi(aux) : bflo(aux)) * LOG2E));
    const float a2 = (dt * bflo(rar)) * LOG2E, bq = (dt * bflo(rai)) * 0.15915494309189535f;
    const f32x2 z1 = cpow(a2, bq, 1.f), z8 = cpow(a2, bq, 8.f), z256 = cpow(a2, bq, 256.f);
    const f32x2 z16c = cmul(z8, z8) * f32x2{1.f, -1.f}, z512 = cmul(z256, z256), z768 = cmul(z512, z256);
    f32x2 u = cpow(a2, bq, 1024.f), q = {1.f, 0.f};
    f32x2 r = cmul(f32x2{bflo(rcr), -bflo(rci)}, f32x2{bflo(rbr), -bflo(rbi)} * dt);
#pragma unroll
    for (int e = 0; e < 16; ++e) {
      const f32x2 rn = cmul(r, z16c), qn = cmul(q, z1);
      P[e * PROW] = pk2(r);
      P[(16 + e) * PROW] = pk2(q);
      if (!(e & 1)) P[(32 + e / 2) * PROW] = pk2(cmul(q, z256));
      if (!(e & 3)) { P[(40 + e / 4) * PROW] = pk2(cmul(q, z512)); P[(44 + e / 4) * PROW] = pk2(cmul(q, z768)); }
      if ((e & 3) == 1) { P[(48 + e / 2) * PROW] = pk2(u); P[(49 + e / 2) * PROW] = pk2(cmul(u, z8)); }
      if ((e & 3) == 3) u = cmul(u, z256);
      r = rn; q = qn;
    }
    P[56 * PROW] = pk2(u);
    STAMP(3);
    quad_arrive(flg, w, 1u);
#pragma unroll
    for (int i = 0; i < 11; ++i) PIN(ga[i]);
    quad_wait(flg, 1u);
    STAMP(4);
    if (w < 3) {
      LAS const u32* fb = (LAS const u32*)(smem + LD_P) + (lane & 15) * PROW + ((lane >> 4) & 1) * 64 + (lane >> 5) * 32;
      u32x4 fa[16], fc[16];
      f32x4 acc[2] = {{0.f, 0.f, 0.f, 0.f}, {0.f, 0.f, 0.f, 0.f}};
#pragma unroll
      for (int ks = 0; ks < 22; ++ks) {
        if (ks < 16) {
          const int o = (ks & 1) * 64 * PROW + (ks >> 1) * 4;
          fa[ks] = *(LAS const u32x4*)(fb + o); fc[ks] = *(LAS const u32x4*)(fb + (w + 1) * 16 * PROW + o);
        }
        SCHED();
        if (ks >= 6) acc[ks & 1] = mfma16(fa[ks - 6], fc[ks - 6], acc[ks & 1]);
        SCHED();
      }
#pragma unroll
      for (int r4 = 0; r4 < 4; ++r4) *(LAS u32*)(smem + ka[r4]) = pk(acc[0][r4], acc[1][r4]);
    }
    STAMP(5);
    quad_arrive(flg, w, 2u);
    quad_wait(flg, 2u);
    STAMP(6);
    u32 e[11];
#pragma unroll
    for (int i = 0; i < 11; ++i) e[i] = *(LAS const u32*)(smem + ga[i]);
#pragma unroll
    for (int c = 0; c < 2; ++c)
#pragma unroll
      for (int s = 0; s < 4; ++s) {
        u32x4 o;
#pragma unroll
        for (int z = 0; z < 4; ++z) o[z] = __builtin_amdgcn_perm(e[s + 2 * z + 1], e[s + 2 * z], c ? 0x07060302u : 0x05040100u);
        *(LAS u32x4*)(smem + LD_T + (c * 4 + s) * TCOPY + tid * 16) = o;
      }
    quad_arrive(flg, w, 3u);
  }
  epoch += 0x9E3779B97F4A7C15ull;
  const u32 ta = (u32)epoch, tb = (u32)(epoch >> 32);
  {
    const int r = lane & 15, g = lane >> 4, ch = w >> 1, half = w & 1;
    LAS const char* Tb = smem + LD_T + (ch * 4 + ((3 - r) & 3)) * TCOPY + 2 * (2028 - 4 * (r >> 2) + 8 * g - 128 * half) - 7 * 512 - 192;
    LAS const char* Ub = smem + LD_U + ch * 4608 + 2 * (256 + 16 * r - 16 + 8 * g - 128 * half) - 192;
    LAS f32x4* red = (LAS f32x4*)(smem + (ch ? LD_RED1 : LD_RED0)) + half * 512 + (lane ^ ((lane >> 4) << 2));
    PIN(Ub); PIN(red);
    f32x4 acc[8];
#pragma unroll
    for (int n = 0; n < 8; ++n) acc[n] = f32x4{0.f, 0.f, 0.f, 0.f};
    u32x4 uf[2][8], tf[4][8];
    quad_wait(smem + LD_FLG, 3u);
#define LD_TH(e, a, hh) do { const u32x2 l_ = *(volatile LAS const u32x2*)(Tb + 64 * (3 - (e)) + 512 * (7 - (a)) + 8 * (hh)); tf[e][a][2 * (hh)] = l_[0]; tf[e][a][2 * (hh) + 1] = l_[1]; } while (0)
#pragma unroll
    for (int e = 0; e < TF_PRE; ++e)
#pragma unroll
      for (int a = 0; a < 8; ++a) { LD_TH(e, a, 0); LD_TH(e, a, 1); }
#define LD_UF(n, e) uf[(e) & 1][n] = *(LAS const u32x4*)(Ub + 64 * (3 - (e)) + 512 * (n))
#define CM(e, a, m) do { SCHED(); acc[(a) + (m)] = mfma16(tf[e][a], uf[(e) & 1][m], acc[(a) + (m)]); SCHED(); } while (0)
    STAMP(10);
    u32 ft;
    quad_wait(smem + LD_FLG + 16, FT_AT);
    __builtin_amdgcn_s_sleep(FT_DELAY);
    TOUCH(ft, ((b * 2 + (w & 1)) * 28 + min(lane, 27)) * 128, w < 2 ? p.w1 : p.w2);
    BARRIER();
    STAMP(11);
#pragma unroll
    for (int n = 0; n < 8; ++n) LD_UF(n, 0);
#pragma unroll
    for (int e = 0; e < 3; ++e) {
      int cnt = 0;
#pragma unroll
      for (int m = 0; m < 8; ++m)
#pragma unroll
        for (int a = 0; a + m < 8; ++a) {
          CM(e, a, m);
          if (cnt >= 4 && cnt < 12) LD_UF(cnt - 4, e + 1);
          if (cnt >= 12 && cnt < 28 && e + TF_PRE < 4) LD_TH(e + TF_PRE, (cnt - 12) >> 1, (cnt - 12) & 1);
          ++cnt;
        }
    }
#pragma unroll
    for (int n = 0; n < 8; ++n)
#pragma unroll
      for (int a = 0; a <= n; ++a) { CM(3, a, n - a); if (a == 1) red[(n - 1) * 64] = acc[n - 1]; }
    STAMP(12);
    red[7 * 64] = acc[7];
    BARRIER();
    STAMP(14);
    WAIT_VM(0);
    asm volatile("" :: "v"(ft));
  }
  if (helper) {
    const int hb = xcd * 4 + j;
    u32x4 v[16];
    const int rg = tid >> 3;
    LAS float* rd = (LAS float*)(smem + 4096);
    { const u32x4 m = poll16(rws, tid * 16, WS_MF, ta, tb); *(LAS u32x2*)(smem + tid * 8) = u32x2{m[0], m[2]}; }
    BARRIER();
#pragma unroll
    for (int i = 0; i < 16; ++i) v[i] = *(LAS const u32x4*)(smem + LD_WR + i * 4096 + tid * 16);
    float a[16];
#pragma unroll
    for (int e = 0; e < 16; ++e) a[e] = 0.f;
#pragma unroll
    for (int i = 0; i < 16; ++i) {
      const u32 gs = *(LAS const u32*)(smem + (i * 32 + rg) * 4);
      const u32 gl = gs & 0xffffu, gh = gs << 16, sl = gs >> 16, sh = gs & 0xffff0000u;
#pragma unroll
      for (int z = 0; z < 4; ++z) {
        a[4 * z] = dot2(v[i][z], gl, a[4 * z]); a[4 * z + 1] = dot2(v[i][z], sl, a[4 * z + 1]);
        a[4 * z + 2] = dot2(v[i][z], gh, a[4 * z + 2]); a[4 * z + 3] = dot2(v[i][z], sh, a[4 * z + 3]);
      }
    }
    fold_cols(a, rd + w * 128, lane);
    STAMP(18);
    poll16(rws, tid * 64, WS_F2 + (xcd >> 1) * 16384, ta, tb);
    BARRIER();
    STAMP(19);
#pragma unroll
    for (int i = 0; i < 16; ++i) v[i] = ld_sc1(rws, tid * 16, WS_X1 + hb * ST_X1 + (i >> 3) * ST_X1H + (i & 7) * 4096);
    if (w == 0 && hb < 28) {
      const f32x2 s = sum4(rd + lane * 2, 128);
      st_sc1(rws, lane * 16, WS_S12 + hb * 1024, u32x4{__float_as_uint(s[0]), ta, __float_as_uint(s[1] * -LOG2E), tb});
    }
#pragma unroll
    for (int e = 0; e < 16; ++e) a[e] = 0.f;
#pragma unroll
    for (int i = 0; i < 16; ++i)
#pragma unroll
      for (int z = 0; z < 4; ++z) {
        const int o = (i >> 3) * 8 + 2 * z;
        a[o] = dot2(v[i][z], 0x3f803f80u, a[o]); a[o + 1] = dot2(v[i][z], v[i][z], a[o + 1]);
      }
    STAMP(20);
    fold_cols(a, rd + 512 + w * 128, lane);
    BARRIER();
    if (w == 0) {
      const f32x2 s = sum4(rd + 512 + lane * 2, 128);
      const u32 st = ln_stat(s[0], s[1]);
      const u32 st2 = __builtin_amdgcn_mov_dpp(st, 0xB1, 0xf, 0xf, true);
      if (!(lane & 1)) st_sc1(rws, lane * 8, WS_G2 + hb * 512, u32x4{st, ta, st2, tb});
    }
    BARRIER();
    STAMP(22);
    __builtin_amdgcn_s_sleep(H_SLEEP);
  } else {
    const int wt = w & 1, wf = w >> 1;
    const int h = lane >> 5, blk = (lane >> 4) & 1, q = (lane & 15) >> 2, pp = lane & 3;
    int ax[2][2], aw[2][2];
#pragma unroll
    for (int c = 0; c < 2; ++c)
#pragma unroll
      for (int t = 0; t < 2; ++t) {
        const int base = 256 * (8 * h + 4 * t + q) + 16 * ((2 * blk + (pp >> 1)) ^ (2 * h + t)) + 8 * (pp & 1);
        ax[c][t] = LD_XR + base + 64 * ((2 * wt + c) ^ q);
        aw[c][t] = LD_WR + base + 64 * ((2 * wf + c) ^ q);
        PIN(ax[c][t]); PIN(aw[c][t]);
      }
    f32x16 acc[2][2];
#pragma unroll
    for (int a = 0; a < 2; ++a)
#pragma unroll
      for (int c = 0; c < 2; ++c)
#pragma unroll
        for (int r = 0; r < 16; ++r) acc[a][c][r] = 0.f;
    u32x4 xf[2][2], wfr[2][2];
    u32 st[2];
    int ho = WS_H + ((rbk * 4 + wt * 2) * 112 + cbk * 8 + wf * 4) * 1024 + lane * 16, ho1 = ho + 112 * 1024;
    int fo = only(lane < 8, WS_F3 + ((rbk * 4 + w) * 16 + cbk) * 128 + lane * 16);
    PIN(ho); PIN(ho1); PIN(fo);
#define RD_X(i, c) xf[(i) & 1][c] = tr_frag(smem + ax[c][0] + ((i) & 15) * 4096, smem + ax[c][1] + ((i) & 15) * 4096)
#define RD_W(i, c) wfr[(i) & 1][c] = tr_frag(smem + aw[c][0] + ((i) & 15) * 4096, smem + aw[c][1] + ((i) & 15) * 4096)
#define MM(i, a, c) do { SCHED(); acc[a][c] = mfma32(wfr[(i) & 1][a], xf[(i) & 1][c], acc[a][c]); SCHED(); } while (0)
    BARRIER();
    STAMP(19);
    RD_W(0, 0); RD_X(0, 0); RD_X(0, 1); RD_W(0, 1);
#pragma unroll
    for (int i = 0; i < 32; ++i) {
      if ((i & 3) != 3) {
        MM(i, 0, 0); RD_W(i + 1, 0); MM(i, 0, 1); RD_X(i + 1, 0); MM(i, 1, 0); RD_X(i + 1, 1); MM(i, 1, 1); RD_W(i + 1, 1);
      } else {
        MM(i, 0, 0); MM(i, 0, 1);
        BARRIER();
        if (i == 31) { st[0] = *(LAS const u32*)(smem + LD_MISC + (wt * 64 + (lane & 31)) * 4); st[1] = *(LAS const u32*)(smem + LD_MISC + (wt * 64 + 32 + (lane & 31)) * 4); }
        MM(i, 1, 0);
        if (i < 31) { RD_W(i + 1, 0); RD_X(i + 1, 0); }
        MM(i, 1, 1);
        if (i < 31) { RD_X(i + 1, 1); RD_W(i + 1, 1); }
      }
    }
    STAMP(21);
    float rstd[2], mr[2];
#pragma unroll
    for (int c = 0; c < 2; ++c) {
      rstd[c] = __builtin_amdgcn_rcpf(bfhi(st[c])) * -LOG2E;
      mr[c] = -bflo(st[c]) * rstd[c];
    }
    STAMP(22);
    LAS const float* s12 = (LAS const float*)(smem + LD_MISC + 512) + wf * 64 + 4 * h;
#pragma unroll
    for (int a = 0; a < 2; ++a)
#pragma unroll
      for (int sp = 0; sp < 2; ++sp) {
        f32x4 s1[2], s2[2];
#pragma unroll
        for (int z = 0; z < 2; ++z) { s1[z] = *(LAS const f32x4*)(s12 + a * 32 + sp * 16 + z * 8); s2[z] = *(LAS const f32x4*)(s12 + 128 + a * 32 + sp * 16 + z * 8); }
#pragma unroll
        for (int c = 0; c < 2; ++c) {
          u32x4 o;
#pragma unroll
          for (int e = 0; e < 4; ++e) {
            const int z = e >> 1, k = (e & 1) * 2;
            const f32x2 av = {acc[a][c][sp * 8 + 2 * e], acc[a][c][sp * 8 + 2 * e + 1]};
            const f32x2 cc = f32x2{s1[z][k], s1[z][k + 1]} * mr[c] + f32x2{s2[z][k], s2[z][k + 1]};
            const f32x2 ar = av * rstd[c] + cc;
            const f32x2 dn = f32x2{__builtin_amdgcn_exp2f(ar[0]), __builtin_amdgcn_exp2f(ar[1])} + 1.f;
            o[e] = pk2(ar * f32x2{__builtin_amdgcn_rcpf(dn[0]), __builtin_amdgcn_rcpf(dn[1])});
          }
          st_sc1(rws, c ? ho1 : ho, (a * 2 + sp) * 1024, o);
        }
      }
    STAMP(23);
    WAIT_VM(0);
    STAMP(24);
    st_sc1(rws, fo, 0, u32x4{ta, ta, tb, tb});
  }

  {
    const int wt = w & 1, kh = w >> 1, h = lane >> 5, tl = lane & 31;
    const int blk = (lane >> 4) & 1, q = (lane & 15) >> 2, pp = lane & 3;
    LAS char* aw[3][2];
#pragma unroll
    for (int c = 0; c < 6; ++c) {
      aw[c >> 1][c & 1] = smem + (c >> 1) * 65536 + kh * 8192 + 128 * (4 * h + q) + 64 * ((c & 1) ^ kh ^ (q >> 1)) + 32 * blk + 8 * pp;
      asm volatile("" : "+v"(aw[c >> 1][c & 1]));
    }
    const rsrc_t ro = __builtin_amdgcn_make_buffer_rsrc(p.out, 0, L * D * 2, 0x00020000);
    int oo[2];
#pragma unroll
    for (int i = 0; i < 2; ++i) {
      const int r = tbk * 64 + (tid >> 3) + 32 * i;
      oo[i] = ((r & ~511) + 128 * ((r >> 1) & 3) + 2 * ((r >> 3) & 63) + (r & 1)) * (D * 2) + db * 128 + (tid & 7) * 16;
      PIN(oo[i]);
    }
    f32x16 acc[2];
#pragma unroll
    for (int r = 0; r < 16; ++r) { acc[0][r] = 0.f; acc[1][r] = 0.f; }
    STAMP(25);
    BARRIER();
    constexpr int NW = G2_NW;
    u32x4 hf[NW], wf[3][2];
#define RD_W2C(i, c) do { LAS char* a_ = aw[w2_unit((i) >> 2) >> 2][c] + (w2_unit((i) >> 2) & 3) * UNIT + ((i) & 3) * 2048; wf[(i) % 3][c] = tr_frag(a_, a_ + 1024); } while (0)
#define RD_W2(i) do { RD_W2C(i, 0); RD_W2C(i, 1); } while (0)
    RD_W2(0); RD_W2(1);
    const int sh = WS_H + ((tbk * 2 + wt) * 112 + 56 * kh) * 1024;
#define LD_H(i) hf[(i) % NW] = ld_sc1(rws, lane * 16 + ((i) & 3) * 1024, sh + w2_phys((i) >> 2) * 4096)
    if (F3_DELAY) __builtin_amdgcn_s_sleep(F3_DELAY);
    poll16(rws, ((lane & 1) * 32 + min(lane >> 1, 6)) * 128, WS_F3 + ((tbk >> 1) * 4 + (tbk & 1)) * 2048 + kh * 896, ta, tb);
    STAMP(26);
#pragma unroll
    for (int i = 0; i < G2_N0; ++i) LD_H(i);
    SCHED();
    u32 xr[8]; f32x2 gb[8];
#pragma unroll
    for (int i = 0; i < 8; ++i) {
      const int pr = 4 * (i >> 1) + 2 * h + (i & 1);
      xr[i] = *(LAS const u32*)(smem + LD_MISC + 2048 + w * 2048 + ((tl >> 2) & 1) * 1024 + pr * 64 + (tl >> 3) * 16 + (tl & 3) * 4);
      gb[i] = *(LAS const f32x2*)(smem + LD_MISC + 1536 + (kh * 16 + pr) * 8);
    }
    SCHED();
#pragma unroll
    for (int i = 0; i < 56; ++i) {
      SCHED();
      acc[0] = mfma32(wf[i % 3][0], hf[i % NW], acc[0]);
      SCHED();
      if (i == 0) STAMP(29);
      if (i == 18) STAMP(30);
      if (i == 42) STAMP(31);
      if (i == 6 || i == 18 || i == 42) BARRIER();
      if (i + 2 < 56) RD_W2C(i + 2, 0);
      if (h_next(i) < h_next(i + 1)) LD_H(h_next(i));
      SCHED();
      acc[1] = mfma32(wf[i % 3][1], hf[i % NW], acc[1]);
      SCHED();
      if (i + 2 < 56) RD_W2C(i + 2, 1);
      if (h_next(i) + 1 < h_next(i + 1)) LD_H(h_next(i) + 1);
      asm volatile("" :: "v"(wf[i % 3][0]), "v"(wf[i % 3][1]), "v"(hf[i % NW]));
    }
    STAMP(27);
    LAS f32x4* ex = (LAS f32x4*)(smem + w2_unit(5) * UNIT) + lane;
#pragma unroll
    for (int qq = 0; qq < 4; ++qq) ex[(w * 4 + qq) * 64] = f32x4{acc[1][4 * qq], acc[1][4 * qq + 1], acc[1][4 * qq + 2], acc[1][4 * qq + 3]};
    BARRIER();
    LAS char* ot = smem + w2_unit(6) * UNIT;
    f32x4 oth[4];
#pragma unroll
    for (int qq = 0; qq < 4; ++qq) oth[qq] = ex[((w ^ 2) * 4 + qq) * 64];
    SCHED();
#pragma unroll
    for (int qq = 0; qq < 4; ++qq) {
      const f32x4 o4 = oth[qq];
      u32x2 o;
#pragma unroll
      for (int z = 0; z < 2; ++z)
        o[z] = pk2((f32x2{acc[0][4 * qq + 2 * z], acc[0][4 * qq + 2 * z + 1]} + f32x2{o4[2 * z], o4[2 * z + 1]}) * gb[2 * qq + z] + up(xr[2 * qq + z]));
      *(LAS u32x2*)(ot + (wt * 32 + tl) * 144 + kh * 64 + qq * 16 + h * 8) = o;
    }
    BARRIER();
#pragma unroll
    for (int i = 0; i < 2; ++i) {
      const int row = (tid >> 3) + 32 * i;
      __builtin_amdgcn_raw_buffer_store_b128(*(LAS const u32x4*)(ot + row * 144 + (tid & 7) * 16), ro, oo[i], 0, OUT_AUX);
    }
    STAMP(28);
    STAMP_FLUSH(p.ws, 0);
  }
}

extern "C" __global__ void fused_block_end() {}

extern "C" void kernel_launch(void* const* d_in, const int* in_sizes, int n_in, void* d_out, int out_size, void* d_ws, size_t ws_size,
                              hipStream_t stream) {
  Params p;
  p.x = (const u16*)d_in[0]; p.Are = (const u16*)d_in[1]; p.Aim = (const u16*)d_in[2]; p.Bre = (const u16*)d_in[3];
  p.Bim = (const u16*)d_in[4]; p.Cre = (const u16*)d_in[5]; p.Cim = (const u16*)d_in[6]; p.ldt = (const u16*)d_in[7];
  p.dskip = (const u16*)d_in[8]; p.w1 = (const u16*)d_in[10]; p.w2 = (const u16*)d_in[11]; p.c = (const u16*)d_in[12];
  p.wmod = (const u16*)d_in[13]; p.out = (u16*)d_out; p.ws = (char*)d_ws;
  (void)hipFuncSetAttribute((const void*)fused_block, hipFuncAttributeMaxDynamicSharedMemorySize, LDS_BYTES + ST_LDS_EXTRA);
  fused_block<<<256, 512, LDS_BYTES + ST_LDS_EXTRA, stream>>>(p);
}
```

```cpp
#define LIGHT 11
#include <hip/hip_runtime.h>
#define ST_OFF ((size_t)128 << 20)
#define ST_SLOTS 32
#define ST_MAXW 8
#define ST_MAXB 512
#define ST_LDS_EXTRA 2048
typedef unsigned st_u2 __attribute__((ext_vector_type(2)));
#define STAMP_DECL __attribute__((address_space(3))) st_u2* st_ = (__attribute__((address_space(3))) st_u2*)(smem + LDS_BYTES) + (threadIdx.x >> 6) * 32; \
  if ((threadIdx.x & 63) < 32) st_[threadIdx.x & 31] = st_u2{0u, 0u};
#define ST_ON(s) ((s) == 0 || (s) == 28 || (s) == LIGHT)
#define STAMP(s) do { unsigned long long rt_, cy_; __builtin_amdgcn_sched_barrier(0); if (!ST_ON(s)) break; \
  asm volatile("s_memrealtime %0\n\ts_memtime %1\n\ts_waitcnt lgkmcnt(0)" : "=&s"(rt_), "=&s"(cy_) :: "memory"); \
  st_[s] = st_u2{(unsigned)rt_ | 0x80000000u, (unsigned)cy_}; __builtin_amdgcn_sched_barrier(0); } while (0)
#define STAMP_FLUSH(ws, kern) do { if ((threadIdx.x & 63) < 32) { \
  unsigned long long* p_ = (unsigned long long*)((char*)(ws) + ST_OFF) + ((((size_t)(kern) * ST_MAXB + blockIdx.x) * ST_MAXW + (threadIdx.x >> 6)) * ST_SLOTS + (threadIdx.x & 31)) * 2; \
  st_u2 v_ = st_[threadIdx.x & 31]; p_[0] = v_[0]; p_[1] = v_[1]; } } while (0)
#include <hip/hip_runtime.h>
#include <hip/hip_bf16.h>

#define ST_RANGES {{0, 32, 256, "compute waves"}, {1, 32, 256, "loader waves"}, {0, 0, 32, "helper WGs, waves 0-3"}, {1, 0, 32, "helper WGs, waves 4-7"}}

typedef unsigned short u16;
typedef unsigned int u32;
typedef unsigned long long u64;
typedef u32 u32x2 __attribute__((ext_vector_type(2)));
typedef u32 u32x4 __attribute__((ext_vector_type(4)));
typedef float f32x2 __attribute__((ext_vector_type(2)));
typedef float f32x4 __attribute__((ext_vector_type(4)));
typedef float f32x16 __attribute__((ext_vector_type(16)));
typedef short s16x4 __attribute__((ext_vector_type(4)));
typedef __bf16 bf16x2 __attribute__((ext_vector_type(2)));
typedef __bf16 bf16x8 __attribute__((ext_vector_type(8)));
typedef __amdgpu_buffer_rsrc_t rsrc_t;
#define GAS __attribute__((address_space(1)))
#define LAS __attribute__((address_space(3)))
#define CAS __attribute__((address_space(4)))

constexpr int L = 2048, D = 512, NS = 128, F = 1792;

constexpr int WS_EPOCH = 0;
constexpr int WS_SG = 0x1000;
constexpr int WS_MG = 0x80000;
constexpr int WS_F2 = 0x40000;
constexpr int WS_F3 = 0x50000;
constexpr int WS_G2 = 0x70000;
constexpr int WS_S12 = 0x74000;
constexpr int WS_MF = 0x7c000;
constexpr int WS_XG = 2 << 20;
constexpr int WS_X1 = 0x440000;
constexpr int WS_X1S = 0x680000;
constexpr int WS_H = 9 << 20;
constexpr int WS_END = 16 << 20;
#define PADS 1
constexpr int ST_XG = 8192 + 128 * PADS, ST_MG = 1536 * 16 + 128 * PADS, ST_X1H = 32768 + 128 * PADS, ST_X1 = 2 * ST_X1H, ST_X1S = 131072 + 384 * PADS;
constexpr int OOB = 0x7ff00000;

constexpr int UNIT = 16384;
constexpr int LD_XR = 0, LD_WR = 4 * UNIT, LD_MISC = 9 * UNIT, LD_FLG = LD_MISC + 10240, LD_MV = LD_FLG + 32, LDS_BYTES = LD_MV + 64;
constexpr int TCOPY = 4160, PROW = 132;
constexpr int LD_T = 0, LD_U = 8 * TCOPY, LD_KD = LD_U + 2 * 4608, LD_P = LD_WR, LD_RED0 = 3 * UNIT, LD_RED1 = 8 * UNIT;
constexpr int KD_ZERO = 900, LD_XT = LD_RED0;
static_assert(LD_KD + 904 * 4 <= LD_RED0, "LDS map");
__host__ __device__ constexpr int w2_phys(int u) { return (u + 5) % 14; }
__host__ __device__ constexpr int w2_unit(int u) { return u % 9 == 8 ? 8 : (u % 9) / 2 + 4 * (u % 9 & 1); }
#define G2_NW 14
#define G2_N0 14
__host__ __device__ constexpr int h_next(int i) { int a = G2_N0 + 2 * i, b = i > 0 ? i - 1 + G2_NW : G2_NW; a = a < b ? a : b; return a < 56 ? a : 56; }
#define TF_PRE 4
#define F3_DELAY 10
#define XG_DELAY 0
#define OUT_AUX 2
#define G_EARLY 1
#define G_SIG 5
#define FT_AT 3
#define FT_DELAY 0
#define H_SLEEP 110
#define S1_SLEEP 2
#define F2_DELAY 16
#define POLL_SLEEP 2
#define LDNT(p) __builtin_nontemporal_load(p)
#define AUX_NT 2
#define WF ""
#define AUX_W 0
constexpr int SPIN_CAP = 1 << 15;
constexpr float LOG2E = 1.44269504088896341f;

__device__ __forceinline__ float bflo(u32 v) { return __uint_as_float(v << 16); }
__device__ __forceinline__ float bfhi(u32 v) { return __uint_as_float(v & 0xffff0000u); }
__device__ __forceinline__ u32 pk(float lo, float hi) {
  f32x2 v = {lo, hi};
  bf16x2 r = __builtin_convertvector(v, bf16x2);
  return __builtin_bit_cast(u32, r);
}
__device__ __forceinline__ float rb(float f) { return bflo(pk(f, f)); }
__device__ __forceinline__ float dot2(u32 a, u32 b, float c) {
  return __builtin_amdgcn_fdot2_f32_bf16(__builtin_bit_cast(bf16x2, a), __builtin_bit_cast(bf16x2, b), c, false);
}
__device__ __forceinline__ f32x4 mfma16(u32x4 a, u32x4 b, f32x4 c) {
  return __builtin_amdgcn_mfma_f32_16x16x32_bf16(__builtin_bit_cast(bf16x8, a), __builtin_bit_cast(bf16x8, b), c, 0, 0, 0);
}
__device__ __forceinline__ f32x16 mfma32(u32x4 a, u32x4 b, f32x16 c) {
  return __builtin_amdgcn_mfma_f32_32x32x16_bf16(__builtin_bit_cast(bf16x8, a), __builtin_bit_cast(bf16x8, b), c, 0, 0, 0);
}
template <int CTRL> __device__ __forceinline__ float dpp(float x) {
  return __builtin_bit_cast(float, __builtin_amdgcn_mov_dpp(__builtin_bit_cast(int, x), CTRL, 0xf, 0xf, true));
}
__device__ __forceinline__ float row_sum(float x) {
  x += dpp<0xB1>(x); x += dpp<0x4E>(x); x += dpp<0x124>(x); x += dpp<0x128>(x);
  return x;
}
__device__ __forceinline__ float fold32(float a, float b) {
  auto s = __builtin_amdgcn_permlane32_swap(__float_as_uint(a), __float_as_uint(b), false, false);
  return __uint_as_float(s[0]) + __uint_as_float(s[1]);
}
__device__ __forceinline__ float fold16(float a, float b) {
  auto s = __builtin_amdgcn_permlane16_swap(__float_as_uint(a), __float_as_uint(b), false, false);
  return __uint_as_float(s[0]) + __uint_as_float(s[1]);
}
__device__ __forceinline__ u32x4 mkrs(const void* p, u32 bytes) {
  const u64 a = (u64)p;
  return u32x4{(u32)a, (u32)(a >> 32) & 0xffffu, bytes, 0x00020000u};
}
__device__ __forceinline__ void dma4(u32x4 rs, int lds, int voff, int soff) {
  asm volatile("s_mov_b32 m0, %0\n\ts_nop 0\n\tbuffer_load_dword %1, %2, %3 offen lds" :: "s"(lds), "v"(voff), "s"(rs), "s"(soff) : "memory");
}
__device__ __forceinline__ void dma16_sc1(u32x4 rs, int lds, int voff, int soff) {
  asm volatile("s_mov_b32 m0, %0\n\ts_nop 0\n\tbuffer_load_dwordx4 %1, %2, %3 offen sc1 lds" :: "s"(lds), "v"(voff), "s"(rs), "s"(soff) : "memory");
}
#define DMA4_TEXT(F) "s_mov_b32 m0, %0\n\ts_nop 0\n\tbuffer_load_dwordx4 %1, %5, %6 offen" F " lds\n\tbuffer_load_dwordx4 %2, %5, %6 offen offset:1024" F " lds\n\t" \
                     "buffer_load_dwordx4 %3, %5, %6 offen offset:2048" F " lds\n\tbuffer_load_dwordx4 %4, %5, %6 offen offset:3072" F " lds"
__device__ __forceinline__ void dma4k(u32x4 rs, int lds, const int (&v)[4], int soff) {
  asm volatile(DMA4_TEXT(WF) :: "s"(lds), "v"(v[0]), "v"(v[1]), "v"(v[2]), "v"(v[3]), "s"(rs), "s"(soff) : "memory");
}
__device__ __forceinline__ void dma4k_sc1(u32x4 rs, int lds, const int (&v)[4], int soff) {
  asm volatile(DMA4_TEXT(" sc1") :: "s"(lds), "v"(v[0]), "v"(v[1]), "v"(v[2]), "v"(v[3]), "s"(rs), "s"(soff) : "memory");
}
__device__ __forceinline__ u32x4 ld_sc1(rsrc_t rs, int voff, int soff) { return __builtin_amdgcn_raw_buffer_load_b128(rs, voff, soff, 16); }
__device__ __forceinline__ void st_sc1(rsrc_t rs, int voff, int soff, u32x4 v) { __builtin_amdgcn_raw_buffer_store_b128(v, rs, voff + soff, 0, 16); }
__device__ __forceinline__ u32x4 tr_frag(LAS char* a0, LAS char* a1) {
  const s16x4 lo = __builtin_amdgcn_ds_read_tr16_b64_v4i16((LAS s16x4*)a0), hi = __builtin_amdgcn_ds_read_tr16_b64_v4i16((LAS s16x4*)a1);
  const u32x2 l = __builtin_bit_cast(u32x2, lo), h = __builtin_bit_cast(u32x2, hi);
  return u32x4{l[0], l[1], h[0], h[1]};
}
#define SCHED() __builtin_amdgcn_sched_barrier(0)
#define FENCE() asm volatile("" ::: "memory")
#define WAIT_VM(n) asm volatile("s_waitcnt vmcnt(" #n ")" ::: "memory")
#define BARRIER() do { asm volatile("s_waitcnt lgkmcnt(0)" ::: "memory"); __builtin_amdgcn_s_barrier(); FENCE(); } while (0)

__device__ __forceinline__ f32x2 cmul(f32x2 a, f32x2 b) {
  const f32x2 t = f32x2{a[0], a[0]} * b;
  return __builtin_elementwise_fma(f32x2{a[1], a[1]}, f32x2{-b[1], b[0]}, t);
}
__device__ __forceinline__ f32x2 cpow(float a2, float bq, float l) {
  const float m = __builtin_amdgcn_exp2f(a2 * l);
  const float ph = __builtin_amdgcn_fractf(bq * l);
  return f32x2{__builtin_amdgcn_cosf(ph), __builtin_amdgcn_sinf(ph)} * m;
}
__device__ __forceinline__ u32 pk2(f32x2 a) { return pk(a[0], a[1]); }
__device__ __forceinline__ f32x2 up(u32 v) { return f32x2{bflo(v), bfhi(v)}; }
__device__ __forceinline__ u32 ln_stat(float sum, float sq) {
  const float m = sum * (1.f / D);
  const float var = fmaxf(sq * (1.f / D) - m * m, 0.f);
  return pk(m, __builtin_amdgcn_sqrtf(rb(rb(var) + 1e-5f)));
}
__device__ __forceinline__ u32 tagdiff(u32x4 v, u32 ta, u32 tb) { return (v[1] ^ ta) | (v[3] ^ tb); }
__device__ __forceinline__ u32x4 poll16(rsrc_t rs, int voff, int soff, u32 ta, u32 tb) {
  u32x4 v;
  int spins = 0;
  for (;;) {
    FENCE();
    v = ld_sc1(rs, voff, soff);
    if (tagdiff(v, ta, tb) == 0u || ++spins >= SPIN_CAP) break;
    if (POLL_SLEEP) __builtin_amdgcn_s_sleep(POLL_SLEEP);
  }
  FENCE();
  return v;
}
__device__ __forceinline__ bool bad4(const u32x4 (&v)[4], u32 ta, u32 tb) {
  return (tagdiff(v[0], ta, tb) | tagdiff(v[1], ta, tb) | tagdiff(v[2], ta, tb) | tagdiff(v[3], ta, tb)) != 0u;
}
__device__ __forceinline__ void retry4(u32x4 (&v)[4], rsrc_t rs, int voff, int step, u32 ta, u32 tb) {
#pragma unroll 1
  for (int spins = 0; spins < SPIN_CAP; ++spins) {
    __builtin_amdgcn_s_sleep(S1_SLEEP);
    FENCE();
#pragma unroll
    for (int i = 0; i < 4; ++i) if (tagdiff(v[i], ta, tb)) v[i] = ld_sc1(rs, voff, i * step);
    if (!__any(bad4(v, ta, tb))) break;
  }
}
__device__ __forceinline__ void fold_cols(const float (&v)[16], LAS float* dst, int lane) {
  float u[8]; f32x4 t;
#pragma unroll
  for (int k = 0; k < 8; ++k) u[k] = fold32(v[k], v[k + 8]);
#pragma unroll
  for (int k = 0; k < 4; ++k) { const float a = fold16(u[k], u[k + 4]); t[k] = a + dpp<0x128>(a); }
  if (!(lane & 8)) *(LAS f32x4*)(dst + (lane & 7) * 16 + (lane >> 4) * 4) = t;
}
__device__ __forceinline__ f32x2 sum4(LAS const float* src, int stride) {
  return (*(LAS const f32x2*)src + *(LAS const f32x2*)(src + stride)) + (*(LAS const f32x2*)(src + 2 * stride) + *(LAS const f32x2*)(src + 3 * stride));
}
__device__ __forceinline__ u32 silu_bf(u32 c) {
  const float f = bflo(c), s = f * __builtin_amdgcn_rcpf(1.f + __builtin_amdgcn_exp2f(f * -LOG2E));
  return pk(s, s);
}
template <int CTRL> __device__ __forceinline__ void gv_row(u32x4 v, u32 sp, float (&acc)[8]) {
  const u32 sb = __builtin_amdgcn_mov_dpp(sp, CTRL, 0xf, 0xf, true), al = sb & 0xffffu, ah = sb & 0xffff0000u;
#pragma unroll
  for (int q = 0; q < 4; ++q) { acc[2 * q] = dot2(v[q], al, acc[2 * q]); acc[2 * q + 1] = dot2(v[q], ah, acc[2 * q + 1]); }
}
__device__ __forceinline__ f32x2 gv_fold(const float (&acc)[8]) {
  const float m0 = fold16(fold32(acc[0], acc[4]), fold32(acc[2], acc[6])), m1 = fold16(fold32(acc[1], acc[5]), fold32(acc[3], acc[7]));
  return f32x2{m0 + dpp<0x128>(m0), m1 + dpp<0x128>(m1)};
}
#define TOUCH(dst, voff, base) asm volatile("global_load_dword %0, %1, %2" : "=v"(dst) : "v"(voff), "s"(base))

__device__ __forceinline__ void quad_arrive(LAS char* f, int i, u32 n) {
  asm volatile("s_waitcnt lgkmcnt(0)" ::: "memory");
  *(volatile LAS u32*)(f + i * 4) = n;
}
__device__ __forceinline__ void quad_wait(LAS char* f, u32 n) {
  for (;;) {
    const u32x4 v = *(volatile LAS u32x4*)f;
    if (__builtin_amdgcn_readfirstlane(min(min(v[0], v[1]), min(v[2], v[3]))) >= n) break;
    __builtin_amdgcn_s_sleep(1);
  }
  FENCE();
}
__device__ __forceinline__ int only(bool c, int off) { return off | (c ? 0 : OOB); }
#define PIN(x) asm volatile("" : "+v"(x))

struct Params {
  const u16 *x, *Are, *Aim, *Bre, *Bim, *Cre, *Cim, *ldt, *dskip, *w1, *w2, *c, *wmod;
  u16* out;
  char* ws;
};

extern "C" __global__ void __launch_bounds__(512, 2) fused_block(Params p) {
  extern __shared__ __attribute__((aligned(16))) char smem_[];
  LAS char* smem = (LAS char*)smem_;
  const int tid = threadIdx.x, lane = tid & 63, w = __builtin_amdgcn_readfirstlane(tid >> 6), lw = w & 3;
  STAMP_DECL
  STAMP(0);
  *(LAS u32*)(smem + LD_FLG + w * 4) = 0u;
  BARRIER();
  asm volatile("" :: "s"(p.x), "s"(p.Are), "s"(p.Aim), "s"(p.Bre), "s"(p.Bim), "s"(p.Cre), "s"(p.Cim), "s"(p.ldt), "s"(p.dskip), "s"(p.w1), "s"(p.w2), "s"(p.c), "s"(p.wmod),
               "s"(p.out), "s"(p.ws));
#define SCAL_ISSUE(q) u64 epoch; u32 aux; \
  asm volatile("s_load_dwordx2 %0, %2, 0x0\n\ts_load_dword %1, %3, 0x0" : "=&s"(epoch), "=&s"(aux) : "s"(p.ws + WS_EPOCH), "s"(q)); SCHED()
#define SCAL_WAIT() asm volatile("s_waitcnt lgkmcnt(0)" : "+s"(epoch), "+s"(aux)); \
  epoch += 0x9E3779B97F4A7C15ull; ta = (u32)epoch; tb = (u32)(epoch >> 32); dsk = aux
#define ROLES() int b = blockIdx.x; asm volatile("" : "+s"(b)); \
  const int xcd = b & 7, j = b >> 3, pair = xcd * 32 + j, d0 = pair * 2; \
  const bool helper = j < 4; \
  const int rbk = (xcd >> 1) * 4 + (j & 3), cbk = (xcd & 1) * 7 + ((j - 4) >> 2), tbk = xcd * 4 + (j & 3), db = ((j >> 2) + 3 - (int)((u64)p.w2 >> 7)) & 7; \
  const rsrc_t rws = __builtin_amdgcn_make_buffer_rsrc(p.ws, 0, WS_END, 0x00020000)

  if (__builtin_expect(w >= 4, 1)) {
    LAS char* flg = smem + LD_FLG + 16;
    u32x4 sg[4];
    u32x2 xg[4];
    ROLES();
    const int go = lw * 4096 + lane * 16;
    u32 ta, tb, dsk;
    const u32x4 qws = mkrs(p.ws, WS_END);
#define POLL_ISSUE() asm volatile("buffer_load_dwordx4 %0, %4, %5, 0 offen sc1\n\tbuffer_load_dwordx4 %1, %4, %5, 0 offen offset:1024 sc1\n\t" \
      "buffer_load_dwordx4 %2, %4, %5, 0 offen offset:2048 sc1\n\tbuffer_load_dwordx4 %3, %4, %5, 0 offen offset:3072 sc1" \
      : "=&v"(sg[0]), "=&v"(sg[1]), "=&v"(sg[2]), "=&v"(sg[3]) : "v"(go + WS_SG), "s"(qws) : "memory")
    int vx[4], vw1[4], vw2[4], so, xo0, xo1, fo, mfo;
    LAS char *ra[2], *rc[2];
    auto addresses = [&]() {
      const int hm = (j - 4) >> 31;
#pragma unroll
      for (int i = 0; i < 4; ++i) {
        const int chk = 16 * ((lane & 15) ^ ((lane >> 4) << 2) ^ i);
        vx[i] = (lane >> 4) * 256 + chk;
        vw1[i] = (lane >> (4 + hm)) * (F * 2) + ((chk & ~hm) | ((lane & 7) * 16 & hm)) + i * ((4 + (4 & hm)) * F * 2 - 1024);
        vw2[i] = (lane >> 3) * 1024 + db * 128 + 16 * ((lane & 7) ^ (((lane >> 4) & 1) << 2)) + i * 7168;
        PIN(vx[i]); PIN(vw1[i]); PIN(vw2[i]);
      }
      const int g = (lane >> 1) & 3;
      ra[0] = smem + LD_RED0 + (2 * lw * 64 + 16 * g + ((lane >> 3) ^ (4 * g))) * 16 + 8 * (lane & 1);
      ra[1] = smem + LD_RED0 + (2 * lw * 64 + 16 * g + ((lane >> 3) ^ (4 * g) ^ 8)) * 16 + 8 * (lane & 1);
      rc[0] = ra[0] + (LD_RED1 - LD_RED0); rc[1] = ra[1] + (LD_RED1 - LD_RED0);
      so = WS_X1S + (lw * 4 + (lane >> 4)) * ST_X1S + d0 * 256 + (lane & 15) * 16;
      xo0 = WS_X1 + (lw * 8 + (lane >> 3)) * ST_X1 + pair * 128 + (lane & 7) * 16; xo1 = xo0 + ST_X1H;
      fo = only(lane < 4, WS_F2 + (lw * 256 + pair) * 64 + lane * 16);
      mfo = only((tid >> 1) == 248, WS_MF + (~lane & 1) * 4096 + pair * 16);
      PIN(mfo); PIN(ra[0]); PIN(ra[1]); PIN(rc[0]); PIN(rc[1]); PIN(so); PIN(xo0); PIN(xo1); PIN(fo);
      SCHED();
    };
    f32x2 xf[4][2], hf[4][2], ca[4][2];
    u32x4 mv1, mv2;
    auto coefficients = [&]() {
      __builtin_amdgcn_s_setprio(2);
      STAMP(7);
      if (__builtin_expect(__any(bad4(sg, ta, tb)), 0)) retry4(sg, rws, go + WS_SG, 1024, ta, tb);
      float rs[4][2];
#pragma unroll
      for (int i = 0; i < 4; ++i)
#pragma unroll
        for (int e = 0; e < 2; ++e) { rs[i][e] = __builtin_amdgcn_rcpf(bfhi(sg[i][2 * e])); PIN(rs[i][e]); }
      quad_wait(flg, 3u);
      const u32x4 mv0 = *(LAS const u32x4*)(smem + LD_MV);
      mv1 = *(LAS const u32x4*)(smem + LD_MV + 32); mv2 = *(LAS const u32x4*)(smem + LD_MV + 48);
      if (lw < 2) {
#pragma unroll
        for (int i = 0; i < 4; ++i) xg[i] = *(LAS const u32x2*)(smem + LD_XT + lw * 2048 + i * 512 + lane * 8);
      }
      const f32x2 sa = up(mv0[0]), g1 = up(mv0[3]);
#pragma unroll
      for (int i = 0; i < 4; ++i)
#pragma unroll
        for (int e = 0; e < 2; ++e) {
          ca[i][e] = g1 * rs[i][e];
          hf[i][e] = sa - ca[i][e] * bflo(sg[i][2 * e]);
          PIN(ca[i][e]); PIN(hf[i][e]);
        }
      STAMP(8);
    };
    if (lw >= 2) {
      u32 xt;
      TOUCH(xt, (j * 32 + (lw & 1) * 16 + (lane & 15)) * 1024 + xcd * 128, p.x);
      SCAL_ISSUE(p.dskip + d0);
      addresses();
      const u32x4 qx = mkrs(p.x, L * D * 2);
      SCAL_WAIT();
      quad_arrive(flg, lw, 1u);
#pragma unroll
      for (int i = 0; i < 8; ++i) {
        if (i == G_EARLY) quad_wait(flg, 1u);
        if (i == G_SIG) *(volatile LAS u32*)(flg + lw * 4) = 2u;
        dma4(qx, LD_XT + (lw & 1) * 2048 + i * 256, lane * 1024 + pair * 4, ((lw & 1) * 8 + i) * 65536);
        WAIT_VM(2);
      }
      if (XG_DELAY) __builtin_amdgcn_s_sleep(XG_DELAY);
      STAMP(3);
      POLL_ISSUE();
      const int xo = WS_XG + ((lw & 1) * 128 + (lane >> 1)) * ST_XG + pair * 32 + (lane & 1) * 16;
      u32x4 xe[4];
#pragma unroll
      for (int i = 0; i < 4; ++i) xe[i] = ld_sc1(rws, xo, i * 32 * ST_XG);
      asm volatile("s_waitcnt vmcnt(8)" :: "v"(xt) : "memory");
      quad_arrive(flg, lw, 3u);
      asm volatile("s_waitcnt vmcnt(4)" : "+v"(sg[0]), "+v"(sg[1]), "+v"(sg[2]), "+v"(sg[3]) :: "memory");
      coefficients();
      if (__builtin_expect(__any(bad4(xe, ta, tb)), 0)) retry4(xe, rws, xo, 32 * ST_XG, ta, tb);
#pragma unroll
      for (int i = 0; i < 4; ++i) xg[i] = u32x2{xe[i][0], xe[i][2]};
    } else {
      u32x4 xv[4], wm[8];
      const int gcb = (b + (b >> 4)) & 15;
#pragma unroll
      for (int i = 0; i < 4; ++i) xv[i] = LDNT((const u32x4*)(p.x + (size_t)(lw * 1024 + b * 4 + i) * D) + lane);
      SCHED();
      SCAL_ISSUE(p.dskip + d0);
      const u32 c0 = p.c[(b >> 4) * 32 + (lane >> 3) + 8 * (lane & 3)];
      SCHED();
      {
        const rsrc_t ra = __builtin_amdgcn_make_buffer_rsrc((void*)p.wmod, 0, D * 3072 * 2, 0x00020000);
        const rsrc_t rb_ = __builtin_amdgcn_make_buffer_rsrc((void*)p.wmod, 0, lw == 0 ? D * 3072 * 2 : 0, 0x00020000);
#pragma unroll
        for (int i = 0; i < 8; ++i)
          wm[i] = __builtin_amdgcn_raw_buffer_load_b128(i < 4 ? ra : rb_, (lane >> 3) * 6144 + (lane & 7) * 16,
                                                        ((b >> 4) * 32 + 8 * (i & 3)) * 6144 + ((lw + (i >> 2) * 2) * 16 + gcb) * 128, AUX_NT);
      }
      SCHED();
      *(LAS u32x4*)(smem + LD_U + (lane >> 5) * 4608 + (lane & 31) * 16) = u32x4{0u, 0u, 0u, 0u};
      int so_ = only(!(lane & 31), WS_SG + lw * 8192 + b * 32 + (lane >> 5) * 16);
      int mo_ = only(!(lane & 8), WS_MG + (b >> 4) * ST_MG + ((lw * 16 + gcb) * 32 + (lane >> 4) * 8 + (lane & 7)) * 16);
      PIN(so_); PIN(mo_);
      SCAL_WAIT();
      __builtin_amdgcn_s_setprio(3);
      STAMP(1);
      LAS char* xw = smem + LD_XT + 4096;
      if (lw) {
#pragma unroll
        for (int i = 0; i < 4; ++i) *(LAS u32x4*)(xw + i * 1056 + lane * 16) = xv[i];
      }
      float s[4], q[4];
#pragma unroll
      for (int i = 0; i < 4; ++i) {
        s[i] = 0.f; q[i] = 0.f;
#pragma unroll
        for (int z = 0; z < 4; ++z) { s[i] = dot2(xv[i][z], 0x3f803f80u, s[i]); q[i] = dot2(xv[i][z], xv[i][z], q[i]); }
      }
      const u32 st = ln_stat(row_sum(fold16(fold32(s[0], s[2]), fold32(s[1], s[3]))), row_sum(fold16(fold32(q[0], q[2]), fold32(q[1], q[3]))));
      const auto sw = __builtin_amdgcn_permlane16_swap(st, st, false, false);
      st_sc1(rws, so_, 0, u32x4{sw[0], ta, sw[1], tb});
      STAMP(2);
      auto gv_tile = [&](int z, u32 sp) {
        float acc[8] = {0.f, 0.f, 0.f, 0.f, 0.f, 0.f, 0.f, 0.f};
        gv_row<0x00>(wm[4 * z], sp, acc); gv_row<0x55>(wm[4 * z + 1], sp, acc); gv_row<0xAA>(wm[4 * z + 2], sp, acc); gv_row<0xFF>(wm[4 * z + 3], sp, acc);
        const f32x2 m = gv_fold(acc);
        st_sc1(rws, mo_ + z * 16384, 0, u32x4{__float_as_uint(m[0]), ta, __float_as_uint(m[1]), tb});
        SCHED();
      };
      if (lw) {
        u32 a[8][2];
#pragma unroll
        for (int i = 0; i < 8; ++i) {
          LAS const u32* src = (LAS const u32*)(xw + (lane & 1) * 2112 + i * 128 + (lane >> 1) * 4);
          a[i][0] = src[0]; a[i][1] = src[264];
        }
#pragma unroll
        for (int i = 0; i < 8; ++i) st_sc1(rws, WS_XG + b * ST_XG + lane * 16 + (i >> 2) * 4096, (i & 3) * 1024, u32x4{a[i][0], ta, a[i][1], tb});
        SCHED();
        gv_tile(0, silu_bf(c0));
        PIN(wm[4]); PIN(wm[5]); PIN(wm[6]); PIN(wm[7]);
      } else {
        const u32 sp = silu_bf(c0);
        gv_tile(0, sp);
        gv_tile(1, sp);
      }
      __builtin_amdgcn_s_setprio(0);
      quad_arrive(flg, lw, 2u);
      STAMP(4);
      addresses();
      quad_wait(flg, 2u);
      const int mo = WS_MG + (lane & 15) * ST_MG + ((((lane >> 4) & (2 * lw + 1)) + 2 * lw) * 256 + (pair >> 5) * 32 + (pair & 3) * 8 + ((pair >> 2) & 7)) * 16;
      u32x4 mg;
      asm volatile("buffer_load_dwordx4 %0, %1, %2, 0 offen sc1" : "=v"(mg) : "v"(mo), "s"(qws) : "memory");
      POLL_ISSUE();
      asm volatile("s_waitcnt vmcnt(4)" : "+v"(mg) :: "memory");
      if (__builtin_expect(__any(tagdiff(mg, ta, tb) != 0u), 0)) {
        asm volatile("s_waitcnt vmcnt(0)" : "+v"(sg[0]), "+v"(sg[1]), "+v"(sg[2]), "+v"(sg[3]) :: "memory");
        mg = poll16(rws, mo, 0, ta, tb);
      }
      const u32 mb = pk(row_sum(__uint_as_float(mg[0])), row_sum(__uint_as_float(mg[2])));
      *(LAS u32x2*)(smem + LD_MV + lw * 32 + (lane >> 4) * 8) = u32x2{mb, pk2(up(mb) + 1.f)};
      quad_arrive(flg, lw, 3u);
      asm volatile("s_waitcnt vmcnt(0)" : "+v"(sg[0]), "+v"(sg[1]), "+v"(sg[2]), "+v"(sg[3]) :: "memory");
      coefficients();
    }
    const u32x4 qw1 = mkrs(p.w1, D * F * 2), qw2 = mkrs(p.w2, D * F * 2);
    const int sw2 = ((lw >> 1) * 896 + (lw & 1) * 32) * 1024;
    auto w2_stage = [&](int u) { dma4k(qw2, w2_unit(u) * UNIT + lw * 4096, vw2, sw2 + w2_phys(u) * 65536); };
    const int wstep = (helper ? 32 : 16) * F * 2, wbase = helper ? min(tbk, 27) * 128 : cbk * 256;
    auto w1_stage = [&](int s) { dma4k(qw1, LD_WR + (s & 3) * UNIT + lw * 4096, vw1, (s * 4 + lw) * wstep + wbase); };
    auto x_stage = [&](int s) { dma4k_sc1(qws, LD_XR + (s & 3) * UNIT + lw * 4096, vx, WS_X1S + rbk * ST_X1S + s * 16384 + lw * 4096); };
    *(volatile LAS u32*)(flg + lw * 4) = 4u;
    STAMP(9);
#pragma unroll
    for (int i = 0; i < 4; ++i) {
#pragma unroll
      for (int e = 0; e < 2; ++e) { xf[i][e] = up(xg[i][e]); hf[i][e] += xf[i][e] * ca[i][e]; }
      LAS u32* U = (LAS u32*)(smem + LD_U + 512 + lw * 1024 + i * 256) + lane;
      U[0] = pk(hf[i][0][0], hf[i][1][0]);
      U[4608 / 4] = pk(hf[i][0][1], hf[i][1][1]);
    }
    STAMP(10);
    BARRIER();
    STAMP(11);
    w1_stage(0); w1_stage(1); w1_stage(2); w1_stage(3);
    const f32x2 gav = up(mv1[0]), gcv = up(mv2[1]);
    f32x2 xc[4][2];
    float gd[2] = {gav[0] * bflo(dsk), gav[1] * bfhi(dsk)}, n0 = bflo(mv2[2]) * (-1.f / LOG2E), n1 = bfhi(mv2[2]) * (-1.f / LOG2E);
    u32 k0 = __builtin_amdgcn_perm(mv1[2], mv2[1], 0x05040100u), k1 = __builtin_amdgcn_perm(mv1[2], mv2[1], 0x07060302u);
    PIN(gd[0]); PIN(gd[1]); PIN(n0); PIN(n1); PIN(k0); PIN(k1);
    const u32x4 mfv = {lane & 1 ? k0 : __float_as_uint(n0), ta, lane & 1 ? k1 : __float_as_uint(n1), tb};
    st_sc1(rws, mfo, 0, mfv);
#pragma unroll
    for (int i = 0; i < 4; ++i)
#pragma unroll
      for (int c = 0; c < 2; ++c) {
        float a0 = __builtin_fmaf(hf[i][0][c], gd[c], xf[i][0][c]), a1 = __builtin_fmaf(hf[i][1][c], gd[c], xf[i][1][c]);
        PIN(a0); PIN(a1);
        xc[i][c] = f32x2{a0, a1};
      }
    STAMP(12);
    SCHED();
    u32x4 wr[20];
    auto wr_load = [&](bool on) {
      const rsrc_t rw2 = __builtin_amdgcn_make_buffer_rsrc((void*)p.w2, 0, on ? D * F * 2 : 0, 0x00020000);
#pragma unroll
      for (int i = 0; i < 20; ++i) wr[i] = __builtin_amdgcn_raw_buffer_load_b128(rw2, vw2[i & 3] + (i & 3) * 1024, sw2 + (i / 4) * 65536, AUX_W);
    };
    wr_load(!helper);
    SCHED();
    STAMP(13);
    BARRIER();
    STAMP(14);
    {
      u32x4 o[2], s0, s1;
      f32x2 yp[4][4], c0[4], c1[4];
#pragma unroll
      for (int i = 0; i < 4; ++i) {
        yp[i][0] = *(volatile LAS const f32x2*)(ra[i & 1] + (i >> 1) * 1024); yp[i][1] = *(volatile LAS const f32x2*)(ra[i & 1] + (i >> 1) * 1024 + 8192);
      }
#pragma unroll
      for (int i = 0; i < 4; ++i) {
        yp[i][2] = *(volatile LAS const f32x2*)(rc[i & 1] + (i >> 1) * 1024); yp[i][3] = *(volatile LAS const f32x2*)(rc[i & 1] + (i >> 1) * 1024 + 8192);
      }
      SCHED();
#pragma unroll
      for (int i = 0; i < 4; ++i) { c0[i] = (yp[i][0] + yp[i][1]) * gav[0] + xc[i][0]; s0[i] = pk2(c0[i] * gcv[0]); }
      st_sc1(rws, so, 0, s0);
      SCHED();
#pragma unroll
      for (int i = 0; i < 4; ++i) { c1[i] = (yp[i][2] + yp[i][3]) * gav[1] + xc[i][1]; s1[i] = pk2(c1[i] * gcv[1]); }
      st_sc1(rws, so, 256, s1);
      SCHED();
#pragma unroll
      for (int i = 0; i < 4; ++i) { o[i >> 1][2 * (i & 1)] = pk(c0[i][0], c1[i][0]); o[i >> 1][2 * (i & 1) + 1] = pk(c0[i][1], c1[i][1]); }
      st_sc1(rws, xo0, 0, o[0]);
      st_sc1(rws, xo1, 0, o[1]);
    }
    STAMP(15);
    WAIT_VM(0);
    STAMP(17);
    st_sc1(rws, fo, 0, u32x4{ta, ta, tb, tb});
    __builtin_amdgcn_s_setprio(0);
    const int kind = helper ? 3 : lw;
    int e0 = WS_G2 + rbk * 1024, e1 = WS_S12 + (cbk * 128 + (kind - 1) * 64) * 16, e3 = WS_MF + 4096 + db * 512;
    asm volatile("" : "+s"(e0), "+s"(e1), "+s"(e3));
    const bool mid = (kind + 1) & 2;
    const int eo = (mid ? e1 : kind ? e3 : e0) + (lane & (kind == 3 ? 31 : 63)) * 16;
    LAS char* ea = smem + LD_MISC + (mid ? 256 + kind * 256 : kind * 512) + lane * (mid ? 4 : 8);
    u32x4 ev;
    if (helper) {
      BARRIER();
      ev = poll16(rws, eo, 0, ta, tb);
      *(LAS u32*)ea = ev[0]; *(LAS u32*)(ea + 4) = ev[2];
      BARRIER();
      __builtin_amdgcn_s_sleep(20);
      w2_stage(1); w2_stage(2); w2_stage(3);
      BARRIER(); BARRIER();
      w2_stage(0); w2_stage(4); w2_stage(5);
      wr_load(true);
    } else {
      __builtin_amdgcn_s_sleep(F2_DELAY);
      poll16(rws, ((lane >> 3) * 32 + lw * 8 + (lane & 7)) * 64, WS_F2 + (xcd >> 1) * 16384, ta, tb);
      STAMP(19);
      x_stage(0); x_stage(1); WAIT_VM(4); BARRIER();
      x_stage(2); x_stage(3); WAIT_VM(8); BARRIER();
      x_stage(4); w1_stage(4); WAIT_VM(12); BARRIER();
#pragma unroll 1
      for (int s = 5; s < 8; ++s) { x_stage(s); w1_stage(s); WAIT_VM(16); BARRIER(); }
      w2_stage(0); w2_stage(1); WAIT_VM(16); BARRIER();
      asm volatile("buffer_load_dwordx4 %0, %1, %2, 0 offen sc1" : "=v"(ev) : "v"(eo), "s"(qws) : "memory");
      w2_stage(2); w2_stage(3); WAIT_VM(17); BARRIER();
      asm volatile("s_waitcnt vmcnt(8)" : "+v"(ev) :: "memory");
      if (__any(tagdiff(ev, ta, tb) != 0u)) ev = poll16(rws, eo, 0, ta, tb);
      *(LAS u32*)ea = ev[0]; *(LAS u32*)(ea + (mid ? 512 : 4)) = ev[2];
      w2_stage(4); w2_stage(5); BARRIER();
    }
    STAMP(22);
    w2_stage(6); w2_stage(7); w2_stage(8);
    {
      const int xs = WS_X1 + tbk * ST_X1 + (db * 32 + (lw >> 1) * 16) * 128 + (lw & 1) * 64;
      dma16_sc1(qws, LD_MISC + 2048 + lw * 2048, (lane >> 2) * 128 + (lane & 3) * 16, xs);
      dma16_sc1(qws, LD_MISC + 3072 + lw * 2048, (lane >> 2) * 128 + (lane & 3) * 16, xs + ST_X1H);
    }
    WAIT_VM(0);
    STAMP(25);
    BARRIER();
    BARRIER();
#pragma unroll
    for (int i = 0; i < 20; ++i) {
      if (i == 8) BARRIER();
      *(LAS u32x4*)(smem + w2_unit(9 + i / 4) * UNIT + lw * 4096 + (i & 3) * 1024 + lane * 16) = wr[i];
    }
    BARRIER();
    if (b == 0 && tid == 256) *(u32x2*)(p.ws + WS_EPOCH) = u32x2{ta, tb};
    STAMP(26);
    STAMP_FLUSH(p.ws, 1);
    return;
  }

  ROLES();
  const int pch = w >> 1, pn = (w & 1) * 64 + lane, po = (d0 + pch) * NS + pn;
  u32 rar = LDNT(p.Are + po), rai = LDNT(p.Aim + po), rbr = LDNT(p.Bre + po), rbi = LDNT(p.Bim + po), rcr = LDNT(p.Cre + po), rci = LDNT(p.Cim + po);
  SCHED();
  SCAL_ISSUE(p.ldt + d0);
  {
    u32 cp0;
    {
      u64 pc; int r0, r1;
      asm volatile("s_getpc_b64 %0\n\ts_mov_b32 %1, fused_block@rel32@lo+4\n\ts_mov_b32 %2, fused_block_end@rel32@lo+12" : "=s"(pc), "=s"(r0), "=s"(r1));
      TOUCH(cp0, min(((w * 4 + (lane & 3)) * 32 + j) * 128, r1 - r0), pc + (long long)r0);
    }
    LAS char* flg = smem + LD_FLG;
    *(LAS u32x4*)(smem + LD_T + ((lane >> 1) & 7) * TCOPY + 4096 + (lane & 1) * 16) = u32x4{0u, 0u, 0u, 0u};
    *(LAS u32*)(smem + LD_KD + KD_ZERO * 4) = 0u;
    asm volatile("s_waitcnt lgkmcnt(0)" : "+s"(epoch), "+s"(aux));
    int ga[11], ka[4];
#pragma unroll
    for (int i = 0; i < 11; ++i) {
      const int l = 2047 - (tid * 8 + i);
      const int bits = (int)(pk((float)l, 0.f) & 0xffffu);
      const int idx = l < 128 ? l : bits - 0x4300 + 128;
      ga[i] = LD_KD + 4 * (l < 0 ? KD_ZERO : idx + (idx >> 3));
    }
    {
      const int e = lane & 15, g = lane >> 4;
      const u32 cfg = e < 8 ? (w == 0 ? 0x100004u : w == 1 ? 0x080004u : 0x022001u) : (w == 0 ? 0x100004u : w == 1 ? 0x044002u : 0x088003u);
      const int sh = cfg & 15, base = 256 * w + ((cfg >> 8) & 0xff) * (e >> sh) + (e & ((1 << sh) - 1));
#pragma unroll
      for (int r = 0; r < 4; ++r) { const int idx = base + (int)(cfg >> 16) * (4 * g + r); ka[r] = LD_KD + 4 * (idx + (idx >> 3)); PIN(ka[r]); }
    }
    STAMP(1);
    asm volatile("" : "+v"(rar), "+v"(rai), "+v"(rbr), "+v"(rbi), "+v"(rcr), "+v"(rci) : "v"(cp0));
    STAMP(2);
    SCHED();
    volatile LAS u32* P = (LAS u32*)(smem + LD_P) + pch * 64 * PROW + pn;
    const float dt = rb(__builtin_amdgcn_exp2f((pch ? bfhi(aux) : bflo(aux)) * LOG2E));
    const float a2 = (dt * bflo(rar)) * LOG2E, bq = (dt * bflo(rai)) * 0.15915494309189535f;
    const f32x2 z1 = cpow(a2, bq, 1.f), z8 = cpow(a2, bq, 8.f), z256 = cpow(a2, bq, 256.f);
    const f32x2 z16c = cmul(z8, z8) * f32x2{1.f, -1.f}, z512 = cmul(z256, z256), z768 = cmul(z512, z256);
    f32x2 u = cpow(a2, bq, 1024.f), q = {1.f, 0.f};
    f32x2 r = cmul(f32x2{bflo(rcr), -bflo(rci)}, f32x2{bflo(rbr), -bflo(rbi)} * dt);
#pragma unroll
    for (int e = 0; e < 16; ++e) {
      const f32x2 rn = cmul(r, z16c), qn = cmul(q, z1);
      P[e * PROW] = pk2(r);
      P[(16 + e) * PROW] = pk2(q);
      if (!(e & 1)) P[(32 + e / 2) * PROW] = pk2(cmul(q, z256));
      if (!(e & 3)) { P[(40 + e / 4) * PROW] = pk2(cmul(q, z512)); P[(44 + e / 4) * PROW] = pk2(cmul(q, z768)); }
      if ((e & 3) == 1) { P[(48 + e / 2) * PROW] = pk2(u); P[(49 + e / 2) * PROW] = pk2(cmul(u, z8)); }
      if ((e & 3) == 3) u = cmul(u, z256);
      r = rn; q = qn;
    }
    P[56 * PROW] = pk2(u);
    STAMP(3);
    quad_arrive(flg, w, 1u);
#pragma unroll
    for (int i = 0; i < 11; ++i) PIN(ga[i]);
    quad_wait(flg, 1u);
    STAMP(4);
    if (w < 3) {
      LAS const u32* fb = (LAS const u32*)(smem + LD_P) + (lane & 15) * PROW + ((lane >> 4) & 1) * 64 + (lane >> 5) * 32;
      u32x4 fa[16], fc[16];
      f32x4 acc[2] = {{0.f, 0.f, 0.f, 0.f}, {0.f, 0.f, 0.f, 0.f}};
#pragma unroll
      for (int ks = 0; ks < 22; ++ks) {
        if (ks < 16) {
          const int o = (ks & 1) * 64 * PROW + (ks >> 1) * 4;
          fa[ks] = *(LAS const u32x4*)(fb + o); fc[ks] = *(LAS const u32x4*)(fb + (w + 1) * 16 * PROW + o);
        }
        SCHED();
        if (ks >= 6) acc[ks & 1] = mfma16(fa[ks - 6], fc[ks - 6], acc[ks & 1]);
        SCHED();
      }
#pragma unroll
      for (int r4 = 0; r4 < 4; ++r4) *(LAS u32*)(smem + ka[r4]) = pk(acc[0][r4], acc[1][r4]);
    }
    STAMP(5);
    quad_arrive(flg, w, 2u);
    quad_wait(flg, 2u);
    STAMP(6);
    u32 e[11];
#pragma unroll
    for (int i = 0; i < 11; ++i) e[i] = *(LAS const u32*)(smem + ga[i]);
#pragma unroll
    for (int c = 0; c < 2; ++c)
#pragma unroll
      for (int s = 0; s < 4; ++s) {
        u32x4 o;
#pragma unroll
        for (int z = 0; z < 4; ++z) o[z] = __builtin_amdgcn_perm(e[s + 2 * z + 1], e[s + 2 * z], c ? 0x07060302u : 0x05040100u);
        *(LAS u32x4*)(smem + LD_T + (c * 4 + s) * TCOPY + tid * 16) = o;
      }
    quad_arrive(flg, w, 3u);
  }
  epoch += 0x9E3779B97F4A7C15ull;
  const u32 ta = (u32)epoch, tb = (u32)(epoch >> 32);
  {
    const int r = lane & 15, g = lane >> 4, ch = w >> 1, half = w & 1;
    LAS const char* Tb = smem + LD_T + (ch * 4 + ((3 - r) & 3)) * TCOPY + 2 * (2028 - 4 * (r >> 2) + 8 * g - 128 * half) - 7 * 512 - 192;
    LAS const char* Ub = smem + LD_U + ch * 4608 + 2 * (256 + 16 * r - 16 + 8 * g - 128 * half) - 192;
    LAS f32x4* red = (LAS f32x4*)(smem + (ch ? LD_RED1 : LD_RED0)) + half * 512 + (lane ^ ((lane >> 4) << 2));
    PIN(Ub); PIN(red);
    f32x4 acc[8];
#pragma unroll
    for (int n = 0; n < 8; ++n) acc[n] = f32x4{0.f, 0.f, 0.f, 0.f};
    u32x4 uf[2][8], tf[4][8];
    quad_wait(smem + LD_FLG, 3u);
#define LD_TH(e, a, hh) do { const u32x2 l_ = *(volatile LAS const u32x2*)(Tb + 64 * (3 - (e)) + 512 * (7 - (a)) + 8 * (hh)); tf[e][a][2 * (hh)] = l_[0]; tf[e][a][2 * (hh) + 1] = l_[1]; } while (0)
#pragma unroll
    for (int e = 0; e < TF_PRE; ++e)
#pragma unroll
      for (int a = 0; a < 8; ++a) { LD_TH(e, a, 0); LD_TH(e, a, 1); }
#define LD_UF(n, e) uf[(e) & 1][n] = *(LAS const u32x4*)(Ub + 64 * (3 - (e)) + 512 * (n))
#define CM(e, a, m) do { SCHED(); acc[(a) + (m)] = mfma16(tf[e][a], uf[(e) & 1][m], acc[(a) + (m)]); SCHED(); } while (0)
    STAMP(10);
    u32 ft;
    quad_wait(smem + LD_FLG + 16, FT_AT);
    __builtin_amdgcn_s_sleep(FT_DELAY);
    TOUCH(ft, ((b * 2 + (w & 1)) * 28 + min(lane, 27)) * 128, w < 2 ? p.w1 : p.w2);
    BARRIER();
    STAMP(11);
#pragma unroll
    for (int n = 0; n < 8; ++n) LD_UF(n, 0);
#pragma unroll
    for (int e = 0; e < 3; ++e) {
      int cnt = 0;
#pragma unroll
      for (int m = 0; m < 8; ++m)
#pragma unroll
        for (int a = 0; a + m < 8; ++a) {
          CM(e, a, m);
          if (cnt >= 4 && cnt < 12) LD_UF(cnt - 4, e + 1);
          if (cnt >= 12 && cnt < 28 && e + TF_PRE < 4) LD_TH(e + TF_PRE, (cnt - 12) >> 1, (cnt - 12) & 1);
          ++cnt;
        }
    }
#pragma unroll
    for (int n = 0; n < 8; ++n)
#pragma unroll
      for (int a = 0; a <= n; ++a) { CM(3, a, n - a); if (a == 1) red[(n - 1) * 64] = acc[n - 1]; }
    STAMP(12);
    red[7 * 64] = acc[7];
    BARRIER();
    STAMP(14);
    WAIT_VM(0);
    asm volatile("" :: "v"(ft));
  }
  if (helper) {
    const int hb = xcd * 4 + j;
    u32x4 v[16];
    const int rg = tid >> 3;
    LAS float* rd = (LAS float*)(smem + 4096);
    { const u32x4 m = poll16(rws, tid * 16, WS_MF, ta, tb); *(LAS u32x2*)(smem + tid * 8) = u32x2{m[0], m[2]}; }
    BARRIER();
#pragma unroll
    for (int i = 0; i < 16; ++i) v[i] = *(LAS const u32x4*)(smem + LD_WR + i * 4096 + tid * 16);
    float a[16];
#pragma unroll
    for (int e = 0; e < 16; ++e) a[e] = 0.f;
#pragma unroll
    for (int i = 0; i < 16; ++i) {
      const u32 gs = *(LAS const u32*)(smem + (i * 32 + rg) * 4);
      const u32 gl = gs & 0xffffu, gh = gs << 16, sl = gs >> 16, sh = gs & 0xffff0000u;
#pragma unroll
      for (int z = 0; z < 4; ++z) {
        a[4 * z] = dot2(v[i][z], gl, a[4 * z]); a[4 * z + 1] = dot2(v[i][z], sl, a[4 * z + 1]);
        a[4 * z + 2] = dot2(v[i][z], gh, a[4 * z + 2]); a[4 * z + 3] = dot2(v[i][z], sh, a[4 * z + 3]);
      }
    }
    fold_cols(a, rd + w * 128, lane);
    STAMP(18);
    poll16(rws, tid * 64, WS_F2 + (xcd >> 1) * 16384, ta, tb);
    BARRIER();
    STAMP(19);
#pragma unroll
    for (int i = 0; i < 16; ++i) v[i] = ld_sc1(rws, tid * 16, WS_X1 + hb * ST_X1 + (i >> 3) * ST_X1H + (i & 7) * 4096);
    if (w == 0 && hb < 28) {
      const f32x2 s = sum4(rd + lane * 2, 128);
      st_sc1(rws, lane * 16, WS_S12 + hb * 1024, u32x4{__float_as_uint(s[0]), ta, __float_as_uint(s[1] * -LOG2E), tb});
    }
#pragma unroll
    for (int e = 0; e < 16; ++e) a[e] = 0.f;
#pragma unroll
    for (int i = 0; i < 16; ++i)
#pragma unroll
      for (int z = 0; z < 4; ++z) {
        const int o = (i >> 3) * 8 + 2 * z;
        a[o] = dot2(v[i][z], 0x3f803f80u, a[o]); a[o + 1] = dot2(v[i][z], v[i][z], a[o + 1]);
      }
    STAMP(20);
    fold_cols(a, rd + 512 + w * 128, lane);
    BARRIER();
    if (w == 0) {
      const f32x2 s = sum4(rd + 512 + lane * 2, 128);
      const u32 st = ln_stat(s[0], s[1]);
      const u32 st2 = __builtin_amdgcn_mov_dpp(st, 0xB1, 0xf, 0xf, true);
      if (!(lane & 1)) st_sc1(rws, lane * 8, WS_G2 + hb * 512, u32x4{st, ta, st2, tb});
    }
    BARRIER();
    STAMP(22);
    __builtin_amdgcn_s_sleep(H_SLEEP);
  } else {
    const int wt = w & 1, wf = w >> 1;
    const int h = lane >> 5, blk = (lane >> 4) & 1, q = (lane & 15) >> 2, pp = lane & 3;
    int ax[2][2], aw[2][2];
#pragma unroll
    for (int c = 0; c < 2; ++c)
#pragma unroll
      for (int t = 0; t < 2; ++t) {
        const int base = 256 * (8 * h + 4 * t + q) + 16 * ((2 * blk + (pp >> 1)) ^ (2 * h + t)) + 8 * (pp & 1);
        ax[c][t] = LD_XR + base + 64 * ((2 * wt + c) ^ q);
        aw[c][t] = LD_WR + base + 64 * ((2 * wf + c) ^ q);
        PIN(ax[c][t]); PIN(aw[c][t]);
      }
    f32x16 acc[2][2];
#pragma unroll
    for (int a = 0; a < 2; ++a)
#pragma unroll
      for (int c = 0; c < 2; ++c)
#pragma unroll
        for (int r = 0; r < 16; ++r) acc[a][c][r] = 0.f;
    u32x4 xf[2][2], wfr[2][2];
    u32 st[2];
    int ho = WS_H + ((rbk * 4 + wt * 2) * 112 + cbk * 8 + wf * 4) * 1024 + lane * 16, ho1 = ho + 112 * 1024;
    int fo = only(lane < 8, WS_F3 + ((rbk * 4 + w) * 16 + cbk) * 128 + lane * 16);
    PIN(ho); PIN(ho1); PIN(fo);
#define RD_X(i, c) xf[(i) & 1][c] = tr_frag(smem + ax[c][0] + ((i) & 15) * 4096, smem + ax[c][1] + ((i) & 15) * 4096)
#define RD_W(i, c) wfr[(i) & 1][c] = tr_frag(smem + aw[c][0] + ((i) & 15) * 4096, smem + aw[c][1] + ((i) & 15) * 4096)
#define MM(i, a, c) do { SCHED(); acc[a][c] = mfma32(wfr[(i) & 1][a], xf[(i) & 1][c], acc[a][c]); SCHED(); } while (0)
    BARRIER();
    STAMP(19);
    RD_W(0, 0); RD_X(0, 0); RD_X(0, 1); RD_W(0, 1);
#pragma unroll
    for (int i = 0; i < 32; ++i) {
      if ((i & 3) != 3) {
        MM(i, 0, 0); RD_W(i + 1, 0); MM(i, 0, 1); RD_X(i + 1, 0); MM(i, 1, 0); RD_X(i + 1, 1); MM(i, 1, 1); RD_W(i + 1, 1);
      } else {
        MM(i, 0, 0); MM(i, 0, 1);
        BARRIER();
        if (i == 31) { st[0] = *(LAS const u32*)(smem + LD_MISC + (wt * 64 + (lane & 31)) * 4); st[1] = *(LAS const u32*)(smem + LD_MISC + (wt * 64 + 32 + (lane & 31)) * 4); }
        MM(i, 1, 0);
        if (i < 31) { RD_W(i + 1, 0); RD_X(i + 1, 0); }
        MM(i, 1, 1);
        if (i < 31) { RD_X(i + 1, 1); RD_W(i + 1, 1); }
      }
    }
    STAMP(21);
    float rstd[2], mr[2];
#pragma unroll
    for (int c = 0; c < 2; ++c) {
      rstd[c] = __builtin_amdgcn_rcpf(bfhi(st[c])) * -LOG2E;
      mr[c] = -bflo(st[c]) * rstd[c];
    }
    STAMP(22);
    LAS const float* s12 = (LAS const float*)(smem + LD_MISC + 512) + wf * 64 + 4 * h;
#pragma unroll
    for (int a = 0; a < 2; ++a)
#pragma unroll
      for (int sp = 0; sp < 2; ++sp) {
        f32x4 s1[2], s2[2];
#pragma unroll
        for (int z = 0; z < 2; ++z) { s1[z] = *(LAS const f32x4*)(s12 + a * 32 + sp * 16 + z * 8); s2[z] = *(LAS const f32x4*)(s12 + 128 + a * 32 + sp * 16 + z * 8); }
#pragma unroll
        for (int c = 0; c < 2; ++c) {
          u32x4 o;
#pragma unroll
          for (int e = 0; e < 4; ++e) {
            const int z = e >> 1, k = (e & 1) * 2;
            const f32x2 av = {acc[a][c][sp * 8 + 2 * e], acc[a][c][sp * 8 + 2 * e + 1]};
            const f32x2 cc = f32x2{s1[z][k], s1[z][k + 1]} * mr[c] + f32x2{s2[z][k], s2[z][k + 1]};
            const f32x2 ar = av * rstd[c] + cc;
            const f32x2 dn = f32x2{__builtin_amdgcn_exp2f(ar[0]), __builtin_amdgcn_exp2f(ar[1])} + 1.f;
            o[e] = pk2(ar * f32x2{__builtin_amdgcn_rcpf(dn[0]), __builtin_amdgcn_rcpf(dn[1])});
          }
          st_sc1(rws, c ? ho1 : ho, (a * 2 + sp) * 1024, o);
        }
      }
    STAMP(23);
    WAIT_VM(0);
    STAMP(24);
    st_sc1(rws, fo, 0, u32x4{ta, ta, tb, tb});
  }

  {
    const int wt = w & 1, kh = w >> 1, h = lane >> 5, tl = lane & 31;
    const int blk = (lane >> 4) & 1, q = (lane & 15) >> 2, pp = lane & 3;
    LAS char* aw[3][2];
#pragma unroll
    for (int c = 0; c < 6; ++c) {
      aw[c >> 1][c & 1] = smem + (c >> 1) * 65536 + kh * 8192 + 128 * (4 * h + q) + 64 * ((c & 1) ^ kh ^ (q >> 1)) + 32 * blk + 8 * pp;
      asm volatile("" : "+v"(aw[c >> 1][c & 1]));
    }
    const rsrc_t ro = __builtin_amdgcn_make_buffer_rsrc(p.out, 0, L * D * 2, 0x00020000);
    int oo[2];
#pragma unroll
    for (int i = 0; i < 2; ++i) {
      const int r = tbk * 64 + (tid >> 3) + 32 * i;
      oo[i] = ((r & ~511) + 128 * ((r >> 1) & 3) + 2 * ((r >> 3) & 63) + (r & 1)) * (D * 2) + db * 128 + (tid & 7) * 16;
      PIN(oo[i]);
    }
    f32x16 acc[2];
#pragma unroll
    for (int r = 0; r < 16; ++r) { acc[0][r] = 0.f; acc[1][r] = 0.f; }
    STAMP(25);
    BARRIER();
    constexpr int NW = G2_NW;
    u32x4 hf[NW], wf[3][2];
#define RD_W2C(i, c) do { LAS char* a_ = aw[w2_unit((i) >> 2) >> 2][c] + (w2_unit((i) >> 2) & 3) * UNIT + ((i) & 3) * 2048; wf[(i) % 3][c] = tr_frag(a_, a_ + 1024); } while (0)
#define RD_W2(i) do { RD_W2C(i, 0); RD_W2C(i, 1); } while (0)
    RD_W2(0); RD_W2(1);
    const int sh = WS_H + ((tbk * 2 + wt) * 112 + 56 * kh) * 1024;
#define LD_H(i) hf[(i) % NW] = ld_sc1(rws, lane * 16 + ((i) & 3) * 1024, sh + w2_phys((i) >> 2) * 4096)
    if (F3_DELAY) __builtin_amdgcn_s_sleep(F3_DELAY);
    poll16(rws, ((lane & 1) * 32 + min(lane >> 1, 6)) * 128, WS_F3 + ((tbk >> 1) * 4 + (tbk & 1)) * 2048 + kh * 896, ta, tb);
    STAMP(26);
#pragma unroll
    for (int i = 0; i < G2_N0; ++i) LD_H(i);
    SCHED();
    u32 xr[8]; f32x2 gb[8];
#pragma unroll
    for (int i = 0; i < 8; ++i) {
      const int pr = 4 * (i >> 1) + 2 * h + (i & 1);
      xr[i] = *(LAS const u32*)(smem + LD_MISC + 2048 + w * 2048 + ((tl >> 2) & 1) * 1024 + pr * 64 + (tl >> 3) * 16 + (tl & 3) * 4);
      gb[i] = *(LAS const f32x2*)(smem + LD_MISC + 1536 + (kh * 16 + pr) * 8);
    }
    SCHED();
#pragma unroll
    for (int i = 0; i < 56; ++i) {
      SCHED();
      acc[0] = mfma32(wf[i % 3][0], hf[i % NW], acc[0]);
      SCHED();
      if (i == 0) STAMP(29);
      if (i == 18) STAMP(30);
      if (i == 42) STAMP(31);
      if (i == 6 || i == 18 || i == 42) BARRIER();
      if (i + 2 < 56) RD_W2C(i + 2, 0);
      if (h_next(i) < h_next(i + 1)) LD_H(h_next(i));
      SCHED();
      acc[1] = mfma32(wf[i % 3][1], hf[i % NW], acc[1]);
      SCHED();
      if (i + 2 < 56) RD_W2C(i + 2, 1);
      if (h_next(i) + 1 < h_next(i + 1)) LD_H(h_next(i) + 1);
      asm volatile("" :: "v"(wf[i % 3][0]), "v"(wf[i % 3][1]), "v"(hf[i % NW]));
    }
    STAMP(27);
    LAS f32x4* ex = (LAS f32x4*)(smem + w2_unit(5) * UNIT) + lane;
#pragma unroll
    for (int qq = 0; qq < 4; ++qq) ex[(w * 4 + qq) * 64] = f32x4{acc[1][4 * qq], acc[1][4 * qq + 1], acc[1][4 * qq + 2], acc[1][4 * qq + 3]};
    BARRIER();
    LAS char* ot = smem + w2_unit(6) * UNIT;
    f32x4 oth[4];
#pragma unroll
    for (int qq = 0; qq < 4; ++qq) oth[qq] = ex[((w ^ 2) * 4 + qq) * 64];
    SCHED();
#pragma unroll
    for (int qq = 0; qq < 4; ++qq) {
      const f32x4 o4 = oth[qq];
      u32x2 o;
#pragma unroll
      for (int z = 0; z < 2; ++z)
        o[z] = pk2((f32x2{acc[0][4 * qq + 2 * z], acc[0][4 * qq + 2 * z + 1]} + f32x2{o4[2 * z], o4[2 * z + 1]}) * gb[2 * qq + z] + up(xr[2 * qq + z]));
      *(LAS u32x2*)(ot + (wt * 32 + tl) * 144 + kh * 64 + qq * 16 + h * 8) = o;
    }
    BARRIER();
#pragma unroll
    for (int i = 0; i < 2; ++i) {
      const int row = (tid >> 3) + 32 * i;
      __builtin_amdgcn_raw_buffer_store_b128(*(LAS const u32x4*)(ot + row * 144 + (tid & 7) * 16), ro, oo[i], 0, OUT_AUX);
    }
    STAMP(28);
    STAMP_FLUSH(p.ws, 0);
  }
}

extern "C" __global__ void fused_block_end() {}

extern "C" __global__ void __launch_bounds__(1024) probe_k(char* ws) {
  __shared__ unsigned mn, mx; __shared__ unsigned long long sum; __shared__ unsigned cnt;
  if (threadIdx.x == 0) { mn = 0xffffffffu; mx = 0u; sum = 0ull; cnt = 0u; }
  __syncthreads();
  const unsigned long long* p = (const unsigned long long*)(ws + ST_OFF);
  const int b = threadIdx.x >> 2, w = (threadIdx.x & 3) + 0 * 4;
  const size_t base = (((size_t)0 * ST_MAXB + b) * ST_MAXW + w) * ST_SLOTS * 2;
  const unsigned t0 = (unsigned)p[base], ts = (unsigned)p[base + 2 * 11];
  atomicMin(&mn, t0);
  if (ts) { atomicMax(&mx, ts); atomicAdd(&sum, (unsigned long long)ts); atomicAdd(&cnt, 1u); }
  __syncthreads();
  unsigned d = mx - mn;
  if (d > 10000u) d = 10000u;
  for (int i = 0; i < 1000000; ++i) if ((((unsigned)__builtin_amdgcn_s_memrealtime() | 0x80000000u) - mn) >= d + 5000u) break;
}
extern "C" void kernel_launch(void* const* d_in, const int* in_sizes, int n_in, void* d_out, int out_size, void* d_ws, size_t ws_size,
                              hipStream_t stream) {
  Params p;
  p.x = (const u16*)d_in[0]; p.Are = (const u16*)d_in[1]; p.Aim = (const u16*)d_in[2]; p.Bre = (const u16*)d_in[3];
  p.Bim = (const u16*)d_in[4]; p.Cre = (const u16*)d_in[5]; p.Cim = (const u16*)d_in[6]; p.ldt = (const u16*)d_in[7];
  p.dskip = (const u16*)d_in[8]; p.w1 = (const u16*)d_in[10]; p.w2 = (const u16*)d_in[11]; p.c = (const u16*)d_in[12];
  p.wmod = (const u16*)d_in[13]; p.out = (u16*)d_out; p.ws = (char*)d_ws;
  (void)hipFuncSetAttribute((const void*)fused_block, hipFuncAttributeMaxDynamicSharedMemorySize, LDS_BYTES + ST_LDS_EXTRA);
  fused_block<<<256, 512, LDS_BYTES + ST_LDS_EXTRA, stream>>>(p);
  probe_k<<<1, 1024, 0, stream>>>(p.ws);
}
```

```cpp
#define F2_DELAY 26
#define F3_DELAY 13
#include <hip/hip_runtime.h>
#include <hip/hip_bf16.h>

#define STAMP_DECL
#define STAMP(s) __builtin_amdgcn_sched_barrier(0)
#define STAMP_FLUSH(ws, k)
#define ST_LDS_EXTRA 0
#define ST_RANGES {{0, 32, 256, "compute waves"}, {1, 32, 256, "loader waves"}, {0, 0, 32, "helper WGs, waves 0-3"}, {1, 0, 32, "helper WGs, waves 4-7"}}

typedef unsigned short u16;
typedef unsigned int u32;
typedef unsigned long long u64;
typedef u32 u32x2 __attribute__((ext_vector_type(2)));
typedef u32 u32x4 __attribute__((ext_vector_type(4)));
typedef float f32x2 __attribute__((ext_vector_type(2)));
typedef float f32x4 __attribute__((ext_vector_type(4)));
typedef float f32x16 __attribute__((ext_vector_type(16)));
typedef short s16x4 __attribute__((ext_vector_type(4)));
typedef __bf16 bf16x2 __attribute__((ext_vector_type(2)));
typedef __bf16 bf16x8 __attribute__((ext_vector_type(8)));
typedef __amdgpu_buffer_rsrc_t rsrc_t;
#define GAS __attribute__((address_space(1)))
#define LAS __attribute__((address_space(3)))
#define CAS __attribute__((address_space(4)))

constexpr int L = 2048, D = 512, NS = 128, F = 1792;

constexpr int WS_EPOCH = 0;
constexpr int WS_SG = 0x1000;
constexpr int WS_MG = 0x80000;
constexpr int WS_F2 = 0x40000;
constexpr int WS_F3 = 0x50000;
constexpr int WS_G2 = 0x70000;
constexpr int WS_S12 = 0x74000;
constexpr int WS_MF = 0x7c000;
constexpr int WS_XG = 2 << 20;
constexpr int WS_X1 = 0x440000;
constexpr int WS_X1S = 0x680000;
constexpr int WS_H = 9 << 20;
constexpr int WS_END = 16 << 20;
#define PADS 1
constexpr int ST_XG = 8192 + 128 * PADS, ST_MG = 1536 * 16 + 128 * PADS, ST_X1H = 32768 + 128 * PADS, ST_X1 = 2 * ST_X1H, ST_X1S = 131072 + 384 * PADS;
constexpr int OOB = 0x7ff00000;

constexpr int UNIT = 16384;
constexpr int LD_XR = 0, LD_WR = 4 * UNIT, LD_MISC = 9 * UNIT, LD_FLG = LD_MISC + 10240, LD_MV = LD_FLG + 32, LDS_BYTES = LD_MV + 64;
constexpr int TCOPY = 4160, PROW = 132;
constexpr int LD_T = 0, LD_U = 8 * TCOPY, LD_KD = LD_U + 2 * 4608, LD_P = LD_WR, LD_RED0 = 3 * UNIT, LD_RED1 = 8 * UNIT;
constexpr int KD_ZERO = 900, LD_XT = LD_RED0;
static_assert(LD_KD + 904 * 4 <= LD_RED0, "LDS map");
__host__ __device__ constexpr int w2_phys(int u) { return (u + 5) % 14; }
__host__ __device__ constexpr int w2_unit(int u) { return u % 9 == 8 ? 8 : (u % 9) / 2 + 4 * (u % 9 & 1); }
#define G2_NW 14
#define G2_N0 14
__host__ __device__ constexpr int h_next(int i) { int a = G2_N0 + 2 * i, b = i > 0 ? i - 1 + G2_NW : G2_NW; a = a < b ? a : b; return a < 56 ? a : 56; }
#define TF_PRE 4
#define XG_DELAY 0
#define OUT_AUX 2
#define G_EARLY 1
#define G_SIG 5
#define FT_AT 3
#define FT_DELAY 0
#define H_SLEEP 110
#define S1_SLEEP 2
#define POLL_SLEEP 2
#define LDNT(p) __builtin_nontemporal_load(p)
#define AUX_NT 2
#define WF ""
#define AUX_W 0
constexpr int SPIN_CAP = 1 << 15;
constexpr float LOG2E = 1.44269504088896341f;

__device__ __forceinline__ float bflo(u32 v) { return __uint_as_float(v << 16); }
__device__ __forceinline__ float bfhi(u32 v) { return __uint_as_float(v & 0xffff0000u); }
__device__ __forceinline__ u32 pk(float lo, float hi) {
  f32x2 v = {lo, hi};
  bf16x2 r = __builtin_convertvector(v, bf16x2);
  return __builtin_bit_cast(u32, r);
}
__device__ __forceinline__ float rb(float f) { return bflo(pk(f, f)); }
__device__ __forceinline__ float dot2(u32 a, u32 b, float c) {
  return __builtin_amdgcn_fdot2_f32_bf16(__builtin_bit_cast(bf16x2, a), __builtin_bit_cast(bf16x2, b), c, false);
}
__device__ __forceinline__ f32x4 mfma16(u32x4 a, u32x4 b, f32x4 c) {
  return __builtin_amdgcn_mfma_f32_16x16x32_bf16(__builtin_bit_cast(bf16x8, a), __builtin_bit_cast(bf16x8, b), c, 0, 0, 0);
}
__device__ __forceinline__ f32x16 mfma32(u32x4 a, u32x4 b, f32x16 c) {
  return __builtin_amdgcn_mfma_f32_32x32x16_bf16(__builtin_bit_cast(bf16x8, a), __builtin_bit_cast(bf16x8, b), c, 0, 0, 0);
}
template <int CTRL> __device__ __forceinline__ float dpp(float x) {
  return __builtin_bit_cast(float, __builtin_amdgcn_mov_dpp(__builtin_bit_cast(int, x), CTRL, 0xf, 0xf, true));
}
__device__ __forceinline__ float row_sum(float x) {
  x += dpp<0xB1>(x); x += dpp<0x4E>(x); x += dpp<0x124>(x); x += dpp<0x128>(x);
  return x;
}
__device__ __forceinline__ float fold32(float a, float b) {
  auto s = __builtin_amdgcn_permlane32_swap(__float_as_uint(a), __float_as_uint(b), false, false);
  return __uint_as_float(s[0]) + __uint_as_float(s[1]);
}
__device__ __forceinline__ float fold16(float a, float b) {
  auto s = __builtin_amdgcn_permlane16_swap(__float_as_uint(a), __float_as_uint(b), false, false);
  return __uint_as_float(s[0]) + __uint_as_float(s[1]);
}
__device__ __forceinline__ u32x4 mkrs(const void* p, u32 bytes) {
  const u64 a = (u64)p;
  return u32x4{(u32)a, (u32)(a >> 32) & 0xffffu, bytes, 0x00020000u};
}
__device__ __forceinline__ void dma4(u32x4 rs, int lds, int voff, int soff) {
  asm volatile("s_mov_b32 m0, %0\n\ts_nop 0\n\tbuffer_load_dword %1, %2, %3 offen lds" :: "s"(lds), "v"(voff), "s"(rs), "s"(soff) : "memory");
}
__device__ __forceinline__ void dma16_sc1(u32x4 rs, int lds, int voff, int soff) {
  asm volatile("s_mov_b32 m0, %0\n\ts_nop 0\n\tbuffer_load_dwordx4 %1, %2, %3 offen sc1 lds" :: "s"(lds), "v"(voff), "s"(rs), "s"(soff) : "memory");
}
#define DMA4_TEXT(F) "s_mov_b32 m0, %0\n\ts_nop 0\n\tbuffer_load_dwordx4 %1, %5, %6 offen" F " lds\n\tbuffer_load_dwordx4 %2, %5, %6 offen offset:1024" F " lds\n\t" \
                     "buffer_load_dwordx4 %3, %5, %6 offen offset:2048" F " lds\n\tbuffer_load_dwordx4 %4, %5, %6 offen offset:3072" F " lds"
__device__ __forceinline__ void dma4k(u32x4 rs, int lds, const int (&v)[4], int soff) {
  asm volatile(DMA4_TEXT(WF) :: "s"(lds), "v"(v[0]), "v"(v[1]), "v"(v[2]), "v"(v[3]), "s"(rs), "s"(soff) : "memory");
}
__device__ __forceinline__ void dma4k_sc1(u32x4 rs, int lds, const int (&v)[4], int soff) {
  asm volatile(DMA4_TEXT(" sc1") :: "s"(lds), "v"(v[0]), "v"(v[1]), "v"(v[2]), "v"(v[3]), "s"(rs), "s"(soff) : "memory");
}
__device__ __forceinline__ u32x4 ld_sc1(rsrc_t rs, int voff, int soff) { return __builtin_amdgcn_raw_buffer_load_b128(rs, voff, soff, 16); }
__device__ __forceinline__ void st_sc1(rsrc_t rs, int voff, int soff, u32x4 v) { __builtin_amdgcn_raw_buffer_store_b128(v, rs, voff + soff, 0, 16); }
__device__ __forceinline__ u32x4 tr_frag(LAS char* a0, LAS char* a1) {
  const s16x4 lo = __builtin_amdgcn_ds_read_tr16_b64_v4i16((LAS s16x4*)a0), hi = __builtin_amdgcn_ds_read_tr16_b64_v4i16((LAS s16x4*)a1);
  const u32x2 l = __builtin_bit_cast(u32x2, lo), h = __builtin_bit_cast(u32x2, hi);
  return u32x4{l[0], l[1], h[0], h[1]};
}
#define SCHED() __builtin_amdgcn_sched_barrier(0)
#define FENCE() asm volatile("" ::: "memory")
#define WAIT_VM(n) asm volatile("s_waitcnt vmcnt(" #n ")" ::: "memory")
#define BARRIER() do { asm volatile("s_waitcnt lgkmcnt(0)" ::: "memory"); __builtin_amdgcn_s_barrier(); FENCE(); } while (0)

__device__ __forceinline__ f32x2 cmul(f32x2 a, f32x2 b) {
  const f32x2 t = f32x2{a[0], a[0]} * b;
  return __builtin_elementwise_fma(f32x2{a[1], a[1]}, f32x2{-b[1], b[0]}, t);
}
__device__ __forceinline__ f32x2 cpow(float a2, float bq, float l) {
  const float m = __builtin_amdgcn_exp2f(a2 * l);
  const float ph = __builtin_amdgcn_fractf(bq * l);
  return f32x2{__builtin_amdgcn_cosf(ph), __builtin_amdgcn_sinf(ph)} * m;
}
__device__ __forceinline__ u32 pk2(f32x2 a) { return pk(a[0], a[1]); }
__device__ __forceinline__ f32x2 up(u32 v) { return f32x2{bflo(v), bfhi(v)}; }
__device__ __forceinline__ u32 ln_stat(float sum, float sq) {
  const float m = sum * (1.f / D);
  const float var = fmaxf(sq * (1.f / D) - m * m, 0.f);
  return pk(m, __builtin_amdgcn_sqrtf(rb(rb(var) + 1e-5f)));
}
__device__ __forceinline__ u32 tagdiff(u32x4 v, u32 ta, u32 tb) { return (v[1] ^ ta) | (v[3] ^ tb); }
__device__ __forceinline__ u32x4 poll16(rsrc_t rs, int voff, int soff, u32 ta, u32 tb) {
  u32x4 v;
  int spins = 0;
  for (;;) {
    FENCE();
    v = ld_sc1(rs, voff, soff);
    if (tagdiff(v, ta, tb) == 0u || ++spins >= SPIN_CAP) break;
    if (POLL_SLEEP) __builtin_amdgcn_s_sleep(POLL_SLEEP);
  }
  FENCE();
  return v;
}
__device__ __forceinline__ bool bad4(const u32x4 (&v)[4], u32 ta, u32 tb) {
  return (tagdiff(v[0], ta, tb) | tagdiff(v[1], ta, tb) | tagdiff(v[2], ta, tb) | tagdiff(v[3], ta, tb)) != 0u;
}
__device__ __forceinline__ void retry4(u32x4 (&v)[4], rsrc_t rs, int voff, int step, u32 ta, u32 tb) {
#pragma unroll 1
  for (int spins = 0; spins < SPIN_CAP; ++spins) {
    __builtin_amdgcn_s_sleep(S1_SLEEP);
    FENCE();
#pragma unroll
    for (int i = 0; i < 4; ++i) if (tagdiff(v[i], ta, tb)) v[i] = ld_sc1(rs, voff, i * step);
    if (!__any(bad4(v, ta, tb))) break;
  }
}
__device__ __forceinline__ void fold_cols(const float (&v)[16], LAS float* dst, int lane) {
  float u[8]; f32x4 t;
#pragma unroll
  for (int k = 0; k < 8; ++k) u[k] = fold32(v[k], v[k + 8]);
#pragma unroll
  for (int k = 0; k < 4; ++k) { const float a = fold16(u[k], u[k + 4]); t[k] = a + dpp<0x128>(a); }
  if (!(lane & 8)) *(LAS f32x4*)(dst + (lane & 7) * 16 + (lane >> 4) * 4) = t;
}
__device__ __forceinline__ f32x2 sum4(LAS const float* src, int stride) {
  return (*(LAS const f32x2*)src + *(LAS const f32x2*)(src + stride)) + (*(LAS const f32x2*)(src + 2 * stride) + *(LAS const f32x2*)(src + 3 * stride));
}
__device__ __forceinline__ u32 silu_bf(u32 c) {
  const float f = bflo(c), s = f * __builtin_amdgcn_rcpf(1.f + __builtin_amdgcn_exp2f(f * -LOG2E));
  return pk(s, s);
}
template <int CTRL> __device__ __forceinline__ void gv_row(u32x4 v, u32 sp, float (&acc)[8]) {
  const u32 sb = __builtin_amdgcn_mov_dpp(sp, CTRL, 0xf, 0xf, true), al = sb & 0xffffu, ah = sb & 0xffff0000u;
#pragma unroll
  for (int q = 0; q < 4; ++q) { acc[2 * q] = dot2(v[q], al, acc[2 * q]); acc[2 * q + 1] = dot2(v[q], ah, acc[2 * q + 1]); }
}
__device__ __forceinline__ f32x2 gv_fold(const float (&acc)[8]) {
  const float m0 = fold16(fold32(acc[0], acc[4]), fold32(acc[2], acc[6])), m1 = fold16(fold32(acc[1], acc[5]), fold32(acc[3], acc[7]));
  return f32x2{m0 + dpp<0x128>(m0), m1 + dpp<0x128>(m1)};
}
#define TOUCH(dst, voff, base) asm volatile("global_load_dword %0, %1, %2" : "=v"(dst) : "v"(voff), "s"(base))

__device__ __forceinline__ void quad_arrive(LAS char* f, int i, u32 n) {
  asm volatile("s_waitcnt lgkmcnt(0)" ::: "memory");
  *(volatile LAS u32*)(f + i * 4) = n;
}
__device__ __forceinline__ void quad_wait(LAS char* f, u32 n) {
  for (;;) {
    const u32x4 v = *(volatile LAS u32x4*)f;
    if (__builtin_amdgcn_readfirstlane(min(min(v[0], v[1]), min(v[2], v[3]))) >= n) break;
    __builtin_amdgcn_s_sleep(1);
  }
  FENCE();
}
__device__ __forceinline__ int only(bool c, int off) { return off | (c ? 0 : OOB); }
#define PIN(x) asm volatile("" : "+v"(x))

struct Params {
  const u16 *x, *Are, *Aim, *Bre, *Bim, *Cre, *Cim, *ldt, *dskip, *w1, *w2, *c, *wmod;
  u16* out;
  char* ws;
};

extern "C" __global__ void __launch_bounds__(512, 2) fused_block(Params p) {
  extern __shared__ __attribute__((aligned(16))) char smem_[];
  LAS char* smem = (LAS char*)smem_;
  const int tid = threadIdx.x, lane = tid & 63, w = __builtin_amdgcn_readfirstlane(tid >> 6), lw = w & 3;
  STAMP_DECL
  STAMP(0);
  *(LAS u32*)(smem + LD_FLG + w * 4) = 0u;
  BARRIER();
  asm volatile("" :: "s"(p.x), "s"(p.Are), "s"(p.Aim), "s"(p.Bre), "s"(p.Bim), "s"(p.Cre), "s"(p.Cim), "s"(p.ldt), "s"(p.dskip), "s"(p.w1), "s"(p.w2), "s"(p.c), "s"(p.wmod),
               "s"(p.out), "s"(p.ws));
#define SCAL_ISSUE(q) u64 epoch; u32 aux; \
  asm volatile("s_load_dwordx2 %0, %2, 0x0\n\ts_load_dword %1, %3, 0x0" : "=&s"(epoch), "=&s"(aux) : "s"(p.ws + WS_EPOCH), "s"(q)); SCHED()
#define SCAL_WAIT() asm volatile("s_waitcnt lgkmcnt(0)" : "+s"(epoch), "+s"(aux)); \
  epoch += 0x9E3779B97F4A7C15ull; ta = (u32)epoch; tb = (u32)(epoch >> 32); dsk = aux
#define ROLES() int b = blockIdx.x; asm volatile("" : "+s"(b)); \
  const int xcd = b & 7, j = b >> 3, pair = xcd * 32 + j, d0 = pair * 2; \
  const bool helper = j < 4; \
  const int rbk = (xcd >> 1) * 4 + (j & 3), cbk = (xcd & 1) * 7 + ((j - 4) >> 2), tbk = xcd * 4 + (j & 3), db = ((j >> 2) + 3 - (int)((u64)p.w2 >> 7)) & 7; \
  const rsrc_t rws = __builtin_amdgcn_make_buffer_rsrc(p.ws, 0, WS_END, 0x00020000)

  if (__builtin_expect(w >= 4, 1)) {
    LAS char* flg = smem + LD_FLG + 16;
    u32x4 sg[4];
    u32x2 xg[4];
    ROLES();
    const int go = lw * 4096 + lane * 16;
    u32 ta, tb, dsk;
    const u32x4 qws = mkrs(p.ws, WS_END);
#define POLL_ISSUE() asm volatile("buffer_load_dwordx4 %0, %4, %5, 0 offen sc1\n\tbuffer_load_dwordx4 %1, %4, %5, 0 offen offset:1024 sc1\n\t" \
      "buffer_load_dwordx4 %2, %4, %5, 0 offen offset:2048 sc1\n\tbuffer_load_dwordx4 %3, %4, %5, 0 offen offset:3072 sc1" \
      : "=&v"(sg[0]), "=&v"(sg[1]), "=&v"(sg[2]), "=&v"(sg[3]) : "v"(go + WS_SG), "s"(qws) : "memory")
    int vx[4], vw1[4], vw2[4], so, xo0, xo1, fo, mfo;
    LAS char *ra[2], *rc[2];
    auto addresses = [&]() {
      const int hm = (j - 4) >> 31;
#pragma unroll
      for (int i = 0; i < 4; ++i) {
        const int chk = 16 * ((lane & 15) ^ ((lane >> 4) << 2) ^ i);
        vx[i] = (lane >> 4) * 256 + chk;
        vw1[i] = (lane >> (4 + hm)) * (F * 2) + ((chk & ~hm) | ((lane & 7) * 16 & hm)) + i * ((4 + (4 & hm)) * F * 2 - 1024);
        vw2[i] = (lane >> 3) * 1024 + db * 128 + 16 * ((lane & 7) ^ (((lane >> 4) & 1) << 2)) + i * 7168;
        PIN(vx[i]); PIN(vw1[i]); PIN(vw2[i]);
      }
      const int g = (lane >> 1) & 3;
      ra[0] = smem + LD_RED0 + (2 * lw * 64 + 16 * g + ((lane >> 3) ^ (4 * g))) * 16 + 8 * (lane & 1);
      ra[1] = smem + LD_RED0 + (2 * lw * 64 + 16 * g + ((lane >> 3) ^ (4 * g) ^ 8)) * 16 + 8 * (lane & 1);
      rc[0] = ra[0] + (LD_RED1 - LD_RED0); rc[1] = ra[1] + (LD_RED1 - LD_RED0);
      so = WS_X1S + (lw * 4 + (lane >> 4)) * ST_X1S + d0 * 256 + (lane & 15) * 16;
      xo0 = WS_X1 + (lw * 8 + (lane >> 3)) * ST_X1 + pair * 128 + (lane & 7) * 16; xo1 = xo0 + ST_X1H;
      fo = only(lane < 4, WS_F2 + (lw * 256 + pair) * 64 + lane * 16);
      mfo = only((tid >> 1) == 248, WS_MF + (~lane & 1) * 4096 + pair * 16);
      PIN(mfo); PIN(ra[0]); PIN(ra[1]); PIN(rc[0]); PIN(rc[1]); PIN(so); PIN(xo0); PIN(xo1); PIN(fo);
      SCHED();
    };
    f32x2 xf[4][2], hf[4][2], ca[4][2];
    u32x4 mv1, mv2;
    auto coefficients = [&]() {
      __builtin_amdgcn_s_setprio(2);
      STAMP(7);
      if (__builtin_expect(__any(bad4(sg, ta, tb)), 0)) retry4(sg, rws, go + WS_SG, 1024, ta, tb);
      float rs[4][2];
#pragma unroll
      for (int i = 0; i < 4; ++i)
#pragma unroll
        for (int e = 0; e < 2; ++e) { rs[i][e] = __builtin_amdgcn_rcpf(bfhi(sg[i][2 * e])); PIN(rs[i][e]); }
      quad_wait(flg, 3u);
      const u32x4 mv0 = *(LAS const u32x4*)(smem + LD_MV);
      mv1 = *(LAS const u32x4*)(smem + LD_MV + 32); mv2 = *(LAS const u32x4*)(smem + LD_MV + 48);
      if (lw < 2) {
#pragma unroll
        for (int i = 0; i < 4; ++i) xg[i] = *(LAS const u32x2*)(smem + LD_XT + lw * 2048 + i * 512 + lane * 8);
      }
      const f32x2 sa = up(mv0[0]), g1 = up(mv0[3]);
#pragma unroll
      for (int i = 0; i < 4; ++i)
#pragma unroll
        for (int e = 0; e < 2; ++e) {
          ca[i][e] = g1 * rs[i][e];
          hf[i][e] = sa - ca[i][e] * bflo(sg[i][2 * e]);
          PIN(ca[i][e]); PIN(hf[i][e]);
        }
      STAMP(8);
    };
    if (lw >= 2) {
      u32 xt;
      TOUCH(xt, (j * 32 + (lw & 1) * 16 + (lane & 15)) * 1024 + xcd * 128, p.x);
      SCAL_ISSUE(p.dskip + d0);
      addresses();
      const u32x4 qx = mkrs(p.x, L * D * 2);
      SCAL_WAIT();
      quad_arrive(flg, lw, 1u);
#pragma unroll
      for (int i = 0; i < 8; ++i) {
        if (i == G_EARLY) quad_wait(flg, 1u);
        if (i == G_SIG) *(volatile LAS u32*)(flg + lw * 4) = 2u;
        dma4(qx, LD_XT + (lw & 1) * 2048 + i * 256, lane * 1024 + pair * 4, ((lw & 1) * 8 + i) * 65536);
        WAIT_VM(2);
      }
      if (XG_DELAY) __builtin_amdgcn_s_sleep(XG_DELAY);
      STAMP(3);
      POLL_ISSUE();
      const int xo = WS_XG + ((lw & 1) * 128 + (lane >> 1)) * ST_XG + pair * 32 + (lane & 1) * 16;
      u32x4 xe[4];
#pragma unroll
      for (int i = 0; i < 4; ++i) xe[i] = ld_sc1(rws, xo, i * 32 * ST_XG);
      asm volatile("s_waitcnt vmcnt(8)" :: "v"(xt) : "memory");
      quad_arrive(flg, lw, 3u);
      asm volatile("s_waitcnt vmcnt(4)" : "+v"(sg[0]), "+v"(sg[1]), "+v"(sg[2]), "+v"(sg[3]) :: "memory");
      coefficients();
      if (__builtin_expect(__any(bad4(xe, ta, tb)), 0)) retry4(xe, rws, xo, 32 * ST_XG, ta, tb);
#pragma unroll
      for (int i = 0; i < 4; ++i) xg[i] = u32x2{xe[i][0], xe[i][2]};
    } else {
      u32x4 xv[4], wm[8];
      const int gcb = (b + (b >> 4)) & 15;
#pragma unroll
      for (int i = 0; i < 4; ++i) xv[i] = LDNT((const u32x4*)(p.x + (size_t)(lw * 1024 + b * 4 + i) * D) + lane);
      SCHED();
      SCAL_ISSUE(p.dskip + d0);
      const u32 c0 = p.c[(b >> 4) * 32 + (lane >> 3) + 8 * (lane & 3)];
      SCHED();
      {
        const rsrc_t ra = __builtin_amdgcn_make_buffer_rsrc((void*)p.wmod, 0, D * 3072 * 2, 0x00020000);
        const rsrc_t rb_ = __builtin_amdgcn_make_buffer_rsrc((void*)p.wmod, 0, lw == 0 ? D * 3072 * 2 : 0, 0x00020000);
#pragma unroll
        for (int i = 0; i < 8; ++i)
          wm[i] = __builtin_amdgcn_raw_buffer_load_b128(i < 4 ? ra : rb_, (lane >> 3) * 6144 + (lane & 7) * 16,
                                                        ((b >> 4) * 32 + 8 * (i & 3)) * 6144 + ((lw + (i >> 2) * 2) * 16 + gcb) * 128, AUX_NT);
      }
      SCHED();
      *(LAS u32x4*)(smem + LD_U + (lane >> 5) * 4608 + (lane & 31) * 16) = u32x4{0u, 0u, 0u, 0u};
      int so_ = only(!(lane & 31), WS_SG + lw * 8192 + b * 32 + (lane >> 5) * 16);
      int mo_ = only(!(lane & 8), WS_MG + (b >> 4) * ST_MG + ((lw * 16 + gcb) * 32 + (lane >> 4) * 8 + (lane & 7)) * 16);
      PIN(so_); PIN(mo_);
      SCAL_WAIT();
      __builtin_amdgcn_s_setprio(3);
      STAMP(1);
      LAS char* xw = smem + LD_XT + 4096;
      if (lw) {
#pragma unroll
        for (int i = 0; i < 4; ++i) *(LAS u32x4*)(xw + i * 1056 + lane * 16) = xv[i];
      }
      float s[4], q[4];
#pragma unroll
      for (int i = 0; i < 4; ++i) {
        s[i] = 0.f; q[i] = 0.f;
#pragma unroll
        for (int z = 0; z < 4; ++z) { s[i] = dot2(xv[i][z], 0x3f803f80u, s[i]); q[i] = dot2(xv[i][z], xv[i][z], q[i]); }
      }
      const u32 st = ln_stat(row_sum(fold16(fold32(s[0], s[2]), fold32(s[1], s[3]))), row_sum(fold16(fold32(q[0], q[2]), fold32(q[1], q[3]))));
      const auto sw = __builtin_amdgcn_permlane16_swap(st, st, false, false);
      st_sc1(rws, so_, 0, u32x4{sw[0], ta, sw[1], tb});
      STAMP(2);
      auto gv_tile = [&](int z, u32 sp) {
        float acc[8] = {0.f, 0.f, 0.f, 0.f, 0.f, 0.f, 0.f, 0.f};
        gv_row<0x00>(wm[4 * z], sp, acc); gv_row<0x55>(wm[4 * z + 1], sp, acc); gv_row<0xAA>(wm[4 * z + 2], sp, acc); gv_row<0xFF>(wm[4 * z + 3], sp, acc);
        const f32x2 m = gv_fold(acc);
        st_sc1(rws, mo_ + z * 16384, 0, u32x4{__float_as_uint(m[0]), ta, __float_as_uint(m[1]), tb});
        SCHED();
      };
      if (lw) {
        u32 a[8][2];
#pragma unroll
        for (int i = 0; i < 8; ++i) {
          LAS const u32* src = (LAS const u32*)(xw + (lane & 1) * 2112 + i * 128 + (lane >> 1) * 4);
          a[i][0] = src[0]; a[i][1] = src[264];
        }
#pragma unroll
        for (int i = 0; i < 8; ++i) st_sc1(rws, WS_XG + b * ST_XG + lane * 16 + (i >> 2) * 4096, (i & 3) * 1024, u32x4{a[i][0], ta, a[i][1], tb});
        SCHED();
        gv_tile(0, silu_bf(c0));
        PIN(wm[4]); PIN(wm[5]); PIN(wm[6]); PIN(wm[7]);
      } else {
        const u32 sp = silu_bf(c0);
        gv_tile(0, sp);
        gv_tile(1, sp);
      }
      __builtin_amdgcn_s_setprio(0);
      quad_arrive(flg, lw, 2u);
      STAMP(4);
      addresses();
      quad_wait(flg, 2u);
      const int mo = WS_MG + (lane & 15) * ST_MG + ((((lane >> 4) & (2 * lw + 1)) + 2 * lw) * 256 + (pair >> 5) * 32 + (pair & 3) * 8 + ((pair >> 2) & 7)) * 16;
      u32x4 mg;
      asm volatile("buffer_load_dwordx4 %0, %1, %2, 0 offen sc1" : "=v"(mg) : "v"(mo), "s"(qws) : "memory");
      POLL_ISSUE();
      asm volatile("s_waitcnt vmcnt(4)" : "+v"(mg) :: "memory");
      if (__builtin_expect(__any(tagdiff(mg, ta, tb) != 0u), 0)) {
        asm volatile("s_waitcnt vmcnt(0)" : "+v"(sg[0]), "+v"(sg[1]), "+v"(sg[2]), "+v"(sg[3]) :: "memory");
        mg = poll16(rws, mo, 0, ta, tb);
      }
      const u32 mb = pk(row_sum(__uint_as_float(mg[0])), row_sum(__uint_as_float(mg[2])));
      *(LAS u32x2*)(smem + LD_MV + lw * 32 + (lane >> 4) * 8) = u32x2{mb, pk2(up(mb) + 1.f)};
      quad_arrive(flg, lw, 3u);
      asm volatile("s_waitcnt vmcnt(0)" : "+v"(sg[0]), "+v"(sg[1]), "+v"(sg[2]), "+v"(sg[3]) :: "memory");
      coefficients();
    }
    const u32x4 qw1 = mkrs(p.w1, D * F * 2), qw2 = mkrs(p.w2, D * F * 2);
    const int sw2 = ((lw >> 1) * 896 + (lw & 1) * 32) * 1024;
    auto w2_stage = [&](int u) { dma4k(qw2, w2_unit(u) * UNIT + lw * 4096, vw2, sw2 + w2_phys(u) * 65536); };
    const int wstep = (helper ? 32 : 16) * F * 2, wbase = helper ? min(tbk, 27) * 128 : cbk * 256;
    auto w1_stage = [&](int s) { dma4k(qw1, LD_WR + (s & 3) * UNIT + lw * 4096, vw1, (s * 4 + lw) * wstep + wbase); };
    auto x_stage = [&](int s) { dma4k_sc1(qws, LD_XR + (s & 3) * UNIT + lw * 4096, vx, WS_X1S + rbk * ST_X1S + s * 16384 + lw * 4096); };
    *(volatile LAS u32*)(flg + lw * 4) = 4u;
    STAMP(9);
#pragma unroll
    for (int i = 0; i < 4; ++i) {
#pragma unroll
      for (int e = 0; e < 2; ++e) { xf[i][e] = up(xg[i][e]); hf[i][e] += xf[i][e] * ca[i][e]; }
      LAS u32* U = (LAS u32*)(smem + LD_U + 512 + lw * 1024 + i * 256) + lane;
      U[0] = pk(hf[i][0][0], hf[i][1][0]);
      U[4608 / 4] = pk(hf[i][0][1], hf[i][1][1]);
    }
    STAMP(10);
    BARRIER();
    STAMP(11);
    w1_stage(0); w1_stage(1); w1_stage(2); w1_stage(3);
    const f32x2 gav = up(mv1[0]), gcv = up(mv2[1]);
    f32x2 xc[4][2];
    float gd[2] = {gav[0] * bflo(dsk), gav[1] * bfhi(dsk)}, n0 = bflo(mv2[2]) * (-1.f / LOG2E), n1 = bfhi(mv2[2]) * (-1.f / LOG2E);
    u32 k0 = __builtin_amdgcn_perm(mv1[2], mv2[1], 0x05040100u), k1 = __builtin_amdgcn_perm(mv1[2], mv2[1], 0x07060302u);
    PIN(gd[0]); PIN(gd[1]); PIN(n0); PIN(n1); PIN(k0); PIN(k1);
    const u32x4 mfv = {lane & 1 ? k0 : __float_as_uint(n0), ta, lane & 1 ? k1 : __float_as_uint(n1), tb};
    st_sc1(rws, mfo, 0, mfv);
#pragma unroll
    for (int i = 0; i < 4; ++i)
#pragma unroll
      for (int c = 0; c < 2; ++c) {
        float a0 = __builtin_fmaf(hf[i][0][c], gd[c], xf[i][0][c]), a1 = __builtin_fmaf(hf[i][1][c], gd[c], xf[i][1][c]);
        PIN(a0); PIN(a1);
        xc[i][c] = f32x2{a0, a1};
      }
    STAMP(12);
    SCHED();
    u32x4 wr[20];
    auto wr_load = [&](bool on) {
      const rsrc_t rw2 = __builtin_amdgcn_make_buffer_rsrc((void*)p.w2, 0, on ? D * F * 2 : 0, 0x00020000);
#pragma unroll
      for (int i = 0; i < 20; ++i) wr[i] = __builtin_amdgcn_raw_buffer_load_b128(rw2, vw2[i & 3] + (i & 3) * 1024, sw2 + (i / 4) * 65536, AUX_W);
    };
    wr_load(!helper);
    SCHED();
    STAMP(13);
    BARRIER();
    STAMP(14);
    {
      u32x4 o[2], s0, s1;
      f32x2 yp[4][4], c0[4], c1[4];
#pragma unroll
      for (int i = 0; i < 4; ++i) {
        yp[i][0] = *(volatile LAS const f32x2*)(ra[i & 1] + (i >> 1) * 1024); yp[i][1] = *(volatile LAS const f32x2*)(ra[i & 1] + (i >> 1) * 1024 + 8192);
      }
#pragma unroll
      for (int i = 0; i < 4; ++i) {
        yp[i][2] = *(volatile LAS const f32x2*)(rc[i & 1] + (i >> 1) * 1024); yp[i][3] = *(volatile LAS const f32x2*)(rc[i & 1] + (i >> 1) * 1024 + 8192);
      }
      SCHED();
#pragma unroll
      for (int i = 0; i < 4; ++i) { c0[i] = (yp[i][0] + yp[i][1]) * gav[0] + xc[i][0]; s0[i] = pk2(c0[i] * gcv[0]); }
      st_sc1(rws, so, 0, s0);
      SCHED();
#pragma unroll
      for (int i = 0; i < 4; ++i) { c1[i] = (yp[i][2] + yp[i][3]) * gav[1] + xc[i][1]; s1[i] = pk2(c1[i] * gcv[1]); }
      st_sc1(rws, so, 256, s1);
      SCHED();
#pragma unroll
      for (int i = 0; i < 4; ++i) { o[i >> 1][2 * (i & 1)] = pk(c0[i][0], c1[i][0]); o[i >> 1][2 * (i & 1) + 1] = pk(c0[i][1], c1[i][1]); }
      st_sc1(rws, xo0, 0, o[0]);
      st_sc1(rws, xo1, 0, o[1]);
    }
    STAMP(15);
    WAIT_VM(0);
    STAMP(17);
    st_sc1(rws, fo, 0, u32x4{ta, ta, tb, tb});
    __builtin_amdgcn_s_setprio(0);
    const int kind = helper ? 3 : lw;
    int e0 = WS_G2 + rbk * 1024, e1 = WS_S12 + (cbk * 128 + (kind - 1) * 64) * 16, e3 = WS_MF + 4096 + db * 512;
    asm volatile("" : "+s"(e0), "+s"(e1), "+s"(e3));
    const bool mid = (kind + 1) & 2;
    const int eo = (mid ? e1 : kind ? e3 : e0) + (lane & (kind == 3 ? 31 : 63)) * 16;
    LAS char* ea = smem + LD_MISC + (mid ? 256 + kind * 256 : kind * 512) + lane * (mid ? 4 : 8);
    u32x4 ev;
    if (helper) {
      BARRIER();
      ev = poll16(rws, eo, 0, ta, tb);
      *(LAS u32*)ea = ev[0]; *(LAS u32*)(ea + 4) = ev[2];
      BARRIER();
      __builtin_amdgcn_s_sleep(20);
      w2_stage(1); w2_stage(2); w2_stage(3);
      BARRIER(); BARRIER();
      w2_stage(0); w2_stage(4); w2_stage(5);
      wr_load(true);
    } else {
      __builtin_amdgcn_s_sleep(F2_DELAY);
      poll16(rws, ((lane >> 3) * 32 + lw * 8 + (lane & 7)) * 64, WS_F2 + (xcd >> 1) * 16384, ta, tb);
      STAMP(19);
      x_stage(0); x_stage(1); WAIT_VM(4); BARRIER();
      x_stage(2); x_stage(3); WAIT_VM(8); BARRIER();
      x_stage(4); w1_stage(4); WAIT_VM(12); BARRIER();
#pragma unroll 1
      for (int s = 5; s < 8; ++s) { x_stage(s); w1_stage(s); WAIT_VM(16); BARRIER(); }
      w2_stage(0); w2_stage(1); WAIT_VM(16); BARRIER();
      asm volatile("buffer_load_dwordx4 %0, %1, %2, 0 offen sc1" : "=v"(ev) : "v"(eo), "s"(qws) : "memory");
      w2_stage(2); w2_stage(3); WAIT_VM(17); BARRIER();
      asm volatile("s_waitcnt vmcnt(8)" : "+v"(ev) :: "memory");
      if (__any(tagdiff(ev, ta, tb) != 0u)) ev = poll16(rws, eo, 0, ta, tb);
      *(LAS u32*)ea = ev[0]; *(LAS u32*)(ea + (mid ? 512 : 4)) = ev[2];
      w2_stage(4); w2_stage(5); BARRIER();
    }
    STAMP(22);
    w2_stage(6); w2_stage(7); w2_stage(8);
    {
      const int xs = WS_X1 + tbk * ST_X1 + (db * 32 + (lw >> 1) * 16) * 128 + (lw & 1) * 64;
      dma16_sc1(qws, LD_MISC + 2048 + lw * 2048, (lane >> 2) * 128 + (lane & 3) * 16, xs);
      dma16_sc1(qws, LD_MISC + 3072 + lw * 2048, (lane >> 2) * 128 + (lane & 3) * 16, xs + ST_X1H);
    }
    WAIT_VM(0);
    STAMP(25);
    BARRIER();
    BARRIER();
#pragma unroll
    for (int i = 0; i < 20; ++i) {
      if (i == 8) BARRIER();
      *(LAS u32x4*)(smem + w2_unit(9 + i / 4) * UNIT + lw * 4096 + (i & 3) * 1024 + lane * 16) = wr[i];
    }
    BARRIER();
    if (b == 0 && tid == 256) *(u32x2*)(p.ws + WS_EPOCH) = u32x2{ta, tb};
    STAMP(26);
    STAMP_FLUSH(p.ws, 1);
    return;
  }

  ROLES();
  const int pch = w >> 1, pn = (w & 1) * 64 + lane, po = (d0 + pch) * NS + pn;
  u32 rar = LDNT(p.Are + po), rai = LDNT(p.Aim + po), rbr = LDNT(p.Bre + po), rbi = LDNT(p.Bim + po), rcr = LDNT(p.Cre + po), rci = LDNT(p.Cim + po);
  SCHED();
  SCAL_ISSUE(p.ldt + d0);
  {
    u32 cp0;
    {
      u64 pc; int r0, r1;
      asm volatile("s_getpc_b64 %0\n\ts_mov_b32 %1, fused_block@rel32@lo+4\n\ts_mov_b32 %2, fused_block_end@rel32@lo+12" : "=s"(pc), "=s"(r0), "=s"(r1));
      TOUCH(cp0, min(((w * 4 + (lane & 3)) * 32 + j) * 128, r1 - r0), pc + (long long)r0);
    }
    LAS char* flg = smem + LD_FLG;
    *(LAS u32x4*)(smem + LD_T + ((lane >> 1) & 7) * TCOPY + 4096 + (lane & 1) * 16) = u32x4{0u, 0u, 0u, 0u};
    *(LAS u32*)(smem + LD_KD + KD_ZERO * 4) = 0u;
    asm volatile("s_waitcnt lgkmcnt(0)" : "+s"(epoch), "+s"(aux));
    int ga[11], ka[4];
#pragma unroll
    for (int i = 0; i < 11; ++i) {
      const int l = 2047 - (tid * 8 + i);
      const int bits = (int)(pk((float)l, 0.f) & 0xffffu);
      const int idx = l < 128 ? l : bits - 0x4300 + 128;
      ga[i] = LD_KD + 4 * (l < 0 ? KD_ZERO : idx + (idx >> 3));
    }
    {
      const int e = lane & 15, g = lane >> 4;
      const u32 cfg = e < 8 ? (w == 0 ? 0x100004u : w == 1 ? 0x080004u : 0x022001u) : (w == 0 ? 0x100004u : w == 1 ? 0x044002u : 0x088003u);
      const int sh = cfg & 15, base = 256 * w + ((cfg >> 8) & 0xff) * (e >> sh) + (e & ((1 << sh) - 1));
#pragma unroll
      for (int r = 0; r < 4; ++r) { const int idx = base + (int)(cfg >> 16) * (4 * g + r); ka[r] = LD_KD + 4 * (idx + (idx >> 3)); PIN(ka[r]); }
    }
    STAMP(1);
    asm volatile("" : "+v"(rar), "+v"(rai), "+v"(rbr), "+v"(rbi), "+v"(rcr), "+v"(rci) : "v"(cp0));
    STAMP(2);
    SCHED();
    volatile LAS u32* P = (LAS u32*)(smem + LD_P) + pch * 64 * PROW + pn;
    const float dt = rb(__builtin_amdgcn_exp2f((pch ? bfhi(aux) : bflo(aux)) * LOG2E));
    const float a2 = (dt * bflo(rar)) * LOG2E, bq = (dt * bflo(rai)) * 0.15915494309189535f;
    const f32x2 z1 = cpow(a2, bq, 1.f), z8 = cpow(a2, bq, 8.f), z256 = cpow(a2, bq, 256.f);
    const f32x2 z16c = cmul(z8, z8) * f32x2{1.f, -1.f}, z512 = cmul(z256, z256), z768 = cmul(z512, z256);
    f32x2 u = cpow(a2, bq, 1024.f), q = {1.f, 0.f};
    f32x2 r = cmul(f32x2{bflo(rcr), -bflo(rci)}, f32x2{bflo(rbr), -bflo(rbi)} * dt);
#pragma unroll
    for (int e = 0; e < 16; ++e) {
      const f32x2 rn = cmul(r, z16c), qn = cmul(q, z1);
      P[e * PROW] = pk2(r);
      P[(16 + e) * PROW] = pk2(q);
      if (!(e & 1)) P[(32 + e / 2) * PROW] = pk2(cmul(q, z256));
      if (!(e & 3)) { P[(40 + e / 4) * PROW] = pk2(cmul(q, z512)); P[(44 + e / 4) * PROW] = pk2(cmul(q, z768)); }
      if ((e & 3) == 1) { P[(48 + e / 2) * PROW] = pk2(u); P[(49 + e / 2) * PROW] = pk2(cmul(u, z8)); }
      if ((e & 3) == 3) u = cmul(u, z256);
      r = rn; q = qn;
    }
    P[56 * PROW] = pk2(u);
    STAMP(3);
    quad_arrive(flg, w, 1u);
#pragma unroll
    for (int i = 0; i < 11; ++i) PIN(ga[i]);
    quad_wait(flg, 1u);
    STAMP(4);
    if (w < 3) {
      LAS const u32* fb = (LAS const u32*)(smem + LD_P) + (lane & 15) * PROW + ((lane >> 4) & 1) * 64 + (lane >> 5) * 32;
      u32x4 fa[16], fc[16];
      f32x4 acc[2] = {{0.f, 0.f, 0.f, 0.f}, {0.f, 0.f, 0.f, 0.f}};
#pragma unroll
      for (int ks = 0; ks < 22; ++ks) {
        if (ks < 16) {
          const int o = (ks & 1) * 64 * PROW + (ks >> 1) * 4;
          fa[ks] = *(LAS const u32x4*)(fb + o); fc[ks] = *(LAS const u32x4*)(fb + (w + 1) * 16 * PROW + o);
        }
        SCHED();
        if (ks >= 6) acc[ks & 1] = mfma16(fa[ks - 6], fc[ks - 6], acc[ks & 1]);
        SCHED();
      }
#pragma unroll
      for (int r4 = 0; r4 < 4; ++r4) *(LAS u32*)(smem + ka[r4]) = pk(acc[0][r4], acc[1][r4]);
    }
    STAMP(5);
    quad_arrive(flg, w, 2u);
    quad_wait(flg, 2u);
    STAMP(6);
    u32 e[11];
#pragma unroll
    for (int i = 0; i < 11; ++i) e[i] = *(LAS const u32*)(smem + ga[i]);
#pragma unroll
    for (int c = 0; c < 2; ++c)
#pragma unroll
      for (int s = 0; s < 4; ++s) {
        u32x4 o;
#pragma unroll
        for (int z = 0; z < 4; ++z) o[z] = __builtin_amdgcn_perm(e[s + 2 * z + 1], e[s + 2 * z], c ? 0x07060302u : 0x05040100u);
        *(LAS u32x4*)(smem + LD_T + (c * 4 + s) * TCOPY + tid * 16) = o;
      }
    quad_arrive(flg, w, 3u);
  }
  epoch += 0x9E3779B97F4A7C15ull;
  const u32 ta = (u32)epoch, tb = (u32)(epoch >> 32);
  {
    const int r = lane & 15, g = lane >> 4, ch = w >> 1, half = w & 1;
    LAS const char* Tb = smem + LD_T + (ch * 4 + ((3 - r) & 3)) * TCOPY + 2 * (2028 - 4 * (r >> 2) + 8 * g - 128 * half) - 7 * 512 - 192;
    LAS const char* Ub = smem + LD_U + ch * 4608 + 2 * (256 + 16 * r - 16 + 8 * g - 128 * half) - 192;
    LAS f32x4* red = (LAS f32x4*)(smem + (ch ? LD_RED1 : LD_RED0)) + half * 512 + (lane ^ ((lane >> 4) << 2));
    PIN(Ub); PIN(red);
    f32x4 acc[8];
#pragma unroll
    for (int n = 0; n < 8; ++n) acc[n] = f32x4{0.f, 0.f, 0.f, 0.f};
    u32x4 uf[2][8], tf[4][8];
    quad_wait(smem + LD_FLG, 3u);
#define LD_TH(e, a, hh) do { const u32x2 l_ = *(volatile LAS const u32x2*)(Tb + 64 * (3 - (e)) + 512 * (7 - (a)) + 8 * (hh)); tf[e][a][2 * (hh)] = l_[0]; tf[e][a][2 * (hh) + 1] = l_[1]; } while (0)
#pragma unroll
    for (int e = 0; e < TF_PRE; ++e)
#pragma unroll
      for (int a = 0; a < 8; ++a) { LD_TH(e, a, 0); LD_TH(e, a, 1); }
#define LD_UF(n, e) uf[(e) & 1][n] = *(LAS const u32x4*)(Ub + 64 * (3 - (e)) + 512 * (n))
#define CM(e, a, m) do { SCHED(); acc[(a) + (m)] = mfma16(tf[e][a], uf[(e) & 1][m], acc[(a) + (m)]); SCHED(); } while (0)
    STAMP(10);
    u32 ft;
    quad_wait(smem + LD_FLG + 16, FT_AT);
    __builtin_amdgcn_s_sleep(FT_DELAY);
    TOUCH(ft, ((b * 2 + (w & 1)) * 28 + min(lane, 27)) * 128, w < 2 ? p.w1 : p.w2);
    BARRIER();
    STAMP(11);
#pragma unroll
    for (int n = 0; n < 8; ++n) LD_UF(n, 0);
#pragma unroll
    for (int e = 0; e < 3; ++e) {
      int cnt = 0;
#pragma unroll
      for (int m = 0; m < 8; ++m)
#pragma unroll
        for (int a = 0; a + m < 8; ++a) {
          CM(e, a, m);
          if (cnt >= 4 && cnt < 12) LD_UF(cnt - 4, e + 1);
          if (cnt >= 12 && cnt < 28 && e + TF_PRE < 4) LD_TH(e + TF_PRE, (cnt - 12) >> 1, (cnt - 12) & 1);
          ++cnt;
        }
    }
#pragma unroll
    for (int n = 0; n < 8; ++n)
#pragma unroll
      for (int a = 0; a <= n; ++a) { CM(3, a, n - a); if (a == 1) red[(n - 1) * 64] = acc[n - 1]; }
    STAMP(12);
    red[7 * 64] = acc[7];
    BARRIER();
    STAMP(14);
    WAIT_VM(0);
    asm volatile("" :: "v"(ft));
  }
  if (helper) {
    const int hb = xcd * 4 + j;
    u32x4 v[16];
    const int rg = tid >> 3;
    LAS float* rd = (LAS float*)(smem + 4096);
    { const u32x4 m = poll16(rws, tid * 16, WS_MF, ta, tb); *(LAS u32x2*)(smem + tid * 8) = u32x2{m[0], m[2]}; }
    BARRIER();
#pragma unroll
    for (int i = 0; i < 16; ++i) v[i] = *(LAS const u32x4*)(smem + LD_WR + i * 4096 + tid * 16);
    float a[16];
#pragma unroll
    for (int e = 0; e < 16; ++e) a[e] = 0.f;
#pragma unroll
    for (int i = 0; i < 16; ++i) {
      const u32 gs = *(LAS const u32*)(smem + (i * 32 + rg) * 4);
      const u32 gl = gs & 0xffffu, gh = gs << 16, sl = gs >> 16, sh = gs & 0xffff0000u;
#pragma unroll
      for (int z = 0; z < 4; ++z) {
        a[4 * z] = dot2(v[i][z], gl, a[4 * z]); a[4 * z + 1] = dot2(v[i][z], sl, a[4 * z + 1]);
        a[4 * z + 2] = dot2(v[i][z], gh, a[4 * z + 2]); a[4 * z + 3] = dot2(v[i][z], sh, a[4 * z + 3]);
      }
    }
    fold_cols(a, rd + w * 128, lane);
    STAMP(18);
    poll16(rws, tid * 64, WS_F2 + (xcd >> 1) * 16384, ta, tb);
    BARRIER();
    STAMP(19);
#pragma unroll
    for (int i = 0; i < 16; ++i) v[i] = ld_sc1(rws, tid * 16, WS_X1 + hb * ST_X1 + (i >> 3) * ST_X1H + (i & 7) * 4096);
    if (w == 0 && hb < 28) {
      const f32x2 s = sum4(rd + lane * 2, 128);
      st_sc1(rws, lane * 16, WS_S12 + hb * 1024, u32x4{__float_as_uint(s[0]), ta, __float_as_uint(s[1] * -LOG2E), tb});
    }
#pragma unroll
    for (int e = 0; e < 16; ++e) a[e] = 0.f;
#pragma unroll
    for (int i = 0; i < 16; ++i)
#pragma unroll
      for (int z = 0; z < 4; ++z) {
        const int o = (i >> 3) * 8 + 2 * z;
        a[o] = dot2(v[i][z], 0x3f803f80u, a[o]); a[o + 1] = dot2(v[i][z], v[i][z], a[o + 1]);
      }
    STAMP(20);
    fold_cols(a, rd + 512 + w * 128, lane);
    BARRIER();
    if (w == 0) {
      const f32x2 s = sum4(rd + 512 + lane * 2, 128);
      const u32 st = ln_stat(s[0], s[1]);
      const u32 st2 = __builtin_amdgcn_mov_dpp(st, 0xB1, 0xf, 0xf, true);
      if (!(lane & 1)) st_sc1(rws, lane * 8, WS_G2 + hb * 512, u32x4{st, ta, st2, tb});
    }
    BARRIER();
    STAMP(22);
    __builtin_amdgcn_s_sleep(H_SLEEP);
  } else {
    const int wt = w & 1, wf = w >> 1;
    const int h = lane >> 5, blk = (lane >> 4) & 1, q = (lane & 15) >> 2, pp = lane & 3;
    int ax[2][2], aw[2][2];
#pragma unroll
    for (int c = 0; c < 2; ++c)
#pragma unroll
      for (int t = 0; t < 2; ++t) {
        const int base = 256 * (8 * h + 4 * t + q) + 16 * ((2 * blk + (pp >> 1)) ^ (2 * h + t)) + 8 * (pp & 1);
        ax[c][t] = LD_XR + base + 64 * ((2 * wt + c) ^ q);
        aw[c][t] = LD_WR + base + 64 * ((2 * wf + c) ^ q);
        PIN(ax[c][t]); PIN(aw[c][t]);
      }
    f32x16 acc[2][2];
#pragma unroll
    for (int a = 0; a < 2; ++a)
#pragma unroll
      for (int c = 0; c < 2; ++c)
#pragma unroll
        for (int r = 0; r < 16; ++r) acc[a][c][r] = 0.f;
    u32x4 xf[2][2], wfr[2][2];
    u32 st[2];
    int ho = WS_H + ((rbk * 4 + wt * 2) * 112 + cbk * 8 + wf * 4) * 1024 + lane * 16, ho1 = ho + 112 * 1024;
    int fo = only(lane < 8, WS_F3 + ((rbk * 4 + w) * 16 + cbk) * 128 + lane * 16);
    PIN(ho); PIN(ho1); PIN(fo);
#define RD_X(i, c) xf[(i) & 1][c] = tr_frag(smem + ax[c][0] + ((i) & 15) * 4096, smem + ax[c][1] + ((i) & 15) * 4096)
#define RD_W(i, c) wfr[(i) & 1][c] = tr_frag(smem + aw[c][0] + ((i) & 15) * 4096, smem + aw[c][1] + ((i) & 15) * 4096)
#define MM(i, a, c) do { SCHED(); acc[a][c] = mfma32(wfr[(i) & 1][a], xf[(i) & 1][c], acc[a][c]); SCHED(); } while (0)
    BARRIER();
    STAMP(19);
    RD_W(0, 0); RD_X(0, 0); RD_X(0, 1); RD_W(0, 1);
#pragma unroll
    for (int i = 0; i < 32; ++i) {
      if ((i & 3) != 3) {
        MM(i, 0, 0); RD_W(i + 1, 0); MM(i, 0, 1); RD_X(i + 1, 0); MM(i, 1, 0); RD_X(i + 1, 1); MM(i, 1, 1); RD_W(i + 1, 1);
      } else {
        MM(i, 0, 0); MM(i, 0, 1);
        BARRIER();
        if (i == 31) { st[0] = *(LAS const u32*)(smem + LD_MISC + (wt * 64 + (lane & 31)) * 4); st[1] = *(LAS const u32*)(smem + LD_MISC + (wt * 64 + 32 + (lane & 31)) * 4); }
        MM(i, 1, 0);
        if (i < 31) { RD_W(i + 1, 0); RD_X(i + 1, 0); }
        MM(i, 1, 1);
        if (i < 31) { RD_X(i + 1, 1); RD_W(i + 1, 1); }
      }
    }
    STAMP(21);
    float rstd[2], mr[2];
#pragma unroll
    for (int c = 0; c < 2; ++c) {
      rstd[c] = __builtin_amdgcn_rcpf(bfhi(st[c])) * -LOG2E;
      mr[c] = -bflo(st[c]) * rstd[c];
    }
    STAMP(22);
    LAS const float* s12 = (LAS const float*)(smem + LD_MISC + 512) + wf * 64 + 4 * h;
#pragma unroll
    for (int a = 0; a < 2; ++a)
#pragma unroll
      for (int sp = 0; sp < 2; ++sp) {
        f32x4 s1[2], s2[2];
#pragma unroll
        for (int z = 0; z < 2; ++z) { s1[z] = *(LAS const f32x4*)(s12 + a * 32 + sp * 16 + z * 8); s2[z] = *(LAS const f32x4*)(s12 + 128 + a * 32 + sp * 16 + z * 8); }
#pragma unroll
        for (int c = 0; c < 2; ++c) {
          u32x4 o;
#pragma unroll
          for (int e = 0; e < 4; ++e) {
            const int z = e >> 1, k = (e & 1) * 2;
            const f32x2 av = {acc[a][c][sp * 8 + 2 * e], acc[a][c][sp * 8 + 2 * e + 1]};
            const f32x2 cc = f32x2{s1[z][k], s1[z][k + 1]} * mr[c] + f32x2{s2[z][k], s2[z][k + 1]};
            const f32x2 ar = av * rstd[c] + cc;
            const f32x2 dn = f32x2{__builtin_amdgcn_exp2f(ar[0]), __builtin_amdgcn_exp2f(ar[1])} + 1.f;
            o[e] = pk2(ar * f32x2{__builtin_amdgcn_rcpf(dn[0]), __builtin_amdgcn_rcpf(dn[1])});
          }
          st_sc1(rws, c ? ho1 : ho, (a * 2 + sp) * 1024, o);
        }
      }
    STAMP(23);
    WAIT_VM(0);
    STAMP(24);
    st_sc1(rws, fo, 0, u32x4{ta, ta, tb, tb});
  }

  {
    const int wt = w & 1, kh = w >> 1, h = lane >> 5, tl = lane & 31;
    const int blk = (lane >> 4) & 1, q = (lane & 15) >> 2, pp = lane & 3;
    LAS char* aw[3][2];
#pragma unroll
    for (int c = 0; c < 6; ++c) {
      aw[c >> 1][c & 1] = smem + (c >> 1) * 65536 + kh * 8192 + 128 * (4 * h + q) + 64 * ((c & 1) ^ kh ^ (q >> 1)) + 32 * blk + 8 * pp;
      asm volatile("" : "+v"(aw[c >> 1][c & 1]));
    }
    const rsrc_t ro = __builtin_amdgcn_make_buffer_rsrc(p.out, 0, L * D * 2, 0x00020000);
    int oo[2];
#pragma unroll
    for (int i = 0; i < 2; ++i) {
      const int r = tbk * 64 + (tid >> 3) + 32 * i;
      oo[i] = ((r & ~511) + 128 * ((r >> 1) & 3) + 2 * ((r >> 3) & 63) + (r & 1)) * (D * 2) + db * 128 + (tid & 7) * 16;
      PIN(oo[i]);
    }
    f32x16 acc[2];
#pragma unroll
    for (int r = 0; r < 16; ++r) { acc[0][r] = 0.f; acc[1][r] = 0.f; }
    STAMP(25);
    BARRIER();
    constexpr int NW = G2_NW;
    u32x4 hf[NW], wf[3][2];
#define RD_W2C(i, c) do { LAS char* a_ = aw[w2_unit((i) >> 2) >> 2][c] + (w2_unit((i) >> 2) & 3) * UNIT + ((i) & 3) * 2048; wf[(i) % 3][c] = tr_frag(a_, a_ + 1024); } while (0)
#define RD_W2(i) do { RD_W2C(i, 0); RD_W2C(i, 1); } while (0)
    RD_W2(0); RD_W2(1);
    const int sh = WS_H + ((tbk * 2 + wt) * 112 + 56 * kh) * 1024;
#define LD_H(i) hf[(i) % NW] = ld_sc1(rws, lane * 16 + ((i) & 3) * 1024, sh + w2_phys((i) >> 2) * 4096)
    if (F3_DELAY) __builtin_amdgcn_s_sleep(F3_DELAY);
    poll16(rws, ((lane & 1) * 32 + min(lane >> 1, 6)) * 128, WS_F3 + ((tbk >> 1) * 4 + (tbk & 1)) * 2048 + kh * 896, ta, tb);
    STAMP(26);
#pragma unroll
    for (int i = 0; i < G2_N0; ++i) LD_H(i);
    SCHED();
    u32 xr[8]; f32x2 gb[8];
#pragma unroll
    for (int i = 0; i < 8; ++i) {
      const int pr = 4 * (i >> 1) + 2 * h + (i & 1);
      xr[i] = *(LAS const u32*)(smem + LD_MISC + 2048 + w * 2048 + ((tl >> 2) & 1) * 1024 + pr * 64 + (tl >> 3) * 16 + (tl & 3) * 4);
      gb[i] = *(LAS const f32x2*)(smem + LD_MISC + 1536 + (kh * 16 + pr) * 8);
    }
    SCHED();
#pragma unroll
    for (int i = 0; i < 56; ++i) {
      SCHED();
      acc[0] = mfma32(wf[i % 3][0], hf[i % NW], acc[0]);
      SCHED();
      if (i == 0) STAMP(29);
      if (i == 18) STAMP(30);
      if (i == 42) STAMP(31);
      if (i == 6 || i == 18 || i == 42) BARRIER();
      if (i + 2 < 56) RD_W2C(i + 2, 0);
      if (h_next(i) < h_next(i + 1)) LD_H(h_next(i));
      SCHED();
      acc[1] = mfma32(wf[i % 3][1], hf[i % NW], acc[1]);
      SCHED();
      if (i + 2 < 56) RD_W2C(i + 2, 1);
      if (h_next(i) + 1 < h_next(i + 1)) LD_H(h_next(i) + 1);
      asm volatile("" :: "v"(wf[i % 3][0]), "v"(wf[i % 3][1]), "v"(hf[i % NW]));
    }
    STAMP(27);
    LAS f32x4* ex = (LAS f32x4*)(smem + w2_unit(5) * UNIT) + lane;
#pragma unroll
    for (int qq = 0; qq < 4; ++qq) ex[(w * 4 + qq) * 64] = f32x4{acc[1][4 * qq], acc[1][4 * qq + 1], acc[1][4 * qq + 2], acc[1][4 * qq + 3]};
    BARRIER();
    LAS char* ot = smem + w2_unit(6) * UNIT;
    f32x4 oth[4];
#pragma unroll
    for (int qq = 0; qq < 4; ++qq) oth[qq] = ex[((w ^ 2) * 4 + qq) * 64];
    SCHED();
#pragma unroll
    for (int qq = 0; qq < 4; ++qq) {
      const f32x4 o4 = oth[qq];
      u32x2 o;
#pragma unroll
      for (int z = 0; z < 2; ++z)
        o[z] = pk2((f32x2{acc[0][4 * qq + 2 * z], acc[0][4 * qq + 2 * z + 1]} + f32x2{o4[2 * z], o4[2 * z + 1]}) * gb[2 * qq + z] + up(xr[2 * qq + z]));
      *(LAS u32x2*)(ot + (wt * 32 + tl) * 144 + kh * 64 + qq * 16 + h * 8) = o;
    }
    BARRIER();
#pragma unroll
    for (int i = 0; i < 2; ++i) {
      const int row = (tid >> 3) + 32 * i;
      __builtin_amdgcn_raw_buffer_store_b128(*(LAS const u32x4*)(ot + row * 144 + (tid & 7) * 16), ro, oo[i], 0, OUT_AUX);
    }
    STAMP(28);
    STAMP_FLUSH(p.ws, 0);
  }
}

extern "C" __global__ void fused_block_end() {}

extern "C" void kernel_launch(void* const* d_in, const int* in_sizes, int n_in, void* d_out, int out_size, void* d_ws, size_t ws_size,
                              hipStream_t stream) {
  Params p;
  p.x = (const u16*)d_in[0]; p.Are = (const u16*)d_in[1]; p.Aim = (const u16*)d_in[2]; p.Bre = (const u16*)d_in[3];
  p.Bim = (const u16*)d_in[4]; p.Cre = (const u16*)d_in[5]; p.Cim = (const u16*)d_in[6]; p.ldt = (const u16*)d_in[7];
  p.dskip = (const u16*)d_in[8]; p.w1 = (const u16*)d_in[10]; p.w2 = (const u16*)d_in[11]; p.c = (const u16*)d_in[12];
  p.wmod = (const u16*)d_in[13]; p.out = (u16*)d_out; p.ws = (char*)d_ws;
  (void)hipFuncSetAttribute((const void*)fused_block, hipFuncAttributeMaxDynamicSharedMemorySize, LDS_BYTES + ST_LDS_EXTRA);
  fused_block<<<256, 512, LDS_BYTES + ST_LDS_EXTRA, stream>>>(p);
}
```

```cpp
#define XG_DELAY 9
#define OUT_AUX 16
#include <hip/hip_runtime.h>
#include <hip/hip_bf16.h>

#define STAMP_DECL
#define STAMP(s) __builtin_amdgcn_sched_barrier(0)
#define STAMP_FLUSH(ws, k)
#define ST_LDS_EXTRA 0
#define ST_RANGES {{0, 32, 256, "compute waves"}, {1, 32, 256, "loader waves"}, {0, 0, 32, "helper WGs, waves 0-3"}, {1, 0, 32, "helper WGs, waves 4-7"}}

typedef unsigned short u16;
typedef unsigned int u32;
typedef unsigned long long u64;
typedef u32 u32x2 __attribute__((ext_vector_type(2)));
typedef u32 u32x4 __attribute__((ext_vector_type(4)));
typedef float f32x2 __attribute__((ext_vector_type(2)));
typedef float f32x4 __attribute__((ext_vector_type(4)));
typedef float f32x16 __attribute__((ext_vector_type(16)));
typedef short s16x4 __attribute__((ext_vector_type(4)));
typedef __bf16 bf16x2 __attribute__((ext_vector_type(2)));
typedef __bf16 bf16x8 __attribute__((ext_vector_type(8)));
typedef __amdgpu_buffer_rsrc_t rsrc_t;
#define GAS __attribute__((address_space(1)))
#define LAS __attribute__((address_space(3)))
#define CAS __attribute__((address_space(4)))

constexpr int L = 2048, D = 512, NS = 128, F = 1792;

constexpr int WS_EPOCH = 0;
constexpr int WS_SG = 0x1000;
constexpr int WS_MG = 0x80000;
constexpr int WS_F2 = 0x40000;
constexpr int WS_F3 = 1 << 20;
constexpr int WS_G2 = 0x70000;
constexpr int WS_S12 = 0x74000;
constexpr int WS_MF = 0x7c000;
constexpr int WS_XG = 2 << 20;
constexpr int WS_X1 = 0x440000;
constexpr int WS_X1S = 0x680000;
constexpr int WS_H = 9 << 20;
constexpr int WS_END = 16 << 20;
#define PADS 1
constexpr int ST_XG = 8192 + 128 * PADS, ST_MG = 1536 * 16 + 128 * PADS, ST_X1H = 32768 + 128 * PADS, ST_X1 = 2 * ST_X1H, ST_X1S = 131072 + 384 * PADS;
constexpr int OOB = 0x7ff00000;

constexpr int UNIT = 16384;
constexpr int LD_XR = 0, LD_WR = 4 * UNIT, LD_MISC = 9 * UNIT, LD_FLG = LD_MISC + 10240, LD_MV = LD_FLG + 32, LDS_BYTES = LD_MV + 64;
constexpr int TCOPY = 4160, PROW = 132;
constexpr int LD_T = 0, LD_U = 8 * TCOPY, LD_KD = LD_U + 2 * 4608, LD_P = LD_WR, LD_RED0 = 3 * UNIT, LD_RED1 = 8 * UNIT;
constexpr int KD_ZERO = 900, LD_XT = LD_RED0;
static_assert(LD_KD + 904 * 4 <= LD_RED0, "LDS map");
__host__ __device__ constexpr int w2_row(int u) { return 128 * (u % 7) + 32 * (u / 7); }
__host__ __device__ constexpr int w2_unit(int u) { return u % 9 == 8 ? 8 : (u % 9) / 2 + 4 * (u % 9 & 1); }
#define G2_NW 14
#define G2_N0 14
__host__ __device__ constexpr int h_next(int i) { int a = G2_N0 + 2 * i, b = i > 0 ? i - 1 + G2_NW : G2_NW; a = a < b ? a : b; return a < 56 ? a : 56; }
#define TF_PRE 4
#define F3_DELAY 6
#define P2 5
#define F1_AT 8
#define G_SG 6
#define G_EARLY 1
#define G_SIG 5
#define FT_AT 3
#define FT_DELAY 0
#define H_SLEEP 96
#define S1_SLEEP 2
#define F2_DELAY 20
#define POLL_SLEEP 2
#define LDNT(p) __builtin_nontemporal_load(p)
#define AUX_NT 2
#define WF ""
#define AUX_W 0
constexpr int SPIN_CAP = 1 << 15;
constexpr float LOG2E = 1.44269504088896341f;

__device__ __forceinline__ float bflo(u32 v) { return __uint_as_float(v << 16); }
__device__ __forceinline__ float bfhi(u32 v) { return __uint_as_float(v & 0xffff0000u); }
__device__ __forceinline__ u32 pk(float lo, float hi) {
  f32x2 v = {lo, hi};
  bf16x2 r = __builtin_convertvector(v, bf16x2);
  return __builtin_bit_cast(u32, r);
}
__device__ __forceinline__ float rb(float f) { return bflo(pk(f, f)); }
__device__ __forceinline__ float dot2(u32 a, u32 b, float c) {
  return __builtin_amdgcn_fdot2_f32_bf16(__builtin_bit_cast(bf16x2, a), __builtin_bit_cast(bf16x2, b), c, false);
}
__device__ __forceinline__ f32x4 mfma16(u32x4 a, u32x4 b, f32x4 c) {
  return __builtin_amdgcn_mfma_f32_16x16x32_bf16(__builtin_bit_cast(bf16x8, a), __builtin_bit_cast(bf16x8, b), c, 0, 0, 0);
}
__device__ __forceinline__ f32x16 mfma32(u32x4 a, u32x4 b, f32x16 c) {
  return __builtin_amdgcn_mfma_f32_32x32x16_bf16(__builtin_bit_cast(bf16x8, a), __builtin_bit_cast(bf16x8, b), c, 0, 0, 0);
}
template <int CTRL> __device__ __forceinline__ float dpp(float x) {
  return __builtin_bit_cast(float, __builtin_amdgcn_mov_dpp(__builtin_bit_cast(int, x), CTRL, 0xf, 0xf, true));
}
__device__ __forceinline__ float row_sum(float x) {
  x += dpp<0xB1>(x); x += dpp<0x4E>(x); x += dpp<0x124>(x); x += dpp<0x128>(x);
  return x;
}
__device__ __forceinline__ float fold32(float a, float b) {
  auto s = __builtin_amdgcn_permlane32_swap(__float_as_uint(a), __float_as_uint(b), false, false);
  return __uint_as_float(s[0]) + __uint_as_float(s[1]);
}
__device__ __forceinline__ float fold16(float a, float b) {
  auto s = __builtin_amdgcn_permlane16_swap(__float_as_uint(a), __float_as_uint(b), false, false);
  return __uint_as_float(s[0]) + __uint_as_float(s[1]);
}
__device__ __forceinline__ u32x4 mkrs(const void* p, u32 bytes) {
  const u64 a = (u64)p;
  return u32x4{(u32)a, (u32)(a >> 32) & 0xffffu, bytes, 0x00020000u};
}
__device__ __forceinline__ void dma4(u32x4 rs, int lds, int voff, int soff) {
  asm volatile("s_mov_b32 m0, %0\n\ts_nop 0\n\tbuffer_load_dword %1, %2, %3 offen lds" :: "s"(lds), "v"(voff), "s"(rs), "s"(soff) : "memory");
}
__device__ __forceinline__ void dma16_sc1(u32x4 rs, int lds, int voff, int soff) {
  asm volatile("s_mov_b32 m0, %0\n\ts_nop 0\n\tbuffer_load_dwordx4 %1, %2, %3 offen sc1 lds" :: "s"(lds), "v"(voff), "s"(rs), "s"(soff) : "memory");
}
#define DMA4_TEXT(F) "s_mov_b32 m0, %0\n\ts_nop 0\n\tbuffer_load_dwordx4 %1, %5, %6 offen" F " lds\n\tbuffer_load_dwordx4 %2, %5, %6 offen offset:1024" F " lds\n\t" \
                     "buffer_load_dwordx4 %3, %5, %6 offen offset:2048" F " lds\n\tbuffer_load_dwordx4 %4, %5, %6 offen offset:3072" F " lds"
__device__ __forceinline__ void dma4k(u32x4 rs, int lds, const int (&v)[4], int soff) {
  asm volatile(DMA4_TEXT(WF) :: "s"(lds), "v"(v[0]), "v"(v[1]), "v"(v[2]), "v"(v[3]), "s"(rs), "s"(soff) : "memory");
}
__device__ __forceinline__ void dma4k_sc1(u32x4 rs, int lds, const int (&v)[4], int soff) {
  asm volatile(DMA4_TEXT(" sc1") :: "s"(lds), "v"(v[0]), "v"(v[1]), "v"(v[2]), "v"(v[3]), "s"(rs), "s"(soff) : "memory");
}
__device__ __forceinline__ u32x4 ld_sc1(rsrc_t rs, int voff, int soff) { return __builtin_amdgcn_raw_buffer_load_b128(rs, voff, soff, 16); }
__device__ __forceinline__ void st_sc1(rsrc_t rs, int voff, int soff, u32x4 v) { __builtin_amdgcn_raw_buffer_store_b128(v, rs, voff + soff, 0, 16); }
__device__ __forceinline__ u32x4 tr_frag(LAS char* a0, LAS char* a1) {
  const s16x4 lo = __builtin_amdgcn_ds_read_tr16_b64_v4i16((LAS s16x4*)a0), hi = __builtin_amdgcn_ds_read_tr16_b64_v4i16((LAS s16x4*)a1);
  const u32x2 l = __builtin_bit_cast(u32x2, lo), h = __builtin_bit_cast(u32x2, hi);
  return u32x4{l[0], l[1], h[0], h[1]};
}
#define SCHED() __builtin_amdgcn_sched_barrier(0)
#define FENCE() asm volatile("" ::: "memory")
#define WAIT_VM(n) asm volatile("s_waitcnt vmcnt(" #n ")" ::: "memory")
#define BARRIER() do { asm volatile("s_waitcnt lgkmcnt(0)" ::: "memory"); __builtin_amdgcn_s_barrier(); FENCE(); } while (0)

__device__ __forceinline__ f32x2 cmul(f32x2 a, f32x2 b) {
  const f32x2 t = f32x2{a[0], a[0]} * b;
  return __builtin_elementwise_fma(f32x2{a[1], a[1]}, f32x2{-b[1], b[0]}, t);
}
__device__ __forceinline__ f32x2 cpow(float a2, float bq, float l) {
  const float m = __builtin_amdgcn_exp2f(a2 * l);
  const float ph = __builtin_amdgcn_fractf(bq * l);
  return f32x2{__builtin_amdgcn_cosf(ph), __builtin_amdgcn_sinf(ph)} * m;
}
__device__ __forceinline__ u32 pk2(f32x2 a) { return pk(a[0], a[1]); }
__device__ __forceinline__ f32x2 up(u32 v) { return f32x2{bflo(v), bfhi(v)}; }
__device__ __forceinline__ u32 ln_stat(float sum, float sq) {
  const float m = sum * (1.f / D);
  const float var = fmaxf(sq * (1.f / D) - m * m, 0.f);
  return pk(m, __builtin_amdgcn_sqrtf(rb(rb(var) + 1e-5f)));
}
__device__ __forceinline__ u32 tagdiff(u32x4 v, u32 ta, u32 tb) { return (v[1] ^ ta) | (v[3] ^ tb); }
__device__ __forceinline__ u32x4 poll16(rsrc_t rs, int voff, int soff, u32 ta, u32 tb) {
  u32x4 v;
  int spins = 0;
  for (;;) {
    FENCE();
    v = ld_sc1(rs, voff, soff);
    if (tagdiff(v, ta, tb) == 0u || ++spins >= SPIN_CAP) break;
    if (POLL_SLEEP) __builtin_amdgcn_s_sleep(POLL_SLEEP);
  }
  FENCE();
  return v;
}
__device__ __forceinline__ bool bad4(const u32x4 (&v)[4], u32 ta, u32 tb) {
  return (tagdiff(v[0], ta, tb) | tagdiff(v[1], ta, tb) | tagdiff(v[2], ta, tb) | tagdiff(v[3], ta, tb)) != 0u;
}
__device__ __forceinline__ void retry4(u32x4 (&v)[4], rsrc_t rs, int voff, int step, u32 ta, u32 tb) {
#pragma unroll 1
  for (int spins = 0; spins < SPIN_CAP; ++spins) {
    __builtin_amdgcn_s_sleep(S1_SLEEP);
    FENCE();
#pragma unroll
    for (int i = 0; i < 4; ++i) if (tagdiff(v[i], ta, tb)) v[i] = ld_sc1(rs, voff, i * step);
    if (!__any(bad4(v, ta, tb))) break;
  }
}
__device__ __forceinline__ void fold_cols(const float (&v)[16], LAS float* dst, int lane) {
  float u[8]; f32x4 t;
#pragma unroll
  for (int k = 0; k < 8; ++k) u[k] = fold32(v[k], v[k + 8]);
#pragma unroll
  for (int k = 0; k < 4; ++k) { const float a = fold16(u[k], u[k + 4]); t[k] = a + dpp<0x128>(a); }
  if (!(lane & 8)) *(LAS f32x4*)(dst + (lane & 7) * 16 + (lane >> 4) * 4) = t;
}
__device__ __forceinline__ f32x2 sum4(LAS const float* src, int stride) {
  return (*(LAS const f32x2*)src + *(LAS const f32x2*)(src + stride)) + (*(LAS const f32x2*)(src + 2 * stride) + *(LAS const f32x2*)(src + 3 * stride));
}
__device__ __forceinline__ u32 silu_bf(u32 c) {
  const float f = bflo(c), s = f * __builtin_amdgcn_rcpf(1.f + __builtin_amdgcn_exp2f(f * -LOG2E));
  return pk(s, s);
}
template <int CTRL> __device__ __forceinline__ void gv_row(u32x4 v, u32 sp, float (&acc)[8]) {
  const u32 sb = __builtin_amdgcn_mov_dpp(sp, CTRL, 0xf, 0xf, true), al = sb & 0xffffu, ah = sb & 0xffff0000u;
#pragma unroll
  for (int q = 0; q < 4; ++q) { acc[2 * q] = dot2(v[q], al, acc[2 * q]); acc[2 * q + 1] = dot2(v[q], ah, acc[2 * q + 1]); }
}
__device__ __forceinline__ f32x2 gv_fold(const float (&acc)[8]) {
  const float m0 = fold16(fold32(acc[0], acc[4]), fold32(acc[2], acc[6])), m1 = fold16(fold32(acc[1], acc[5]), fold32(acc[3], acc[7]));
  return f32x2{m0 + dpp<0x128>(m0), m1 + dpp<0x128>(m1)};
}
#define TOUCH(dst, voff, base) asm volatile("global_load_dword %0, %1, %2" : "=v"(dst) : "v"(voff), "s"(base))

__device__ __forceinline__ void quad_arrive(LAS char* f, int i, u32 n) {
  asm volatile("s_waitcnt lgkmcnt(0)" ::: "memory");
  *(volatile LAS u32*)(f + i * 4) = n;
}
__device__ __forceinline__ void quad_wait(LAS char* f, u32 n) {
  for (;;) {
    const u32x4 v = *(volatile LAS u32x4*)f;
    if (__builtin_amdgcn_readfirstlane(min(min(v[0], v[1]), min(v[2], v[3]))) >= n) break;
    __builtin_amdgcn_s_sleep(1);
  }
  FENCE();
}
__device__ __forceinline__ int only(bool c, int off) { return off | (c ? 0 : OOB); }
#define PIN(x) asm volatile("" : "+v"(x))

struct Params {
  const u16 *x, *Are, *Aim, *Bre, *Bim, *Cre, *Cim, *ldt, *dskip, *w1, *w2, *c, *wmod;
  u16* out;
  char* ws;
};

extern "C" __global__ void __launch_bounds__(512, 2) fused_block(Params p) {
  extern __shared__ __attribute__((aligned(16))) char smem_[];
  LAS char* smem = (LAS char*)smem_;
  const int tid = threadIdx.x, lane = tid & 63, w = __builtin_amdgcn_readfirstlane(tid >> 6), lw = w & 3;
  STAMP_DECL
  STAMP(0);
  *(LAS u32*)(smem + LD_FLG + w * 4) = 0u;
  BARRIER();
  asm volatile("" :: "s"(p.x), "s"(p.Are), "s"(p.Aim), "s"(p.Bre), "s"(p.Bim), "s"(p.Cre), "s"(p.Cim), "s"(p.ldt), "s"(p.dskip), "s"(p.w1), "s"(p.w2), "s"(p.c), "s"(p.wmod),
               "s"(p.out), "s"(p.ws));
#define SCAL_ISSUE(q) u64 epoch; u32 aux; \
  asm volatile("s_load_dwordx2 %0, %2, 0x0\n\ts_load_dword %1, %3, 0x0" : "=&s"(epoch), "=&s"(aux) : "s"(p.ws + WS_EPOCH), "s"(q)); SCHED()
#define SCAL_WAIT() asm volatile("s_waitcnt lgkmcnt(0)" : "+s"(epoch), "+s"(aux)); \
  epoch += 0x9E3779B97F4A7C15ull; ta = (u32)epoch; tb = (u32)(epoch >> 32); dsk = aux
#define ROLES() int b = blockIdx.x; asm volatile("" : "+s"(b)); \
  const int xcd = b & 7, j = b >> 3, pair = xcd * 32 + j, d0 = pair * 2; \
  const bool helper = j < 4; \
  const int rbk = (xcd >> 1) * 4 + (j & 3), cbk = (xcd & 1) * 7 + ((j - 4) >> 2), tbk = xcd * 4 + (j & 3), db = ((j >> 2) + 3 - (int)((u64)p.w2 >> 7)) & 7; \
  const rsrc_t rws = __builtin_amdgcn_make_buffer_rsrc(p.ws, 0, WS_END, 0x00020000)

  if (__builtin_expect(w >= 4, 1)) {
    LAS char* flg = smem + LD_FLG + 16;
    u32x4 sg[4];
    u32x2 xg[4];
    ROLES();
    const int go = lw * 4096 + lane * 16;
    u32 ta, tb, dsk;
    const u32x4 qws = mkrs(p.ws, WS_END);
#define POLL_ISSUE() asm volatile("buffer_load_dwordx4 %0, %4, %5, 0 offen sc1\n\tbuffer_load_dwordx4 %1, %4, %5, 0 offen offset:1024 sc1\n\t" \
      "buffer_load_dwordx4 %2, %4, %5, 0 offen offset:2048 sc1\n\tbuffer_load_dwordx4 %3, %4, %5, 0 offen offset:3072 sc1" \
      : "=&v"(sg[0]), "=&v"(sg[1]), "=&v"(sg[2]), "=&v"(sg[3]) : "v"(go + WS_SG), "s"(qws) : "memory")
    int vx[4], vw1[4], vw2[4], so, xo0, xo1, fo, mfo;
    LAS char *ra[2], *rc[2];
    auto addresses = [&]() {
      const int hm = (j - 4) >> 31;
#pragma unroll
      for (int i = 0; i < 4; ++i) {
        const int chk = 16 * ((lane & 15) ^ ((lane >> 4) << 2) ^ i);
        vx[i] = (lane >> 4) * 256 + chk;
        vw1[i] = (lane >> (4 + hm)) * (F * 2) + ((chk & ~hm) | ((lane & 7) * 16 & hm)) + i * ((4 + (4 & hm)) * F * 2 - 1024);
        vw2[i] = (lane >> 3) * 1024 + db * 128 + 16 * ((lane & 7) ^ (((lane >> 4) & 1) << 2)) + i * 7168;
        PIN(vx[i]); PIN(vw1[i]); PIN(vw2[i]);
      }
      const int g = (lane >> 1) & 3;
      ra[0] = smem + LD_RED0 + (2 * lw * 64 + 16 * g + ((lane >> 3) ^ (4 * g))) * 16 + 8 * (lane & 1);
      ra[1] = smem + LD_RED0 + (2 * lw * 64 + 16 * g + ((lane >> 3) ^ (4 * g) ^ 8)) * 16 + 8 * (lane & 1);
      rc[0] = ra[0] + (LD_RED1 - LD_RED0); rc[1] = ra[1] + (LD_RED1 - LD_RED0);
      so = WS_X1S + (lw * 4 + (lane >> 4)) * ST_X1S + d0 * 256 + (lane & 15) * 16;
      xo0 = WS_X1 + (lw * 8 + (lane >> 3)) * ST_X1 + pair * 128 + (lane & 7) * 16; xo1 = xo0 + ST_X1H;
      fo = only(lane < 4, WS_F2 + (lw * 256 + pair) * 64 + lane * 16);
      mfo = only((tid >> 1) == 248, WS_MF + (~lane & 1) * 4096 + pair * 16);
      PIN(mfo); PIN(ra[0]); PIN(ra[1]); PIN(rc[0]); PIN(rc[1]); PIN(so); PIN(xo0); PIN(xo1); PIN(fo);
      SCHED();
    };
    f32x2 xf[4][2], hf[4][2], ca[4][2];
    u32x4 mv1, mv2;
    auto coefficients = [&]() {
      __builtin_amdgcn_s_setprio(2);
      STAMP(7);
      if (__builtin_expect(__any(bad4(sg, ta, tb)), 0)) retry4(sg, rws, go + WS_SG, 1024, ta, tb);
      float rs[4][2];
#pragma unroll
      for (int i = 0; i < 4; ++i)
#pragma unroll
        for (int e = 0; e < 2; ++e) { rs[i][e] = __builtin_amdgcn_rcpf(bfhi(sg[i][2 * e])); PIN(rs[i][e]); }
      if (lw >= 2) { WAIT_VM(4); quad_arrive(flg, lw, 3u); }
      quad_wait(flg, 3u);
      const u32x4 mv0 = *(LAS const u32x4*)(smem + LD_MV);
      mv1 = *(LAS const u32x4*)(smem + LD_MV + 32); mv2 = *(LAS const u32x4*)(smem + LD_MV + 48);
      if (lw < 2) {
#pragma unroll
        for (int i = 0; i < 4; ++i) xg[i] = *(LAS const u32x2*)(smem + LD_XT + lw * 2048 + i * 512 + lane * 8);
      }
      const f32x2 sa = up(mv0[0]), g1 = up(mv0[3]);
#pragma unroll
      for (int i = 0; i < 4; ++i)
#pragma unroll
        for (int e = 0; e < 2; ++e) {
          ca[i][e] = g1 * rs[i][e];
          hf[i][e] = sa - ca[i][e] * bflo(sg[i][2 * e]);
          PIN(ca[i][e]); PIN(hf[i][e]);
        }
      STAMP(8);
    };
    if (lw >= 2) {
      u32 xt;
      TOUCH(xt, (j * 32 + (lw & 1) * 16 + (lane & 15)) * 1024 + xcd * 128, p.x);
      SCAL_ISSUE(p.dskip + d0);
      addresses();
      const u32x4 qx = mkrs(p.x, L * D * 2);
      SCAL_WAIT();
      quad_arrive(flg, lw, 1u);
#pragma unroll
      for (int i = 0; i < 8; ++i) {
        if (i == G_EARLY) quad_wait(flg, 1u);
        if (i == G_SIG) *(volatile LAS u32*)(flg + lw * 4) = 2u;
        if (i == G_SG) POLL_ISSUE();
        dma4(qx, LD_XT + (lw & 1) * 2048 + i * 256, lane * 1024 + pair * 4, ((lw & 1) * 8 + i) * 65536);
        if (i < G_SG) WAIT_VM(2);
      }
      if (XG_DELAY) __builtin_amdgcn_s_sleep(XG_DELAY);
      STAMP(3);
      const int xo = WS_XG + ((lw & 1) * 128 + (lane >> 1)) * ST_XG + pair * 32 + (lane & 1) * 16;
      u32x4 xe[4];
#pragma unroll
      for (int i = 0; i < 4; ++i) xe[i] = ld_sc1(rws, xo, i * 32 * ST_XG);
      asm volatile("s_waitcnt vmcnt(%5)" : "+v"(sg[0]), "+v"(sg[1]), "+v"(sg[2]), "+v"(sg[3]) : "v"(xt), "n"(12 - G_SG) : "memory");
      coefficients();
      if (__builtin_expect(__any(bad4(xe, ta, tb)), 0)) retry4(xe, rws, xo, 32 * ST_XG, ta, tb);
#pragma unroll
      for (int i = 0; i < 4; ++i) xg[i] = u32x2{xe[i][0], xe[i][2]};
    } else {
      u32x4 xv[4], wm[8];
      const int gcb = (b + (b >> 4)) & 15;
#pragma unroll
      for (int i = 0; i < 4; ++i) xv[i] = LDNT((const u32x4*)(p.x + (size_t)(lw * 1024 + b * 4 + i) * D) + lane);
      SCHED();
      SCAL_ISSUE(p.dskip + d0);
      const u32 c0 = p.c[(b >> 4) * 32 + (lane >> 3) + 8 * (lane & 3)];
      SCHED();
      {
        const rsrc_t ra = __builtin_amdgcn_make_buffer_rsrc((void*)p.wmod, 0, D * 3072 * 2, 0x00020000);
        const rsrc_t rb_ = __builtin_amdgcn_make_buffer_rsrc((void*)p.wmod, 0, lw == 0 ? D * 3072 * 2 : 0, 0x00020000);
#pragma unroll
        for (int i = 0; i < 8; ++i)
          wm[i] = __builtin_amdgcn_raw_buffer_load_b128(i < 4 ? ra : rb_, (lane >> 3) * 6144 + (lane & 7) * 16,
                                                        ((b >> 4) * 32 + 8 * (i & 3)) * 6144 + ((lw + (i >> 2) * 2) * 16 + gcb) * 128, AUX_NT);
      }
      SCHED();
      *(LAS u32x4*)(smem + LD_U + (lane >> 5) * 4608 + (lane & 31) * 16) = u32x4{0u, 0u, 0u, 0u};
      int so_ = only(!(lane & 31), WS_SG + lw * 8192 + b * 32 + (lane >> 5) * 16);
      int mo_ = only(!(lane & 8), WS_MG + (b >> 4) * ST_MG + ((lw * 16 + gcb) * 32 + (lane >> 4) * 8 + (lane & 7)) * 16);
      PIN(so_); PIN(mo_);
      SCAL_WAIT();
      __builtin_amdgcn_s_setprio(3);
      STAMP(1);
      LAS char* xw = smem + LD_XT + 4096;
      if (lw) {
#pragma unroll
        for (int i = 0; i < 4; ++i) *(LAS u32x4*)(xw + i * 1056 + lane * 16) = xv[i];
      }
      float s[4], q[4];
#pragma unroll
      for (int i = 0; i < 4; ++i) {
        s[i] = 0.f; q[i] = 0.f;
#pragma unroll
        for (int z = 0; z < 4; ++z) { s[i] = dot2(xv[i][z], 0x3f803f80u, s[i]); q[i] = dot2(xv[i][z], xv[i][z], q[i]); }
      }
      const u32 st = ln_stat(row_sum(fold16(fold32(s[0], s[2]), fold32(s[1], s[3]))), row_sum(fold16(fold32(q[0], q[2]), fold32(q[1], q[3]))));
      const auto sw = __builtin_amdgcn_permlane16_swap(st, st, false, false);
      st_sc1(rws, so_, 0, u32x4{sw[0], ta, sw[1], tb});
      STAMP(2);
      auto gv_tile = [&](int z, u32 sp) {
        float acc[8] = {0.f, 0.f, 0.f, 0.f, 0.f, 0.f, 0.f, 0.f};
        gv_row<0x00>(wm[4 * z], sp, acc); gv_row<0x55>(wm[4 * z + 1], sp, acc); gv_row<0xAA>(wm[4 * z + 2], sp, acc); gv_row<0xFF>(wm[4 * z + 3], sp, acc);
        const f32x2 m = gv_fold(acc);
        st_sc1(rws, mo_ + z * 16384, 0, u32x4{__float_as_uint(m[0]), ta, __float_as_uint(m[1]), tb});
        SCHED();
      };
      if (lw) {
        u32 a[8][2];
#pragma unroll
        for (int i = 0; i < 8; ++i) {
          LAS const u32* src = (LAS const u32*)(xw + (lane & 1) * 2112 + i * 128 + (lane >> 1) * 4);
          a[i][0] = src[0]; a[i][1] = src[264];
        }
#pragma unroll
        for (int i = 0; i < 8; ++i) st_sc1(rws, WS_XG + b * ST_XG + lane * 16 + (i >> 2) * 4096, (i & 3) * 1024, u32x4{a[i][0], ta, a[i][1], tb});
        SCHED();
        gv_tile(0, silu_bf(c0));
        PIN(wm[4]); PIN(wm[5]); PIN(wm[6]); PIN(wm[7]);
      } else {
        const u32 sp = silu_bf(c0);
        gv_tile(0, sp);
        gv_tile(1, sp);
      }
      __builtin_amdgcn_s_setprio(0);
      quad_arrive(flg, lw, 2u);
      STAMP(4);
      addresses();
      quad_wait(flg, 2u);
      const int mo = WS_MG + (lane & 15) * ST_MG + ((((lane >> 4) & (2 * lw + 1)) + 2 * lw) * 256 + (pair >> 5) * 32 + (pair & 3) * 8 + ((pair >> 2) & 7)) * 16;
      u32x4 mg;
      asm volatile("buffer_load_dwordx4 %0, %1, %2, 0 offen sc1" : "=v"(mg) : "v"(mo), "s"(qws) : "memory");
      POLL_ISSUE();
      asm volatile("s_waitcnt vmcnt(4)" : "+v"(mg) :: "memory");
      if (__builtin_expect(__any(tagdiff(mg, ta, tb) != 0u), 0)) {
        asm volatile("s_waitcnt vmcnt(0)" : "+v"(sg[0]), "+v"(sg[1]), "+v"(sg[2]), "+v"(sg[3]) :: "memory");
        mg = poll16(rws, mo, 0, ta, tb);
      }
      const u32 mb = pk(row_sum(__uint_as_float(mg[0])), row_sum(__uint_as_float(mg[2])));
      *(LAS u32x2*)(smem + LD_MV + lw * 32 + (lane >> 4) * 8) = u32x2{mb, pk2(up(mb) + 1.f)};
      quad_arrive(flg, lw, 3u);
      asm volatile("s_waitcnt vmcnt(0)" : "+v"(sg[0]), "+v"(sg[1]), "+v"(sg[2]), "+v"(sg[3]) :: "memory");
      coefficients();
    }
    const u32x4 qw1 = mkrs(p.w1, D * F * 2), qw2 = mkrs(p.w2, D * F * 2);
    const int sw2 = ((lw >> 1) * 896 + (lw & 1) * 64) * 1024;
    auto w2_stage = [&](int u) { dma4k(qw2, w2_unit(u) * UNIT + lw * 4096, vw2, sw2 + w2_row(u) * 1024); };
    const int wstep = (helper ? 32 : 16) * F * 2, wbase = helper ? min(tbk, 27) * 128 : cbk * 256;
    auto w1_stage = [&](int s) { dma4k(qw1, LD_WR + (s & 3) * UNIT + lw * 4096, vw1, (s * 4 + lw) * wstep + wbase); };
    auto x_stage = [&](int s) { dma4k_sc1(qws, LD_XR + (s & 3) * UNIT + lw * 4096, vx, WS_X1S + rbk * ST_X1S + s * 16384 + lw * 4096); };
    *(volatile LAS u32*)(flg + lw * 4) = 4u;
    STAMP(9);
#pragma unroll
    for (int i = 0; i < 4; ++i) {
#pragma unroll
      for (int e = 0; e < 2; ++e) { xf[i][e] = up(xg[i][e]); hf[i][e] += xf[i][e] * ca[i][e]; }
      LAS u32* U = (LAS u32*)(smem + LD_U + 512 + lw * 1024 + i * 256) + lane;
      U[0] = pk(hf[i][0][0], hf[i][1][0]);
      U[4608 / 4] = pk(hf[i][0][1], hf[i][1][1]);
    }
    STAMP(10);
    BARRIER();
    STAMP(11);
    w1_stage(0); w1_stage(1); w1_stage(2); w1_stage(3);
    const f32x2 gav = up(mv1[0]), gcv = up(mv2[1]);
    f32x2 xc[4][2];
    float gd[2] = {gav[0] * bflo(dsk), gav[1] * bfhi(dsk)}, n0 = bflo(mv2[2]) * (-1.f / LOG2E), n1 = bfhi(mv2[2]) * (-1.f / LOG2E);
    u32 k0 = __builtin_amdgcn_perm(mv1[2], mv2[1], 0x05040100u), k1 = __builtin_amdgcn_perm(mv1[2], mv2[1], 0x07060302u);
    PIN(gd[0]); PIN(gd[1]); PIN(n0); PIN(n1); PIN(k0); PIN(k1);
    const u32x4 mfv = {lane & 1 ? k0 : __float_as_uint(n0), ta, lane & 1 ? k1 : __float_as_uint(n1), tb};
    st_sc1(rws, mfo, 0, mfv);
#pragma unroll
    for (int i = 0; i < 4; ++i)
#pragma unroll
      for (int c = 0; c < 2; ++c) {
        float a0 = __builtin_fmaf(hf[i][0][c], gd[c], xf[i][0][c]), a1 = __builtin_fmaf(hf[i][1][c], gd[c], xf[i][1][c]);
        PIN(a0); PIN(a1);
        xc[i][c] = f32x2{a0, a1};
      }
    STAMP(12);
    SCHED();
    u32x4 wr[20];
    auto wr_load = [&](bool on) {
      const rsrc_t rw2 = __builtin_amdgcn_make_buffer_rsrc((void*)p.w2, 0, on ? D * F * 2 : 0, 0x00020000);
#pragma unroll
      for (int i = 0; i < 20; ++i) wr[i] = __builtin_amdgcn_raw_buffer_load_b128(rw2, vw2[i & 3] + (i & 3) * 1024, sw2 + w2_row(9 + i / 4) * 1024, AUX_W);
    };
    wr_load(!helper);
    SCHED();
    STAMP(13);
    BARRIER();
    STAMP(14);
    {
      u32x4 o[2], s0, s1;
      f32x2 yp[4][4], c0[4], c1[4];
#pragma unroll
      for (int i = 0; i < 4; ++i) {
        yp[i][0] = *(volatile LAS const f32x2*)(ra[i & 1] + (i >> 1) * 1024); yp[i][1] = *(volatile LAS const f32x2*)(ra[i & 1] + (i >> 1) * 1024 + 8192);
      }
#pragma unroll
      for (int i = 0; i < 4; ++i) {
        yp[i][2] = *(volatile LAS const f32x2*)(rc[i & 1] + (i >> 1) * 1024); yp[i][3] = *(volatile LAS const f32x2*)(rc[i & 1] + (i >> 1) * 1024 + 8192);
      }
      SCHED();
#pragma unroll
      for (int i = 0; i < 4; ++i) { c0[i] = (yp[i][0] + yp[i][1]) * gav[0] + xc[i][0]; s0[i] = pk2(c0[i] * gcv[0]); }
      st_sc1(rws, so, 0, s0);
      SCHED();
#pragma unroll
      for (int i = 0; i < 4; ++i) { c1[i] = (yp[i][2] + yp[i][3]) * gav[1] + xc[i][1]; s1[i] = pk2(c1[i] * gcv[1]); }
      st_sc1(rws, so, 256, s1);
      SCHED();
#pragma unroll
      for (int i = 0; i < 4; ++i) { o[i >> 1][2 * (i & 1)] = pk(c0[i][0], c1[i][0]); o[i >> 1][2 * (i & 1) + 1] = pk(c0[i][1], c1[i][1]); }
      st_sc1(rws, xo0, 0, o[0]);
      st_sc1(rws, xo1, 0, o[1]);
    }
    STAMP(15);
    WAIT_VM(0);
    STAMP(17);
    st_sc1(rws, fo, 0, u32x4{ta, ta, tb, tb});
    __builtin_amdgcn_s_setprio(0);
    const int kind = helper ? 3 : lw;
    int e0 = WS_G2 + rbk * 1024, e1 = WS_S12 + (cbk * 128 + (kind - 1) * 64) * 16, e3 = WS_MF + 4096 + db * 512;
    asm volatile("" : "+s"(e0), "+s"(e1), "+s"(e3));
    const bool mid = (kind + 1) & 2;
    const int eo = (mid ? e1 : kind ? e3 : e0) + (lane & (kind == 3 ? 31 : 63)) * 16;
    LAS char* ea = smem + LD_MISC + (mid ? 256 + kind * 256 : kind * 512) + lane * (mid ? 4 : 8);
    u32x4 ev;
    if (helper) {
      BARRIER();
      ev = poll16(rws, eo, 0, ta, tb);
      *(LAS u32*)ea = ev[0]; *(LAS u32*)(ea + 4) = ev[2];
      BARRIER();
      __builtin_amdgcn_s_sleep(20);
      w2_stage(1); w2_stage(2); w2_stage(3);
      BARRIER(); BARRIER();
      w2_stage(0); w2_stage(4); w2_stage(5);
      wr_load(true);
    } else {
      __builtin_amdgcn_s_sleep(F2_DELAY);
      poll16(rws, ((lane >> 3) * 32 + lw * 8 + (lane & 7)) * 64, WS_F2 + (xcd >> 1) * 16384, ta, tb);
      STAMP(19);
      x_stage(0); x_stage(1); WAIT_VM(4); BARRIER();
      x_stage(2); x_stage(3); WAIT_VM(8); BARRIER();
      x_stage(4); w1_stage(4); WAIT_VM(12); BARRIER();
#pragma unroll 1
      for (int s = 5; s < 8; ++s) { x_stage(s); w1_stage(s); WAIT_VM(16); BARRIER(); }
      w2_stage(0); w2_stage(1); WAIT_VM(16); BARRIER();
      asm volatile("buffer_load_dwordx4 %0, %1, %2, 0 offen sc1" : "=v"(ev) : "v"(eo), "s"(qws) : "memory");
      w2_stage(2); w2_stage(3); WAIT_VM(17); BARRIER();
      asm volatile("s_waitcnt vmcnt(8)" : "+v"(ev) :: "memory");
      if (__any(tagdiff(ev, ta, tb) != 0u)) ev = poll16(rws, eo, 0, ta, tb);
      *(LAS u32*)ea = ev[0]; *(LAS u32*)(ea + (mid ? 512 : 4)) = ev[2];
      w2_stage(4); w2_stage(5); BARRIER();
    }
    STAMP(22);
    w2_stage(6); w2_stage(7); w2_stage(8);
    {
      const int xs = WS_X1 + tbk * ST_X1 + (db * 32 + (lw >> 1) * 16) * 128 + (lw & 1) * 64;
      dma16_sc1(qws, LD_MISC + 2048 + lw * 2048, (lane >> 2) * 128 + (lane & 3) * 16, xs);
      dma16_sc1(qws, LD_MISC + 3072 + lw * 2048, (lane >> 2) * 128 + (lane & 3) * 16, xs + ST_X1H);
    }
    WAIT_VM(0);
    STAMP(25);
    BARRIER();
    BARRIER();
#pragma unroll
    for (int i = 0; i < 20; ++i) {
      if (i == 8) BARRIER();
      *(LAS u32x4*)(smem + w2_unit(9 + i / 4) * UNIT + lw * 4096 + (i & 3) * 1024 + lane * 16) = wr[i];
    }
    BARRIER();
    if (b == 0 && tid == 256) *(u32x2*)(p.ws + WS_EPOCH) = u32x2{ta, tb};
    STAMP(26);
    STAMP_FLUSH(p.ws, 1);
    return;
  }

  ROLES();
  const int pch = w >> 1, pn = (w & 1) * 64 + lane, po = (d0 + pch) * NS + pn;
  u32 rar = LDNT(p.Are + po), rai = LDNT(p.Aim + po), rbr = LDNT(p.Bre + po), rbi = LDNT(p.Bim + po), rcr = LDNT(p.Cre + po), rci = LDNT(p.Cim + po);
  SCHED();
  SCAL_ISSUE(p.ldt + d0);
  {
    u32 cp0;
    {
      u64 pc; int r0, r1;
      asm volatile("s_getpc_b64 %0\n\ts_mov_b32 %1, fused_block@rel32@lo+4\n\ts_mov_b32 %2, fused_block_end@rel32@lo+12" : "=s"(pc), "=s"(r0), "=s"(r1));
      TOUCH(cp0, min(((w * 4 + (lane & 3)) * 32 + j) * 128, r1 - r0), pc + (long long)r0);
    }
    LAS char* flg = smem + LD_FLG;
    *(LAS u32x4*)(smem + LD_T + ((lane >> 1) & 7) * TCOPY + 4096 + (lane & 1) * 16) = u32x4{0u, 0u, 0u, 0u};
    *(LAS u32*)(smem + LD_KD + KD_ZERO * 4) = 0u;
    asm volatile("s_waitcnt lgkmcnt(0)" : "+s"(epoch), "+s"(aux));
    int ga[11], ka[4];
#pragma unroll
    for (int i = 0; i < 11; ++i) {
      const int l = 2047 - (tid * 8 + i);
      const int bits = (int)(pk((float)l, 0.f) & 0xffffu);
      const int idx = l < 128 ? l : bits - 0x4300 + 128;
      ga[i] = LD_KD + 4 * (l < 0 ? KD_ZERO : idx + (idx >> 3));
    }
    {
      const int e = lane & 15, g = lane >> 4;
      const u32 cfg = e < 8 ? (w == 0 ? 0x100004u : w == 1 ? 0x080004u : 0x022001u) : (w == 0 ? 0x100004u : w == 1 ? 0x044002u : 0x088003u);
      const int sh = cfg & 15, base = 256 * w + ((cfg >> 8) & 0xff) * (e >> sh) + (e & ((1 << sh) - 1));
#pragma unroll
      for (int r = 0; r < 4; ++r) { const int idx = base + (int)(cfg >> 16) * (4 * g + r); ka[r] = LD_KD + 4 * (idx + (idx >> 3)); PIN(ka[r]); }
    }
    STAMP(1);
    asm volatile("" : "+v"(rar), "+v"(rai), "+v"(rbr), "+v"(rbi), "+v"(rcr), "+v"(rci) : "v"(cp0));
    STAMP(2);
    SCHED();
    volatile LAS u32* P = (LAS u32*)(smem + LD_P) + pch * 64 * PROW + pn;
    const float dt = rb(__builtin_amdgcn_exp2f((pch ? bfhi(aux) : bflo(aux)) * LOG2E));
    const float a2 = (dt * bflo(rar)) * LOG2E, bq = (dt * bflo(rai)) * 0.15915494309189535f;
    const f32x2 z1 = cpow(a2, bq, 1.f), z8 = cpow(a2, bq, 8.f), z256 = cpow(a2, bq, 256.f);
    const f32x2 z16c = cmul(z8, z8) * f32x2{1.f, -1.f}, z512 = cmul(z256, z256), z768 = cmul(z512, z256);
    f32x2 u = cpow(a2, bq, 1024.f), q = {1.f, 0.f};
    f32x2 r = cmul(f32x2{bflo(rcr), -bflo(rci)}, f32x2{bflo(rbr), -bflo(rbi)} * dt);
#pragma unroll
    for (int e = 0; e < 16; ++e) {
      const f32x2 rn = cmul(r, z16c), qn = cmul(q, z1);
      P[e * PROW] = pk2(r);
      P[(16 + e) * PROW] = pk2(q);
      if (!(e & 1)) P[(32 + e / 2) * PROW] = pk2(cmul(q, z256));
      if (!(e & 3)) { P[(40 + e / 4) * PROW] = pk2(cmul(q, z512)); P[(44 + e / 4) * PROW] = pk2(cmul(q, z768)); }
      if ((e & 3) == 1) { P[(48 + e / 2) * PROW] = pk2(u); P[(49 + e / 2) * PROW] = pk2(cmul(u, z8)); }
      if ((e & 3) == 3) u = cmul(u, z256);
      r = rn; q = qn;
    }
    P[56 * PROW] = pk2(u);
    STAMP(3);
    quad_arrive(flg, w, 1u);
#pragma unroll
    for (int i = 0; i < 11; ++i) PIN(ga[i]);
    quad_wait(flg, 1u);
    STAMP(4);
    if (w < 3) {
      LAS const u32* fb = (LAS const u32*)(smem + LD_P) + (lane & 15) * PROW + ((lane >> 4) & 1) * 64 + (lane >> 5) * 32;
      u32x4 fa[16], fc[16];
      f32x4 acc[2] = {{0.f, 0.f, 0.f, 0.f}, {0.f, 0.f, 0.f, 0.f}};
#pragma unroll
      for (int ks = 0; ks < 22; ++ks) {
        if (ks < 16) {
          const int o = (ks & 1) * 64 * PROW + (ks >> 1) * 4;
          fa[ks] = *(LAS const u32x4*)(fb + o); fc[ks] = *(LAS const u32x4*)(fb + (w + 1) * 16 * PROW + o);
        }
        SCHED();
        if (ks >= 6) acc[ks & 1] = mfma16(fa[ks - 6], fc[ks - 6], acc[ks & 1]);
        SCHED();
      }
#pragma unroll
      for (int r4 = 0; r4 < 4; ++r4) *(LAS u32*)(smem + ka[r4]) = pk(acc[0][r4], acc[1][r4]);
    }
    STAMP(5);
    quad_arrive(flg, w, 2u);
    quad_wait(flg, 2u);
    STAMP(6);
    u32 e[11];
#pragma unroll
    for (int i = 0; i < 11; ++i) e[i] = *(LAS const u32*)(smem + ga[i]);
#pragma unroll
    for (int c = 0; c < 2; ++c)
#pragma unroll
      for (int s = 0; s < 4; ++s) {
        u32x4 o;
#pragma unroll
        for (int z = 0; z < 4; ++z) o[z] = __builtin_amdgcn_perm(e[s + 2 * z + 1], e[s + 2 * z], c ? 0x07060302u : 0x05040100u);
        *(LAS u32x4*)(smem + LD_T + (c * 4 + s) * TCOPY + tid * 16) = o;
      }
    quad_arrive(flg, w, 3u);
  }
  epoch += 0x9E3779B97F4A7C15ull;
  const u32 ta = (u32)epoch, tb = (u32)(epoch >> 32);
  {
    const int r = lane & 15, g = lane >> 4, ch = w >> 1, half = w & 1;
    LAS const char* Tb = smem + LD_T + (ch * 4 + ((3 - r) & 3)) * TCOPY + 2 * (2028 - 4 * (r >> 2) + 8 * g - 128 * half) - 7 * 512 - 192;
    LAS const char* Ub = smem + LD_U + ch * 4608 + 2 * (256 + 16 * r - 16 + 8 * g - 128 * half) - 192;
    LAS f32x4* red = (LAS f32x4*)(smem + (ch ? LD_RED1 : LD_RED0)) + half * 512 + (lane ^ ((lane >> 4) << 2));
    PIN(Ub); PIN(red);
    f32x4 acc[8];
#pragma unroll
    for (int n = 0; n < 8; ++n) acc[n] = f32x4{0.f, 0.f, 0.f, 0.f};
    u32x4 uf[2][8], tf[4][8];
    quad_wait(smem + LD_FLG, 3u);
#define LD_TH(e, a, hh) do { const u32x2 l_ = *(volatile LAS const u32x2*)(Tb + 64 * (3 - (e)) + 512 * (7 - (a)) + 8 * (hh)); tf[e][a][2 * (hh)] = l_[0]; tf[e][a][2 * (hh) + 1] = l_[1]; } while (0)
#pragma unroll
    for (int e = 0; e < TF_PRE; ++e)
#pragma unroll
      for (int a = 0; a < 8; ++a) { LD_TH(e, a, 0); LD_TH(e, a, 1); }
#define LD_UF(n, e) uf[(e) & 1][n] = *(LAS const u32x4*)(Ub + 64 * (3 - (e)) + 512 * (n))
#define CM(e, a, m) do { SCHED(); acc[(a) + (m)] = mfma16(tf[e][a], uf[(e) & 1][m], acc[(a) + (m)]); SCHED(); } while (0)
    STAMP(10);
    u32 ft;
    quad_wait(smem + LD_FLG + 16, FT_AT);
    __builtin_amdgcn_s_sleep(FT_DELAY);
    TOUCH(ft, ((b * 2 + (w & 1)) * 28 + min(lane, 27)) * 128, w < 2 ? p.w1 : p.w2);
    BARRIER();
    STAMP(11);
#pragma unroll
    for (int n = 0; n < 8; ++n) LD_UF(n, 0);
#pragma unroll
    for (int e = 0; e < 3; ++e) {
      int cnt = 0;
#pragma unroll
      for (int m = 0; m < 8; ++m)
#pragma unroll
        for (int a = 0; a + m < 8; ++a) {
          CM(e, a, m);
          if (cnt >= 4 && cnt < 12) LD_UF(cnt - 4, e + 1);
          if (cnt >= 12 && cnt < 28 && e + TF_PRE < 4) LD_TH(e + TF_PRE, (cnt - 12) >> 1, (cnt - 12) & 1);
          ++cnt;
        }
    }
#pragma unroll
    for (int n = 0; n < 8; ++n)
#pragma unroll
      for (int a = 0; a <= n; ++a) { CM(3, a, n - a); if (a == 1) red[(n - 1) * 64] = acc[n - 1]; }
    STAMP(12);
    red[7 * 64] = acc[7];
    BARRIER();
    STAMP(14);
    WAIT_VM(0);
    asm volatile("" :: "v"(ft));
  }
  int f3w = OOB;
  if (helper) {
    const int hb = xcd * 4 + j;
    u32x4 v[16];
    const int rg = tid >> 3;
    LAS float* rd = (LAS float*)(smem + 4096);
    { const u32x4 m = poll16(rws, tid * 16, WS_MF, ta, tb); *(LAS u32x2*)(smem + tid * 8) = u32x2{m[0], m[2]}; }
    BARRIER();
#pragma unroll
    for (int i = 0; i < 16; ++i) v[i] = *(LAS const u32x4*)(smem + LD_WR + i * 4096 + tid * 16);
    float a[16];
#pragma unroll
    for (int e = 0; e < 16; ++e) a[e] = 0.f;
#pragma unroll
    for (int i = 0; i < 16; ++i) {
      const u32 gs = *(LAS const u32*)(smem + (i * 32 + rg) * 4);
      const u32 gl = gs & 0xffffu, gh = gs << 16, sl = gs >> 16, sh = gs & 0xffff0000u;
#pragma unroll
      for (int z = 0; z < 4; ++z) {
        a[4 * z] = dot2(v[i][z], gl, a[4 * z]); a[4 * z + 1] = dot2(v[i][z], sl, a[4 * z + 1]);
        a[4 * z + 2] = dot2(v[i][z], gh, a[4 * z + 2]); a[4 * z + 3] = dot2(v[i][z], sh, a[4 * z + 3]);
      }
    }
    fold_cols(a, rd + w * 128, lane);
    STAMP(18);
    poll16(rws, tid * 64, WS_F2 + (xcd >> 1) * 16384, ta, tb);
    BARRIER();
    STAMP(19);
#pragma unroll
    for (int i = 0; i < 16; ++i) v[i] = ld_sc1(rws, tid * 16, WS_X1 + hb * ST_X1 + (i >> 3) * ST_X1H + (i & 7) * 4096);
    if (w == 0 && hb < 28) {
      const f32x2 s = sum4(rd + lane * 2, 128);
      st_sc1(rws, lane * 16, WS_S12 + hb * 1024, u32x4{__float_as_uint(s[0]), ta, __float_as_uint(s[1] * -LOG2E), tb});
    }
#pragma unroll
    for (int e = 0; e < 16; ++e) a[e] = 0.f;
#pragma unroll
    for (int i = 0; i < 16; ++i)
#pragma unroll
      for (int z = 0; z < 4; ++z) {
        const int o = (i >> 3) * 8 + 2 * z;
        a[o] = dot2(v[i][z], 0x3f803f80u, a[o]); a[o + 1] = dot2(v[i][z], v[i][z], a[o + 1]);
      }
    STAMP(20);
    fold_cols(a, rd + 512 + w * 128, lane);
    BARRIER();
    if (w == 0) {
      const f32x2 s = sum4(rd + 512 + lane * 2, 128);
      const u32 st = ln_stat(s[0], s[1]);
      const u32 st2 = __builtin_amdgcn_mov_dpp(st, 0xB1, 0xf, 0xf, true);
      if (!(lane & 1)) st_sc1(rws, lane * 8, WS_G2 + hb * 512, u32x4{st, ta, st2, tb});
    }
    BARRIER();
    STAMP(22);
    __builtin_amdgcn_s_sleep(H_SLEEP);
  } else {
    const int wt = w & 1, wf = w >> 1;
    const int h = lane >> 5, blk = (lane >> 4) & 1, q = (lane & 15) >> 2, pp = lane & 3;
    int ax[2][2], aw[2][2];
#pragma unroll
    for (int c = 0; c < 2; ++c)
#pragma unroll
      for (int t = 0; t < 2; ++t) {
        const int base = 256 * (8 * h + 4 * t + q) + 16 * ((2 * blk + (pp >> 1)) ^ (2 * h + t)) + 8 * (pp & 1);
        ax[c][t] = LD_XR + base + 64 * ((2 * wt + c) ^ q);
        aw[c][t] = LD_WR + base + 64 * ((2 * wf + c) ^ q);
        PIN(ax[c][t]); PIN(aw[c][t]);
      }
    f32x16 acc[2][2];
#pragma unroll
    for (int a = 0; a < 2; ++a)
#pragma unroll
      for (int c = 0; c < 2; ++c)
#pragma unroll
        for (int r = 0; r < 16; ++r) acc[a][c][r] = 0.f;
    u32x4 xf[2][2], wfr[2][2];
    u32 st[2];
    int ho[4];
#pragma unroll
    for (int z = 0; z < 4; ++z) { ho[z] = WS_H + ((rbk * 4 + wt * 2) * 112 + z * 56 + cbk * 4 + wf * 2) * 1024 + lane * 16; PIN(ho[z]); }
    int fo = only(lane < 8, WS_F3 + ((rbk * 4 + w) * 16 + cbk) * 128 + lane * 16);
    int fo2 = fo + 0x20000;
    PIN(fo); PIN(fo2);
#define RD_X(i, c) xf[(i) & 1][c] = tr_frag(smem + ax[c][0] + ((i) & 15) * 4096, smem + ax[c][1] + ((i) & 15) * 4096)
#define RD_W(i, c) wfr[(i) & 1][c] = tr_frag(smem + aw[c][0] + ((i) & 15) * 4096, smem + aw[c][1] + ((i) & 15) * 4096)
#define MM(i, a, c) do { SCHED(); acc[a][c] = mfma32(wfr[(i) & 1][a], xf[(i) & 1][c], acc[a][c]); SCHED(); } while (0)
    BARRIER();
    STAMP(19);
    RD_W(0, 0); RD_X(0, 0); RD_X(0, 1); RD_W(0, 1);
#pragma unroll
    for (int i = 0; i < 32; ++i) {
      if ((i & 3) != 3) {
        MM(i, 0, 0); RD_W(i + 1, 0); MM(i, 0, 1); RD_X(i + 1, 0); MM(i, 1, 0); RD_X(i + 1, 1); MM(i, 1, 1); RD_W(i + 1, 1);
      } else {
        MM(i, 0, 0); MM(i, 0, 1);
        BARRIER();
        if (i == 31) { st[0] = *(LAS const u32*)(smem + LD_MISC + (wt * 64 + (lane & 31)) * 4); st[1] = *(LAS const u32*)(smem + LD_MISC + (wt * 64 + 32 + (lane & 31)) * 4); }
        MM(i, 1, 0);
        if (i < 31) { RD_W(i + 1, 0); RD_X(i + 1, 0); }
        MM(i, 1, 1);
        if (i < 31) { RD_X(i + 1, 1); RD_W(i + 1, 1); }
      }
    }
    STAMP(21);
    float rstd[2], mr[2];
#pragma unroll
    for (int c = 0; c < 2; ++c) {
      rstd[c] = __builtin_amdgcn_rcpf(bfhi(st[c])) * -LOG2E;
      mr[c] = -bflo(st[c]) * rstd[c];
    }
    STAMP(22);
    LAS const float* s12 = (LAS const float*)(smem + LD_MISC + 512) + wf * 64 + 4 * h;
#pragma unroll
    for (int a = 0; a < 2; ++a)
#pragma unroll
      for (int sp = 0; sp < 2; ++sp) {
        f32x4 s1[2], s2[2];
#pragma unroll
        for (int z = 0; z < 2; ++z) { s1[z] = *(LAS const f32x4*)(s12 + a * 32 + sp * 16 + z * 8); s2[z] = *(LAS const f32x4*)(s12 + 128 + a * 32 + sp * 16 + z * 8); }
#pragma unroll
        for (int c = 0; c < 2; ++c) {
          u32x4 o;
#pragma unroll
          for (int e = 0; e < 4; ++e) {
            const int z = e >> 1, k = (e & 1) * 2;
            const f32x2 av = {acc[a][c][sp * 8 + 2 * e], acc[a][c][sp * 8 + 2 * e + 1]};
            const f32x2 cc = f32x2{s1[z][k], s1[z][k + 1]} * mr[c] + f32x2{s2[z][k], s2[z][k + 1]};
            const f32x2 ar = av * rstd[c] + cc;
            const f32x2 dn = f32x2{__builtin_amdgcn_exp2f(ar[0]), __builtin_amdgcn_exp2f(ar[1])} + 1.f;
            o[e] = pk2(ar * f32x2{__builtin_amdgcn_rcpf(dn[0]), __builtin_amdgcn_rcpf(dn[1])});
          }
          st_sc1(rws, ho[2 * c + a], sp * 1024, o);
          if (a * 4 + sp * 2 + c + 1 == F1_AT) { asm volatile("s_waitcnt vmcnt(%0)" :: "n"(F1_AT - 4) : "memory"); st_sc1(rws, fo, 0, u32x4{ta, ta, tb, tb}); SCHED(); }
        }
        if (a == 0 && sp == 1) FENCE();
      }
    STAMP(23);
    f3w = fo2;
  }

  {
    const int wt = w & 1, kh = w >> 1, h = lane >> 5, tl = lane & 31;
    const int blk = (lane >> 4) & 1, q = (lane & 15) >> 2, pp = lane & 3;
    LAS char* aw[3][2];
#pragma unroll
    for (int c = 0; c < 6; ++c) {
      aw[c >> 1][c & 1] = smem + (c >> 1) * 65536 + kh * 8192 + 128 * (4 * h + q) + 64 * ((c & 1) ^ kh ^ (q >> 1)) + 32 * blk + 8 * pp;
      asm volatile("" : "+v"(aw[c >> 1][c & 1]));
    }
    const rsrc_t ro = __builtin_amdgcn_make_buffer_rsrc(p.out, 0, L * D * 2, 0x00020000);
    int oo[2];
#pragma unroll
    for (int i = 0; i < 2; ++i) {
      const int r = tbk * 64 + (tid >> 3) + 32 * i;
      oo[i] = ((r & ~511) + 128 * ((r >> 1) & 3) + 2 * ((r >> 3) & 63) + (r & 1)) * (D * 2) + db * 128 + (tid & 7) * 16;
      PIN(oo[i]);
    }
    f32x16 acc[2];
#pragma unroll
    for (int r = 0; r < 16; ++r) { acc[0][r] = 0.f; acc[1][r] = 0.f; }
    SCHED();
    WAIT_VM(1);
    STAMP(24);
    st_sc1(rws, f3w, 0, u32x4{ta, ta, tb, tb});
    STAMP(25);
    BARRIER();
    constexpr int NW = G2_NW;
    u32x4 hf[NW], wf[3][2];
#define RD_W2C(i, c) do { LAS char* a_ = aw[w2_unit((i) >> 2) >> 2][c] + (w2_unit((i) >> 2) & 3) * UNIT + ((i) & 3) * 2048; wf[(i) % 3][c] = tr_frag(a_, a_ + 1024); } while (0)
#define RD_W2(i) do { RD_W2C(i, 0); RD_W2C(i, 1); } while (0)
    RD_W2(0); RD_W2(1);
    const int sh = WS_H + ((tbk * 2 + wt) * 112 + 28 * kh) * 1024;
#define LD_H(i) hf[(i) % NW] = ld_sc1(rws, lane * 16 + ((i) & 3) * 1024, sh + (((i) >> 2) + (i) / 28 * 7) * 4096)
    const int f3o = WS_F3 + ((tbk >> 1) * 4 + (tbk & 1)) * 2048 + kh * 896 + ((lane & 1) * 32 + min(lane >> 1, 6)) * 128;
    if (F3_DELAY) __builtin_amdgcn_s_sleep(F3_DELAY);
    poll16(rws, f3o, 0, ta, tb);
    u32x4 f2;
    STAMP(26);
#pragma unroll
    for (int i = 0; i < G2_N0; ++i) LD_H(i);
    SCHED();
    f32x2 xr[8], gb[8];
#pragma unroll
    for (int i = 0; i < 8; ++i) {
      const int pr = 4 * (i >> 1) + 2 * h + (i & 1);
      xr[i] = up(*(LAS const u32*)(smem + LD_MISC + 2048 + w * 2048 + ((tl >> 2) & 1) * 1024 + pr * 64 + (tl >> 3) * 16 + (tl & 3) * 4));
      gb[i] = *(LAS const f32x2*)(smem + LD_MISC + 1536 + (kh * 16 + pr) * 8);
    }
#pragma unroll
    for (int i = 0; i < 8; ++i) PIN(xr[i]);
    LAS f32x4* exw = (LAS f32x4*)(smem + w2_unit(5) * UNIT) + w * 256 + lane;
    LAS f32x4* exr = (LAS f32x4*)(smem + w2_unit(5) * UNIT) + (w ^ 2) * 256 + lane;
    LAS char* otw = smem + w2_unit(6) * UNIT + (wt * 32 + tl) * 144 + kh * 64 + h * 8;
    LAS char* otr = smem + w2_unit(6) * UNIT + (tid >> 3) * 144 + (tid & 7) * 16;
    PIN(exw); PIN(exr); PIN(otw); PIN(otr);
    SCHED();
#define G2_STEP(i) do { \
      SCHED(); acc[0] = mfma32(wf[(i) % 3][0], hf[(i) % NW], acc[0]); SCHED(); \
      if ((i) == 0) STAMP(29); \
      if ((i) == 18) STAMP(30); \
      if ((i) == 42) STAMP(31); \
      if ((i) == 6 || (i) == 18 || (i) == 42) BARRIER(); \
      if ((i) + 2 < 56) RD_W2C((i) + 2, 0); \
      if ((i) == P2) f2 = ld_sc1(rws, f3o, 0x20000); \
      if ((i) > 0 && (i) - 1 + NW < 56) LD_H((i) - 1 + NW); \
      SCHED(); acc[1] = mfma32(wf[(i) % 3][1], hf[(i) % NW], acc[1]); SCHED(); \
      if ((i) + 2 < 56) RD_W2C((i) + 2, 1); \
      asm volatile("" :: "v"(wf[(i) % 3][0]), "v"(wf[(i) % 3][1]), "v"(hf[(i) % NW])); } while (0)
#pragma unroll
    for (int i = 0; i < 29 - NW; ++i) G2_STEP(i);
    if (__builtin_expect(__any(tagdiff(f2, ta, tb) != 0u), 0)) poll16(rws, f3o, 0x20000, ta, tb);
    FENCE();
#pragma unroll
    for (int i = 29 - NW; i < 54; ++i) G2_STEP(i);
    SCHED();
    acc[1] = mfma32(wf[54 % 3][1], hf[54 % NW], acc[1]);
    acc[1] = mfma32(wf[55 % 3][1], hf[55 % NW], acc[1]);
    SCHED();
    acc[0] = mfma32(wf[54 % 3][0], hf[54 % NW], acc[0]);
    acc[0] = mfma32(wf[55 % 3][0], hf[55 % NW], acc[0]);
    SCHED();
    STAMP(27);
#pragma unroll
    for (int qq = 0; qq < 4; ++qq) exw[qq * 64] = f32x4{acc[1][4 * qq], acc[1][4 * qq + 1], acc[1][4 * qq + 2], acc[1][4 * qq + 3]};
    SCHED();
#pragma unroll
    for (int i = 0; i < 8; ++i) { xr[i] += f32x2{acc[0][2 * i], acc[0][2 * i + 1]} * gb[i]; PIN(xr[i]); }
    BARRIER();
    f32x4 oth[4];
#pragma unroll
    for (int qq = 0; qq < 4; ++qq) oth[qq] = exr[qq * 64];
    SCHED();
#pragma unroll
    for (int qq = 0; qq < 4; ++qq)
      *(LAS u32x2*)(otw + qq * 16) = u32x2{pk2(f32x2{oth[qq][0], oth[qq][1]} * gb[2 * qq] + xr[2 * qq]), pk2(f32x2{oth[qq][2], oth[qq][3]} * gb[2 * qq + 1] + xr[2 * qq + 1])};
    BARRIER();
#pragma unroll
    for (int i = 0; i < 2; ++i) __builtin_amdgcn_raw_buffer_store_b128(*(LAS const u32x4*)(otr + i * 32 * 144), ro, oo[i], 0, OUT_AUX);
    STAMP(28);
    STAMP_FLUSH(p.ws, 0);
  }
}

extern "C" __global__ void fused_block_end() {}

extern "C" void kernel_launch(void* const* d_in, const int* in_sizes, int n_in, void* d_out, int out_size, void* d_ws, size_t ws_size,
                              hipStream_t stream) {
  Params p;
  p.x = (const u16*)d_in[0]; p.Are = (const u16*)d_in[1]; p.Aim = (const u16*)d_in[2]; p.Bre = (const u16*)d_in[3];
  p.Bim = (const u16*)d_in[4]; p.Cre = (const u16*)d_in[5]; p.Cim = (const u16*)d_in[6]; p.ldt = (const u16*)d_in[7];
  p.dskip = (const u16*)d_in[8]; p.w1 = (const u16*)d_in[10]; p.w2 = (const u16*)d_in[11]; p.c = (const u16*)d_in[12];
  p.wmod = (const u16*)d_in[13]; p.out = (u16*)d_out; p.ws = (char*)d_ws;
  (void)hipFuncSetAttribute((const void*)fused_block, hipFuncAttributeMaxDynamicSharedMemorySize, LDS_BYTES + ST_LDS_EXTRA);
  fused_block<<<256, 512, LDS_BYTES + ST_LDS_EXTRA, stream>>>(p);
}
```

```cpp
#define LIGHT 1
#include <hip/hip_runtime.h>
#define ST_OFF ((size_t)128 << 20)
#define ST_SLOTS 32
#define ST_MAXW 8
#define ST_MAXB 512
#define ST_LDS_EXTRA 2048
typedef unsigned st_u2 __attribute__((ext_vector_type(2)));
#define STAMP_DECL __attribute__((address_space(3))) st_u2* st_ = (__attribute__((address_space(3))) st_u2*)(smem + LDS_BYTES) + (threadIdx.x >> 6) * 32; \
  if ((threadIdx.x & 63) < 32) st_[threadIdx.x & 31] = st_u2{0u, 0u};
#define ST_ON(s) ((s) == 0 || (s) == 28 || (s) == LIGHT)
#define STAMP(s) do { unsigned long long rt_, cy_; __builtin_amdgcn_sched_barrier(0); if (!ST_ON(s)) break; \
  asm volatile("s_memrealtime %0\n\ts_memtime %1\n\ts_waitcnt lgkmcnt(0)" : "=&s"(rt_), "=&s"(cy_) :: "memory"); \
  st_[s] = st_u2{(unsigned)rt_ | 0x80000000u, (unsigned)cy_}; __builtin_amdgcn_sched_barrier(0); } while (0)
#define STAMP_FLUSH(ws, kern) do { if ((threadIdx.x & 63) < 32) { \
  unsigned long long* p_ = (unsigned long long*)((char*)(ws) + ST_OFF) + ((((size_t)(kern) * ST_MAXB + blockIdx.x) * ST_MAXW + (threadIdx.x >> 6)) * ST_SLOTS + (threadIdx.x & 31)) * 2; \
  st_u2 v_ = st_[threadIdx.x & 31]; p_[0] = v_[0]; p_[1] = v_[1]; } } while (0)
#include <hip/hip_runtime.h>
#include <hip/hip_bf16.h>

#define ST_RANGES {{0, 32, 256, "compute waves"}, {1, 32, 256, "loader waves"}, {0, 0, 32, "helper WGs, waves 0-3"}, {1, 0, 32, "helper WGs, waves 4-7"}}

typedef unsigned short u16;
typedef unsigned int u32;
typedef unsigned long long u64;
typedef u32 u32x2 __attribute__((ext_vector_type(2)));
typedef u32 u32x4 __attribute__((ext_vector_type(4)));
typedef float f32x2 __attribute__((ext_vector_type(2)));
typedef float f32x4 __attribute__((ext_vector_type(4)));
typedef float f32x16 __attribute__((ext_vector_type(16)));
typedef short s16x4 __attribute__((ext_vector_type(4)));
typedef __bf16 bf16x2 __attribute__((ext_vector_type(2)));
typedef __bf16 bf16x8 __attribute__((ext_vector_type(8)));
typedef __amdgpu_buffer_rsrc_t rsrc_t;
#define GAS __attribute__((address_space(1)))
#define LAS __attribute__((address_space(3)))
#define CAS __attribute__((address_space(4)))

constexpr int L = 2048, D = 512, NS = 128, F = 1792;

constexpr int WS_EPOCH = 0;
constexpr int WS_SG = 0x1000;
constexpr int WS_MG = 0x80000;
constexpr int WS_F2 = 0x40000;
constexpr int WS_F3 = 1 << 20;
constexpr int WS_G2 = 0x70000;
constexpr int WS_S12 = 0x74000;
constexpr int WS_MF = 0x7c000;
constexpr int WS_XG = 2 << 20;
constexpr int WS_X1 = 0x440000;
constexpr int WS_X1S = 0x680000;
constexpr int WS_H = 9 << 20;
constexpr int WS_END = 16 << 20;
#define PADS 1
constexpr int ST_XG = 8192 + 128 * PADS, ST_MG = 1536 * 16 + 128 * PADS, ST_X1H = 32768 + 128 * PADS, ST_X1 = 2 * ST_X1H, ST_X1S = 131072 + 384 * PADS;
constexpr int OOB = 0x7ff00000;

constexpr int UNIT = 16384;
constexpr int LD_XR = 0, LD_WR = 4 * UNIT, LD_MISC = 9 * UNIT, LD_FLG = LD_MISC + 10240, LD_MV = LD_FLG + 32, LDS_BYTES = LD_MV + 64;
constexpr int TCOPY = 4160, PROW = 132;
constexpr int LD_T = 0, LD_U = 8 * TCOPY, LD_KD = LD_U + 2 * 4608, LD_P = LD_WR, LD_RED0 = 3 * UNIT, LD_RED1 = 8 * UNIT;
constexpr int KD_ZERO = 900, LD_XT = LD_RED0;
static_assert(LD_KD + 904 * 4 <= LD_RED0, "LDS map");
__host__ __device__ constexpr int w2_row(int u) { return 128 * (u % 7) + 32 * (u / 7); }
__host__ __device__ constexpr int w2_unit(int u) { return u % 9 == 8 ? 8 : (u % 9) / 2 + 4 * (u % 9 & 1); }
#define G2_NW 14
#define G2_N0 14
__host__ __device__ constexpr int h_next(int i) { int a = G2_N0 + 2 * i, b = i > 0 ? i - 1 + G2_NW : G2_NW; a = a < b ? a : b; return a < 56 ? a : 56; }
#define TF_PRE 4
#define F3_DELAY 6
#define XG_DELAY 10
#define OUT_AUX 2
#define P2 5
#define F1_AT 8
#define G_SG 6
#define G_EARLY 1
#define G_SIG 5
#define FT_AT 3
#define FT_DELAY 0
#define H_SLEEP 96
#define S1_SLEEP 2
#define F2_DELAY 20
#define POLL_SLEEP 2
#define LDNT(p) __builtin_nontemporal_load(p)
#define AUX_NT 2
#define WF ""
#define AUX_W 0
constexpr int SPIN_CAP = 1 << 15;
constexpr float LOG2E = 1.44269504088896341f;

__device__ __forceinline__ float bflo(u32 v) { return __uint_as_float(v << 16); }
__device__ __forceinline__ float bfhi(u32 v) { return __uint_as_float(v & 0xffff0000u); }
__device__ __forceinline__ u32 pk(float lo, float hi) {
  f32x2 v = {lo, hi};
  bf16x2 r = __builtin_convertvector(v, bf16x2);
  return __builtin_bit_cast(u32, r);
}
__device__ __forceinline__ float rb(float f) { return bflo(pk(f, f)); }
__device__ __forceinline__ float dot2(u32 a, u32 b, float c) {
  return __builtin_amdgcn_fdot2_f32_bf16(__builtin_bit_cast(bf16x2, a), __builtin_bit_cast(bf16x2, b), c, false);
}
__device__ __forceinline__ f32x4 mfma16(u32x4 a, u32x4 b, f32x4 c) {
  return __builtin_amdgcn_mfma_f32_16x16x32_bf16(__builtin_bit_cast(bf16x8, a), __builtin_bit_cast(bf16x8, b), c, 0, 0, 0);
}
__device__ __forceinline__ f32x16 mfma32(u32x4 a, u32x4 b, f32x16 c) {
  return __builtin_amdgcn_mfma_f32_32x32x16_bf16(__builtin_bit_cast(bf16x8, a), __builtin_bit_cast(bf16x8, b), c, 0, 0, 0);
}
template <int CTRL> __device__ __forceinline__ float dpp(float x) {
  return __builtin_bit_cast(float, __builtin_amdgcn_mov_dpp(__builtin_bit_cast(int, x), CTRL, 0xf, 0xf, true));
}
__device__ __forceinline__ float row_sum(float x) {
  x += dpp<0xB1>(x); x += dpp<0x4E>(x); x += dpp<0x124>(x); x += dpp<0x128>(x);
  return x;
}
__device__ __forceinline__ float fold32(float a, float b) {
  auto s = __builtin_amdgcn_permlane32_swap(__float_as_uint(a), __float_as_uint(b), false, false);
  return __uint_as_float(s[0]) + __uint_as_float(s[1]);
}
__device__ __forceinline__ float fold16(float a, float b) {
  auto s = __builtin_amdgcn_permlane16_swap(__float_as_uint(a), __float_as_uint(b), false, false);
  return __uint_as_float(s[0]) + __uint_as_float(s[1]);
}
__device__ __forceinline__ u32x4 mkrs(const void* p, u32 bytes) {
  const u64 a = (u64)p;
  return u32x4{(u32)a, (u32)(a >> 32) & 0xffffu, bytes, 0x00020000u};
}
__device__ __forceinline__ void dma4(u32x4 rs, int lds, int voff, int soff) {
  asm volatile("s_mov_b32 m0, %0\n\ts_nop 0\n\tbuffer_load_dword %1, %2, %3 offen lds" :: "s"(lds), "v"(voff), "s"(rs), "s"(soff) : "memory");
}
__device__ __forceinline__ void dma16_sc1(u32x4 rs, int lds, int voff, int soff) {
  asm volatile("s_mov_b32 m0, %0\n\ts_nop 0\n\tbuffer_load_dwordx4 %1, %2, %3 offen sc1 lds" :: "s"(lds), "v"(voff), "s"(rs), "s"(soff) : "memory");
}
#define DMA4_TEXT(F) "s_mov_b32 m0, %0\n\ts_nop 0\n\tbuffer_load_dwordx4 %1, %5, %6 offen" F " lds\n\tbuffer_load_dwordx4 %2, %5, %6 offen offset:1024" F " lds\n\t" \
                     "buffer_load_dwordx4 %3, %5, %6 offen offset:2048" F " lds\n\tbuffer_load_dwordx4 %4, %5, %6 offen offset:3072" F " lds"
__device__ __forceinline__ void dma4k(u32x4 rs, int lds, const int (&v)[4], int soff) {
  asm volatile(DMA4_TEXT(WF) :: "s"(lds), "v"(v[0]), "v"(v[1]), "v"(v[2]), "v"(v[3]), "s"(rs), "s"(soff) : "memory");
}
__device__ __forceinline__ void dma4k_sc1(u32x4 rs, int lds, const int (&v)[4], int soff) {
  asm volatile(DMA4_TEXT(" sc1") :: "s"(lds), "v"(v[0]), "v"(v[1]), "v"(v[2]), "v"(v[3]), "s"(rs), "s"(soff) : "memory");
}
__device__ __forceinline__ u32x4 ld_sc1(rsrc_t rs, int voff, int soff) { return __builtin_amdgcn_raw_buffer_load_b128(rs, voff, soff, 16); }
__device__ __forceinline__ void st_sc1(rsrc_t rs, int voff, int soff, u32x4 v) { __builtin_amdgcn_raw_buffer_store_b128(v, rs, voff + soff, 0, 16); }
__device__ __forceinline__ u32x4 tr_frag(LAS char* a0, LAS char* a1) {
  const s16x4 lo = __builtin_amdgcn_ds_read_tr16_b64_v4i16((LAS s16x4*)a0), hi = __builtin_amdgcn_ds_read_tr16_b64_v4i16((LAS s16x4*)a1);
  const u32x2 l = __builtin_bit_cast(u32x2, lo), h = __builtin_bit_cast(u32x2, hi);
  return u32x4{l[0], l[1], h[0], h[1]};
}
#define SCHED() __builtin_amdgcn_sched_barrier(0)
#define FENCE() asm volatile("" ::: "memory")
#define WAIT_VM(n) asm volatile("s_waitcnt vmcnt(" #n ")" ::: "memory")
#define BARRIER() do { asm volatile("s_waitcnt lgkmcnt(0)" ::: "memory"); __builtin_amdgcn_s_barrier(); FENCE(); } while (0)

__device__ __forceinline__ f32x2 cmul(f32x2 a, f32x2 b) {
  const f32x2 t = f32x2{a[0], a[0]} * b;
  return __builtin_elementwise_fma(f32x2{a[1], a[1]}, f32x2{-b[1], b[0]}, t);
}
__device__ __forceinline__ f32x2 cpow(float a2, float bq, float l) {
  const float m = __builtin_amdgcn_exp2f(a2 * l);
  const float ph = __builtin_amdgcn_fractf(bq * l);
  return f32x2{__builtin_amdgcn_cosf(ph), __builtin_amdgcn_sinf(ph)} * m;
}
__device__ __forceinline__ u32 pk2(f32x2 a) { return pk(a[0], a[1]); }
__device__ __forceinline__ f32x2 up(u32 v) { return f32x2{bflo(v), bfhi(v)}; }
__device__ __forceinline__ u32 ln_stat(float sum, float sq) {
  const float m = sum * (1.f / D);
  const float var = fmaxf(sq * (1.f / D) - m * m, 0.f);
  return pk(m, __builtin_amdgcn_sqrtf(rb(rb(var) + 1e-5f)));
}
__device__ __forceinline__ u32 tagdiff(u32x4 v, u32 ta, u32 tb) { return (v[1] ^ ta) | (v[3] ^ tb); }
__device__ __forceinline__ u32x4 poll16(rsrc_t rs, int voff, int soff, u32 ta, u32 tb) {
  u32x4 v;
  int spins = 0;
  for (;;) {
    FENCE();
    v = ld_sc1(rs, voff, soff);
    if (tagdiff(v, ta, tb) == 0u || ++spins >= SPIN_CAP) break;
    if (POLL_SLEEP) __builtin_amdgcn_s_sleep(POLL_SLEEP);
  }
  FENCE();
  return v;
}
__device__ __forceinline__ bool bad4(const u32x4 (&v)[4], u32 ta, u32 tb) {
  return (tagdiff(v[0], ta, tb) | tagdiff(v[1], ta, tb) | tagdiff(v[2], ta, tb) | tagdiff(v[3], ta, tb)) != 0u;
}
__device__ __forceinline__ void retry4(u32x4 (&v)[4], rsrc_t rs, int voff, int step, u32 ta, u32 tb) {
#pragma unroll 1
  for (int spins = 0; spins < SPIN_CAP; ++spins) {
    __builtin_amdgcn_s_sleep(S1_SLEEP);
    FENCE();
#pragma unroll
    for (int i = 0; i < 4; ++i) if (tagdiff(v[i], ta, tb)) v[i] = ld_sc1(rs, voff, i * step);
    if (!__any(bad4(v, ta, tb))) break;
  }
}
__device__ __forceinline__ void fold_cols(const float (&v)[16], LAS float* dst, int lane) {
  float u[8]; f32x4 t;
#pragma unroll
  for (int k = 0; k < 8; ++k) u[k] = fold32(v[k], v[k + 8]);
#pragma unroll
  for (int k = 0; k < 4; ++k) { const float a = fold16(u[k], u[k + 4]); t[k] = a + dpp<0x128>(a); }
  if (!(lane & 8)) *(LAS f32x4*)(dst + (lane & 7) * 16 + (lane >> 4) * 4) = t;
}
__device__ __forceinline__ f32x2 sum4(LAS const float* src, int stride) {
  return (*(LAS const f32x2*)src + *(LAS const f32x2*)(src + stride)) + (*(LAS const f32x2*)(src + 2 * stride) + *(LAS const f32x2*)(src + 3 * stride));
}
__device__ __forceinline__ u32 silu_bf(u32 c) {
  const float f = bflo(c), s = f * __builtin_amdgcn_rcpf(1.f + __builtin_amdgcn_exp2f(f * -LOG2E));
  return pk(s, s);
}
template <int CTRL> __device__ __forceinline__ void gv_row(u32x4 v, u32 sp, float (&acc)[8]) {
  const u32 sb = __builtin_amdgcn_mov_dpp(sp, CTRL, 0xf, 0xf, true), al = sb & 0xffffu, ah = sb & 0xffff0000u;
#pragma unroll
  for (int q = 0; q < 4; ++q) { acc[2 * q] = dot2(v[q], al, acc[2 * q]); acc[2 * q + 1] = dot2(v[q], ah, acc[2 * q + 1]); }
}
__device__ __forceinline__ f32x2 gv_fold(const float (&acc)[8]) {
  const float m0 = fold16(fold32(acc[0], acc[4]), fold32(acc[2], acc[6])), m1 = fold16(fold32(acc[1], acc[5]), fold32(acc[3], acc[7]));
  return f32x2{m0 + dpp<0x128>(m0), m1 + dpp<0x128>(m1)};
}
#define TOUCH(dst, voff, base) asm volatile("global_load_dword %0, %1, %2" : "=v"(dst) : "v"(voff), "s"(base))

__device__ __forceinline__ void quad_arrive(LAS char* f, int i, u32 n) {
  asm volatile("s_waitcnt lgkmcnt(0)" ::: "memory");
  *(volatile LAS u32*)(f + i * 4) = n;
}
__device__ __forceinline__ void quad_wait(LAS char* f, u32 n) {
  for (;;) {
    const u32x4 v = *(volatile LAS u32x4*)f;
    if (__builtin_amdgcn_readfirstlane(min(min(v[0], v[1]), min(v[2], v[3]))) >= n) break;
    __builtin_amdgcn_s_sleep(1);
  }
  FENCE();
}
__device__ __forceinline__ int only(bool c, int off) { return off | (c ? 0 : OOB); }
#define PIN(x) asm volatile("" : "+v"(x))

struct Params {
  const u16 *x, *Are, *Aim, *Bre, *Bim, *Cre, *Cim, *ldt, *dskip, *w1, *w2, *c, *wmod;
  u16* out;
  char* ws;
};

extern "C" __global__ void __launch_bounds__(512, 2) fused_block(Params p) {
  extern __shared__ __attribute__((aligned(16))) char smem_[];
  LAS char* smem = (LAS char*)smem_;
  const int tid = threadIdx.x, lane = tid & 63, w = __builtin_amdgcn_readfirstlane(tid >> 6), lw = w & 3;
  STAMP_DECL
  STAMP(0);
  *(LAS u32*)(smem + LD_FLG + w * 4) = 0u;
  BARRIER();
  asm volatile("" :: "s"(p.x), "s"(p.Are), "s"(p.Aim), "s"(p.Bre), "s"(p.Bim), "s"(p.Cre), "s"(p.Cim), "s"(p.ldt), "s"(p.dskip), "s"(p.w1), "s"(p.w2), "s"(p.c), "s"(p.wmod),
               "s"(p.out), "s"(p.ws));
#define SCAL_ISSUE(q) u64 epoch; u32 aux; \
  asm volatile("s_load_dwordx2 %0, %2, 0x0\n\ts_load_dword %1, %3, 0x0" : "=&s"(epoch), "=&s"(aux) : "s"(p.ws + WS_EPOCH), "s"(q)); SCHED()
#define SCAL_WAIT() asm volatile("s_waitcnt lgkmcnt(0)" : "+s"(epoch), "+s"(aux)); \
  epoch += 0x9E3779B97F4A7C15ull; ta = (u32)epoch; tb = (u32)(epoch >> 32); dsk = aux
#define ROLES() int b = blockIdx.x; asm volatile("" : "+s"(b)); \
  const int xcd = b & 7, j = b >> 3, pair = xcd * 32 + j, d0 = pair * 2; \
  const bool helper = j < 4; \
  const int rbk = (xcd >> 1) * 4 + (j & 3), cbk = (xcd & 1) * 7 + ((j - 4) >> 2), tbk = xcd * 4 + (j & 3), db = ((j >> 2) + 3 - (int)((u64)p.w2 >> 7)) & 7; \
  const rsrc_t rws = __builtin_amdgcn_make_buffer_rsrc(p.ws, 0, WS_END, 0x00020000)

  if (__builtin_expect(w >= 4, 1)) {
    LAS char* flg = smem + LD_FLG + 16;
    u32x4 sg[4];
    u32x2 xg[4];
    ROLES();
    const int go = lw * 4096 + lane * 16;
    u32 ta, tb, dsk;
    const u32x4 qws = mkrs(p.ws, WS_END);
#define POLL_ISSUE() asm volatile("buffer_load_dwordx4 %0, %4, %5, 0 offen sc1\n\tbuffer_load_dwordx4 %1, %4, %5, 0 offen offset:1024 sc1\n\t" \
      "buffer_load_dwordx4 %2, %4, %5, 0 offen offset:2048 sc1\n\tbuffer_load_dwordx4 %3, %4, %5, 0 offen offset:3072 sc1" \
      : "=&v"(sg[0]), "=&v"(sg[1]), "=&v"(sg[2]), "=&v"(sg[3]) : "v"(go + WS_SG), "s"(qws) : "memory")
    int vx[4], vw1[4], vw2[4], so, xo0, xo1, fo, mfo;
    LAS char *ra[2], *rc[2];
    auto addresses = [&]() {
      const int hm = (j - 4) >> 31;
#pragma unroll
      for (int i = 0; i < 4; ++i) {
        const int chk = 16 * ((lane & 15) ^ ((lane >> 4) << 2) ^ i);
        vx[i] = (lane >> 4) * 256 + chk;
        vw1[i] = (lane >> (4 + hm)) * (F * 2) + ((chk & ~hm) | ((lane & 7) * 16 & hm)) + i * ((4 + (4 & hm)) * F * 2 - 1024);
        vw2[i] = (lane >> 3) * 1024 + db * 128 + 16 * ((lane & 7) ^ (((lane >> 4) & 1) << 2)) + i * 7168;
        PIN(vx[i]); PIN(vw1[i]); PIN(vw2[i]);
      }
      const int g = (lane >> 1) & 3;
      ra[0] = smem + LD_RED0 + (2 * lw * 64 + 16 * g + ((lane >> 3) ^ (4 * g))) * 16 + 8 * (lane & 1);
      ra[1] = smem + LD_RED0 + (2 * lw * 64 + 16 * g + ((lane >> 3) ^ (4 * g) ^ 8)) * 16 + 8 * (lane & 1);
      rc[0] = ra[0] + (LD_RED1 - LD_RED0); rc[1] = ra[1] + (LD_RED1 - LD_RED0);
      so = WS_X1S + (lw * 4 + (lane >> 4)) * ST_X1S + d0 * 256 + (lane & 15) * 16;
      xo0 = WS_X1 + (lw * 8 + (lane >> 3)) * ST_X1 + pair * 128 + (lane & 7) * 16; xo1 = xo0 + ST_X1H;
      fo = only(lane < 4, WS_F2 + (lw * 256 + pair) * 64 + lane * 16);
      mfo = only((tid >> 1) == 248, WS_MF + (~lane & 1) * 4096 + pair * 16);
      PIN(mfo); PIN(ra[0]); PIN(ra[1]); PIN(rc[0]); PIN(rc[1]); PIN(so); PIN(xo0); PIN(xo1); PIN(fo);
      SCHED();
    };
    f32x2 xf[4][2], hf[4][2], ca[4][2];
    u32x4 mv1, mv2;
    auto coefficients = [&]() {
      __builtin_amdgcn_s_setprio(2);
      STAMP(7);
      if (__builtin_expect(__any(bad4(sg, ta, tb)), 0)) retry4(sg, rws, go + WS_SG, 1024, ta, tb);
      float rs[4][2];
#pragma unroll
      for (int i = 0; i < 4; ++i)
#pragma unroll
        for (int e = 0; e < 2; ++e) { rs[i][e] = __builtin_amdgcn_rcpf(bfhi(sg[i][2 * e])); PIN(rs[i][e]); }
      if (lw >= 2) { WAIT_VM(4); quad_arrive(flg, lw, 3u); }
      quad_wait(flg, 3u);
      const u32x4 mv0 = *(LAS const u32x4*)(smem + LD_MV);
      mv1 = *(LAS const u32x4*)(smem + LD_MV + 32); mv2 = *(LAS const u32x4*)(smem + LD_MV + 48);
      if (lw < 2) {
#pragma unroll
        for (int i = 0; i < 4; ++i) xg[i] = *(LAS const u32x2*)(smem + LD_XT + lw * 2048 + i * 512 + lane * 8);
      }
      const f32x2 sa = up(mv0[0]), g1 = up(mv0[3]);
#pragma unroll
      for (int i = 0; i < 4; ++i)
#pragma unroll
        for (int e = 0; e < 2; ++e) {
          ca[i][e] = g1 * rs[i][e];
          hf[i][e] = sa - ca[i][e] * bflo(sg[i][2 * e]);
          PIN(ca[i][e]); PIN(hf[i][e]);
        }
      STAMP(8);
    };
    if (lw >= 2) {
      u32 xt;
      TOUCH(xt, (j * 32 + (lw & 1) * 16 + (lane & 15)) * 1024 + xcd * 128, p.x);
      SCAL_ISSUE(p.dskip + d0);
      addresses();
      const u32x4 qx = mkrs(p.x, L * D * 2);
      SCAL_WAIT();
      quad_arrive(flg, lw, 1u);
#pragma unroll
      for (int i = 0; i < 8; ++i) {
        if (i == G_EARLY) quad_wait(flg, 1u);
        if (i == G_SIG) *(volatile LAS u32*)(flg + lw * 4) = 2u;
        if (i == G_SG) POLL_ISSUE();
        dma4(qx, LD_XT + (lw & 1) * 2048 + i * 256, lane * 1024 + pair * 4, ((lw & 1) * 8 + i) * 65536);
        if (i < G_SG) WAIT_VM(2);
      }
      if (XG_DELAY) __builtin_amdgcn_s_sleep(XG_DELAY);
      STAMP(3);
      const int xo = WS_XG + ((lw & 1) * 128 + (lane >> 1)) * ST_XG + pair * 32 + (lane & 1) * 16;
      u32x4 xe[4];
#pragma unroll
      for (int i = 0; i < 4; ++i) xe[i] = ld_sc1(rws, xo, i * 32 * ST_XG);
      asm volatile("s_waitcnt vmcnt(%5)" : "+v"(sg[0]), "+v"(sg[1]), "+v"(sg[2]), "+v"(sg[3]) : "v"(xt), "n"(12 - G_SG) : "memory");
      coefficients();
      if (__builtin_expect(__any(bad4(xe, ta, tb)), 0)) retry4(xe, rws, xo, 32 * ST_XG, ta, tb);
#pragma unroll
      for (int i = 0; i < 4; ++i) xg[i] = u32x2{xe[i][0], xe[i][2]};
    } else {
      u32x4 xv[4], wm[8];
      const int gcb = (b + (b >> 4)) & 15;
#pragma unroll
      for (int i = 0; i < 4; ++i) xv[i] = LDNT((const u32x4*)(p.x + (size_t)(lw * 1024 + b * 4 + i) * D) + lane);
      SCHED();
      SCAL_ISSUE(p.dskip + d0);
      const u32 c0 = p.c[(b >> 4) * 32 + (lane >> 3) + 8 * (lane & 3)];
      SCHED();
      {
        const rsrc_t ra = __builtin_amdgcn_make_buffer_rsrc((void*)p.wmod, 0, D * 3072 * 2, 0x00020000);
        const rsrc_t rb_ = __builtin_amdgcn_make_buffer_rsrc((void*)p.wmod, 0, lw == 0 ? D * 3072 * 2 : 0, 0x00020000);
#pragma unroll
        for (int i = 0; i < 8; ++i)
          wm[i] = __builtin_amdgcn_raw_buffer_load_b128(i < 4 ? ra : rb_, (lane >> 3) * 6144 + (lane & 7) * 16,
                                                        ((b >> 4) * 32 + 8 * (i & 3)) * 6144 + ((lw + (i >> 2) * 2) * 16 + gcb) * 128, AUX_NT);
      }
      SCHED();
      *(LAS u32x4*)(smem + LD_U + (lane >> 5) * 4608 + (lane & 31) * 16) = u32x4{0u, 0u, 0u, 0u};
      int so_ = only(!(lane & 31), WS_SG + lw * 8192 + b * 32 + (lane >> 5) * 16);
      int mo_ = only(!(lane & 8), WS_MG + (b >> 4) * ST_MG + ((lw * 16 + gcb) * 32 + (lane >> 4) * 8 + (lane & 7)) * 16);
      PIN(so_); PIN(mo_);
      SCAL_WAIT();
      __builtin_amdgcn_s_setprio(3);
      STAMP(1);
      LAS char* xw = smem + LD_XT + 4096;
      if (lw) {
#pragma unroll
        for (int i = 0; i < 4; ++i) *(LAS u32x4*)(xw + i * 1056 + lane * 16) = xv[i];
      }
      float s[4], q[4];
#pragma unroll
      for (int i = 0; i < 4; ++i) {
        s[i] = 0.f; q[i] = 0.f;
#pragma unroll
        for (int z = 0; z < 4; ++z) { s[i] = dot2(xv[i][z], 0x3f803f80u, s[i]); q[i] = dot2(xv[i][z], xv[i][z], q[i]); }
      }
      const u32 st = ln_stat(row_sum(fold16(fold32(s[0], s[2]), fold32(s[1], s[3]))), row_sum(fold16(fold32(q[0], q[2]), fold32(q[1], q[3]))));
      const auto sw = __builtin_amdgcn_permlane16_swap(st, st, false, false);
      st_sc1(rws, so_, 0, u32x4{sw[0], ta, sw[1], tb});
      STAMP(2);
      auto gv_tile = [&](int z, u32 sp) {
        float acc[8] = {0.f, 0.f, 0.f, 0.f, 0.f, 0.f, 0.f, 0.f};
        gv_row<0x00>(wm[4 * z], sp, acc); gv_row<0x55>(wm[4 * z + 1], sp, acc); gv_row<0xAA>(wm[4 * z + 2], sp, acc); gv_row<0xFF>(wm[4 * z + 3], sp, acc);
        const f32x2 m = gv_fold(acc);
        st_sc1(rws, mo_ + z * 16384, 0, u32x4{__float_as_uint(m[0]), ta, __float_as_uint(m[1]), tb});
        SCHED();
      };
      if (lw) {
        u32 a[8][2];
#pragma unroll
        for (int i = 0; i < 8; ++i) {
          LAS const u32* src = (LAS const u32*)(xw + (lane & 1) * 2112 + i * 128 + (lane >> 1) * 4);
          a[i][0] = src[0]; a[i][1] = src[264];
        }
#pragma unroll
        for (int i = 0; i < 8; ++i) st_sc1(rws, WS_XG + b * ST_XG + lane * 16 + (i >> 2) * 4096, (i & 3) * 1024, u32x4{a[i][0], ta, a[i][1], tb});
        SCHED();
        gv_tile(0, silu_bf(c0));
        PIN(wm[4]); PIN(wm[5]); PIN(wm[6]); PIN(wm[7]);
      } else {
        const u32 sp = silu_bf(c0);
        gv_tile(0, sp);
        gv_tile(1, sp);
      }
      __builtin_amdgcn_s_setprio(0);
      quad_arrive(flg, lw, 2u);
      STAMP(4);
      addresses();
      quad_wait(flg, 2u);
      const int mo = WS_MG + (lane & 15) * ST_MG + ((((lane >> 4) & (2 * lw + 1)) + 2 * lw) * 256 + (pair >> 5) * 32 + (pair & 3) * 8 + ((pair >> 2) & 7)) * 16;
      u32x4 mg;
      asm volatile("buffer_load_dwordx4 %0, %1, %2, 0 offen sc1" : "=v"(mg) : "v"(mo), "s"(qws) : "memory");
      POLL_ISSUE();
      asm volatile("s_waitcnt vmcnt(4)" : "+v"(mg) :: "memory");
      if (__builtin_expect(__any(tagdiff(mg, ta, tb) != 0u), 0)) {
        asm volatile("s_waitcnt vmcnt(0)" : "+v"(sg[0]), "+v"(sg[1]), "+v"(sg[2]), "+v"(sg[3]) :: "memory");
        mg = poll16(rws, mo, 0, ta, tb);
      }
      const u32 mb = pk(row_sum(__uint_as_float(mg[0])), row_sum(__uint_as_float(mg[2])));
      *(LAS u32x2*)(smem + LD_MV + lw * 32 + (lane >> 4) * 8) = u32x2{mb, pk2(up(mb) + 1.f)};
      quad_arrive(flg, lw, 3u);
      asm volatile("s_waitcnt vmcnt(0)" : "+v"(sg[0]), "+v"(sg[1]), "+v"(sg[2]), "+v"(sg[3]) :: "memory");
      coefficients();
    }
    const u32x4 qw1 = mkrs(p.w1, D * F * 2), qw2 = mkrs(p.w2, D * F * 2);
    const int sw2 = ((lw >> 1) * 896 + (lw & 1) * 64) * 1024;
    auto w2_stage = [&](int u) { dma4k(qw2, w2_unit(u) * UNIT + lw * 4096, vw2, sw2 + w2_row(u) * 1024); };
    const int wstep = (helper ? 32 : 16) * F * 2, wbase = helper ? min(tbk, 27) * 128 : cbk * 256;
    auto w1_stage = [&](int s) { dma4k(qw1, LD_WR + (s & 3) * UNIT + lw * 4096, vw1, (s * 4 + lw) * wstep + wbase); };
    auto x_stage = [&](int s) { dma4k_sc1(qws, LD_XR + (s & 3) * UNIT + lw * 4096, vx, WS_X1S + rbk * ST_X1S + s * 16384 + lw * 4096); };
    *(volatile LAS u32*)(flg + lw * 4) = 4u;
    STAMP(9);
#pragma unroll
    for (int i = 0; i < 4; ++i) {
#pragma unroll
      for (int e = 0; e < 2; ++e) { xf[i][e] = up(xg[i][e]); hf[i][e] += xf[i][e] * ca[i][e]; }
      LAS u32* U = (LAS u32*)(smem + LD_U + 512 + lw * 1024 + i * 256) + lane;
      U[0] = pk(hf[i][0][0], hf[i][1][0]);
      U[4608 / 4] = pk(hf[i][0][1], hf[i][1][1]);
    }
    STAMP(10);
    BARRIER();
    STAMP(11);
    w1_stage(0); w1_stage(1); w1_stage(2); w1_stage(3);
    const f32x2 gav = up(mv1[0]), gcv = up(mv2[1]);
    f32x2 xc[4][2];
    float gd[2] = {gav[0] * bflo(dsk), gav[1] * bfhi(dsk)}, n0 = bflo(mv2[2]) * (-1.f / LOG2E), n1 = bfhi(mv2[2]) * (-1.f / LOG2E);
    u32 k0 = __builtin_amdgcn_perm(mv1[2], mv2[1], 0x05040100u), k1 = __builtin_amdgcn_perm(mv1[2], mv2[1], 0x07060302u);
    PIN(gd[0]); PIN(gd[1]); PIN(n0); PIN(n1); PIN(k0); PIN(k1);
    const u32x4 mfv = {lane & 1 ? k0 : __float_as_uint(n0), ta, lane & 1 ? k1 : __float_as_uint(n1), tb};
    st_sc1(rws, mfo, 0, mfv);
#pragma unroll
    for (int i = 0; i < 4; ++i)
#pragma unroll
      for (int c = 0; c < 2; ++c) {
        float a0 = __builtin_fmaf(hf[i][0][c], gd[c], xf[i][0][c]), a1 = __builtin_fmaf(hf[i][1][c], gd[c], xf[i][1][c]);
        PIN(a0); PIN(a1);
        xc[i][c] = f32x2{a0, a1};
      }
    STAMP(12);
    SCHED();
    u32x4 wr[20];
    auto wr_load = [&](bool on) {
      const rsrc_t rw2 = __builtin_amdgcn_make_buffer_rsrc((void*)p.w2, 0, on ? D * F * 2 : 0, 0x00020000);
#pragma unroll
      for (int i = 0; i < 20; ++i) wr[i] = __builtin_amdgcn_raw_buffer_load_b128(rw2, vw2[i & 3] + (i & 3) * 1024, sw2 + w2_row(9 + i / 4) * 1024, AUX_W);
    };
    wr_load(!helper);
    SCHED();
    STAMP(13);
    BARRIER();
    STAMP(14);
    {
      u32x4 o[2], s0, s1;
      f32x2 yp[4][4], c0[4], c1[4];
#pragma unroll
      for (int i = 0; i < 4; ++i) {
        yp[i][0] = *(volatile LAS const f32x2*)(ra[i & 1] + (i >> 1) * 1024); yp[i][1] = *(volatile LAS const f32x2*)(ra[i & 1] + (i >> 1) * 1024 + 8192);
      }
#pragma unroll
      for (int i = 0; i < 4; ++i) {
        yp[i][2] = *(volatile LAS const f32x2*)(rc[i & 1] + (i >> 1) * 1024); yp[i][3] = *(volatile LAS const f32x2*)(rc[i & 1] + (i >> 1) * 1024 + 8192);
      }
      SCHED();
#pragma unroll
      for (int i = 0; i < 4; ++i) { c0[i] = (yp[i][0] + yp[i][1]) * gav[0] + xc[i][0]; s0[i] = pk2(c0[i] * gcv[0]); }
      st_sc1(rws, so, 0, s0);
      SCHED();
#pragma unroll
      for (int i = 0; i < 4; ++i) { c1[i] = (yp[i][2] + yp[i][3]) * gav[1] + xc[i][1]; s1[i] = pk2(c1[i] * gcv[1]); }
      st_sc1(rws, so, 256, s1);
      SCHED();
#pragma unroll
      for (int i = 0; i < 4; ++i) { o[i >> 1][2 * (i & 1)] = pk(c0[i][0], c1[i][0]); o[i >> 1][2 * (i & 1) + 1] = pk(c0[i][1], c1[i][1]); }
      st_sc1(rws, xo0, 0, o[0]);
      st_sc1(rws, xo1, 0, o[1]);
    }
    STAMP(15);
    WAIT_VM(0);
    STAMP(17);
    st_sc1(rws, fo, 0, u32x4{ta, ta, tb, tb});
    __builtin_amdgcn_s_setprio(0);
    const int kind = helper ? 3 : lw;
    int e0 = WS_G2 + rbk * 1024, e1 = WS_S12 + (cbk * 128 + (kind - 1) * 64) * 16, e3 = WS_MF + 4096 + db * 512;
    asm volatile("" : "+s"(e0), "+s"(e1), "+s"(e3));
    const bool mid = (kind + 1) & 2;
    const int eo = (mid ? e1 : kind ? e3 : e0) + (lane & (kind == 3 ? 31 : 63)) * 16;
    LAS char* ea = smem + LD_MISC + (mid ? 256 + kind * 256 : kind * 512) + lane * (mid ? 4 : 8);
    u32x4 ev;
    if (helper) {
      BARRIER();
      ev = poll16(rws, eo, 0, ta, tb);
      *(LAS u32*)ea = ev[0]; *(LAS u32*)(ea + 4) = ev[2];
      BARRIER();
      __builtin_amdgcn_s_sleep(20);
      w2_stage(1); w2_stage(2); w2_stage(3);
      BARRIER(); BARRIER();
      w2_stage(0); w2_stage(4); w2_stage(5);
      wr_load(true);
    } else {
      __builtin_amdgcn_s_sleep(F2_DELAY);
      poll16(rws, ((lane >> 3) * 32 + lw * 8 + (lane & 7)) * 64, WS_F2 + (xcd >> 1) * 16384, ta, tb);
      STAMP(19);
      x_stage(0); x_stage(1); WAIT_VM(4); BARRIER();
      x_stage(2); x_stage(3); WAIT_VM(8); BARRIER();
      x_stage(4); w1_stage(4); WAIT_VM(12); BARRIER();
#pragma unroll 1
      for (int s = 5; s < 8; ++s) { x_stage(s); w1_stage(s); WAIT_VM(16); BARRIER(); }
      w2_stage(0); w2_stage(1); WAIT_VM(16); BARRIER();
      asm volatile("buffer_load_dwordx4 %0, %1, %2, 0 offen sc1" : "=v"(ev) : "v"(eo), "s"(qws) : "memory");
      w2_stage(2); w2_stage(3); WAIT_VM(17); BARRIER();
      asm volatile("s_waitcnt vmcnt(8)" : "+v"(ev) :: "memory");
      if (__any(tagdiff(ev, ta, tb) != 0u)) ev = poll16(rws, eo, 0, ta, tb);
      *(LAS u32*)ea = ev[0]; *(LAS u32*)(ea + (mid ? 512 : 4)) = ev[2];
      w2_stage(4); w2_stage(5); BARRIER();
    }
    STAMP(22);
    w2_stage(6); w2_stage(7); w2_stage(8);
    {
      const int xs = WS_X1 + tbk * ST_X1 + (db * 32 + (lw >> 1) * 16) * 128 + (lw & 1) * 64;
      dma16_sc1(qws, LD_MISC + 2048 + lw * 2048, (lane >> 2) * 128 + (lane & 3) * 16, xs);
      dma16_sc1(qws, LD_MISC + 3072 + lw * 2048, (lane >> 2) * 128 + (lane & 3) * 16, xs + ST_X1H);
    }
    WAIT_VM(0);
    STAMP(25);
    BARRIER();
    BARRIER();
#pragma unroll
    for (int i = 0; i < 20; ++i) {
      if (i == 8) BARRIER();
      *(LAS u32x4*)(smem + w2_unit(9 + i / 4) * UNIT + lw * 4096 + (i & 3) * 1024 + lane * 16) = wr[i];
    }
    BARRIER();
    if (b == 0 && tid == 256) *(u32x2*)(p.ws + WS_EPOCH) = u32x2{ta, tb};
    STAMP(26);
    STAMP_FLUSH(p.ws, 1);
    return;
  }

  ROLES();
  const int pch = w >> 1, pn = (w & 1) * 64 + lane, po = (d0 + pch) * NS + pn;
  u32 rar = LDNT(p.Are + po), rai = LDNT(p.Aim + po), rbr = LDNT(p.Bre + po), rbi = LDNT(p.Bim + po), rcr = LDNT(p.Cre + po), rci = LDNT(p.Cim + po);
  SCHED();
  SCAL_ISSUE(p.ldt + d0);
  {
    u32 cp0;
    {
      u64 pc; int r0, r1;
      asm volatile("s_getpc_b64 %0\n\ts_mov_b32 %1, fused_block@rel32@lo+4\n\ts_mov_b32 %2, fused_block_end@rel32@lo+12" : "=s"(pc), "=s"(r0), "=s"(r1));
      TOUCH(cp0, min(((w * 4 + (lane & 3)) * 32 + j) * 128, r1 - r0), pc + (long long)r0);
    }
    LAS char* flg = smem + LD_FLG;
    *(LAS u32x4*)(smem + LD_T + ((lane >> 1) & 7) * TCOPY + 4096 + (lane & 1) * 16) = u32x4{0u, 0u, 0u, 0u};
    *(LAS u32*)(smem + LD_KD + KD_ZERO * 4) = 0u;
    asm volatile("s_waitcnt lgkmcnt(0)" : "+s"(epoch), "+s"(aux));
    int ga[11], ka[4];
#pragma unroll
    for (int i = 0; i < 11; ++i) {
      const int l = 2047 - (tid * 8 + i);
      const int bits = (int)(pk((float)l, 0.f) & 0xffffu);
      const int idx = l < 128 ? l : bits - 0x4300 + 128;
      ga[i] = LD_KD + 4 * (l < 0 ? KD_ZERO : idx + (idx >> 3));
    }
    {
      const int e = lane & 15, g = lane >> 4;
      const u32 cfg = e < 8 ? (w == 0 ? 0x100004u : w == 1 ? 0x080004u : 0x022001u) : (w == 0 ? 0x100004u : w == 1 ? 0x044002u : 0x088003u);
      const int sh = cfg & 15, base = 256 * w + ((cfg >> 8) & 0xff) * (e >> sh) + (e & ((1 << sh) - 1));
#pragma unroll
      for (int r = 0; r < 4; ++r) { const int idx = base + (int)(cfg >> 16) * (4 * g + r); ka[r] = LD_KD + 4 * (idx + (idx >> 3)); PIN(ka[r]); }
    }
    STAMP(1);
    asm volatile("" : "+v"(rar), "+v"(rai), "+v"(rbr), "+v"(rbi), "+v"(rcr), "+v"(rci) : "v"(cp0));
    STAMP(2);
    SCHED();
    volatile LAS u32* P = (LAS u32*)(smem + LD_P) + pch * 64 * PROW + pn;
    const float dt = rb(__builtin_amdgcn_exp2f((pch ? bfhi(aux) : bflo(aux)) * LOG2E));
    const float a2 = (dt * bflo(rar)) * LOG2E, bq = (dt * bflo(rai)) * 0.15915494309189535f;
    const f32x2 z1 = cpow(a2, bq, 1.f), z8 = cpow(a2, bq, 8.f), z256 = cpow(a2, bq, 256.f);
    const f32x2 z16c = cmul(z8, z8) * f32x2{1.f, -1.f}, z512 = cmul(z256, z256), z768 = cmul(z512, z256);
    f32x2 u = cpow(a2, bq, 1024.f), q = {1.f, 0.f};
    f32x2 r = cmul(f32x2{bflo(rcr), -bflo(rci)}, f32x2{bflo(rbr), -bflo(rbi)} * dt);
#pragma unroll
    for (int e = 0; e < 16; ++e) {
      const f32x2 rn = cmul(r, z16c), qn = cmul(q, z1);
      P[e * PROW] = pk2(r);
      P[(16 + e) * PROW] = pk2(q);
      if (!(e & 1)) P[(32 + e / 2) * PROW] = pk2(cmul(q, z256));
      if (!(e & 3)) { P[(40 + e / 4) * PROW] = pk2(cmul(q, z512)); P[(44 + e / 4) * PROW] = pk2(cmul(q, z768)); }
      if ((e & 3) == 1) { P[(48 + e / 2) * PROW] = pk2(u); P[(49 + e / 2) * PROW] = pk2(cmul(u, z8)); }
      if ((e & 3) == 3) u = cmul(u, z256);
      r = rn; q = qn;
    }
    P[56 * PROW] = pk2(u);
    STAMP(3);
    quad_arrive(flg, w, 1u);
#pragma unroll
    for (int i = 0; i < 11; ++i) PIN(ga[i]);
    quad_wait(flg, 1u);
    STAMP(4);
    if (w < 3) {
      LAS const u32* fb = (LAS const u32*)(smem + LD_P) + (lane & 15) * PROW + ((lane >> 4) & 1) * 64 + (lane >> 5) * 32;
      u32x4 fa[16], fc[16];
      f32x4 acc[2] = {{0.f, 0.f, 0.f, 0.f}, {0.f, 0.f, 0.f, 0.f}};
#pragma unroll
      for (int ks = 0; ks < 22; ++ks) {
        if (ks < 16) {
          const int o = (ks & 1) * 64 * PROW + (ks >> 1) * 4;
          fa[ks] = *(LAS const u32x4*)(fb + o); fc[ks] = *(LAS const u32x4*)(fb + (w + 1) * 16 * PROW + o);
        }
        SCHED();
        if (ks >= 6) acc[ks & 1] = mfma16(fa[ks - 6], fc[ks - 6], acc[ks & 1]);
        SCHED();
      }
#pragma unroll
      for (int r4 = 0; r4 < 4; ++r4) *(LAS u32*)(smem + ka[r4]) = pk(acc[0][r4], acc[1][r4]);
    }
    STAMP(5);
    quad_arrive(flg, w, 2u);
    quad_wait(flg, 2u);
    STAMP(6);
    u32 e[11];
#pragma unroll
    for (int i = 0; i < 11; ++i) e[i] = *(LAS const u32*)(smem + ga[i]);
#pragma unroll
    for (int c = 0; c < 2; ++c)
#pragma unroll
      for (int s = 0; s < 4; ++s) {
        u32x4 o;
#pragma unroll
        for (int z = 0; z < 4; ++z) o[z] = __builtin_amdgcn_perm(e[s + 2 * z + 1], e[s + 2 * z], c ? 0x07060302u : 0x05040100u);
        *(LAS u32x4*)(smem + LD_T + (c * 4 + s) * TCOPY + tid * 16) = o;
      }
    quad_arrive(flg, w, 3u);
  }
  epoch += 0x9E3779B97F4A7C15ull;
  const u32 ta = (u32)epoch, tb = (u32)(epoch >> 32);
  {
    const int r = lane & 15, g = lane >> 4, ch = w >> 1, half = w & 1;
    LAS const char* Tb = smem + LD_T + (ch * 4 + ((3 - r) & 3)) * TCOPY + 2 * (2028 - 4 * (r >> 2) + 8 * g - 128 * half) - 7 * 512 - 192;
    LAS const char* Ub = smem + LD_U + ch * 4608 + 2 * (256 + 16 * r - 16 + 8 * g - 128 * half) - 192;
    LAS f32x4* red = (LAS f32x4*)(smem + (ch ? LD_RED1 : LD_RED0)) + half * 512 + (lane ^ ((lane >> 4) << 2));
    PIN(Ub); PIN(red);
    f32x4 acc[8];
#pragma unroll
    for (int n = 0; n < 8; ++n) acc[n] = f32x4{0.f, 0.f, 0.f, 0.f};
    u32x4 uf[2][8], tf[4][8];
    quad_wait(smem + LD_FLG, 3u);
#define LD_TH(e, a, hh) do { const u32x2 l_ = *(volatile LAS const u32x2*)(Tb + 64 * (3 - (e)) + 512 * (7 - (a)) + 8 * (hh)); tf[e][a][2 * (hh)] = l_[0]; tf[e][a][2 * (hh) + 1] = l_[1]; } while (0)
#pragma unroll
    for (int e = 0; e < TF_PRE; ++e)
#pragma unroll
      for (int a = 0; a < 8; ++a) { LD_TH(e, a, 0); LD_TH(e, a, 1); }
#define LD_UF(n, e) uf[(e) & 1][n] = *(LAS const u32x4*)(Ub + 64 * (3 - (e)) + 512 * (n))
#define CM(e, a, m) do { SCHED(); acc[(a) + (m)] = mfma16(tf[e][a], uf[(e) & 1][m], acc[(a) + (m)]); SCHED(); } while (0)
    STAMP(10);
    u32 ft;
    quad_wait(smem + LD_FLG + 16, FT_AT);
    __builtin_amdgcn_s_sleep(FT_DELAY);
    TOUCH(ft, ((b * 2 + (w & 1)) * 28 + min(lane, 27)) * 128, w < 2 ? p.w1 : p.w2);
    BARRIER();
    STAMP(11);
#pragma unroll
    for (int n = 0; n < 8; ++n) LD_UF(n, 0);
#pragma unroll
    for (int e = 0; e < 3; ++e) {
      int cnt = 0;
#pragma unroll
      for (int m = 0; m < 8; ++m)
#pragma unroll
        for (int a = 0; a + m < 8; ++a) {
          CM(e, a, m);
          if (cnt >= 4 && cnt < 12) LD_UF(cnt - 4, e + 1);
          if (cnt >= 12 && cnt < 28 && e + TF_PRE < 4) LD_TH(e + TF_PRE, (cnt - 12) >> 1, (cnt - 12) & 1);
          ++cnt;
        }
    }
#pragma unroll
    for (int n = 0; n < 8; ++n)
#pragma unroll
      for (int a = 0; a <= n; ++a) { CM(3, a, n - a); if (a == 1) red[(n - 1) * 64] = acc[n - 1]; }
    STAMP(12);
    red[7 * 64] = acc[7];
    BARRIER();
    STAMP(14);
    WAIT_VM(0);
    asm volatile("" :: "v"(ft));
  }
  int f3w = OOB;
  if (helper) {
    const int hb = xcd * 4 + j;
    u32x4 v[16];
    const int rg = tid >> 3;
    LAS float* rd = (LAS float*)(smem + 4096);
    { const u32x4 m = poll16(rws, tid * 16, WS_MF, ta, tb); *(LAS u32x2*)(smem + tid * 8) = u32x2{m[0], m[2]}; }
    BARRIER();
#pragma unroll
    for (int i = 0; i < 16; ++i) v[i] = *(LAS const u32x4*)(smem + LD_WR + i * 4096 + tid * 16);
    float a[16];
#pragma unroll
    for (int e = 0; e < 16; ++e) a[e] = 0.f;
#pragma unroll
    for (int i = 0; i < 16; ++i) {
      const u32 gs = *(LAS const u32*)(smem + (i * 32 + rg) * 4);
      const u32 gl = gs & 0xffffu, gh = gs << 16, sl = gs >> 16, sh = gs & 0xffff0000u;
#pragma unroll
      for (int z = 0; z < 4; ++z) {
        a[4 * z] = dot2(v[i][z], gl, a[4 * z]); a[4 * z + 1] = dot2(v[i][z], sl, a[4 * z + 1]);
        a[4 * z + 2] = dot2(v[i][z], gh, a[4 * z + 2]); a[4 * z + 3] = dot2(v[i][z], sh, a[4 * z + 3]);
      }
    }
    fold_cols(a, rd + w * 128, lane);
    STAMP(18);
    poll16(rws, tid * 64, WS_F2 + (xcd >> 1) * 16384, ta, tb);
    BARRIER();
    STAMP(19);
#pragma unroll
    for (int i = 0; i < 16; ++i) v[i] = ld_sc1(rws, tid * 16, WS_X1 + hb * ST_X1 + (i >> 3) * ST_X1H + (i & 7) * 4096);
    if (w == 0 && hb < 28) {
      const f32x2 s = sum4(rd + lane * 2, 128);
      st_sc1(rws, lane * 16, WS_S12 + hb * 1024, u32x4{__float_as_uint(s[0]), ta, __float_as_uint(s[1] * -LOG2E), tb});
    }
#pragma unroll
    for (int e = 0; e < 16; ++e) a[e] = 0.f;
#pragma unroll
    for (int i = 0; i < 16; ++i)
#pragma unroll
      for (int z = 0; z < 4; ++z) {
        const int o = (i >> 3) * 8 + 2 * z;
        a[o] = dot2(v[i][z], 0x3f803f80u, a[o]); a[o + 1] = dot2(v[i][z], v[i][z], a[o + 1]);
      }
    STAMP(20);
    fold_cols(a, rd + 512 + w * 128, lane);
    BARRIER();
    if (w == 0) {
      const f32x2 s = sum4(rd + 512 + lane * 2, 128);
      const u32 st = ln_stat(s[0], s[1]);
      const u32 st2 = __builtin_amdgcn_mov_dpp(st, 0xB1, 0xf, 0xf, true);
      if (!(lane & 1)) st_sc1(rws, lane * 8, WS_G2 + hb * 512, u32x4{st, ta, st2, tb});
    }
    BARRIER();
    STAMP(22);
    __builtin_amdgcn_s_sleep(H_SLEEP);
  } else {
    const int wt = w & 1, wf = w >> 1;
    const int h = lane >> 5, blk = (lane >> 4) & 1, q = (lane & 15) >> 2, pp = lane & 3;
    int ax[2][2], aw[2][2];
#pragma unroll
    for (int c = 0; c < 2; ++c)
#pragma unroll
      for (int t = 0; t < 2; ++t) {
        const int base = 256 * (8 * h + 4 * t + q) + 16 * ((2 * blk + (pp >> 1)) ^ (2 * h + t)) + 8 * (pp & 1);
        ax[c][t] = LD_XR + base + 64 * ((2 * wt + c) ^ q);
        aw[c][t] = LD_WR + base + 64 * ((2 * wf + c) ^ q);
        PIN(ax[c][t]); PIN(aw[c][t]);
      }
    f32x16 acc[2][2];
#pragma unroll
    for (int a = 0; a < 2; ++a)
#pragma unroll
      for (int c = 0; c < 2; ++c)
#pragma unroll
        for (int r = 0; r < 16; ++r) acc[a][c][r] = 0.f;
    u32x4 xf[2][2], wfr[2][2];
    u32 st[2];
    int ho[4];
#pragma unroll
    for (int z = 0; z < 4; ++z) { ho[z] = WS_H + ((rbk * 4 + wt * 2) * 112 + z * 56 + cbk * 4 + wf * 2) * 1024 + lane * 16; PIN(ho[z]); }
    int fo = only(lane < 8, WS_F3 + ((rbk * 4 + w) * 16 + cbk) * 128 + lane * 16);
    int fo2 = fo + 0x20000;
    PIN(fo); PIN(fo2);
#define RD_X(i, c) xf[(i) & 1][c] = tr_frag(smem + ax[c][0] + ((i) & 15) * 4096, smem + ax[c][1] + ((i) & 15) * 4096)
#define RD_W(i, c) wfr[(i) & 1][c] = tr_frag(smem + aw[c][0] + ((i) & 15) * 4096, smem + aw[c][1] + ((i) & 15) * 4096)
#define MM(i, a, c) do { SCHED(); acc[a][c] = mfma32(wfr[(i) & 1][a], xf[(i) & 1][c], acc[a][c]); SCHED(); } while (0)
    BARRIER();
    STAMP(19);
    RD_W(0, 0); RD_X(0, 0); RD_X(0, 1); RD_W(0, 1);
#pragma unroll
    for (int i = 0; i < 32; ++i) {
      if ((i & 3) != 3) {
        MM(i, 0, 0); RD_W(i + 1, 0); MM(i, 0, 1); RD_X(i + 1, 0); MM(i, 1, 0); RD_X(i + 1, 1); MM(i, 1, 1); RD_W(i + 1, 1);
      } else {
        MM(i, 0, 0); MM(i, 0, 1);
        BARRIER();
        if (i == 31) { st[0] = *(LAS const u32*)(smem + LD_MISC + (wt * 64 + (lane & 31)) * 4); st[1] = *(LAS const u32*)(smem + LD_MISC + (wt * 64 + 32 + (lane & 31)) * 4); }
        MM(i, 1, 0);
        if (i < 31) { RD_W(i + 1, 0); RD_X(i + 1, 0); }
        MM(i, 1, 1);
        if (i < 31) { RD_X(i + 1, 1); RD_W(i + 1, 1); }
      }
    }
    STAMP(21);
    float rstd[2], mr[2];
#pragma unroll
    for (int c = 0; c < 2; ++c) {
      rstd[c] = __builtin_amdgcn_rcpf(bfhi(st[c])) * -LOG2E;
      mr[c] = -bflo(st[c]) * rstd[c];
    }
    STAMP(22);
    LAS const float* s12 = (LAS const float*)(smem + LD_MISC + 512) + wf * 64 + 4 * h;
#pragma unroll
    for (int a = 0; a < 2; ++a)
#pragma unroll
      for (int sp = 0; sp < 2; ++sp) {
        f32x4 s1[2], s2[2];
#pragma unroll
        for (int z = 0; z < 2; ++z) { s1[z] = *(LAS const f32x4*)(s12 + a * 32 + sp * 16 + z * 8); s2[z] = *(LAS const f32x4*)(s12 + 128 + a * 32 + sp * 16 + z * 8); }
#pragma unroll
        for (int c = 0; c < 2; ++c) {
          u32x4 o;
#pragma unroll
          for (int e = 0; e < 4; ++e) {
            const int z = e >> 1, k = (e & 1) * 2;
            const f32x2 av = {acc[a][c][sp * 8 + 2 * e], acc[a][c][sp * 8 + 2 * e + 1]};
            const f32x2 cc = f32x2{s1[z][k], s1[z][k + 1]} * mr[c] + f32x2{s2[z][k], s2[z][k + 1]};
            const f32x2 ar = av * rstd[c] + cc;
            const f32x2 dn = f32x2{__builtin_amdgcn_exp2f(ar[0]), __builtin_amdgcn_exp2f(ar[1])} + 1.f;
            o[e] = pk2(ar * f32x2{__builtin_amdgcn_rcpf(dn[0]), __builtin_amdgcn_rcpf(dn[1])});
          }
          st_sc1(rws, ho[2 * c + a], sp * 1024, o);
          if (a * 4 + sp * 2 + c + 1 == F1_AT) { asm volatile("s_waitcnt vmcnt(%0)" :: "n"(F1_AT - 4) : "memory"); st_sc1(rws, fo, 0, u32x4{ta, ta, tb, tb}); SCHED(); }
        }
        if (a == 0 && sp == 1) FENCE();
      }
    STAMP(23);
    f3w = fo2;
  }

  {
    const int wt = w & 1, kh = w >> 1, h = lane >> 5, tl = lane & 31;
    const int blk = (lane >> 4) & 1, q = (lane & 15) >> 2, pp = lane & 3;
    LAS char* aw[3][2];
#pragma unroll
    for (int c = 0; c < 6; ++c) {
      aw[c >> 1][c & 1] = smem + (c >> 1) * 65536 + kh * 8192 + 128 * (4 * h + q) + 64 * ((c & 1) ^ kh ^ (q >> 1)) + 32 * blk + 8 * pp;
      asm volatile("" : "+v"(aw[c >> 1][c & 1]));
    }
    const rsrc_t ro = __builtin_amdgcn_make_buffer_rsrc(p.out, 0, L * D * 2, 0x00020000);
    int oo[2];
#pragma unroll
    for (int i = 0; i < 2; ++i) {
      const int r = tbk * 64 + (tid >> 3) + 32 * i;
      oo[i] = ((r & ~511) + 128 * ((r >> 1) & 3) + 2 * ((r >> 3) & 63) + (r & 1)) * (D * 2) + db * 128 + (tid & 7) * 16;
      PIN(oo[i]);
    }
    f32x16 acc[2];
#pragma unroll
    for (int r = 0; r < 16; ++r) { acc[0][r] = 0.f; acc[1][r] = 0.f; }
    SCHED();
    WAIT_VM(1);
    STAMP(24);
    st_sc1(rws, f3w, 0, u32x4{ta, ta, tb, tb});
    STAMP(25);
    BARRIER();
    constexpr int NW = G2_NW;
    u32x4 hf[NW], wf[3][2];
#define RD_W2C(i, c) do { LAS char* a_ = aw[w2_unit((i) >> 2) >> 2][c] + (w2_unit((i) >> 2) & 3) * UNIT + ((i) & 3) * 2048; wf[(i) % 3][c] = tr_frag(a_, a_ + 1024); } while (0)
#define RD_W2(i) do { RD_W2C(i, 0); RD_W2C(i, 1); } while (0)
    RD_W2(0); RD_W2(1);
    const int sh = WS_H + ((tbk * 2 + wt) * 112 + 28 * kh) * 1024;
#define LD_H(i) hf[(i) % NW] = ld_sc1(rws, lane * 16 + ((i) & 3) * 1024, sh + (((i) >> 2) + (i) / 28 * 7) * 4096)
    const int f3o = WS_F3 + ((tbk >> 1) * 4 + (tbk & 1)) * 2048 + kh * 896 + ((lane & 1) * 32 + min(lane >> 1, 6)) * 128;
    if (F3_DELAY) __builtin_amdgcn_s_sleep(F3_DELAY);
    poll16(rws, f3o, 0, ta, tb);
    u32x4 f2;
    STAMP(26);
#pragma unroll
    for (int i = 0; i < G2_N0; ++i) LD_H(i);
    SCHED();
    f32x2 xr[8], gb[8];
#pragma unroll
    for (int i = 0; i < 8; ++i) {
      const int pr = 4 * (i >> 1) + 2 * h + (i & 1);
      xr[i] = up(*(LAS const u32*)(smem + LD_MISC + 2048 + w * 2048 + ((tl >> 2) & 1) * 1024 + pr * 64 + (tl >> 3) * 16 + (tl & 3) * 4));
      gb[i] = *(LAS const f32x2*)(smem + LD_MISC + 1536 + (kh * 16 + pr) * 8);
    }
#pragma unroll
    for (int i = 0; i < 8; ++i) PIN(xr[i]);
    LAS f32x4* exw = (LAS f32x4*)(smem + w2_unit(5) * UNIT) + w * 256 + lane;
    LAS f32x4* exr = (LAS f32x4*)(smem + w2_unit(5) * UNIT) + (w ^ 2) * 256 + lane;
    LAS char* otw = smem + w2_unit(6) * UNIT + (wt * 32 + tl) * 144 + kh * 64 + h * 8;
    LAS char* otr = smem + w2_unit(6) * UNIT + (tid >> 3) * 144 + (tid & 7) * 16;
    PIN(exw); PIN(exr); PIN(otw); PIN(otr);
    SCHED();
#define G2_STEP(i) do { \
      SCHED(); acc[0] = mfma32(wf[(i) % 3][0], hf[(i) % NW], acc[0]); SCHED(); \
      if ((i) == 0) STAMP(29); \
      if ((i) == 18) STAMP(30); \
      if ((i) == 42) STAMP(31); \
      if ((i) == 6 || (i) == 18 || (i) == 42) BARRIER(); \
      if ((i) + 2 < 56) RD_W2C((i) + 2, 0); \
      if ((i) == P2) f2 = ld_sc1(rws, f3o, 0x20000); \
      if ((i) > 0 && (i) - 1 + NW < 56) LD_H((i) - 1 + NW); \
      SCHED(); acc[1] = mfma32(wf[(i) % 3][1], hf[(i) % NW], acc[1]); SCHED(); \
      if ((i) + 2 < 56) RD_W2C((i) + 2, 1); \
      asm volatile("" :: "v"(wf[(i) % 3][0]), "v"(wf[(i) % 3][1]), "v"(hf[(i) % NW])); } while (0)
#pragma unroll
    for (int i = 0; i < 29 - NW; ++i) G2_STEP(i);
    if (__builtin_expect(__any(tagdiff(f2, ta, tb) != 0u), 0)) poll16(rws, f3o, 0x20000, ta, tb);
    FENCE();
#pragma unroll
    for (int i = 29 - NW; i < 54; ++i) G2_STEP(i);
    SCHED();
    acc[1] = mfma32(wf[54 % 3][1], hf[54 % NW], acc[1]);
    acc[1] = mfma32(wf[55 % 3][1], hf[55 % NW], acc[1]);
    SCHED();
    acc[0] = mfma32(wf[54 % 3][0], hf[54 % NW], acc[0]);
    acc[0] = mfma32(wf[55 % 3][0], hf[55 % NW], acc[0]);
    SCHED();
    STAMP(27);
#pragma unroll
    for (int qq = 0; qq < 4; ++qq) exw[qq * 64] = f32x4{acc[1][4 * qq], acc[1][4 * qq + 1], acc[1][4 * qq + 2], acc[1][4 * qq + 3]};
    SCHED();
#pragma unroll
    for (int i = 0; i < 8; ++i) { xr[i] += f32x2{acc[0][2 * i], acc[0][2 * i + 1]} * gb[i]; PIN(xr[i]); }
    BARRIER();
    f32x4 oth[4];
#pragma unroll
    for (int qq = 0; qq < 4; ++qq) oth[qq] = exr[qq * 64];
    SCHED();
#pragma unroll
    for (int qq = 0; qq < 4; ++qq)
      *(LAS u32x2*)(otw + qq * 16) = u32x2{pk2(f32x2{oth[qq][0], oth[qq][1]} * gb[2 * qq] + xr[2 * qq]), pk2(f32x2{oth[qq][2], oth[qq][3]} * gb[2 * qq + 1] + xr[2 * qq + 1])};
    BARRIER();
#pragma unroll
    for (int i = 0; i < 2; ++i) __builtin_amdgcn_raw_buffer_store_b128(*(LAS const u32x4*)(otr + i * 32 * 144), ro, oo[i], 0, OUT_AUX);
    STAMP(28);
    STAMP_FLUSH(p.ws, 0);
  }
}

extern "C" __global__ void fused_block_end() {}

extern "C" __global__ void __launch_bounds__(1024) probe_k(char* ws) {
  __shared__ unsigned mn, mx; __shared__ unsigned long long sum; __shared__ unsigned cnt;
  if (threadIdx.x == 0) { mn = 0xffffffffu; mx = 0u; sum = 0ull; cnt = 0u; }
  __syncthreads();
  const unsigned long long* p = (const unsigned long long*)(ws + ST_OFF);
  const int b = threadIdx.x >> 2, w = (threadIdx.x & 3) + 1 * 4;
  const size_t base = (((size_t)1 * ST_MAXB + b) * ST_MAXW + w) * ST_SLOTS * 2;
  const unsigned t0 = (unsigned)p[base], ts = (unsigned)p[base + 2 * 1];
  atomicMin(&mn, t0);
  if (ts) { atomicMax(&mx, ts); atomicAdd(&sum, (unsigned long long)ts); atomicAdd(&cnt, 1u); }
  __syncthreads();
  unsigned d = (unsigned)(sum / cnt) - mn;
  if (d > 10000u) d = 10000u;
  for (int i = 0; i < 1000000; ++i) if ((((unsigned)__builtin_amdgcn_s_memrealtime() | 0x80000000u) - mn) >= d + 5000u) break;
}
extern "C" void kernel_launch(void* const* d_in, const int* in_sizes, int n_in, void* d_out, int out_size, void* d_ws, size_t ws_size,
                              hipStream_t stream) {
  Params p;
  p.x = (const u16*)d_in[0]; p.Are = (const u16*)d_in[1]; p.Aim = (const u16*)d_in[2]; p.Bre = (const u16*)d_in[3];
  p.Bim = (const u16*)d_in[4]; p.Cre = (const u16*)d_in[5]; p.Cim = (const u16*)d_in[6]; p.ldt = (const u16*)d_in[7];
  p.dskip = (const u16*)d_in[8]; p.w1 = (const u16*)d_in[10]; p.w2 = (const u16*)d_in[11]; p.c = (const u16*)d_in[12];
  p.wmod = (const u16*)d_in[13]; p.out = (u16*)d_out; p.ws = (char*)d_ws;
  (void)hipFuncSetAttribute((const void*)fused_block, hipFuncAttributeMaxDynamicSharedMemorySize, LDS_BYTES + ST_LDS_EXTRA);
  fused_block<<<256, 512, LDS_BYTES + ST_LDS_EXTRA, stream>>>(p);
  probe_k<<<1, 1024, 0, stream>>>(p.ws);
}
```

```cpp
#define XG_DELAY 14
#include <hip/hip_runtime.h>
#include <hip/hip_bf16.h>

#define STAMP_DECL
#define STAMP(s) __builtin_amdgcn_sched_barrier(0)
#define STAMP_FLUSH(ws, k)
#define ST_LDS_EXTRA 0
#define ST_RANGES {{0, 32, 256, "compute waves"}, {1, 32, 256, "loader waves"}, {0, 0, 32, "helper WGs, waves 0-3"}, {1, 0, 32, "helper WGs, waves 4-7"}}

typedef unsigned short u16;
typedef unsigned int u32;
typedef unsigned long long u64;
typedef u32 u32x2 __attribute__((ext_vector_type(2)));
typedef u32 u32x4 __attribute__((ext_vector_type(4)));
typedef float f32x2 __attribute__((ext_vector_type(2)));
typedef float f32x4 __attribute__((ext_vector_type(4)));
typedef float f32x16 __attribute__((ext_vector_type(16)));
typedef short s16x4 __attribute__((ext_vector_type(4)));
typedef __bf16 bf16x2 __attribute__((ext_vector_type(2)));
typedef __bf16 bf16x8 __attribute__((ext_vector_type(8)));
typedef __amdgpu_buffer_rsrc_t rsrc_t;
#define GAS __attribute__((address_space(1)))
#define LAS __attribute__((address_space(3)))
#define CAS __attribute__((address_space(4)))

constexpr int L = 2048, D = 512, NS = 128, F = 1792;

constexpr int WS_EPOCH = 0;
constexpr int WS_SG = 0x1000;
constexpr int WS_MG = 0x80000;
constexpr int WS_F2 = 0x40000;
constexpr int WS_F3 = 1 << 20;
constexpr int WS_G2 = 0x70000;
constexpr int WS_S12 = 0x74000;
constexpr int WS_MF = 0x7c000;
constexpr int WS_XG = 2 << 20;
constexpr int WS_X1 = 0x440000;
constexpr int WS_X1S = 0x680000;
constexpr int WS_H = 9 << 20;
constexpr int WS_END = 16 << 20;
#define PADS 1
constexpr int ST_XG = 8192 + 128 * PADS, ST_MG = 1536 * 16 + 128 * PADS, ST_X1H = 32768 + 128 * PADS, ST_X1 = 2 * ST_X1H, ST_X1S = 131072 + 384 * PADS;
constexpr int OOB = 0x7ff00000;

constexpr int UNIT = 16384;
constexpr int LD_XR = 0, LD_WR = 4 * UNIT, LD_MISC = 9 * UNIT, LD_FLG = LD_MISC + 10240, LD_MV = LD_FLG + 32, LDS_BYTES = LD_MV + 64;
constexpr int TCOPY = 4160, PROW = 132;
constexpr int LD_T = 0, LD_U = 8 * TCOPY, LD_KD = LD_U + 2 * 4608, LD_P = LD_WR, LD_RED0 = 3 * UNIT, LD_RED1 = 8 * UNIT;
constexpr int KD_ZERO = 900, LD_XT = LD_RED0;
static_assert(LD_KD + 904 * 4 <= LD_RED0, "LDS map");
__host__ __device__ constexpr int w2_row(int u) { return 128 * (u % 7) + 32 * (u / 7); }
__host__ __device__ constexpr int w2_unit(int u) { return u % 9 == 8 ? 8 : (u % 9) / 2 + 4 * (u % 9 & 1); }
#define G2_NW 14
#define G2_N0 14
__host__ __device__ constexpr int h_next(int i) { int a = G2_N0 + 2 * i, b = i > 0 ? i - 1 + G2_NW : G2_NW; a = a < b ? a : b; return a < 56 ? a : 56; }
#define TF_PRE 4
#define F3_DELAY 6
#define OUT_AUX 2
#define P2 5
#define F1_AT 8
#define G_SG 6
#define PUB_WAIT 1
#define G_EARLY 1
#define G_SIG 5
#define FT_AT 3
#define FT_DELAY 0
#define H_SLEEP 96
#define S1_SLEEP 2
#define F2_DELAY 20
#define POLL_SLEEP 2
#define LDNT(p) __builtin_nontemporal_load(p)
#define AUX_NT 2
#define WF ""
#define AUX_W 0
constexpr int SPIN_CAP = 1 << 15;
constexpr float LOG2E = 1.44269504088896341f;

__device__ __forceinline__ float bflo(u32 v) { return __uint_as_float(v << 16); }
__device__ __forceinline__ float bfhi(u32 v) { return __uint_as_float(v & 0xffff0000u); }
__device__ __forceinline__ u32 pk(float lo, float hi) {
  f32x2 v = {lo, hi};
  bf16x2 r = __builtin_convertvector(v, bf16x2);
  return __builtin_bit_cast(u32, r);
}
__device__ __forceinline__ float rb(float f) { return bflo(pk(f, f)); }
__device__ __forceinline__ float dot2(u32 a, u32 b, float c) {
  return __builtin_amdgcn_fdot2_f32_bf16(__builtin_bit_cast(bf16x2, a), __builtin_bit_cast(bf16x2, b), c, false);
}
__device__ __forceinline__ f32x4 mfma16(u32x4 a, u32x4 b, f32x4 c) {
  return __builtin_amdgcn_mfma_f32_16x16x32_bf16(__builtin_bit_cast(bf16x8, a), __builtin_bit_cast(bf16x8, b), c, 0, 0, 0);
}
__device__ __forceinline__ f32x16 mfma32(u32x4 a, u32x4 b, f32x16 c) {
  return __builtin_amdgcn_mfma_f32_32x32x16_bf16(__builtin_bit_cast(bf16x8, a), __builtin_bit_cast(bf16x8, b), c, 0, 0, 0);
}
template <int CTRL> __device__ __forceinline__ float dpp(float x) {
  return __builtin_bit_cast(float, __builtin_amdgcn_mov_dpp(__builtin_bit_cast(int, x), CTRL, 0xf, 0xf, true));
}
__device__ __forceinline__ float row_sum(float x) {
  x += dpp<0xB1>(x); x += dpp<0x4E>(x); x += dpp<0x124>(x); x += dpp<0x128>(x);
  return x;
}
__device__ __forceinline__ float fold32(float a, float b) {
  auto s = __builtin_amdgcn_permlane32_swap(__float_as_uint(a), __float_as_uint(b), false, false);
  return __uint_as_float(s[0]) + __uint_as_float(s[1]);
}
__device__ __forceinline__ float fold16(float a, float b) {
  auto s = __builtin_amdgcn_permlane16_swap(__float_as_uint(a), __float_as_uint(b), false, false);
  return __uint_as_float(s[0]) + __uint_as_float(s[1]);
}
__device__ __forceinline__ u32x4 mkrs(const void* p, u32 bytes) {
  const u64 a = (u64)p;
  return u32x4{(u32)a, (u32)(a >> 32) & 0xffffu, bytes, 0x00020000u};
}
__device__ __forceinline__ void dma4(u32x4 rs, int lds, int voff, int soff) {
  asm volatile("s_mov_b32 m0, %0\n\ts_nop 0\n\tbuffer_load_dword %1, %2, %3 offen lds" :: "s"(lds), "v"(voff), "s"(rs), "s"(soff) : "memory");
}
__device__ __forceinline__ void dma16_sc1(u32x4 rs, int lds, int voff, int soff) {
  asm volatile("s_mov_b32 m0, %0\n\ts_nop 0\n\tbuffer_load_dwordx4 %1, %2, %3 offen sc1 lds" :: "s"(lds), "v"(voff), "s"(rs), "s"(soff) : "memory");
}
#define DMA4_TEXT(F) "s_mov_b32 m0, %0\n\ts_nop 0\n\tbuffer_load_dwordx4 %1, %5, %6 offen" F " lds\n\tbuffer_load_dwordx4 %2, %5, %6 offen offset:1024" F " lds\n\t" \
                     "buffer_load_dwordx4 %3, %5, %6 offen offset:2048" F " lds\n\tbuffer_load_dwordx4 %4, %5, %6 offen offset:3072" F " lds"
__device__ __forceinline__ void dma4k(u32x4 rs, int lds, const int (&v)[4], int soff) {
  asm volatile(DMA4_TEXT(WF) :: "s"(lds), "v"(v[0]), "v"(v[1]), "v"(v[2]), "v"(v[3]), "s"(rs), "s"(soff) : "memory");
}
__device__ __forceinline__ void dma4k_sc1(u32x4 rs, int lds, const int (&v)[4], int soff) {
  asm volatile(DMA4_TEXT(" sc1") :: "s"(lds), "v"(v[0]), "v"(v[1]), "v"(v[2]), "v"(v[3]), "s"(rs), "s"(soff) : "memory");
}
__device__ __forceinline__ u32x4 ld_sc1(rsrc_t rs, int voff, int soff) { return __builtin_amdgcn_raw_buffer_load_b128(rs, voff, soff, 16); }
__device__ __forceinline__ void st_sc1(rsrc_t rs, int voff, int soff, u32x4 v) { __builtin_amdgcn_raw_buffer_store_b128(v, rs, voff + soff, 0, 16); }
__device__ __forceinline__ u32x4 tr_frag(LAS char* a0, LAS char* a1) {
  const s16x4 lo = __builtin_amdgcn_ds_read_tr16_b64_v4i16((LAS s16x4*)a0), hi = __builtin_amdgcn_ds_read_tr16_b64_v4i16((LAS s16x4*)a1);
  const u32x2 l = __builtin_bit_cast(u32x2, lo), h = __builtin_bit_cast(u32x2, hi);
  return u32x4{l[0], l[1], h[0], h[1]};
}
#define SCHED() __builtin_amdgcn_sched_barrier(0)
#define FENCE() asm volatile("" ::: "memory")
#define WAIT_VM(n) asm volatile("s_waitcnt vmcnt(" #n ")" ::: "memory")
#define BARRIER() do { asm volatile("s_waitcnt lgkmcnt(0)" ::: "memory"); __builtin_amdgcn_s_barrier(); FENCE(); } while (0)

__device__ __forceinline__ f32x2 cmul(f32x2 a, f32x2 b) {
  const f32x2 t = f32x2{a[0], a[0]} * b;
  return __builtin_elementwise_fma(f32x2{a[1], a[1]}, f32x2{-b[1], b[0]}, t);
}
__device__ __forceinline__ f32x2 cpow(float a2, float bq, float l) {
  const float m = __builtin_amdgcn_exp2f(a2 * l);
  const float ph = __builtin_amdgcn_fractf(bq * l);
  return f32x2{__builtin_amdgcn_cosf(ph), __builtin_amdgcn_sinf(ph)} * m;
}
__device__ __forceinline__ u32 pk2(f32x2 a) { return pk(a[0], a[1]); }
__device__ __forceinline__ f32x2 up(u32 v) { return f32x2{bflo(v), bfhi(v)}; }
__device__ __forceinline__ u32 ln_stat(float sum, float sq) {
  const float m = sum * (1.f / D);
  const float var = fmaxf(sq * (1.f / D) - m * m, 0.f);
  return pk(m, __builtin_amdgcn_sqrtf(rb(rb(var) + 1e-5f)));
}
__device__ __forceinline__ u32 tagdiff(u32x4 v, u32 ta, u32 tb) { return (v[1] ^ ta) | (v[3] ^ tb); }
__device__ __forceinline__ u32x4 poll16(rsrc_t rs, int voff, int soff, u32 ta, u32 tb) {
  u32x4 v;
  int spins = 0;
  for (;;) {
    FENCE();
    v = ld_sc1(rs, voff, soff);
    if (tagdiff(v, ta, tb) == 0u || ++spins >= SPIN_CAP) break;
    if (POLL_SLEEP) __builtin_amdgcn_s_sleep(POLL_SLEEP);
  }
  FENCE();
  return v;
}
__device__ __forceinline__ bool bad4(const u32x4 (&v)[4], u32 ta, u32 tb) {
  return (tagdiff(v[0], ta, tb) | tagdiff(v[1], ta, tb) | tagdiff(v[2], ta, tb) | tagdiff(v[3], ta, tb)) != 0u;
}
__device__ __forceinline__ void retry4(u32x4 (&v)[4], rsrc_t rs, int voff, int step, u32 ta, u32 tb) {
#pragma unroll 1
  for (int spins = 0; spins < SPIN_CAP; ++spins) {
    __builtin_amdgcn_s_sleep(S1_SLEEP);
    FENCE();
#pragma unroll
    for (int i = 0; i < 4; ++i) if (tagdiff(v[i], ta, tb)) v[i] = ld_sc1(rs, voff, i * step);
    if (!__any(bad4(v, ta, tb))) break;
  }
}
__device__ __forceinline__ void fold_cols(const float (&v)[16], LAS float* dst, int lane) {
  float u[8]; f32x4 t;
#pragma unroll
  for (int k = 0; k < 8; ++k) u[k] = fold32(v[k], v[k + 8]);
#pragma unroll
  for (int k = 0; k < 4; ++k) { const float a = fold16(u[k], u[k + 4]); t[k] = a + dpp<0x128>(a); }
  if (!(lane & 8)) *(LAS f32x4*)(dst + (lane & 7) * 16 + (lane >> 4) * 4) = t;
}
__device__ __forceinline__ f32x2 sum4(LAS const float* src, int stride) {
  return (*(LAS const f32x2*)src + *(LAS const f32x2*)(src + stride)) + (*(LAS const f32x2*)(src + 2 * stride) + *(LAS const f32x2*)(src + 3 * stride));
}
__device__ __forceinline__ u32 silu_bf(u32 c) {
  const float f = bflo(c), s = f * __builtin_amdgcn_rcpf(1.f + __builtin_amdgcn_exp2f(f * -LOG2E));
  return pk(s, s);
}
template <int CTRL> __device__ __forceinline__ void gv_row(u32x4 v, u32 sp, float (&acc)[8]) {
  const u32 sb = __builtin_amdgcn_mov_dpp(sp, CTRL, 0xf, 0xf, true), al = sb & 0xffffu, ah = sb & 0xffff0000u;
#pragma unroll
  for (int q = 0; q < 4; ++q) { acc[2 * q] = dot2(v[q], al, acc[2 * q]); acc[2 * q + 1] = dot2(v[q], ah, acc[2 * q + 1]); }
}
__device__ __forceinline__ f32x2 gv_fold(const float (&acc)[8]) {
  const float m0 = fold16(fold32(acc[0], acc[4]), fold32(acc[2], acc[6])), m1 = fold16(fold32(acc[1], acc[5]), fold32(acc[3], acc[7]));
  return f32x2{m0 + dpp<0x128>(m0), m1 + dpp<0x128>(m1)};
}
#define TOUCH(dst, voff, base) asm volatile("global_load_dword %0, %1, %2" : "=v"(dst) : "v"(voff), "s"(base))

__device__ __forceinline__ void quad_arrive(LAS char* f, int i, u32 n) {
  asm volatile("s_waitcnt lgkmcnt(0)" ::: "memory");
  *(volatile LAS u32*)(f + i * 4) = n;
}
__device__ __forceinline__ void quad_wait(LAS char* f, u32 n) {
  for (;;) {
    const u32x4 v = *(volatile LAS u32x4*)f;
    if (__builtin_amdgcn_readfirstlane(min(min(v[0], v[1]), min(v[2], v[3]))) >= n) break;
    __builtin_amdgcn_s_sleep(1);
  }
  FENCE();
}
__device__ __forceinline__ int only(bool c, int off) { return off | (c ? 0 : OOB); }
#define PIN(x) asm volatile("" : "+v"(x))

struct Params {
  const u16 *x, *Are, *Aim, *Bre, *Bim, *Cre, *Cim, *ldt, *dskip, *w1, *w2, *c, *wmod;
  u16* out;
  char* ws;
};

typedef u64 u64x8 __attribute__((ext_vector_type(8)));
typedef u64 u64x4 __attribute__((ext_vector_type(4)));
typedef u64 u64x2 __attribute__((ext_vector_type(2)));
#define PINS(x) asm volatile("" : "+s"(x))

extern "C" __global__ void __launch_bounds__(512, 2) fused_block(Params args) {
  extern __shared__ __attribute__((aligned(16))) char smem_[];
  LAS char* smem = (LAS char*)smem_;
  u64x8 ka; u64x4 kb; u64x2 kc; u64 kd;
  asm volatile("s_load_dwordx16 %0, %4, 0x0\n\ts_load_dwordx8 %1, %4, 0x40\n\ts_load_dwordx4 %2, %4, 0x60\n\ts_load_dwordx2 %3, %4, 0x70"
               : "=&s"(ka), "=&s"(kb), "=&s"(kc), "=&s"(kd) : "s"(__builtin_amdgcn_kernarg_segment_ptr()));
  const int tid = threadIdx.x, lane = tid & 63, w = __builtin_amdgcn_readfirstlane(tid >> 6), lw = w & 3;
  {
    u64 pc; int r0, r1;
    asm volatile("s_getpc_b64 %0\n\ts_mov_b32 %1, fused_block@rel32@lo+4\n\ts_mov_b32 %2, fused_block_end@rel32@lo+12" : "=s"(pc), "=s"(r0), "=s"(r1));
    dma4(mkrs((const void*)(pc + (long long)r0), r1 - r0), LD_MISC, (lane & 1) * 4096, w * 8192 + (blockIdx.x >> 3) * 128);
  }
  SCHED();
  STAMP_DECL
  STAMP(0);
  u32x4 xv[4], wm[8];
  u32 rar, rai, rbr, rbi, rcr, rci, c0, ldt2, dsk;
  u64 epoch;
  Params p;
  {
    const int b = blockIdx.x;
    int d2 = (b & 7) * 128 + (b >> 3) * 4, role = w >> 1;
    int sxa = (w & 1) * (1024 * 1024) + b * 4096, sxb = (b >> 3) * 32768 + (w & 1) * 16384 + (b & 7) * 128;
    PINS(sxa); PINS(sxb);
    int sx = role == 2 ? sxa : sxb, v16 = lane * 16, vp = d2 * NS + tid * 2, vt = (lane & 15) * 1024;
    PINS(d2); PINS(role); PINS(sx); PIN(v16); PIN(vp); PIN(vt);
    SCHED();
    asm volatile("s_waitcnt lgkmcnt(0)" : "+s"(ka), "+s"(kb), "+s"(kc), "+s"(kd));
    p.x = (const u16*)ka[0]; p.Are = (const u16*)ka[1]; p.Aim = (const u16*)ka[2]; p.Bre = (const u16*)ka[3]; p.Bim = (const u16*)ka[4];
    p.Cre = (const u16*)ka[5]; p.Cim = (const u16*)ka[6]; p.ldt = (const u16*)ka[7]; p.dskip = (const u16*)kb[0]; p.w1 = (const u16*)kb[1];
    p.w2 = (const u16*)kb[2]; p.c = (const u16*)kb[3]; p.wmod = (const u16*)kc[0]; p.out = (u16*)kc[1]; p.ws = (char*)kd;
    asm volatile("s_cmp_lt_u32 %21, 2\n\ts_cbranch_scc0 1f\n\t"
                 "global_load_ushort %0, %10, %13 nt\n\tglobal_load_ushort %1, %10, %14 nt\n\tglobal_load_ushort %2, %10, %15 nt\n\t"
                 "global_load_ushort %3, %10, %16 nt\n\tglobal_load_ushort %4, %10, %17 nt\n\tglobal_load_ushort %5, %10, %18 nt\n\ts_branch 3f\n"
                 "1:\n\ts_cmp_eq_u32 %21, 2\n\ts_cbranch_scc0 2f\n\t"
                 "buffer_load_dwordx4 %6, %11, %19, %20 offen nt\n\tbuffer_load_dwordx4 %7, %11, %19, %20 offen offset:1024 nt\n\t"
                 "buffer_load_dwordx4 %8, %11, %19, %20 offen offset:2048 nt\n\tbuffer_load_dwordx4 %9, %11, %19, %20 offen offset:3072 nt\n\ts_branch 3f\n"
                 "2:\n\tbuffer_load_dword %12, %19, %20 offen lds\n"
                 "3:"
                 : "=&v"(rar), "=&v"(rai), "=&v"(rbr), "=&v"(rbi), "=&v"(rcr), "=&v"(rci), "=&v"(xv[0]), "=&v"(xv[1]), "=&v"(xv[2]), "=&v"(xv[3])
                 : "v"(vp), "v"(v16), "v"(vt), "s"(p.Are), "s"(p.Aim), "s"(p.Bre), "s"(p.Bim), "s"(p.Cre), "s"(p.Cim), "s"(mkrs(p.x, L * D * 2)), "s"(sx), "s"(role)
                 : "scc", "memory");
    SCHED();
    asm volatile("s_load_dwordx2 %0, %3, 0x0\n\ts_load_dword %1, %4, %6\n\ts_load_dword %2, %5, %6"
                 : "=&s"(epoch), "=&s"(ldt2), "=&s"(dsk) : "s"(p.ws + WS_EPOCH), "s"(p.ldt), "s"(p.dskip), "s"(d2) : "memory");
    SCHED();
    int t0, t1, t2;
    const int sw = (b >> 4) * 32 * 6144 + ((w & 1) * 16 + ((b + (b >> 4)) & 15)) * 128, n2 = w == 4 ? D * 3072 * 2 : 0;
    const int vw = (lane >> 3) * 6144 + (lane & 7) * 16, vw2 = vw + 4096, vc = ((b >> 4) * 32 + (lane >> 3) + 8 * (lane & 3)) * 2;
    asm volatile("s_cmp_eq_u32 %18, 2\n\ts_cbranch_scc0 1f\n\t"
                 "global_load_ushort %8, %14, %19\n\t"
                 "s_add_i32 %9, %17, 0xc000\n\ts_add_i32 %10, %17, 0x18000\n\ts_add_i32 %11, %17, 0x24000\n\t"
                 "buffer_load_dwordx4 %0, %12, %15, %17 offen nt\n\tbuffer_load_dwordx4 %1, %12, %15, %9 offen nt\n\t"
                 "buffer_load_dwordx4 %2, %12, %15, %10 offen nt\n\tbuffer_load_dwordx4 %3, %12, %15, %11 offen nt\n\t"
                 "buffer_load_dwordx4 %4, %13, %16, %17 offen nt\n\tbuffer_load_dwordx4 %5, %13, %16, %9 offen nt\n\t"
                 "buffer_load_dwordx4 %6, %13, %16, %10 offen nt\n\tbuffer_load_dwordx4 %7, %13, %16, %11 offen nt\n"
                 "1:"
                 : "=&v"(wm[0]), "=&v"(wm[1]), "=&v"(wm[2]), "=&v"(wm[3]), "=&v"(wm[4]), "=&v"(wm[5]), "=&v"(wm[6]), "=&v"(wm[7]), "=&v"(c0),
                   "=&s"(t0), "=&s"(t1), "=&s"(t2)
                 : "v"(vw), "v"(vw2), "v"(vc), "s"(mkrs(p.wmod, D * 3072 * 2)), "s"(mkrs(p.wmod, n2)), "s"(sw), "s"(role), "s"(p.c)
                 : "scc", "memory");
  }
  *(LAS u32*)(smem + LD_FLG + w * 4) = 0u;
  asm volatile("s_waitcnt lgkmcnt(0)" : "+s"(epoch), "+s"(ldt2), "+s"(dsk) :: "memory");
  BARRIER();
  epoch += 0x9E3779B97F4A7C15ull;
  const u32 ta = (u32)epoch, tb = (u32)(epoch >> 32);
#define ROLES() int b = blockIdx.x; asm volatile("" : "+s"(b)); \
  const int xcd = b & 7, j = b >> 3, pair = xcd * 32 + j, d0 = pair * 2; \
  const bool helper = j < 4; \
  const int rbk = (xcd >> 1) * 4 + (j & 3), cbk = (xcd & 1) * 7 + ((j - 4) >> 2), tbk = xcd * 4 + (j & 3), db = ((j >> 2) + 3 - (int)((u64)p.w2 >> 7)) & 7; \
  const rsrc_t rws = __builtin_amdgcn_make_buffer_rsrc(p.ws, 0, WS_END, 0x00020000)

  if (w < 4) {
  ROLES();
  const int pch = w >> 1, pn = (w & 1) * 64 + lane;
  {
    LAS char* flg = smem + LD_FLG;
    *(LAS u32x4*)(smem + LD_T + ((lane >> 1) & 7) * TCOPY + 4096 + (lane & 1) * 16) = u32x4{0u, 0u, 0u, 0u};
    *(LAS u32*)(smem + LD_KD + KD_ZERO * 4) = 0u;
    int ga[11], ka[4];
#pragma unroll
    for (int i = 0; i < 11; ++i) {
      const int l = 2047 - (tid * 8 + i);
      const int bits = (int)(pk((float)l, 0.f) & 0xffffu);
      const int idx = l < 128 ? l : bits - 0x4300 + 128;
      ga[i] = LD_KD + 4 * (l < 0 ? KD_ZERO : idx + (idx >> 3));
    }
    {
      const int e = lane & 15, g = lane >> 4;
      const u32 cfg = e < 8 ? (w == 0 ? 0x100004u : w == 1 ? 0x080004u : 0x022001u) : (w == 0 ? 0x100004u : w == 1 ? 0x044002u : 0x088003u);
      const int sh = cfg & 15, base = 256 * w + ((cfg >> 8) & 0xff) * (e >> sh) + (e & ((1 << sh) - 1));
#pragma unroll
      for (int r = 0; r < 4; ++r) { const int idx = base + (int)(cfg >> 16) * (4 * g + r); ka[r] = LD_KD + 4 * (idx + (idx >> 3)); PIN(ka[r]); }
    }
    STAMP(1);
    asm volatile("s_waitcnt vmcnt(0)" : "+v"(rar), "+v"(rai), "+v"(rbr), "+v"(rbi), "+v"(rcr), "+v"(rci) :: "memory");
    STAMP(2);
    SCHED();
    volatile LAS u32* P = (LAS u32*)(smem + LD_P) + pch * 64 * PROW + pn;
    const float dt = rb(__builtin_amdgcn_exp2f((pch ? bfhi(ldt2) : bflo(ldt2)) * LOG2E));
    const float a2 = (dt * bflo(rar)) * LOG2E, bq = (dt * bflo(rai)) * 0.15915494309189535f;
    const f32x2 z1 = cpow(a2, bq, 1.f), z8 = cpow(a2, bq, 8.f), z256 = cpow(a2, bq, 256.f);
    const f32x2 z16c = cmul(z8, z8) * f32x2{1.f, -1.f}, z512 = cmul(z256, z256), z768 = cmul(z512, z256);
    f32x2 u = cpow(a2, bq, 1024.f), q = {1.f, 0.f};
    f32x2 r = cmul(f32x2{bflo(rcr), -bflo(rci)}, f32x2{bflo(rbr), -bflo(rbi)} * dt);
#pragma unroll
    for (int e = 0; e < 16; ++e) {
      const f32x2 rn = cmul(r, z16c), qn = cmul(q, z1);
      P[e * PROW] = pk2(r);
      P[(16 + e) * PROW] = pk2(q);
      if (!(e & 1)) P[(32 + e / 2) * PROW] = pk2(cmul(q, z256));
      if (!(e & 3)) { P[(40 + e / 4) * PROW] = pk2(cmul(q, z512)); P[(44 + e / 4) * PROW] = pk2(cmul(q, z768)); }
      if ((e & 3) == 1) { P[(48 + e / 2) * PROW] = pk2(u); P[(49 + e / 2) * PROW] = pk2(cmul(u, z8)); }
      if ((e & 3) == 3) u = cmul(u, z256);
      r = rn; q = qn;
    }
    P[56 * PROW] = pk2(u);
    STAMP(3);
    quad_arrive(flg, w, 1u);
#pragma unroll
    for (int i = 0; i < 11; ++i) PIN(ga[i]);
    quad_wait(flg, 1u);
    STAMP(4);
    if (w < 3) {
      LAS const u32* fb = (LAS const u32*)(smem + LD_P) + (lane & 15) * PROW + ((lane >> 4) & 1) * 64 + (lane >> 5) * 32;
      u32x4 fa[16], fc[16];
      f32x4 acc[2] = {{0.f, 0.f, 0.f, 0.f}, {0.f, 0.f, 0.f, 0.f}};
#pragma unroll
      for (int ks = 0; ks < 22; ++ks) {
        if (ks < 16) {
          const int o = (ks & 1) * 64 * PROW + (ks >> 1) * 4;
          fa[ks] = *(LAS const u32x4*)(fb + o); fc[ks] = *(LAS const u32x4*)(fb + (w + 1) * 16 * PROW + o);
        }
        SCHED();
        if (ks >= 6) acc[ks & 1] = mfma16(fa[ks - 6], fc[ks - 6], acc[ks & 1]);
        SCHED();
      }
#pragma unroll
      for (int r4 = 0; r4 < 4; ++r4) *(LAS u32*)(smem + ka[r4]) = pk(acc[0][r4], acc[1][r4]);
    }
    STAMP(5);
    quad_arrive(flg, w, 2u);
    quad_wait(flg, 2u);
    STAMP(6);
    u32 e[11];
#pragma unroll
    for (int i = 0; i < 11; ++i) e[i] = *(LAS const u32*)(smem + ga[i]);
#pragma unroll
    for (int c = 0; c < 2; ++c)
#pragma unroll
      for (int s = 0; s < 4; ++s) {
        u32x4 o;
#pragma unroll
        for (int z = 0; z < 4; ++z) o[z] = __builtin_amdgcn_perm(e[s + 2 * z + 1], e[s + 2 * z], c ? 0x07060302u : 0x05040100u);
        *(LAS u32x4*)(smem + LD_T + (c * 4 + s) * TCOPY + tid * 16) = o;
      }
    quad_arrive(flg, w, 3u);
  }
  {
    const int r = lane & 15, g = lane >> 4, ch = w >> 1, half = w & 1;
    LAS const char* Tb = smem + LD_T + (ch * 4 + ((3 - r) & 3)) * TCOPY + 2 * (2028 - 4 * (r >> 2) + 8 * g - 128 * half) - 7 * 512 - 192;
    LAS const char* Ub = smem + LD_U + ch * 4608 + 2 * (256 + 16 * r - 16 + 8 * g - 128 * half) - 192;
    LAS f32x4* red = (LAS f32x4*)(smem + (ch ? LD_RED1 : LD_RED0)) + half * 512 + (lane ^ ((lane >> 4) << 2));
    PIN(Ub); PIN(red);
    f32x4 acc[8];
#pragma unroll
    for (int n = 0; n < 8; ++n) acc[n] = f32x4{0.f, 0.f, 0.f, 0.f};
    u32x4 uf[2][8], tf[4][8];
    quad_wait(smem + LD_FLG, 3u);
#define LD_TH(e, a, hh) do { const u32x2 l_ = *(volatile LAS const u32x2*)(Tb + 64 * (3 - (e)) + 512 * (7 - (a)) + 8 * (hh)); tf[e][a][2 * (hh)] = l_[0]; tf[e][a][2 * (hh) + 1] = l_[1]; } while (0)
#pragma unroll
    for (int e = 0; e < TF_PRE; ++e)
#pragma unroll
      for (int a = 0; a < 8; ++a) { LD_TH(e, a, 0); LD_TH(e, a, 1); }
#define LD_UF(n, e) uf[(e) & 1][n] = *(LAS const u32x4*)(Ub + 64 * (3 - (e)) + 512 * (n))
#define CM(e, a, m) do { SCHED(); acc[(a) + (m)] = mfma16(tf[e][a], uf[(e) & 1][m], acc[(a) + (m)]); SCHED(); } while (0)
    STAMP(10);
    u32 ft;
    quad_wait(smem + LD_FLG + 16, FT_AT);
    __builtin_amdgcn_s_sleep(FT_DELAY);
    TOUCH(ft, ((b * 2 + (w & 1)) * 28 + min(lane, 27)) * 128, w < 2 ? p.w1 : p.w2);
    BARRIER();
    STAMP(11);
#pragma unroll
    for (int n = 0; n < 8; ++n) LD_UF(n, 0);
#pragma unroll
    for (int e = 0; e < 3; ++e) {
      int cnt = 0;
#pragma unroll
      for (int m = 0; m < 8; ++m)
#pragma unroll
        for (int a = 0; a + m < 8; ++a) {
          CM(e, a, m);
          if (cnt >= 4 && cnt < 12) LD_UF(cnt - 4, e + 1);
          if (cnt >= 12 && cnt < 28 && e + TF_PRE < 4) LD_TH(e + TF_PRE, (cnt - 12) >> 1, (cnt - 12) & 1);
          ++cnt;
        }
    }
#pragma unroll
    for (int n = 0; n < 8; ++n)
#pragma unroll
      for (int a = 0; a <= n; ++a) { CM(3, a, n - a); if (a == 1) red[(n - 1) * 64] = acc[n - 1]; }
    STAMP(12);
    red[7 * 64] = acc[7];
    BARRIER();
    STAMP(14);
    WAIT_VM(0);
    asm volatile("" :: "v"(ft));
  }
  int f3w = OOB;
  if (helper) {
    const int hb = xcd * 4 + j;
    u32x4 v[16];
    const int rg = tid >> 3;
    LAS float* rd = (LAS float*)(smem + 4096);
    { const u32x4 m = poll16(rws, tid * 16, WS_MF, ta, tb); *(LAS u32x2*)(smem + tid * 8) = u32x2{m[0], m[2]}; }
    BARRIER();
#pragma unroll
    for (int i = 0; i < 16; ++i) v[i] = *(LAS const u32x4*)(smem + LD_WR + i * 4096 + tid * 16);
    float a[16];
#pragma unroll
    for (int e = 0; e < 16; ++e) a[e] = 0.f;
#pragma unroll
    for (int i = 0; i < 16; ++i) {
      const u32 gs = *(LAS const u32*)(smem + (i * 32 + rg) * 4);
      const u32 gl = gs & 0xffffu, gh = gs << 16, sl = gs >> 16, sh = gs & 0xffff0000u;
#pragma unroll
      for (int z = 0; z < 4; ++z) {
        a[4 * z] = dot2(v[i][z], gl, a[4 * z]); a[4 * z + 1] = dot2(v[i][z], sl, a[4 * z + 1]);
        a[4 * z + 2] = dot2(v[i][z], gh, a[4 * z + 2]); a[4 * z + 3] = dot2(v[i][z], sh, a[4 * z + 3]);
      }
    }
    fold_cols(a, rd + w * 128, lane);
    STAMP(18);
    poll16(rws, tid * 64, WS_F2 + (xcd >> 1) * 16384, ta, tb);
    BARRIER();
    STAMP(19);
#pragma unroll
    for (int i = 0; i < 16; ++i) v[i] = ld_sc1(rws, tid * 16, WS_X1 + hb * ST_X1 + (i >> 3) * ST_X1H + (i & 7) * 4096);
    if (w == 0 && hb < 28) {
      const f32x2 s = sum4(rd + lane * 2, 128);
      st_sc1(rws, lane * 16, WS_S12 + hb * 1024, u32x4{__float_as_uint(s[0]), ta, __float_as_uint(s[1] * -LOG2E), tb});
    }
#pragma unroll
    for (int e = 0; e < 16; ++e) a[e] = 0.f;
#pragma unroll
    for (int i = 0; i < 16; ++i)
#pragma unroll
      for (int z = 0; z < 4; ++z) {
        const int o = (i >> 3) * 8 + 2 * z;
        a[o] = dot2(v[i][z], 0x3f803f80u, a[o]); a[o + 1] = dot2(v[i][z], v[i][z], a[o + 1]);
      }
    STAMP(20);
    fold_cols(a, rd + 512 + w * 128, lane);
    BARRIER();
    if (w == 0) {
      const f32x2 s = sum4(rd + 512 + lane * 2, 128);
      const u32 st = ln_stat(s[0], s[1]);
      const u32 st2 = __builtin_amdgcn_mov_dpp(st, 0xB1, 0xf, 0xf, true);
      if (!(lane & 1)) st_sc1(rws, lane * 8, WS_G2 + hb * 512, u32x4{st, ta, st2, tb});
    }
    BARRIER();
    STAMP(22);
    __builtin_amdgcn_s_sleep(H_SLEEP);
  } else {
    const int wt = w & 1, wf = w >> 1;
    const int h = lane >> 5, blk = (lane >> 4) & 1, q = (lane & 15) >> 2, pp = lane & 3;
    int ax[2][2], aw[2][2];
#pragma unroll
    for (int c = 0; c < 2; ++c)
#pragma unroll
      for (int t = 0; t < 2; ++t) {
        const int base = 256 * (8 * h + 4 * t + q) + 16 * ((2 * blk + (pp >> 1)) ^ (2 * h + t)) + 8 * (pp & 1);
        ax[c][t] = LD_XR + base + 64 * ((2 * wt + c) ^ q);
        aw[c][t] = LD_WR + base + 64 * ((2 * wf + c) ^ q);
        PIN(ax[c][t]); PIN(aw[c][t]);
      }
    f32x16 acc[2][2];
#pragma unroll
    for (int a = 0; a < 2; ++a)
#pragma unroll
      for (int c = 0; c < 2; ++c)
#pragma unroll
        for (int r = 0; r < 16; ++r) acc[a][c][r] = 0.f;
    u32x4 xf[2][2], wfr[2][2];
    u32 st[2];
    int ho[4];
#pragma unroll
    for (int z = 0; z < 4; ++z) { ho[z] = WS_H + ((rbk * 4 + wt * 2) * 112 + z * 56 + cbk * 4 + wf * 2) * 1024 + lane * 16; PIN(ho[z]); }
    int fo = only(lane < 8, WS_F3 + ((rbk * 4 + w) * 16 + cbk) * 128 + lane * 16);
    int fo2 = fo + 0x20000;
    PIN(fo); PIN(fo2);
#define RD_X(i, c) xf[(i) & 1][c] = tr_frag(smem + ax[c][0] + ((i) & 15) * 4096, smem + ax[c][1] + ((i) & 15) * 4096)
#define RD_W(i, c) wfr[(i) & 1][c] = tr_frag(smem + aw[c][0] + ((i) & 15) * 4096, smem + aw[c][1] + ((i) & 15) * 4096)
#define MM(i, a, c) do { SCHED(); acc[a][c] = mfma32(wfr[(i) & 1][a], xf[(i) & 1][c], acc[a][c]); SCHED(); } while (0)
    BARRIER();
    STAMP(19);
    RD_W(0, 0); RD_X(0, 0); RD_X(0, 1); RD_W(0, 1);
#pragma unroll
    for (int i = 0; i < 32; ++i) {
      if ((i & 3) != 3) {
        MM(i, 0, 0); RD_W(i + 1, 0); MM(i, 0, 1); RD_X(i + 1, 0); MM(i, 1, 0); RD_X(i + 1, 1); MM(i, 1, 1); RD_W(i + 1, 1);
      } else {
        MM(i, 0, 0); MM(i, 0, 1);
        BARRIER();
        if (i == 31) { st[0] = *(LAS const u32*)(smem + LD_MISC + (wt * 64 + (lane & 31)) * 4); st[1] = *(LAS const u32*)(smem + LD_MISC + (wt * 64 + 32 + (lane & 31)) * 4); }
        MM(i, 1, 0);
        if (i < 31) { RD_W(i + 1, 0); RD_X(i + 1, 0); }
        MM(i, 1, 1);
        if (i < 31) { RD_X(i + 1, 1); RD_W(i + 1, 1); }
      }
    }
    STAMP(21);
    float rstd[2], mr[2];
#pragma unroll
    for (int c = 0; c < 2; ++c) {
      rstd[c] = __builtin_amdgcn_rcpf(bfhi(st[c])) * -LOG2E;
      mr[c] = -bflo(st[c]) * rstd[c];
    }
    STAMP(22);
    LAS const float* s12 = (LAS const float*)(smem + LD_MISC + 512) + wf * 64 + 4 * h;
#pragma unroll
    for (int a = 0; a < 2; ++a)
#pragma unroll
      for (int sp = 0; sp < 2; ++sp) {
        f32x4 s1[2], s2[2];
#pragma unroll
        for (int z = 0; z < 2; ++z) { s1[z] = *(LAS const f32x4*)(s12 + a * 32 + sp * 16 + z * 8); s2[z] = *(LAS const f32x4*)(s12 + 128 + a * 32 + sp * 16 + z * 8); }
#pragma unroll
        for (int c = 0; c < 2; ++c) {
          u32x4 o;
#pragma unroll
          for (int e = 0; e < 4; ++e) {
            const int z = e >> 1, k = (e & 1) * 2;
            const f32x2 av = {acc[a][c][sp * 8 + 2 * e], acc[a][c][sp * 8 + 2 * e + 1]};
            const f32x2 cc = f32x2{s1[z][k], s1[z][k + 1]} * mr[c] + f32x2{s2[z][k], s2[z][k + 1]};
            const f32x2 ar = av * rstd[c] + cc;
            const f32x2 dn = f32x2{__builtin_amdgcn_exp2f(ar[0]), __builtin_amdgcn_exp2f(ar[1])} + 1.f;
            o[e] = pk2(ar * f32x2{__builtin_amdgcn_rcpf(dn[0]), __builtin_amdgcn_rcpf(dn[1])});
          }
          st_sc1(rws, ho[2 * c + a], sp * 1024, o);
          if (a * 4 + sp * 2 + c + 1 == F1_AT) { asm volatile("s_waitcnt vmcnt(%0)" :: "n"(F1_AT - 4) : "memory"); st_sc1(rws, fo, 0, u32x4{ta, ta, tb, tb}); SCHED(); }
        }
        if (a == 0 && sp == 1) FENCE();
      }
    STAMP(23);
    f3w = fo2;
  }

  {
    const int wt = w & 1, kh = w >> 1, h = lane >> 5, tl = lane & 31;
    const int blk = (lane >> 4) & 1, q = (lane & 15) >> 2, pp = lane & 3;
    LAS char* aw[3][2];
#pragma unroll
    for (int c = 0; c < 6; ++c) {
      aw[c >> 1][c & 1] = smem + (c >> 1) * 65536 + kh * 8192 + 128 * (4 * h + q) + 64 * ((c & 1) ^ kh ^ (q >> 1)) + 32 * blk + 8 * pp;
      asm volatile("" : "+v"(aw[c >> 1][c & 1]));
    }
    const rsrc_t ro = __builtin_amdgcn_make_buffer_rsrc(p.out, 0, L * D * 2, 0x00020000);
    int oo[2];
#pragma unroll
    for (int i = 0; i < 2; ++i) {
      const int r = tbk * 64 + (tid >> 3) + 32 * i;
      oo[i] = ((r & ~511) + 128 * ((r >> 1) & 3) + 2 * ((r >> 3) & 63) + (r & 1)) * (D * 2) + db * 128 + (tid & 7) * 16;
      PIN(oo[i]);
    }
    f32x16 acc[2];
#pragma unroll
    for (int r = 0; r < 16; ++r) { acc[0][r] = 0.f; acc[1][r] = 0.f; }
    SCHED();
    WAIT_VM(1);
    STAMP(24);
    st_sc1(rws, f3w, 0, u32x4{ta, ta, tb, tb});
    STAMP(25);
    BARRIER();
    constexpr int NW = G2_NW;
    u32x4 hf[NW], wf[3][2];
#define RD_W2C(i, c) do { LAS char* a_ = aw[w2_unit((i) >> 2) >> 2][c] + (w2_unit((i) >> 2) & 3) * UNIT + ((i) & 3) * 2048; wf[(i) % 3][c] = tr_frag(a_, a_ + 1024); } while (0)
#define RD_W2(i) do { RD_W2C(i, 0); RD_W2C(i, 1); } while (0)
    RD_W2(0); RD_W2(1);
    const int sh = WS_H + ((tbk * 2 + wt) * 112 + 28 * kh) * 1024;
#define LD_H(i) hf[(i) % NW] = ld_sc1(rws, lane * 16 + ((i) & 3) * 1024, sh + (((i) >> 2) + (i) / 28 * 7) * 4096)
    const int f3o = WS_F3 + ((tbk >> 1) * 4 + (tbk & 1)) * 2048 + kh * 896 + ((lane & 1) * 32 + min(lane >> 1, 6)) * 128;
    if (F3_DELAY) __builtin_amdgcn_s_sleep(F3_DELAY);
    poll16(rws, f3o, 0, ta, tb);
    u32x4 f2;
    STAMP(26);
#pragma unroll
    for (int i = 0; i < G2_N0; ++i) LD_H(i);
    SCHED();
    f32x2 xr[8], gb[8];
#pragma unroll
    for (int i = 0; i < 8; ++i) {
      const int pr = 4 * (i >> 1) + 2 * h + (i & 1);
      xr[i] = up(*(LAS const u32*)(smem + LD_MISC + 2048 + w * 2048 + ((tl >> 2) & 1) * 1024 + pr * 64 + (tl >> 3) * 16 + (tl & 3) * 4));
      gb[i] = *(LAS const f32x2*)(smem + LD_MISC + 1536 + (kh * 16 + pr) * 8);
    }
#pragma unroll
    for (int i = 0; i < 8; ++i) PIN(xr[i]);
    LAS f32x4* exw = (LAS f32x4*)(smem + w2_unit(5) * UNIT) + w * 256 + lane;
    LAS f32x4* exr = (LAS f32x4*)(smem + w2_unit(5) * UNIT) + (w ^ 2) * 256 + lane;
    LAS char* otw = smem + w2_unit(6) * UNIT + (wt * 32 + tl) * 144 + kh * 64 + h * 8;
    LAS char* otr = smem + w2_unit(6) * UNIT + (tid >> 3) * 144 + (tid & 7) * 16;
    PIN(exw); PIN(exr); PIN(otw); PIN(otr);
    SCHED();
#define G2_STEP(i) do { \
      SCHED(); acc[0] = mfma32(wf[(i) % 3][0], hf[(i) % NW], acc[0]); SCHED(); \
      if ((i) == 0) STAMP(29); \
      if ((i) == 18) STAMP(30); \
      if ((i) == 42) STAMP(31); \
      if ((i) == 6 || (i) == 18 || (i) == 42) BARRIER(); \
      if ((i) + 2 < 56) RD_W2C((i) + 2, 0); \
      if ((i) == P2) f2 = ld_sc1(rws, f3o, 0x20000); \
      if ((i) > 0 && (i) - 1 + NW < 56) LD_H((i) - 1 + NW); \
      SCHED(); acc[1] = mfma32(wf[(i) % 3][1], hf[(i) % NW], acc[1]); SCHED(); \
      if ((i) + 2 < 56) RD_W2C((i) + 2, 1); \
      asm volatile("" :: "v"(wf[(i) % 3][0]), "v"(wf[(i) % 3][1]), "v"(hf[(i) % NW])); } while (0)
#pragma unroll
    for (int i = 0; i < 29 - NW; ++i) G2_STEP(i);
    if (__builtin_expect(__any(tagdiff(f2, ta, tb) != 0u), 0)) poll16(rws, f3o, 0x20000, ta, tb);
    FENCE();
#pragma unroll
    for (int i = 29 - NW; i < 54; ++i) G2_STEP(i);
    SCHED();
    acc[1] = mfma32(wf[54 % 3][1], hf[54 % NW], acc[1]);
    acc[1] = mfma32(wf[55 % 3][1], hf[55 % NW], acc[1]);
    SCHED();
    acc[0] = mfma32(wf[54 % 3][0], hf[54 % NW], acc[0]);
    acc[0] = mfma32(wf[55 % 3][0], hf[55 % NW], acc[0]);
    SCHED();
    STAMP(27);
#pragma unroll
    for (int qq = 0; qq < 4; ++qq) exw[qq * 64] = f32x4{acc[1][4 * qq], acc[1][4 * qq + 1], acc[1][4 * qq + 2], acc[1][4 * qq + 3]};
    SCHED();
#pragma unroll
    for (int i = 0; i < 8; ++i) { xr[i] += f32x2{acc[0][2 * i], acc[0][2 * i + 1]} * gb[i]; PIN(xr[i]); }
    BARRIER();
    f32x4 oth[4];
#pragma unroll
    for (int qq = 0; qq < 4; ++qq) oth[qq] = exr[qq * 64];
    SCHED();
#pragma unroll
    for (int qq = 0; qq < 4; ++qq)
      *(LAS u32x2*)(otw + qq * 16) = u32x2{pk2(f32x2{oth[qq][0], oth[qq][1]} * gb[2 * qq] + xr[2 * qq]), pk2(f32x2{oth[qq][2], oth[qq][3]} * gb[2 * qq + 1] + xr[2 * qq + 1])};
    BARRIER();
#pragma unroll
    for (int i = 0; i < 2; ++i) __builtin_amdgcn_raw_buffer_store_b128(*(LAS const u32x4*)(otr + i * 32 * 144), ro, oo[i], 0, OUT_AUX);
    STAMP(28);
    STAMP_FLUSH(p.ws, 0);
  }
  return;
  }
  {
    LAS char* flg = smem + LD_FLG + 16;
    u32x4 sg[4];
    u32x2 xg[4];
    ROLES();
    const int go = lw * 4096 + lane * 16;
    const u32x4 qws = mkrs(p.ws, WS_END);
#define POLL_ISSUE() asm volatile("buffer_load_dwordx4 %0, %4, %5, 0 offen sc1\n\tbuffer_load_dwordx4 %1, %4, %5, 0 offen offset:1024 sc1\n\t" \
      "buffer_load_dwordx4 %2, %4, %5, 0 offen offset:2048 sc1\n\tbuffer_load_dwordx4 %3, %4, %5, 0 offen offset:3072 sc1" \
      : "=&v"(sg[0]), "=&v"(sg[1]), "=&v"(sg[2]), "=&v"(sg[3]) : "v"(go + WS_SG), "s"(qws) : "memory")
    int vx[4], vw1[4], vw2[4], so, xo0, xo1, fo, mfo;
    LAS char *ra[2], *rc[2];
    auto addresses = [&]() {
      const int hm = (j - 4) >> 31;
#pragma unroll
      for (int i = 0; i < 4; ++i) {
        const int chk = 16 * ((lane & 15) ^ ((lane >> 4) << 2) ^ i);
        vx[i] = (lane >> 4) * 256 + chk;
        vw1[i] = (lane >> (4 + hm)) * (F * 2) + ((chk & ~hm) | ((lane & 7) * 16 & hm)) + i * ((4 + (4 & hm)) * F * 2 - 1024);
        vw2[i] = (lane >> 3) * 1024 + db * 128 + 16 * ((lane & 7) ^ (((lane >> 4) & 1) << 2)) + i * 7168;
        PIN(vx[i]); PIN(vw1[i]); PIN(vw2[i]);
      }
      const int g = (lane >> 1) & 3;
      ra[0] = smem + LD_RED0 + (2 * lw * 64 + 16 * g + ((lane >> 3) ^ (4 * g))) * 16 + 8 * (lane & 1);
      ra[1] = smem + LD_RED0 + (2 * lw * 64 + 16 * g + ((lane >> 3) ^ (4 * g) ^ 8)) * 16 + 8 * (lane & 1);
      rc[0] = ra[0] + (LD_RED1 - LD_RED0); rc[1] = ra[1] + (LD_RED1 - LD_RED0);
      so = WS_X1S + (lw * 4 + (lane >> 4)) * ST_X1S + d0 * 256 + (lane & 15) * 16;
      xo0 = WS_X1 + (lw * 8 + (lane >> 3)) * ST_X1 + pair * 128 + (lane & 7) * 16; xo1 = xo0 + ST_X1H;
      fo = only(lane < 4, WS_F2 + (lw * 256 + pair) * 64 + lane * 16);
      mfo = only((tid >> 1) == 248, WS_MF + (~lane & 1) * 4096 + pair * 16);
      PIN(mfo); PIN(ra[0]); PIN(ra[1]); PIN(rc[0]); PIN(rc[1]); PIN(so); PIN(xo0); PIN(xo1); PIN(fo);
      SCHED();
    };
    f32x2 xf[4][2], hf[4][2], ca[4][2];
    u32x4 mv1, mv2;
    auto coefficients = [&]() {
      __builtin_amdgcn_s_setprio(2);
      STAMP(7);
      if (__builtin_expect(__any(bad4(sg, ta, tb)), 0)) retry4(sg, rws, go + WS_SG, 1024, ta, tb);
      float rs[4][2];
#pragma unroll
      for (int i = 0; i < 4; ++i)
#pragma unroll
        for (int e = 0; e < 2; ++e) { rs[i][e] = __builtin_amdgcn_rcpf(bfhi(sg[i][2 * e])); PIN(rs[i][e]); }
      if (lw >= 2) { WAIT_VM(4); quad_arrive(flg, lw, 3u); }
      quad_wait(flg, 3u);
      const u32x4 mv0 = *(LAS const u32x4*)(smem + LD_MV);
      mv1 = *(LAS const u32x4*)(smem + LD_MV + 32); mv2 = *(LAS const u32x4*)(smem + LD_MV + 48);
      if (lw < 2) {
#pragma unroll
        for (int i = 0; i < 4; ++i) xg[i] = *(LAS const u32x2*)(smem + LD_XT + lw * 2048 + i * 512 + lane * 8);
      }
      const f32x2 sa = up(mv0[0]), g1 = up(mv0[3]);
#pragma unroll
      for (int i = 0; i < 4; ++i)
#pragma unroll
        for (int e = 0; e < 2; ++e) {
          ca[i][e] = g1 * rs[i][e];
          hf[i][e] = sa - ca[i][e] * bflo(sg[i][2 * e]);
          PIN(ca[i][e]); PIN(hf[i][e]);
        }
      STAMP(8);
    };
    if (lw >= 2) {
      addresses();
      const u32x4 qx = mkrs(p.x, L * D * 2);
      quad_arrive(flg, lw, 1u);
#pragma unroll
      for (int i = 0; i < 8; ++i) {
        if (i == G_EARLY) quad_wait(flg, 1u);
        if (i == G_SIG) *(volatile LAS u32*)(flg + lw * 4) = 2u;
        if (i == G_SG) POLL_ISSUE();
        dma4(qx, LD_XT + (lw & 1) * 2048 + i * 256, lane * 1024 + pair * 4, ((lw & 1) * 8 + i) * 65536);
        if (i < G_SG) WAIT_VM(2);
      }
      if (XG_DELAY) __builtin_amdgcn_s_sleep(XG_DELAY);
      STAMP(3);
      const int xo = WS_XG + ((lw & 1) * 128 + (lane >> 1)) * ST_XG + pair * 32 + (lane & 1) * 16;
      u32x4 xe[4];
#pragma unroll
      for (int i = 0; i < 4; ++i) xe[i] = ld_sc1(rws, xo, i * 32 * ST_XG);
      asm volatile("s_waitcnt vmcnt(%4)" : "+v"(sg[0]), "+v"(sg[1]), "+v"(sg[2]), "+v"(sg[3]) : "n"(12 - G_SG) : "memory");
      coefficients();
      if (__builtin_expect(__any(bad4(xe, ta, tb)), 0)) retry4(xe, rws, xo, 32 * ST_XG, ta, tb);
#pragma unroll
      for (int i = 0; i < 4; ++i) xg[i] = u32x2{xe[i][0], xe[i][2]};
    } else {
      const int gcb = (b + (b >> 4)) & 15;
      *(LAS u32x4*)(smem + LD_U + (lane >> 5) * 4608 + (lane & 31) * 16) = u32x4{0u, 0u, 0u, 0u};
      int so_ = only(!(lane & 31), WS_SG + lw * 8192 + b * 32 + (lane >> 5) * 16);
      int mo_ = only(!(lane & 8), WS_MG + (b >> 4) * ST_MG + ((lw * 16 + gcb) * 32 + (lane >> 4) * 8 + (lane & 7)) * 16);
      PIN(so_); PIN(mo_);
      __builtin_amdgcn_s_setprio(3);
      STAMP(1);
      LAS char* xw = smem + LD_XT + 4096;
      asm volatile("s_waitcnt vmcnt(9)" : "+v"(xv[0]), "+v"(xv[1]), "+v"(xv[2]), "+v"(xv[3]) :: "memory");
      if (lw) {
#pragma unroll
        for (int i = 0; i < 4; ++i) *(LAS u32x4*)(xw + i * 1056 + lane * 16) = xv[i];
      }
      float s[4], q[4];
#pragma unroll
      for (int i = 0; i < 4; ++i) {
        s[i] = 0.f; q[i] = 0.f;
#pragma unroll
        for (int z = 0; z < 4; ++z) { s[i] = dot2(xv[i][z], 0x3f803f80u, s[i]); q[i] = dot2(xv[i][z], xv[i][z], q[i]); }
      }
      const u32 st = ln_stat(row_sum(fold16(fold32(s[0], s[2]), fold32(s[1], s[3]))), row_sum(fold16(fold32(q[0], q[2]), fold32(q[1], q[3]))));
      const auto sw = __builtin_amdgcn_permlane16_swap(st, st, false, false);
      st_sc1(rws, so_, 0, u32x4{sw[0], ta, sw[1], tb});
      STAMP(2);
      auto gv_tile = [&](int z, u32 sp) {
        float acc[8] = {0.f, 0.f, 0.f, 0.f, 0.f, 0.f, 0.f, 0.f};
        gv_row<0x00>(wm[4 * z], sp, acc); gv_row<0x55>(wm[4 * z + 1], sp, acc); gv_row<0xAA>(wm[4 * z + 2], sp, acc); gv_row<0xFF>(wm[4 * z + 3], sp, acc);
        const f32x2 m = gv_fold(acc);
        st_sc1(rws, mo_ + z * 16384, 0, u32x4{__float_as_uint(m[0]), ta, __float_as_uint(m[1]), tb});
        SCHED();
      };
      if (lw) {
        u32 a[8][2];
#pragma unroll
        for (int i = 0; i < 8; ++i) {
          LAS const u32* src = (LAS const u32*)(xw + (lane & 1) * 2112 + i * 128 + (lane >> 1) * 4);
          a[i][0] = src[0]; a[i][1] = src[264];
        }
#pragma unroll
        for (int i = 0; i < 8; ++i) st_sc1(rws, WS_XG + b * ST_XG + lane * 16 + (i >> 2) * 4096, (i & 3) * 1024, u32x4{a[i][0], ta, a[i][1], tb});
      }
      asm volatile("s_cmp_lg_u32 %5, 0\n\ts_cbranch_scc1 1f\n\ts_waitcnt vmcnt(5)\n1:\n\ts_waitcnt vmcnt(13)"
                   : "+v"(c0), "+v"(wm[0]), "+v"(wm[1]), "+v"(wm[2]), "+v"(wm[3]) : "s"(lw) : "scc", "memory");
      const u32 sp = silu_bf(c0);
      gv_tile(0, sp);
      asm volatile("s_cmp_lg_u32 %4, 0\n\ts_cbranch_scc1 1f\n\ts_waitcnt vmcnt(2)\n1:\n\ts_waitcnt vmcnt(10)"
                   : "+v"(wm[4]), "+v"(wm[5]), "+v"(wm[6]), "+v"(wm[7]) : "s"(lw) : "scc", "memory");
      if (!lw) gv_tile(1, sp);
      if (PUB_WAIT) WAIT_VM(1);
      __builtin_amdgcn_s_setprio(0);
      quad_arrive(flg, lw, 2u);
      STAMP(4);
      addresses();
      quad_wait(flg, 2u);
      const int mo = WS_MG + (lane & 15) * ST_MG + ((((lane >> 4) & (2 * lw + 1)) + 2 * lw) * 256 + (pair >> 5) * 32 + (pair & 3) * 8 + ((pair >> 2) & 7)) * 16;
      u32x4 mg;
      asm volatile("buffer_load_dwordx4 %0, %1, %2, 0 offen sc1" : "=v"(mg) : "v"(mo), "s"(qws) : "memory");
      POLL_ISSUE();
      asm volatile("s_waitcnt vmcnt(4)" : "+v"(mg) :: "memory");
      if (__builtin_expect(__any(tagdiff(mg, ta, tb) != 0u), 0)) {
        asm volatile("s_waitcnt vmcnt(0)" : "+v"(sg[0]), "+v"(sg[1]), "+v"(sg[2]), "+v"(sg[3]) :: "memory");
        mg = poll16(rws, mo, 0, ta, tb);
      }
      const u32 mb = pk(row_sum(__uint_as_float(mg[0])), row_sum(__uint_as_float(mg[2])));
      *(LAS u32x2*)(smem + LD_MV + lw * 32 + (lane >> 4) * 8) = u32x2{mb, pk2(up(mb) + 1.f)};
      quad_arrive(flg, lw, 3u);
      asm volatile("s_waitcnt vmcnt(0)" : "+v"(sg[0]), "+v"(sg[1]), "+v"(sg[2]), "+v"(sg[3]) :: "memory");
      coefficients();
    }
    const u32x4 qw1 = mkrs(p.w1, D * F * 2), qw2 = mkrs(p.w2, D * F * 2);
    const int sw2 = ((lw >> 1) * 896 + (lw & 1) * 64) * 1024;
    auto w2_stage = [&](int u) { dma4k(qw2, w2_unit(u) * UNIT + lw * 4096, vw2, sw2 + w2_row(u) * 1024); };
    const int wstep = (helper ? 32 : 16) * F * 2, wbase = helper ? min(tbk, 27) * 128 : cbk * 256;
    auto w1_stage = [&](int s) { dma4k(qw1, LD_WR + (s & 3) * UNIT + lw * 4096, vw1, (s * 4 + lw) * wstep + wbase); };
    auto x_stage = [&](int s) { dma4k_sc1(qws, LD_XR + (s & 3) * UNIT + lw * 4096, vx, WS_X1S + rbk * ST_X1S + s * 16384 + lw * 4096); };
    *(volatile LAS u32*)(flg + lw * 4) = 4u;
    STAMP(9);
#pragma unroll
    for (int i = 0; i < 4; ++i) {
#pragma unroll
      for (int e = 0; e < 2; ++e) { xf[i][e] = up(xg[i][e]); hf[i][e] += xf[i][e] * ca[i][e]; }
      LAS u32* U = (LAS u32*)(smem + LD_U + 512 + lw * 1024 + i * 256) + lane;
      U[0] = pk(hf[i][0][0], hf[i][1][0]);
      U[4608 / 4] = pk(hf[i][0][1], hf[i][1][1]);
    }
    STAMP(10);
    BARRIER();
    STAMP(11);
    w1_stage(0); w1_stage(1); w1_stage(2); w1_stage(3);
    const f32x2 gav = up(mv1[0]), gcv = up(mv2[1]);
    f32x2 xc[4][2];
    float gd[2] = {gav[0] * bflo(dsk), gav[1] * bfhi(dsk)}, n0 = bflo(mv2[2]) * (-1.f / LOG2E), n1 = bfhi(mv2[2]) * (-1.f / LOG2E);
    u32 k0 = __builtin_amdgcn_perm(mv1[2], mv2[1], 0x05040100u), k1 = __builtin_amdgcn_perm(mv1[2], mv2[1], 0x07060302u);
    PIN(gd[0]); PIN(gd[1]); PIN(n0); PIN(n1); PIN(k0); PIN(k1);
    const u32x4 mfv = {lane & 1 ? k0 : __float_as_uint(n0), ta, lane & 1 ? k1 : __float_as_uint(n1), tb};
    st_sc1(rws, mfo, 0, mfv);
#pragma unroll
    for (int i = 0; i < 4; ++i)
#pragma unroll
      for (int c = 0; c < 2; ++c) {
        float a0 = __builtin_fmaf(hf[i][0][c], gd[c], xf[i][0][c]), a1 = __builtin_fmaf(hf[i][1][c], gd[c], xf[i][1][c]);
        PIN(a0); PIN(a1);
        xc[i][c] = f32x2{a0, a1};
      }
    STAMP(12);
    SCHED();
    u32x4 wr[20];
    auto wr_load = [&](bool on) {
      const rsrc_t rw2 = __builtin_amdgcn_make_buffer_rsrc((void*)p.w2, 0, on ? D * F * 2 : 0, 0x00020000);
#pragma unroll
      for (int i = 0; i < 20; ++i) wr[i] = __builtin_amdgcn_raw_buffer_load_b128(rw2, vw2[i & 3] + (i & 3) * 1024, sw2 + w2_row(9 + i / 4) * 1024, AUX_W);
    };
    wr_load(!helper);
    SCHED();
    STAMP(13);
    BARRIER();
    STAMP(14);
    {
      u32x4 o[2], s0, s1;
      f32x2 yp[4][4], c0[4], c1[4];
#pragma unroll
      for (int i = 0; i < 4; ++i) {
        yp[i][0] = *(volatile LAS const f32x2*)(ra[i & 1] + (i >> 1) * 1024); yp[i][1] = *(volatile LAS const f32x2*)(ra[i & 1] + (i >> 1) * 1024 + 8192);
      }
#pragma unroll
      for (int i = 0; i < 4; ++i) {
        yp[i][2] = *(volatile LAS const f32x2*)(rc[i & 1] + (i >> 1) * 1024); yp[i][3] = *(volatile LAS const f32x2*)(rc[i & 1] + (i >> 1) * 1024 + 8192);
      }
      SCHED();
#pragma unroll
      for (int i = 0; i < 4; ++i) { c0[i] = (yp[i][0] + yp[i][1]) * gav[0] + xc[i][0]; s0[i] = pk2(c0[i] * gcv[0]); }
      st_sc1(rws, so, 0, s0);
      SCHED();
#pragma unroll
      for (int i = 0; i < 4; ++i) { c1[i] = (yp[i][2] + yp[i][3]) * gav[1] + xc[i][1]; s1[i] = pk2(c1[i] * gcv[1]); }
      st_sc1(rws, so, 256, s1);
      SCHED();
#pragma unroll
      for (int i = 0; i < 4; ++i) { o[i >> 1][2 * (i & 1)] = pk(c0[i][0], c1[i][0]); o[i >> 1][2 * (i & 1) + 1] = pk(c0[i][1], c1[i][1]); }
      st_sc1(rws, xo0, 0, o[0]);
      st_sc1(rws, xo1, 0, o[1]);
    }
    STAMP(15);
    WAIT_VM(0);
    STAMP(17);
    st_sc1(rws, fo, 0, u32x4{ta, ta, tb, tb});
    __builtin_amdgcn_s_setprio(0);
    const int kind = helper ? 3 : lw;
    int e0 = WS_G2 + rbk * 1024, e1 = WS_S12 + (cbk * 128 + (kind - 1) * 64) * 16, e3 = WS_MF + 4096 + db * 512;
    asm volatile("" : "+s"(e0), "+s"(e1), "+s"(e3));
    const bool mid = (kind + 1) & 2;
    const int eo = (mid ? e1 : kind ? e3 : e0) + (lane & (kind == 3 ? 31 : 63)) * 16;
    LAS char* ea = smem + LD_MISC + (mid ? 256 + kind * 256 : kind * 512) + lane * (mid ? 4 : 8);
    u32x4 ev;
    if (helper) {
      BARRIER();
      ev = poll16(rws, eo, 0, ta, tb);
      *(LAS u32*)ea = ev[0]; *(LAS u32*)(ea + 4) = ev[2];
      BARRIER();
      __builtin_amdgcn_s_sleep(20);
      w2_stage(1); w2_stage(2); w2_stage(3);
      BARRIER(); BARRIER();
      w2_stage(0); w2_stage(4); w2_stage(5);
      wr_load(true);
    } else {
      __builtin_amdgcn_s_sleep(F2_DELAY);
      poll16(rws, ((lane >> 3) * 32 + lw * 8 + (lane & 7)) * 64, WS_F2 + (xcd >> 1) * 16384, ta, tb);
      STAMP(19);
      x_stage(0); x_stage(1); WAIT_VM(4); BARRIER();
      x_stage(2); x_stage(3); WAIT_VM(8); BARRIER();
      x_stage(4); w1_stage(4); WAIT_VM(12); BARRIER();
#pragma unroll 1
      for (int s = 5; s < 8; ++s) { x_stage(s); w1_stage(s); WAIT_VM(16); BARRIER(); }
      w2_stage(0); w2_stage(1); WAIT_VM(16); BARRIER();
      asm volatile("buffer_load_dwordx4 %0, %1, %2, 0 offen sc1" : "=v"(ev) : "v"(eo), "s"(qws) : "memory");
      w2_stage(2); w2_stage(3); WAIT_VM(17); BARRIER();
      asm volatile("s_waitcnt vmcnt(8)" : "+v"(ev) :: "memory");
      if (__any(tagdiff(ev, ta, tb) != 0u)) ev = poll16(rws, eo, 0, ta, tb);
      *(LAS u32*)ea = ev[0]; *(LAS u32*)(ea + (mid ? 512 : 4)) = ev[2];
      w2_stage(4); w2_stage(5); BARRIER();
    }
    STAMP(22);
    w2_stage(6); w2_stage(7); w2_stage(8);
    {
      const int xs = WS_X1 + tbk * ST_X1 + (db * 32 + (lw >> 1) * 16) * 128 + (lw & 1) * 64;
      dma16_sc1(qws, LD_MISC + 2048 + lw * 2048, (lane >> 2) * 128 + (lane & 3) * 16, xs);
      dma16_sc1(qws, LD_MISC + 3072 + lw * 2048, (lane >> 2) * 128 + (lane & 3) * 16, xs + ST_X1H);
    }
    WAIT_VM(0);
    STAMP(25);
    BARRIER();
    BARRIER();
#pragma unroll
    for (int i = 0; i < 20; ++i) {
      if (i == 8) BARRIER();
      *(LAS u32x4*)(smem + w2_unit(9 + i / 4) * UNIT + lw * 4096 + (i & 3) * 1024 + lane * 16) = wr[i];
    }
    BARRIER();
    if (b == 0 && tid == 256) *(GAS u32x2*)(p.ws + WS_EPOCH) = u32x2{ta, tb};
    STAMP(26);
    STAMP_FLUSH(p.ws, 1);
  }
}

extern "C" __global__ void fused_block_end() {}

extern "C" void kernel_launch(void* const* d_in, const int* in_sizes, int n_in, void* d_out, int out_size, void* d_ws, size_t ws_size,
                              hipStream_t stream) {
  Params p;
  p.x = (const u16*)d_in[0]; p.Are = (const u16*)d_in[1]; p.Aim = (const u16*)d_in[2]; p.Bre = (const u16*)d_in[3];
  p.Bim = (const u16*)d_in[4]; p.Cre = (const u16*)d_in[5]; p.Cim = (const u16*)d_in[6]; p.ldt = (const u16*)d_in[7];
  p.dskip = (const u16*)d_in[8]; p.w1 = (const u16*)d_in[10]; p.w2 = (const u16*)d_in[11]; p.c = (const u16*)d_in[12];
  p.wmod = (const u16*)d_in[13]; p.out = (u16*)d_out; p.ws = (char*)d_ws;
  (void)hipFuncSetAttribute((const void*)fused_block, hipFuncAttributeMaxDynamicSharedMemorySize, LDS_BYTES + ST_LDS_EXTRA);
  fused_block<<<256, 512, LDS_BYTES + ST_LDS_EXTRA, stream>>>(p);
}
```

```cpp
#define XG_DELAY 11
#include <hip/hip_runtime.h>
#include <hip/hip_bf16.h>

#define STAMP_DECL
#define STAMP(s) __builtin_amdgcn_sched_barrier(0)
#define STAMP_FLUSH(ws, k)
#define ST_LDS_EXTRA 0
#define ST_RANGES {{0, 32, 256, "compute waves"}, {1, 32, 256, "loader waves"}, {0, 0, 32, "helper WGs, waves 0-3"}, {1, 0, 32, "helper WGs, waves 4-7"}}

typedef unsigned short u16;
typedef unsigned int u32;
typedef unsigned long long u64;
typedef u32 u32x2 __attribute__((ext_vector_type(2)));
typedef u32 u32x4 __attribute__((ext_vector_type(4)));
typedef float f32x2 __attribute__((ext_vector_type(2)));
typedef float f32x4 __attribute__((ext_vector_type(4)));
typedef float f32x16 __attribute__((ext_vector_type(16)));
typedef short s16x4 __attribute__((ext_vector_type(4)));
typedef __bf16 bf16x2 __attribute__((ext_vector_type(2)));
typedef __bf16 bf16x8 __attribute__((ext_vector_type(8)));
typedef __amdgpu_buffer_rsrc_t rsrc_t;
#define GAS __attribute__((address_space(1)))
#define LAS __attribute__((address_space(3)))
#define CAS __attribute__((address_space(4)))

constexpr int L = 2048, D = 512, NS = 128, F = 1792;

constexpr int WS_EPOCH = 0;
constexpr int WS_SG = 0x1000;
constexpr int WS_MG = 0x80000;
constexpr int WS_F2 = 0x40000;
constexpr int WS_F3 = 1 << 20;
constexpr int WS_G2 = 0x70000;
constexpr int WS_S12 = 0x74000;
constexpr int WS_MF = 0x7c000;
constexpr int WS_XG = 2 << 20;
constexpr int WS_X1 = 0x440000;
constexpr int WS_X1S = 0x680000;
constexpr int WS_H = 9 << 20;
constexpr int WS_END = 16 << 20;
#define PADS 1
constexpr int ST_XG = 8192 + 128 * PADS, ST_MG = 1536 * 16 + 128 * PADS, ST_X1H = 32768 + 128 * PADS, ST_X1 = 2 * ST_X1H, ST_X1S = 131072 + 384 * PADS;
constexpr int OOB = 0x7ff00000;

constexpr int UNIT = 16384;
constexpr int LD_XR = 0, LD_WR = 4 * UNIT, LD_MISC = 9 * UNIT, LD_FLG = LD_MISC + 10240, LD_MV = LD_FLG + 32, LDS_BYTES = LD_MV + 64;
constexpr int TCOPY = 4160, PROW = 132;
constexpr int LD_T = 0, LD_U = 8 * TCOPY, LD_KD = LD_U + 2 * 4608, LD_P = LD_WR, LD_RED0 = 3 * UNIT, LD_RED1 = 8 * UNIT;
constexpr int KD_ZERO = 900, LD_XT = LD_RED0;
static_assert(LD_KD + 904 * 4 <= LD_RED0, "LDS map");
__host__ __device__ constexpr int w2_row(int u) { return 128 * (u % 7) + 32 * (u / 7); }
__host__ __device__ constexpr int w2_unit(int u) { return u % 9 == 8 ? 8 : (u % 9) / 2 + 4 * (u % 9 & 1); }
#define G2_NW 14
#define G2_N0 14
__host__ __device__ constexpr int h_next(int i) { int a = G2_N0 + 2 * i, b = i > 0 ? i - 1 + G2_NW : G2_NW; a = a < b ? a : b; return a < 56 ? a : 56; }
#define TF_PRE 4
#define F3_DELAY 6
#define OUT_AUX 2
#define P2 5
#define F1_AT 8
#define G_SG 6
#define G_EARLY 1
#define G_SIG 5
#define FT_AT 3
#define FT_DELAY 0
#define H_SLEEP 96
#define S1_SLEEP 2
#define F2_DELAY 20
#define POLL_SLEEP 2
#define LDNT(p) __builtin_nontemporal_load(p)
#define AUX_NT 2
#define WF ""
#define AUX_W 0
constexpr int SPIN_CAP = 1 << 15;
constexpr float LOG2E = 1.44269504088896341f;

__device__ __forceinline__ float bflo(u32 v) { return __uint_as_float(v << 16); }
__device__ __forceinline__ float bfhi(u32 v) { return __uint_as_float(v & 0xffff0000u); }
__device__ __forceinline__ u32 pk(float lo, float hi) {
  f32x2 v = {lo, hi};
  bf16x2 r = __builtin_convertvector(v, bf16x2);
  return __builtin_bit_cast(u32, r);
}
__device__ __forceinline__ float rb(float f) { return bflo(pk(f, f)); }
__device__ __forceinline__ float dot2(u32 a, u32 b, float c) {
  return __builtin_amdgcn_fdot2_f32_bf16(__builtin_bit_cast(bf16x2, a), __builtin_bit_cast(bf16x2, b), c, false);
}
__device__ __forceinline__ f32x4 mfma16(u32x4 a, u32x4 b, f32x4 c) {
  return __builtin_amdgcn_mfma_f32_16x16x32_bf16(__builtin_bit_cast(bf16x8, a), __builtin_bit_cast(bf16x8, b), c, 0, 0, 0);
}
__device__ __forceinline__ f32x16 mfma32(u32x4 a, u32x4 b, f32x16 c) {
  return __builtin_amdgcn_mfma_f32_32x32x16_bf16(__builtin_bit_cast(bf16x8, a), __builtin_bit_cast(bf16x8, b), c, 0, 0, 0);
}
template <int CTRL> __device__ __forceinline__ float dpp(float x) {
  return __builtin_bit_cast(float, __builtin_amdgcn_mov_dpp(__builtin_bit_cast(int, x), CTRL, 0xf, 0xf, true));
}
__device__ __forceinline__ float row_sum(float x) {
  x += dpp<0xB1>(x); x += dpp<0x4E>(x); x += dpp<0x124>(x); x += dpp<0x128>(x);
  return x;
}
__device__ __forceinline__ float fold32(float a, float b) {
  auto s = __builtin_amdgcn_permlane32_swap(__float_as_uint(a), __float_as_uint(b), false, false);
  return __uint_as_float(s[0]) + __uint_as_float(s[1]);
}
__device__ __forceinline__ float fold16(float a, float b) {
  auto s = __builtin_amdgcn_permlane16_swap(__float_as_uint(a), __float_as_uint(b), false, false);
  return __uint_as_float(s[0]) + __uint_as_float(s[1]);
}
__device__ __forceinline__ u32x4 mkrs(const void* p, u32 bytes) {
  const u64 a = (u64)p;
  return u32x4{(u32)a, (u32)(a >> 32) & 0xffffu, bytes, 0x00020000u};
}
__device__ __forceinline__ void dma4(u32x4 rs, int lds, int voff, int soff) {
  asm volatile("s_mov_b32 m0, %0\n\ts_nop 0\n\tbuffer_load_dword %1, %2, %3 offen lds" :: "s"(lds), "v"(voff), "s"(rs), "s"(soff) : "memory");
}
__device__ __forceinline__ void dma16_sc1(u32x4 rs, int lds, int voff, int soff) {
  asm volatile("s_mov_b32 m0, %0\n\ts_nop 0\n\tbuffer_load_dwordx4 %1, %2, %3 offen sc1 lds" :: "s"(lds), "v"(voff), "s"(rs), "s"(soff) : "memory");
}
#define DMA4_TEXT(F) "s_mov_b32 m0, %0\n\ts_nop 0\n\tbuffer_load_dwordx4 %1, %5, %6 offen" F " lds\n\tbuffer_load_dwordx4 %2, %5, %6 offen offset:1024" F " lds\n\t" \
                     "buffer_load_dwordx4 %3, %5, %6 offen offset:2048" F " lds\n\tbuffer_load_dwordx4 %4, %5, %6 offen offset:3072" F " lds"
__device__ __forceinline__ void dma4k(u32x4 rs, int lds, const int (&v)[4], int soff) {
  asm volatile(DMA4_TEXT(WF) :: "s"(lds), "v"(v[0]), "v"(v[1]), "v"(v[2]), "v"(v[3]), "s"(rs), "s"(soff) : "memory");
}
__device__ __forceinline__ void dma4k_sc1(u32x4 rs, int lds, const int (&v)[4], int soff) {
  asm volatile(DMA4_TEXT(" sc1") :: "s"(lds), "v"(v[0]), "v"(v[1]), "v"(v[2]), "v"(v[3]), "s"(rs), "s"(soff) : "memory");
}
__device__ __forceinline__ u32x4 ld_sc1(rsrc_t rs, int voff, int soff) { return __builtin_amdgcn_raw_buffer_load_b128(rs, voff, soff, 16); }
__device__ __forceinline__ void st_sc1(rsrc_t rs, int voff, int soff, u32x4 v) { __builtin_amdgcn_raw_buffer_store_b128(v, rs, voff + soff, 0, 16); }
__device__ __forceinline__ u32x4 tr_frag(LAS char* a0, LAS char* a1) {
  const s16x4 lo = __builtin_amdgcn_ds_read_tr16_b64_v4i16((LAS s16x4*)a0), hi = __builtin_amdgcn_ds_read_tr16_b64_v4i16((LAS s16x4*)a1);
  const u32x2 l = __builtin_bit_cast(u32x2, lo), h = __builtin_bit_cast(u32x2, hi);
  return u32x4{l[0], l[1], h[0], h[1]};
}
#define SCHED() __builtin_amdgcn_sched_barrier(0)
#define FENCE() asm volatile("" ::: "memory")
#define WAIT_VM(n) asm volatile("s_waitcnt vmcnt(" #n ")" ::: "memory")
#define BARRIER() do { asm volatile("s_waitcnt lgkmcnt(0)" ::: "memory"); __builtin_amdgcn_s_barrier(); FENCE(); } while (0)

__device__ __forceinline__ f32x2 cmul(f32x2 a, f32x2 b) {
  const f32x2 t = f32x2{a[0], a[0]} * b;
  return __builtin_elementwise_fma(f32x2{a[1], a[1]}, f32x2{-b[1], b[0]}, t);
}
__device__ __forceinline__ f32x2 cpow(float a2, float bq, float l) {
  const float m = __builtin_amdgcn_exp2f(a2 * l);
  const float ph = __builtin_amdgcn_fractf(bq * l);
  return f32x2{__builtin_amdgcn_cosf(ph), __builtin_amdgcn_sinf(ph)} * m;
}
__device__ __forceinline__ u32 pk2(f32x2 a) { return pk(a[0], a[1]); }
__device__ __forceinline__ f32x2 up(u32 v) { return f32x2{bflo(v), bfhi(v)}; }
__device__ __forceinline__ u32 ln_stat(float sum, float sq) {
  const float m = sum * (1.f / D);
  const float var = fmaxf(sq * (1.f / D) - m * m, 0.f);
  return pk(m, __builtin_amdgcn_sqrtf(rb(rb(var) + 1e-5f)));
}
__device__ __forceinline__ u32 tagdiff(u32x4 v, u32 ta, u32 tb) { return (v[1] ^ ta) | (v[3] ^ tb); }
__device__ __forceinline__ u32x4 poll16(rsrc_t rs, int voff, int soff, u32 ta, u32 tb) {
  u32x4 v;
  int spins = 0;
  for (;;) {
    FENCE();
    v = ld_sc1(rs, voff, soff);
    if (tagdiff(v, ta, tb) == 0u || ++spins >= SPIN_CAP) break;
    if (POLL_SLEEP) __builtin_amdgcn_s_sleep(POLL_SLEEP);
  }
  FENCE();
  return v;
}
__device__ __forceinline__ bool bad4(const u32x4 (&v)[4], u32 ta, u32 tb) {
  return (tagdiff(v[0], ta, tb) | tagdiff(v[1], ta, tb) | tagdiff(v[2], ta, tb) | tagdiff(v[3], ta, tb)) != 0u;
}
__device__ __forceinline__ void retry4(u32x4 (&v)[4], rsrc_t rs, int voff, int step, u32 ta, u32 tb) {
#pragma unroll 1
  for (int spins = 0; spins < SPIN_CAP; ++spins) {
    __builtin_amdgcn_s_sleep(S1_SLEEP);
    FENCE();
#pragma unroll
    for (int i = 0; i < 4; ++i) if (tagdiff(v[i], ta, tb)) v[i] = ld_sc1(rs, voff, i * step);
    if (!__any(bad4(v, ta, tb))) break;
  }
}
__device__ __forceinline__ void fold_cols(const float (&v)[16], LAS float* dst, int lane) {
  float u[8]; f32x4 t;
#pragma unroll
  for (int k = 0; k < 8; ++k) u[k] = fold32(v[k], v[k + 8]);
#pragma unroll
  for (int k = 0; k < 4; ++k) { const float a = fold16(u[k], u[k + 4]); t[k] = a + dpp<0x128>(a); }
  if (!(lane & 8)) *(LAS f32x4*)(dst + (lane & 7) * 16 + (lane >> 4) * 4) = t;
}
__device__ __forceinline__ f32x2 sum4(LAS const float* src, int stride) {
  return (*(LAS const f32x2*)src + *(LAS const f32x2*)(src + stride)) + (*(LAS const f32x2*)(src + 2 * stride) + *(LAS const f32x2*)(src + 3 * stride));
}
__device__ __forceinline__ u32 silu_bf(u32 c) {
  const float f = bflo(c), s = f * __builtin_amdgcn_rcpf(1.f + __builtin_amdgcn_exp2f(f * -LOG2E));
  return pk(s, s);
}
template <int CTRL> __device__ __forceinline__ void gv_row(u32x4 v, u32 sp, float (&acc)[8]) {
  const u32 sb = __builtin_amdgcn_mov_dpp(sp, CTRL, 0xf, 0xf, true), al = sb & 0xffffu, ah = sb & 0xffff0000u;
#pragma unroll
  for (int q = 0; q < 4; ++q) { acc[2 * q] = dot2(v[q], al, acc[2 * q]); acc[2 * q + 1] = dot2(v[q], ah, acc[2 * q + 1]); }
}
__device__ __forceinline__ f32x2 gv_fold(const float (&acc)[8]) {
  const float m0 = fold16(fold32(acc[0], acc[4]), fold32(acc[2], acc[6])), m1 = fold16(fold32(acc[1], acc[5]), fold32(acc[3], acc[7]));
  return f32x2{m0 + dpp<0x128>(m0), m1 + dpp<0x128>(m1)};
}
#define TOUCH(dst, voff, base) asm volatile("global_load_dword %0, %1, %2" : "=v"(dst) : "v"(voff), "s"(base))

__device__ __forceinline__ void quad_arrive(LAS char* f, int i, u32 n) {
  asm volatile("s_waitcnt lgkmcnt(0)" ::: "memory");
  *(volatile LAS u32*)(f + i * 4) = n;
}
__device__ __forceinline__ void quad_wait(LAS char* f, u32 n) {
  for (;;) {
    const u32x4 v = *(volatile LAS u32x4*)f;
    if (__builtin_amdgcn_readfirstlane(min(min(v[0], v[1]), min(v[2], v[3]))) >= n) break;
    __builtin_amdgcn_s_sleep(1);
  }
  FENCE();
}
__device__ __forceinline__ int only(bool c, int off) { return off | (c ? 0 : OOB); }
#define PIN(x) asm volatile("" : "+v"(x))

struct Params {
  const u16 *x, *Are, *Aim, *Bre, *Bim, *Cre, *Cim, *ldt, *dskip, *w1, *w2, *c, *wmod;
  u16* out;
  char* ws;
};

extern "C" __global__ void __launch_bounds__(512, 2) fused_block(Params p) {
  extern __shared__ __attribute__((aligned(16))) char smem_[];
  LAS char* smem = (LAS char*)smem_;
  const int tid = threadIdx.x, lane = tid & 63, w = __builtin_amdgcn_readfirstlane(tid >> 6), lw = w & 3;
  STAMP_DECL
  STAMP(0);
  *(LAS u32*)(smem + LD_FLG + w * 4) = 0u;
  BARRIER();
  asm volatile("" :: "s"(p.x), "s"(p.Are), "s"(p.Aim), "s"(p.Bre), "s"(p.Bim), "s"(p.Cre), "s"(p.Cim), "s"(p.ldt), "s"(p.dskip), "s"(p.w1), "s"(p.w2), "s"(p.c), "s"(p.wmod),
               "s"(p.out), "s"(p.ws));
#define SCAL_ISSUE(q) u64 epoch; u32 aux; \
  asm volatile("s_load_dwordx2 %0, %2, 0x0\n\ts_load_dword %1, %3, 0x0" : "=&s"(epoch), "=&s"(aux) : "s"(p.ws + WS_EPOCH), "s"(q)); SCHED()
#define SCAL_WAIT() asm volatile("s_waitcnt lgkmcnt(0)" : "+s"(epoch), "+s"(aux)); \
  epoch += 0x9E3779B97F4A7C15ull; ta = (u32)epoch; tb = (u32)(epoch >> 32); dsk = aux
#define ROLES() int b = blockIdx.x; asm volatile("" : "+s"(b)); \
  const int xcd = b & 7, j = b >> 3, pair = xcd * 32 + j, d0 = pair * 2; \
  const bool helper = j < 4; \
  const int rbk = (xcd >> 1) * 4 + (j & 3), cbk = (xcd & 1) * 7 + ((j - 4) >> 2), tbk = xcd * 4 + (j & 3), db = ((j >> 2) + 3 - (int)((u64)p.w2 >> 7)) & 7; \
  const rsrc_t rws = __builtin_amdgcn_make_buffer_rsrc(p.ws, 0, WS_END, 0x00020000)

  if (__builtin_expect(w >= 4, 1)) {
    LAS char* flg = smem + LD_FLG + 16;
    u32x4 sg[4];
    u32x2 xg[4];
    ROLES();
    const int go = lw * 4096 + lane * 16;
    u32 ta, tb, dsk;
    const u32x4 qws = mkrs(p.ws, WS_END);
#define POLL_ISSUE() asm volatile("buffer_load_dwordx4 %0, %4, %5, 0 offen sc1\n\tbuffer_load_dwordx4 %1, %4, %5, 0 offen offset:1024 sc1\n\t" \
      "buffer_load_dwordx4 %2, %4, %5, 0 offen offset:2048 sc1\n\tbuffer_load_dwordx4 %3, %4, %5, 0 offen offset:3072 sc1" \
      : "=&v"(sg[0]), "=&v"(sg[1]), "=&v"(sg[2]), "=&v"(sg[3]) : "v"(go + WS_SG), "s"(qws) : "memory")
    int vx[4], vw1[4], vw2[4], so, xo0, xo1, fo, mfo;
    LAS char *ra[2], *rc[2];
    auto addresses = [&]() {
      const int hm = (j - 4) >> 31;
#pragma unroll
      for (int i = 0; i < 4; ++i) {
        const int chk = 16 * ((lane & 15) ^ ((lane >> 4) << 2) ^ i);
        vx[i] = (lane >> 4) * 256 + chk;
        vw1[i] = (lane >> (4 + hm)) * (F * 2) + ((chk & ~hm) | ((lane & 7) * 16 & hm)) + i * ((4 + (4 & hm)) * F * 2 - 1024);
        vw2[i] = (lane >> 3) * 1024 + db * 128 + 16 * ((lane & 7) ^ (((lane >> 4) & 1) << 2)) + i * 7168;
        PIN(vx[i]); PIN(vw1[i]); PIN(vw2[i]);
      }
      const int g = (lane >> 1) & 3;
      ra[0] = smem + LD_RED0 + (2 * lw * 64 + 16 * g + ((lane >> 3) ^ (4 * g))) * 16 + 8 * (lane & 1);
      ra[1] = smem + LD_RED0 + (2 * lw * 64 + 16 * g + ((lane >> 3) ^ (4 * g) ^ 8)) * 16 + 8 * (lane & 1);
      rc[0] = ra[0] + (LD_RED1 - LD_RED0); rc[1] = ra[1] + (LD_RED1 - LD_RED0);
      so = WS_X1S + (lw * 4 + (lane >> 4)) * ST_X1S + d0 * 256 + (lane & 15) * 16;
      xo0 = WS_X1 + (lw * 8 + (lane >> 3)) * ST_X1 + pair * 128 + (lane & 7) * 16; xo1 = xo0 + ST_X1H;
      fo = only(lane < 4, WS_F2 + (lw * 256 + pair) * 64 + lane * 16);
      mfo = only((tid >> 1) == 248, WS_MF + (~lane & 1) * 4096 + pair * 16);
      PIN(mfo); PIN(ra[0]); PIN(ra[1]); PIN(rc[0]); PIN(rc[1]); PIN(so); PIN(xo0); PIN(xo1); PIN(fo);
      SCHED();
    };
    f32x2 xf[4][2], hf[4][2], ca[4][2];
    u32x4 mv1, mv2;
    auto coefficients = [&]() {
      __builtin_amdgcn_s_setprio(2);
      STAMP(7);
      if (__builtin_expect(__any(bad4(sg, ta, tb)), 0)) retry4(sg, rws, go + WS_SG, 1024, ta, tb);
      float rs[4][2];
#pragma unroll
      for (int i = 0; i < 4; ++i)
#pragma unroll
        for (int e = 0; e < 2; ++e) { rs[i][e] = __builtin_amdgcn_rcpf(bfhi(sg[i][2 * e])); PIN(rs[i][e]); }
      if (lw >= 2) { WAIT_VM(4); quad_arrive(flg, lw, 3u); }
      quad_wait(flg, 3u);
      const u32x4 mv0 = *(LAS const u32x4*)(smem + LD_MV);
      mv1 = *(LAS const u32x4*)(smem + LD_MV + 32); mv2 = *(LAS const u32x4*)(smem + LD_MV + 48);
      if (lw < 2) {
#pragma unroll
        for (int i = 0; i < 4; ++i) xg[i] = *(LAS const u32x2*)(smem + LD_XT + lw * 2048 + i * 512 + lane * 8);
      }
      const f32x2 sa = up(mv0[0]), g1 = up(mv0[3]);
#pragma unroll
      for (int i = 0; i < 4; ++i)
#pragma unroll
        for (int e = 0; e < 2; ++e) {
          ca[i][e] = g1 * rs[i][e];
          hf[i][e] = sa - ca[i][e] * bflo(sg[i][2 * e]);
          PIN(ca[i][e]); PIN(hf[i][e]);
        }
      STAMP(8);
    };
    if (lw >= 2) {
      u32 xt;
      TOUCH(xt, (j * 32 + (lw & 1) * 16 + (lane & 15)) * 1024 + xcd * 128, p.x);
      SCAL_ISSUE(p.dskip + d0);
      addresses();
      const u32x4 qx = mkrs(p.x, L * D * 2);
      SCAL_WAIT();
      quad_arrive(flg, lw, 1u);
#pragma unroll
      for (int i = 0; i < 8; ++i) {
        if (i == G_EARLY) quad_wait(flg, 1u);
        if (i == G_SIG) *(volatile LAS u32*)(flg + lw * 4) = 2u;
        if (i == G_SG) POLL_ISSUE();
        dma4(qx, LD_XT + (lw & 1) * 2048 + i * 256, lane * 1024 + pair * 4, ((lw & 1) * 8 + i) * 65536);
        if (i < G_SG) WAIT_VM(2);
      }
      if (XG_DELAY) __builtin_amdgcn_s_sleep(XG_DELAY);
      STAMP(3);
      const int xo = WS_XG + ((lw & 1) * 128 + (lane >> 1)) * ST_XG + pair * 32 + (lane & 1) * 16;
      u32x4 xe[4];
#pragma unroll
      for (int i = 0; i < 4; ++i) xe[i] = ld_sc1(rws, xo, i * 32 * ST_XG);
      asm volatile("s_waitcnt vmcnt(%5)" : "+v"(sg[0]), "+v"(sg[1]), "+v"(sg[2]), "+v"(sg[3]) : "v"(xt), "n"(12 - G_SG) : "memory");
      coefficients();
      if (__builtin_expect(__any(bad4(xe, ta, tb)), 0)) retry4(xe, rws, xo, 32 * ST_XG, ta, tb);
#pragma unroll
      for (int i = 0; i < 4; ++i) xg[i] = u32x2{xe[i][0], xe[i][2]};
    } else {
      u32x4 xv[4], wm[8];
      const int gcb = (b + (b >> 4)) & 15;
#pragma unroll
      for (int i = 0; i < 4; ++i) xv[i] = LDNT((const u32x4*)(p.x + (size_t)(lw * 1024 + b * 4 + i) * D) + lane);
      SCHED();
      SCAL_ISSUE(p.dskip + d0);
      const u32 c0 = p.c[(b >> 4) * 32 + (lane >> 3) + 8 * (lane & 3)];
      SCHED();
      {
        const rsrc_t ra = __builtin_amdgcn_make_buffer_rsrc((void*)p.wmod, 0, D * 3072 * 2, 0x00020000);
        const rsrc_t rb_ = __builtin_amdgcn_make_buffer_rsrc((void*)p.wmod, 0, lw == 0 ? D * 3072 * 2 : 0, 0x00020000);
#pragma unroll
        for (int i = 0; i < 8; ++i)
          wm[i] = __builtin_amdgcn_raw_buffer_load_b128(i < 4 ? ra : rb_, (lane >> 3) * 6144 + (lane & 7) * 16,
                                                        ((b >> 4) * 32 + 8 * (i & 3)) * 6144 + ((lw + (i >> 2) * 2) * 16 + gcb) * 128, AUX_NT);
      }
      SCHED();
      *(LAS u32x4*)(smem + LD_U + (lane >> 5) * 4608 + (lane & 31) * 16) = u32x4{0u, 0u, 0u, 0u};
      int so_ = only(!(lane & 31), WS_SG + lw * 8192 + b * 32 + (lane >> 5) * 16);
      int mo_ = only(!(lane & 8), WS_MG + (b >> 4) * ST_MG + ((lw * 16 + gcb) * 32 + (lane >> 4) * 8 + (lane & 7)) * 16);
      PIN(so_); PIN(mo_);
      SCAL_WAIT();
      __builtin_amdgcn_s_setprio(3);
      STAMP(1);
      LAS char* xw = smem + LD_XT + 4096;
      if (lw) {
#pragma unroll
        for (int i = 0; i < 4; ++i) *(LAS u32x4*)(xw + i * 1056 + lane * 16) = xv[i];
      }
      float s[4], q[4];
#pragma unroll
      for (int i = 0; i < 4; ++i) {
        s[i] = 0.f; q[i] = 0.f;
#pragma unroll
        for (int z = 0; z < 4; ++z) { s[i] = dot2(xv[i][z], 0x3f803f80u, s[i]); q[i] = dot2(xv[i][z], xv[i][z], q[i]); }
      }
      const u32 st = ln_stat(row_sum(fold16(fold32(s[0], s[2]), fold32(s[1], s[3]))), row_sum(fold16(fold32(q[0], q[2]), fold32(q[1], q[3]))));
      const auto sw = __builtin_amdgcn_permlane16_swap(st, st, false, false);
      st_sc1(rws, so_, 0, u32x4{sw[0], ta, sw[1], tb});
      STAMP(2);
      auto gv_tile = [&](int z, u32 sp) {
        float acc[8] = {0.f, 0.f, 0.f, 0.f, 0.f, 0.f, 0.f, 0.f};
        gv_row<0x00>(wm[4 * z], sp, acc); gv_row<0x55>(wm[4 * z + 1], sp, acc); gv_row<0xAA>(wm[4 * z + 2], sp, acc); gv_row<0xFF>(wm[4 * z + 3], sp, acc);
        const f32x2 m = gv_fold(acc);
        st_sc1(rws, mo_ + z * 16384, 0, u32x4{__float_as_uint(m[0]), ta, __float_as_uint(m[1]), tb});
        SCHED();
      };
      if (lw) {
        u32 a[8][2];
#pragma unroll
        for (int i = 0; i < 8; ++i) {
          LAS const u32* src = (LAS const u32*)(xw + (lane & 1) * 2112 + i * 128 + (lane >> 1) * 4);
          a[i][0] = src[0]; a[i][1] = src[264];
        }
#pragma unroll
        for (int i = 0; i < 8; ++i) st_sc1(rws, WS_XG + b * ST_XG + lane * 16 + (i >> 2) * 4096, (i & 3) * 1024, u32x4{a[i][0], ta, a[i][1], tb});
        SCHED();
        gv_tile(0, silu_bf(c0));
        PIN(wm[4]); PIN(wm[5]); PIN(wm[6]); PIN(wm[7]);
      } else {
        const u32 sp = silu_bf(c0);
        gv_tile(0, sp);
        gv_tile(1, sp);
      }
      __builtin_amdgcn_s_setprio(0);
      quad_arrive(flg, lw, 2u);
      STAMP(4);
      addresses();
      quad_wait(flg, 2u);
      const int mo = WS_MG + (lane & 15) * ST_MG + ((((lane >> 4) & (2 * lw + 1)) + 2 * lw) * 256 + (pair >> 5) * 32 + (pair & 3) * 8 + ((pair >> 2) & 7)) * 16;
      u32x4 mg;
      asm volatile("buffer_load_dwordx4 %0, %1, %2, 0 offen sc1" : "=v"(mg) : "v"(mo), "s"(qws) : "memory");
      POLL_ISSUE();
      asm volatile("s_waitcnt vmcnt(4)" : "+v"(mg) :: "memory");
      if (__builtin_expect(__any(tagdiff(mg, ta, tb) != 0u), 0)) {
        asm volatile("s_waitcnt vmcnt(0)" : "+v"(sg[0]), "+v"(sg[1]), "+v"(sg[2]), "+v"(sg[3]) :: "memory");
        mg = poll16(rws, mo, 0, ta, tb);
      }
      const u32 mb = pk(row_sum(__uint_as_float(mg[0])), row_sum(__uint_as_float(mg[2])));
      *(LAS u32x2*)(smem + LD_MV + lw * 32 + (lane >> 4) * 8) = u32x2{mb, pk2(up(mb) + 1.f)};
      quad_arrive(flg, lw, 3u);
      asm volatile("s_waitcnt vmcnt(0)" : "+v"(sg[0]), "+v"(sg[1]), "+v"(sg[2]), "+v"(sg[3]) :: "memory");
      coefficients();
    }
    const u32x4 qw1 = mkrs(p.w1, D * F * 2), qw2 = mkrs(p.w2, D * F * 2);
    const int sw2 = ((lw >> 1) * 896 + (lw & 1) * 64) * 1024;
    auto w2_stage = [&](int u) { dma4k(qw2, w2_unit(u) * UNIT + lw * 4096, vw2, sw2 + w2_row(u) * 1024); };
    const int wstep = (helper ? 32 : 16) * F * 2, wbase = helper ? min(tbk, 27) * 128 : cbk * 256;
    auto w1_stage = [&](int s) { dma4k(qw1, LD_WR + (s & 3) * UNIT + lw * 4096, vw1, (s * 4 + lw) * wstep + wbase); };
    auto x_stage = [&](int s) { dma4k_sc1(qws, LD_XR + (s & 3) * UNIT + lw * 4096, vx, WS_X1S + rbk * ST_X1S + s * 16384 + lw * 4096); };
    *(volatile LAS u32*)(flg + lw * 4) = 4u;
    STAMP(9);
#pragma unroll
    for (int i = 0; i < 4; ++i) {
#pragma unroll
      for (int e = 0; e < 2; ++e) { xf[i][e] = up(xg[i][e]); hf[i][e] += xf[i][e] * ca[i][e]; }
      LAS u32* U = (LAS u32*)(smem + LD_U + 512 + lw * 1024 + i * 256) + lane;
      U[0] = pk(hf[i][0][0], hf[i][1][0]);
      U[4608 / 4] = pk(hf[i][0][1], hf[i][1][1]);
    }
    STAMP(10);
    BARRIER();
    STAMP(11);
    w1_stage(0); w1_stage(1); w1_stage(2); w1_stage(3);
    const f32x2 gav = up(mv1[0]), gcv = up(mv2[1]);
    f32x2 xc[4][2];
    float gd[2] = {gav[0] * bflo(dsk), gav[1] * bfhi(dsk)}, n0 = bflo(mv2[2]) * (-1.f / LOG2E), n1 = bfhi(mv2[2]) * (-1.f / LOG2E);
    u32 k0 = __builtin_amdgcn_perm(mv1[2], mv2[1], 0x05040100u), k1 = __builtin_amdgcn_perm(mv1[2], mv2[1], 0x07060302u);
    PIN(gd[0]); PIN(gd[1]); PIN(n0); PIN(n1); PIN(k0); PIN(k1);
    const u32x4 mfv = {lane & 1 ? k0 : __float_as_uint(n0), ta, lane & 1 ? k1 : __float_as_uint(n1), tb};
    st_sc1(rws, mfo, 0, mfv);
#pragma unroll
    for (int i = 0; i < 4; ++i)
#pragma unroll
      for (int c = 0; c < 2; ++c) {
        float a0 = __builtin_fmaf(hf[i][0][c], gd[c], xf[i][0][c]), a1 = __builtin_fmaf(hf[i][1][c], gd[c], xf[i][1][c]);
        PIN(a0); PIN(a1);
        xc[i][c] = f32x2{a0, a1};
      }
    STAMP(12);
    SCHED();
    u32x4 wr[20];
    auto wr_load = [&](bool on) {
      const rsrc_t rw2 = __builtin_amdgcn_make_buffer_rsrc((void*)p.w2, 0, on ? D * F * 2 : 0, 0x00020000);
#pragma unroll
      for (int i = 0; i < 20; ++i) wr[i] = __builtin_amdgcn_raw_buffer_load_b128(rw2, vw2[i & 3] + (i & 3) * 1024, sw2 + w2_row(9 + i / 4) * 1024, AUX_W);
    };
    wr_load(!helper);
    SCHED();
    STAMP(13);
    BARRIER();
    STAMP(14);
    {
      u32x4 o[2], s0, s1;
      f32x2 yp[4][4], c0[4], c1[4];
#pragma unroll
      for (int i = 0; i < 4; ++i) {
        yp[i][0] = *(volatile LAS const f32x2*)(ra[i & 1] + (i >> 1) * 1024); yp[i][1] = *(volatile LAS const f32x2*)(ra[i & 1] + (i >> 1) * 1024 + 8192);
      }
#pragma unroll
      for (int i = 0; i < 4; ++i) {
        yp[i][2] = *(volatile LAS const f32x2*)(rc[i & 1] + (i >> 1) * 1024); yp[i][3] = *(volatile LAS const f32x2*)(rc[i & 1] + (i >> 1) * 1024 + 8192);
      }
      SCHED();
#pragma unroll
      for (int i = 0; i < 4; ++i) { c0[i] = (yp[i][0] + yp[i][1]) * gav[0] + xc[i][0]; s0[i] = pk2(c0[i] * gcv[0]); }
      st_sc1(rws, so, 0, s0);
      SCHED();
#pragma unroll
      for (int i = 0; i < 4; ++i) { c1[i] = (yp[i][2] + yp[i][3]) * gav[1] + xc[i][1]; s1[i] = pk2(c1[i] * gcv[1]); }
      st_sc1(rws, so, 256, s1);
      SCHED();
#pragma unroll
      for (int i = 0; i < 4; ++i) { o[i >> 1][2 * (i & 1)] = pk(c0[i][0], c1[i][0]); o[i >> 1][2 * (i & 1) + 1] = pk(c0[i][1], c1[i][1]); }
      st_sc1(rws, xo0, 0, o[0]);
      st_sc1(rws, xo1, 0, o[1]);
    }
    STAMP(15);
    WAIT_VM(0);
    STAMP(17);
    st_sc1(rws, fo, 0, u32x4{ta, ta, tb, tb});
    __builtin_amdgcn_s_setprio(0);
    const int kind = helper ? 3 : lw;
    int e0 = WS_G2 + rbk * 1024, e1 = WS_S12 + (cbk * 128 + (kind - 1) * 64) * 16, e3 = WS_MF + 4096 + db * 512;
    asm volatile("" : "+s"(e0), "+s"(e1), "+s"(e3));
    const bool mid = (kind + 1) & 2;
    const int eo = (mid ? e1 : kind ? e3 : e0) + (lane & (kind == 3 ? 31 : 63)) * 16;
    LAS char* ea = smem + LD_MISC + (mid ? 256 + kind * 256 : kind * 512) + lane * (mid ? 4 : 8);
    u32x4 ev;
    if (helper) {
      BARRIER();
      ev = poll16(rws, eo, 0, ta, tb);
      *(LAS u32*)ea = ev[0]; *(LAS u32*)(ea + 4) = ev[2];
      BARRIER();
      __builtin_amdgcn_s_sleep(20);
      w2_stage(1); w2_stage(2); w2_stage(3);
      BARRIER(); BARRIER();
      w2_stage(0); w2_stage(4); w2_stage(5);
      wr_load(true);
    } else {
      __builtin_amdgcn_s_sleep(F2_DELAY);
      poll16(rws, ((lane >> 3) * 32 + lw * 8 + (lane & 7)) * 64, WS_F2 + (xcd >> 1) * 16384, ta, tb);
      STAMP(19);
      x_stage(0); x_stage(1); WAIT_VM(4); BARRIER();
      x_stage(2); x_stage(3); WAIT_VM(8); BARRIER();
      x_stage(4); w1_stage(4); WAIT_VM(12); BARRIER();
#pragma unroll 1
      for (int s = 5; s < 8; ++s) { x_stage(s); w1_stage(s); WAIT_VM(16); BARRIER(); }
      w2_stage(0); w2_stage(1); WAIT_VM(16); BARRIER();
      asm volatile("buffer_load_dwordx4 %0, %1, %2, 0 offen sc1" : "=v"(ev) : "v"(eo), "s"(qws) : "memory");
      w2_stage(2); w2_stage(3); WAIT_VM(17); BARRIER();
      asm volatile("s_waitcnt vmcnt(8)" : "+v"(ev) :: "memory");
      if (__any(tagdiff(ev, ta, tb) != 0u)) ev = poll16(rws, eo, 0, ta, tb);
      *(LAS u32*)ea = ev[0]; *(LAS u32*)(ea + (mid ? 512 : 4)) = ev[2];
      w2_stage(4); w2_stage(5); BARRIER();
    }
    STAMP(22);
    w2_stage(6); w2_stage(7); w2_stage(8);
    {
      const int xs = WS_X1 + tbk * ST_X1 + (db * 32 + (lw >> 1) * 16) * 128 + (lw & 1) * 64;
      dma16_sc1(qws, LD_MISC + 2048 + lw * 2048, (lane >> 2) * 128 + (lane & 3) * 16, xs);
      dma16_sc1(qws, LD_MISC + 3072 + lw * 2048, (lane >> 2) * 128 + (lane & 3) * 16, xs + ST_X1H);
    }
    WAIT_VM(0);
    STAMP(25);
    BARRIER();
    BARRIER();
#pragma unroll
    for (int i = 0; i < 20; ++i) {
      if (i == 8) BARRIER();
      *(LAS u32x4*)(smem + w2_unit(9 + i / 4) * UNIT + lw * 4096 + (i & 3) * 1024 + lane * 16) = wr[i];
    }
    BARRIER();
    if (b == 0 && tid == 256) *(u32x2*)(p.ws + WS_EPOCH) = u32x2{ta, tb};
    STAMP(26);
    STAMP_FLUSH(p.ws, 1);
    return;
  }

  ROLES();
  const int pch = w >> 1, pn = (w & 1) * 64 + lane, po = (d0 + pch) * NS + pn;
  u32 rar = LDNT(p.Are + po), rai = LDNT(p.Aim + po), rbr = LDNT(p.Bre + po), rbi = LDNT(p.Bim + po), rcr = LDNT(p.Cre + po), rci = LDNT(p.Cim + po);
  SCHED();
  SCAL_ISSUE(p.ldt + d0);
  {
    u32 cp0;
    {
      u64 pc; int r0, r1;
      asm volatile("s_getpc_b64 %0\n\ts_mov_b32 %1, fused_block@rel32@lo+4\n\ts_mov_b32 %2, fused_block_end@rel32@lo+12" : "=s"(pc), "=s"(r0), "=s"(r1));
      TOUCH(cp0, min(((w * 4 + (lane & 3)) * 32 + j) * 128, r1 - r0), pc + (long long)r0);
    }
    LAS char* flg = smem + LD_FLG;
    *(LAS u32x4*)(smem + LD_T + ((lane >> 1) & 7) * TCOPY + 4096 + (lane & 1) * 16) = u32x4{0u, 0u, 0u, 0u};
    *(LAS u32*)(smem + LD_KD + KD_ZERO * 4) = 0u;
    asm volatile("s_waitcnt lgkmcnt(0)" : "+s"(epoch), "+s"(aux));
    int ga[11], ka[4];
#pragma unroll
    for (int i = 0; i < 11; ++i) {
      const int l = 2047 - (tid * 8 + i);
      const int bits = (int)(pk((float)l, 0.f) & 0xffffu);
      const int idx = l < 128 ? l : bits - 0x4300 + 128;
      ga[i] = LD_KD + 4 * (l < 0 ? KD_ZERO : idx + (idx >> 3));
    }
    {
      const int e = lane & 15, g = lane >> 4;
      const u32 cfg = e < 8 ? (w == 0 ? 0x100004u : w == 1 ? 0x080004u : 0x022001u) : (w == 0 ? 0x100004u : w == 1 ? 0x044002u : 0x088003u);
      const int sh = cfg & 15, base = 256 * w + ((cfg >> 8) & 0xff) * (e >> sh) + (e & ((1 << sh) - 1));
#pragma unroll
      for (int r = 0; r < 4; ++r) { const int idx = base + (int)(cfg >> 16) * (4 * g + r); ka[r] = LD_KD + 4 * (idx + (idx >> 3)); PIN(ka[r]); }
    }
    STAMP(1);
    asm volatile("" : "+v"(rar), "+v"(rai), "+v"(rbr), "+v"(rbi), "+v"(rcr), "+v"(rci) : "v"(cp0));
    STAMP(2);
    SCHED();
    volatile LAS u32* P = (LAS u32*)(smem + LD_P) + pch * 64 * PROW + pn;
    const float dt = rb(__builtin_amdgcn_exp2f((pch ? bfhi(aux) : bflo(aux)) * LOG2E));
    const float a2 = (dt * bflo(rar)) * LOG2E, bq = (dt * bflo(rai)) * 0.15915494309189535f;
    const f32x2 z1 = cpow(a2, bq, 1.f), z8 = cpow(a2, bq, 8.f), z256 = cpow(a2, bq, 256.f);
    const f32x2 z16c = cmul(z8, z8) * f32x2{1.f, -1.f}, z512 = cmul(z256, z256), z768 = cmul(z512, z256);
    f32x2 u = cpow(a2, bq, 1024.f), q = {1.f, 0.f};
    f32x2 r = cmul(f32x2{bflo(rcr), -bflo(rci)}, f32x2{bflo(rbr), -bflo(rbi)} * dt);
#pragma unroll
    for (int e = 0; e < 16; ++e) {
      const f32x2 rn = cmul(r, z16c), qn = cmul(q, z1);
      P[e * PROW] = pk2(r);
      P[(16 + e) * PROW] = pk2(q);
      if (!(e & 1)) P[(32 + e / 2) * PROW] = pk2(cmul(q, z256));
      if (!(e & 3)) { P[(40 + e / 4) * PROW] = pk2(cmul(q, z512)); P[(44 + e / 4) * PROW] = pk2(cmul(q, z768)); }
      if ((e & 3) == 1) { P[(48 + e / 2) * PROW] = pk2(u); P[(49 + e / 2) * PROW] = pk2(cmul(u, z8)); }
      if ((e & 3) == 3) u = cmul(u, z256);
      r = rn; q = qn;
    }
    P[56 * PROW] = pk2(u);
    STAMP(3);
    quad_arrive(flg, w, 1u);
#pragma unroll
    for (int i = 0; i < 11; ++i) PIN(ga[i]);
    quad_wait(flg, 1u);
    STAMP(4);
    if (w < 3) {
      LAS const u32* fb = (LAS const u32*)(smem + LD_P) + (lane & 15) * PROW + ((lane >> 4) & 1) * 64 + (lane >> 5) * 32;
      u32x4 fa[16], fc[16];
      f32x4 acc[2] = {{0.f, 0.f, 0.f, 0.f}, {0.f, 0.f, 0.f, 0.f}};
#pragma unroll
      for (int ks = 0; ks < 22; ++ks) {
        if (ks < 16) {
          const int o = (ks & 1) * 64 * PROW + (ks >> 1) * 4;
          fa[ks] = *(LAS const u32x4*)(fb + o); fc[ks] = *(LAS const u32x4*)(fb + (w + 1) * 16 * PROW + o);
        }
        SCHED();
        if (ks >= 6) acc[ks & 1] = mfma16(fa[ks - 6], fc[ks - 6], acc[ks & 1]);
        SCHED();
      }
#pragma unroll
      for (int r4 = 0; r4 < 4; ++r4) *(LAS u32*)(smem + ka[r4]) = pk(acc[0][r4], acc[1][r4]);
    }
    STAMP(5);
    quad_arrive(flg, w, 2u);
    quad_wait(flg, 2u);
    STAMP(6);
    u32 e[11];
#pragma unroll
    for (int i = 0; i < 11; ++i) e[i] = *(LAS const u32*)(smem + ga[i]);
#pragma unroll
    for (int c = 0; c < 2; ++c)
#pragma unroll
      for (int s = 0; s < 4; ++s) {
        u32x4 o;
#pragma unroll
        for (int z = 0; z < 4; ++z) o[z] = __builtin_amdgcn_perm(e[s + 2 * z + 1], e[s + 2 * z], c ? 0x07060302u : 0x05040100u);
        *(LAS u32x4*)(smem + LD_T + (c * 4 + s) * TCOPY + tid * 16) = o;
      }
    quad_arrive(flg, w, 3u);
  }
  epoch += 0x9E3779B97F4A7C15ull;
  const u32 ta = (u32)epoch, tb = (u32)(epoch >> 32);
  {
    const int r = lane & 15, g = lane >> 4, ch = w >> 1, half = w & 1;
    LAS const char* Tb = smem + LD_T + (ch * 4 + ((3 - r) & 3)) * TCOPY + 2 * (2028 - 4 * (r >> 2) + 8 * g - 128 * half) - 7 * 512 - 192;
    LAS const char* Ub = smem + LD_U + ch * 4608 + 2 * (256 + 16 * r - 16 + 8 * g - 128 * half) - 192;
    LAS f32x4* red = (LAS f32x4*)(smem + (ch ? LD_RED1 : LD_RED0)) + half * 512 + (lane ^ ((lane >> 4) << 2));
    PIN(Ub); PIN(red);
    f32x4 acc[8];
#pragma unroll
    for (int n = 0; n < 8; ++n) acc[n] = f32x4{0.f, 0.f, 0.f, 0.f};
    u32x4 uf[2][8], tf[4][8];
    quad_wait(smem + LD_FLG, 3u);
#define LD_TH(e, a, hh) do { const u32x2 l_ = *(volatile LAS const u32x2*)(Tb + 64 * (3 - (e)) + 512 * (7 - (a)) + 8 * (hh)); tf[e][a][2 * (hh)] = l_[0]; tf[e][a][2 * (hh) + 1] = l_[1]; } while (0)
#pragma unroll
    for (int e = 0; e < TF_PRE; ++e)
#pragma unroll
      for (int a = 0; a < 8; ++a) { LD_TH(e, a, 0); LD_TH(e, a, 1); }
#define LD_UF(n, e) uf[(e) & 1][n] = *(LAS const u32x4*)(Ub + 64 * (3 - (e)) + 512 * (n))
#define CM(e, a, m) do { SCHED(); acc[(a) + (m)] = mfma16(tf[e][a], uf[(e) & 1][m], acc[(a) + (m)]); SCHED(); } while (0)
    STAMP(10);
    u32 ft;
    quad_wait(smem + LD_FLG + 16, FT_AT);
    __builtin_amdgcn_s_sleep(FT_DELAY);
    TOUCH(ft, ((b * 2 + (w & 1)) * 28 + min(lane, 27)) * 128, w < 2 ? p.w1 : p.w2);
    BARRIER();
    STAMP(11);
#pragma unroll
    for (int n = 0; n < 8; ++n) LD_UF(n, 0);
#pragma unroll
    for (int e = 0; e < 3; ++e) {
      int cnt = 0;
#pragma unroll
      for (int m = 0; m < 8; ++m)
#pragma unroll
        for (int a = 0; a + m < 8; ++a) {
          CM(e, a, m);
          if (cnt >= 4 && cnt < 12) LD_UF(cnt - 4, e + 1);
          if (cnt >= 12 && cnt < 28 && e + TF_PRE < 4) LD_TH(e + TF_PRE, (cnt - 12) >> 1, (cnt - 12) & 1);
          ++cnt;
        }
    }
#pragma unroll
    for (int n = 0; n < 8; ++n)
#pragma unroll
      for (int a = 0; a <= n; ++a) { CM(3, a, n - a); if (a == 1) red[(n - 1) * 64] = acc[n - 1]; }
    STAMP(12);
    red[7 * 64] = acc[7];
    BARRIER();
    STAMP(14);
    WAIT_VM(0);
    asm volatile("" :: "v"(ft));
  }
  int f3w = OOB;
  if (helper) {
    const int hb = xcd * 4 + j;
    u32x4 v[16];
    const int rg = tid >> 3;
    LAS float* rd = (LAS float*)(smem + 4096);
    { const u32x4 m = poll16(rws, tid * 16, WS_MF, ta, tb); *(LAS u32x2*)(smem + tid * 8) = u32x2{m[0], m[2]}; }
    BARRIER();
#pragma unroll
    for (int i = 0; i < 16; ++i) v[i] = *(LAS const u32x4*)(smem + LD_WR + i * 4096 + tid * 16);
    float a[16];
#pragma unroll
    for (int e = 0; e < 16; ++e) a[e] = 0.f;
#pragma unroll
    for (int i = 0; i < 16; ++i) {
      const u32 gs = *(LAS const u32*)(smem + (i * 32 + rg) * 4);
      const u32 gl = gs & 0xffffu, gh = gs << 16, sl = gs >> 16, sh = gs & 0xffff0000u;
#pragma unroll
      for (int z = 0; z < 4; ++z) {
        a[4 * z] = dot2(v[i][z], gl, a[4 * z]); a[4 * z + 1] = dot2(v[i][z], sl, a[4 * z + 1]);
        a[4 * z + 2] = dot2(v[i][z], gh, a[4 * z + 2]); a[4 * z + 3] = dot2(v[i][z], sh, a[4 * z + 3]);
      }
    }
    fold_cols(a, rd + w * 128, lane);
    STAMP(18);
    poll16(rws, tid * 64, WS_F2 + (xcd >> 1) * 16384, ta, tb);
    BARRIER();
    STAMP(19);
#pragma unroll
    for (int i = 0; i < 16; ++i) v[i] = ld_sc1(rws, tid * 16, WS_X1 + hb * ST_X1 + (i >> 3) * ST_X1H + (i & 7) * 4096);
    if (w == 0 && hb < 28) {
      const f32x2 s = sum4(rd + lane * 2, 128);
      st_sc1(rws, lane * 16, WS_S12 + hb * 1024, u32x4{__float_as_uint(s[0]), ta, __float_as_uint(s[1] * -LOG2E), tb});
    }
#pragma unroll
    for (int e = 0; e < 16; ++e) a[e] = 0.f;
#pragma unroll
    for (int i = 0; i < 16; ++i)
#pragma unroll
      for (int z = 0; z < 4; ++z) {
        const int o = (i >> 3) * 8 + 2 * z;
        a[o] = dot2(v[i][z], 0x3f803f80u, a[o]); a[o + 1] = dot2(v[i][z], v[i][z], a[o + 1]);
      }
    STAMP(20);
    fold_cols(a, rd + 512 + w * 128, lane);
    BARRIER();
    if (w == 0) {
      const f32x2 s = sum4(rd + 512 + lane * 2, 128);
      const u32 st = ln_stat(s[0], s[1]);
      const u32 st2 = __builtin_amdgcn_mov_dpp(st, 0xB1, 0xf, 0xf, true);
      if (!(lane & 1)) st_sc1(rws, lane * 8, WS_G2 + hb * 512, u32x4{st, ta, st2, tb});
    }
    BARRIER();
    STAMP(22);
    __builtin_amdgcn_s_sleep(H_SLEEP);
  } else {
    const int wt = w & 1, wf = w >> 1;
    const int h = lane >> 5, blk = (lane >> 4) & 1, q = (lane & 15) >> 2, pp = lane & 3;
    int ax[2][2], aw[2][2];
#pragma unroll
    for (int c = 0; c < 2; ++c)
#pragma unroll
      for (int t = 0; t < 2; ++t) {
        const int base = 256 * (8 * h + 4 * t + q) + 16 * ((2 * blk + (pp >> 1)) ^ (2 * h + t)) + 8 * (pp & 1);
        ax[c][t] = LD_XR + base + 64 * ((2 * wt + c) ^ q);
        aw[c][t] = LD_WR + base + 64 * ((2 * wf + c) ^ q);
        PIN(ax[c][t]); PIN(aw[c][t]);
      }
    f32x16 acc[2][2];
#pragma unroll
    for (int a = 0; a < 2; ++a)
#pragma unroll
      for (int c = 0; c < 2; ++c)
#pragma unroll
        for (int r = 0; r < 16; ++r) acc[a][c][r] = 0.f;
    u32x4 xf[2][2], wfr[2][2];
    u32 st[2];
    int ho[4];
#pragma unroll
    for (int z = 0; z < 4; ++z) { ho[z] = WS_H + ((rbk * 4 + wt * 2) * 112 + z * 56 + cbk * 4 + wf * 2) * 1024 + lane * 16; PIN(ho[z]); }
    int fo = only(lane < 8, WS_F3 + ((rbk * 4 + w) * 16 + cbk) * 128 + lane * 16);
    int fo2 = fo + 0x20000;
    PIN(fo); PIN(fo2);
#define RD_X(i, c) xf[(i) & 1][c] = tr_frag(smem + ax[c][0] + ((i) & 15) * 4096, smem + ax[c][1] + ((i) & 15) * 4096)
#define RD_W(i, c) wfr[(i) & 1][c] = tr_frag(smem + aw[c][0] + ((i) & 15) * 4096, smem + aw[c][1] + ((i) & 15) * 4096)
#define MM(i, a, c) do { SCHED(); acc[a][c] = mfma32(wfr[(i) & 1][a], xf[(i) & 1][c], acc[a][c]); SCHED(); } while (0)
    BARRIER();
    STAMP(19);
    RD_W(0, 0); RD_X(0, 0); RD_X(0, 1); RD_W(0, 1);
#pragma unroll
    for (int i = 0; i < 32; ++i) {
      if ((i & 3) != 3) {
        MM(i, 0, 0); RD_W(i + 1, 0); MM(i, 0, 1); RD_X(i + 1, 0); MM(i, 1, 0); RD_X(i + 1, 1); MM(i, 1, 1); RD_W(i + 1, 1);
      } else {
        MM(i, 0, 0); MM(i, 0, 1);
        BARRIER();
        if (i == 31) { st[0] = *(LAS const u32*)(smem + LD_MISC + (wt * 64 + (lane & 31)) * 4); st[1] = *(LAS const u32*)(smem + LD_MISC + (wt * 64 + 32 + (lane & 31)) * 4); }
        MM(i, 1, 0);
        if (i < 31) { RD_W(i + 1, 0); RD_X(i + 1, 0); }
        MM(i, 1, 1);
        if (i < 31) { RD_X(i + 1, 1); RD_W(i + 1, 1); }
      }
    }
    STAMP(21);
    float rstd[2], mr[2];
#pragma unroll
    for (int c = 0; c < 2; ++c) {
      rstd[c] = __builtin_amdgcn_rcpf(bfhi(st[c])) * -LOG2E;
      mr[c] = -bflo(st[c]) * rstd[c];
    }
    STAMP(22);
    LAS const float* s12 = (LAS const float*)(smem + LD_MISC + 512) + wf * 64 + 4 * h;
#pragma unroll
    for (int a = 0; a < 2; ++a)
#pragma unroll
      for (int sp = 0; sp < 2; ++sp) {
        f32x4 s1[2], s2[2];
#pragma unroll
        for (int z = 0; z < 2; ++z) { s1[z] = *(LAS const f32x4*)(s12 + a * 32 + sp * 16 + z * 8); s2[z] = *(LAS const f32x4*)(s12 + 128 + a * 32 + sp * 16 + z * 8); }
#pragma unroll
        for (int c = 0; c < 2; ++c) {
          u32x4 o;
#pragma unroll
          for (int e = 0; e < 4; ++e) {
            const int z = e >> 1, k = (e & 1) * 2;
            const f32x2 av = {acc[a][c][sp * 8 + 2 * e], acc[a][c][sp * 8 + 2 * e + 1]};
            const f32x2 cc = f32x2{s1[z][k], s1[z][k + 1]} * mr[c] + f32x2{s2[z][k], s2[z][k + 1]};
            const f32x2 ar = av * rstd[c] + cc;
            const f32x2 dn = f32x2{__builtin_amdgcn_exp2f(ar[0]), __builtin_amdgcn_exp2f(ar[1])} + 1.f;
            o[e] = pk2(ar * f32x2{__builtin_amdgcn_rcpf(dn[0]), __builtin_amdgcn_rcpf(dn[1])});
          }
          st_sc1(rws, ho[2 * c + a], sp * 1024, o);
          if (a * 4 + sp * 2 + c + 1 == F1_AT) { asm volatile("s_waitcnt vmcnt(%0)" :: "n"(F1_AT - 4) : "memory"); st_sc1(rws, fo, 0, u32x4{ta, ta, tb, tb}); SCHED(); }
        }
        if (a == 0 && sp == 1) FENCE();
      }
    STAMP(23);
    f3w = fo2;
  }

  {
    const int wt = w & 1, kh = w >> 1, h = lane >> 5, tl = lane & 31;
    const int blk = (lane >> 4) & 1, q = (lane & 15) >> 2, pp = lane & 3;
    LAS char* aw[3][2];
#pragma unroll
    for (int c = 0; c < 6; ++c) {
      aw[c >> 1][c & 1] = smem + (c >> 1) * 65536 + kh * 8192 + 128 * (4 * h + q) + 64 * ((c & 1) ^ kh ^ (q >> 1)) + 32 * blk + 8 * pp;
      asm volatile("" : "+v"(aw[c >> 1][c & 1]));
    }
    const rsrc_t ro = __builtin_amdgcn_make_buffer_rsrc(p.out, 0, L * D * 2, 0x00020000);
    int oo[2];
#pragma unroll
    for (int i = 0; i < 2; ++i) {
      const int r = tbk * 64 + (tid >> 3) + 32 * i;
      oo[i] = ((r & ~511) + 128 * ((r >> 1) & 3) + 2 * ((r >> 3) & 63) + (r & 1)) * (D * 2) + db * 128 + (tid & 7) * 16;
      PIN(oo[i]);
    }
    f32x16 acc[2];
#pragma unroll
    for (int r = 0; r < 16; ++r) { acc[0][r] = 0.f; acc[1][r] = 0.f; }
    SCHED();
    WAIT_VM(1);
    STAMP(24);
    st_sc1(rws, f3w, 0, u32x4{ta, ta, tb, tb});
    STAMP(25);
    BARRIER();
    constexpr int NW = G2_NW;
    u32x4 hf[NW], wf[3][2];
#define RD_W2C(i, c) do { LAS char* a_ = aw[w2_unit((i) >> 2) >> 2][c] + (w2_unit((i) >> 2) & 3) * UNIT + ((i) & 3) * 2048; wf[(i) % 3][c] = tr_frag(a_, a_ + 1024); } while (0)
#define RD_W2(i) do { RD_W2C(i, 0); RD_W2C(i, 1); } while (0)
    RD_W2(0); RD_W2(1);
    const int sh = WS_H + ((tbk * 2 + wt) * 112 + 28 * kh) * 1024;
#define LD_H(i) hf[(i) % NW] = ld_sc1(rws, lane * 16 + ((i) & 3) * 1024, sh + (((i) >> 2) + (i) / 28 * 7) * 4096)
    const int f3o = WS_F3 + ((tbk >> 1) * 4 + (tbk & 1)) * 2048 + kh * 896 + ((lane & 1) * 32 + min(lane >> 1, 6)) * 128;
    if (F3_DELAY) __builtin_amdgcn_s_sleep(F3_DELAY);
    poll16(rws, f3o, 0, ta, tb);
    u32x4 f2;
    STAMP(26);
#pragma unroll
    for (int i = 0; i < G2_N0; ++i) LD_H(i);
    SCHED();
    f32x2 xr[8], gb[8];
#pragma unroll
    for (int i = 0; i < 8; ++i) {
      const int pr = 4 * (i >> 1) + 2 * h + (i & 1);
      xr[i] = up(*(LAS const u32*)(smem + LD_MISC + 2048 + w * 2048 + ((tl >> 2) & 1) * 1024 + pr * 64 + (tl >> 3) * 16 + (tl & 3) * 4));
      gb[i] = *(LAS const f32x2*)(smem + LD_MISC + 1536 + (kh * 16 + pr) * 8);
    }
#pragma unroll
    for (int i = 0; i < 8; ++i) PIN(xr[i]);
    LAS f32x4* exw = (LAS f32x4*)(smem + w2_unit(5) * UNIT) + w * 256 + lane;
    LAS f32x4* exr = (LAS f32x4*)(smem + w2_unit(5) * UNIT) + (w ^ 2) * 256 + lane;
    LAS char* otw = smem + w2_unit(6) * UNIT + (wt * 32 + tl) * 144 + kh * 64 + h * 8;
    LAS char* otr = smem + w2_unit(6) * UNIT + (tid >> 3) * 144 + (tid & 7) * 16;
    PIN(exw); PIN(exr); PIN(otw); PIN(otr);
    SCHED();
#define G2_STEP(i) do { \
      SCHED(); acc[0] = mfma32(wf[(i) % 3][0], hf[(i) % NW], acc[0]); SCHED(); \
      if ((i) == 0) STAMP(29); \
      if ((i) == 18) STAMP(30); \
      if ((i) == 42) STAMP(31); \
      if ((i) == 6 || (i) == 18 || (i) == 42) BARRIER(); \
      if ((i) + 2 < 56) RD_W2C((i) + 2, 0); \
      if ((i) == P2) f2 = ld_sc1(rws, f3o, 0x20000); \
      if ((i) > 0 && (i) - 1 + NW < 56) LD_H((i) - 1 + NW); \
      SCHED(); acc[1] = mfma32(wf[(i) % 3][1], hf[(i) % NW], acc[1]); SCHED(); \
      if ((i) + 2 < 56) RD_W2C((i) + 2, 1); \
      asm volatile("" :: "v"(wf[(i) % 3][0]), "v"(wf[(i) % 3][1]), "v"(hf[(i) % NW])); } while (0)
#pragma unroll
    for (int i = 0; i < 29 - NW; ++i) G2_STEP(i);
    if (__builtin_expect(__any(tagdiff(f2, ta, tb) != 0u), 0)) poll16(rws, f3o, 0x20000, ta, tb);
    FENCE();
#pragma unroll
    for (int i = 29 - NW; i < 54; ++i) G2_STEP(i);
    SCHED();
    acc[1] = mfma32(wf[54 % 3][1], hf[54 % NW], acc[1]);
    acc[1] = mfma32(wf[55 % 3][1], hf[55 % NW], acc[1]);
    SCHED();
    acc[0] = mfma32(wf[54 % 3][0], hf[54 % NW], acc[0]);
    acc[0] = mfma32(wf[55 % 3][0], hf[55 % NW], acc[0]);
    SCHED();
    STAMP(27);
#pragma unroll
    for (int qq = 0; qq < 4; ++qq) exw[qq * 64] = f32x4{acc[1][4 * qq], acc[1][4 * qq + 1], acc[1][4 * qq + 2], acc[1][4 * qq + 3]};
    SCHED();
#pragma unroll
    for (int i = 0; i < 8; ++i) { xr[i] += f32x2{acc[0][2 * i], acc[0][2 * i + 1]} * gb[i]; PIN(xr[i]); }
    BARRIER();
    f32x4 oth[4];
#pragma unroll
    for (int qq = 0; qq < 4; ++qq) oth[qq] = exr[qq * 64];
    SCHED();
#pragma unroll
    for (int qq = 0; qq < 4; ++qq)
      *(LAS u32x2*)(otw + qq * 16) = u32x2{pk2(f32x2{oth[qq][0], oth[qq][1]} * gb[2 * qq] + xr[2 * qq]), pk2(f32x2{oth[qq][2], oth[qq][3]} * gb[2 * qq + 1] + xr[2 * qq + 1])};
    BARRIER();
#pragma unroll
    for (int i = 0; i < 2; ++i) __builtin_amdgcn_raw_buffer_store_b128(*(LAS const u32x4*)(otr + i * 32 * 144), ro, oo[i], 0, OUT_AUX);
    STAMP(28);
    STAMP_FLUSH(p.ws, 0);
  }
}

extern "C" __global__ void fused_block_end() {}

extern "C" void kernel_launch(void* const* d_in, const int* in_sizes, int n_in, void* d_out, int out_size, void* d_ws, size_t ws_size,
                              hipStream_t stream) {
  Params p;
  p.x = (const u16*)d_in[0]; p.Are = (const u16*)d_in[1]; p.Aim = (const u16*)d_in[2]; p.Bre = (const u16*)d_in[3];
  p.Bim = (const u16*)d_in[4]; p.Cre = (const u16*)d_in[5]; p.Cim = (const u16*)d_in[6]; p.ldt = (const u16*)d_in[7];
  p.dskip = (const u16*)d_in[8]; p.w1 = (const u16*)d_in[10]; p.w2 = (const u16*)d_in[11]; p.c = (const u16*)d_in[12];
  p.wmod = (const u16*)d_in[13]; p.out = (u16*)d_out; p.ws = (char*)d_ws;
  (void)hipFuncSetAttribute((const void*)fused_block, hipFuncAttributeMaxDynamicSharedMemorySize, LDS_BYTES + ST_LDS_EXTRA);
  fused_block<<<256, 512, LDS_BYTES + ST_LDS_EXTRA, stream>>>(p);
}
```
